```python
import torch

def _softmax(x, axis=-1):
    return torch.softmax(x, dim=axis)
def _gelu(x):
    return torch.nn.functional.gelu(x, approximate="tanh")
def _silu(x):
    return torch.nn.functional.silu(x)
def _sigmoid(x):
    return torch.sigmoid(x)
def _layernorm(x, axis=-1, eps=1e-5):
    m = x.mean(dim=axis, keepdim=True)
    v = x.var(dim=axis, keepdim=True, unbiased=False)
    return (x - m) / torch.sqrt(v + eps)
def _scatter_add(tgt, idx, src, axis=0):
    return tgt.clone().index_add_(axis, idx, src)
def _segment_sum(src, seg, n):
    out = torch.zeros((n,) + src.shape[1:], dtype=src.dtype)
    return out.index_add_(0, seg, src)
def _pad(x, pw):
    flat = tuple(v for pair in reversed(pw) for v in pair)
    return torch.nn.functional.pad(x, flat)

kernel_name = 'recurrent_scan__compressed_kv_cla__dense_activation_squared_relu__prompt'

def setup_inputs():

    _w = (lambda a: (lambda t: t.to(torch.bfloat16) if t.is_floating_point() else t)(torch.as_tensor(a)))
    def _A_setup():
        B, H, Sq, Sk, D, M = 1, 16, 2048, 2048, 256, 448
        Dc = 4 * D
        return {
            "x": _w((lambda *s: torch.randn(*s, dtype=torch.bfloat16) * 0.05)(B, Sq, M)),
            "c_kv": _w((lambda *s: torch.randn(*s, dtype=torch.bfloat16) * 0.2)(B, Sk, Dc)),
            "w_q": _w((lambda *s: torch.randn(*s, dtype=torch.bfloat16) * 3.2)(M, H, D) * (M ** -0.5)),
            "w_uk": _w((lambda *s: torch.randn(*s, dtype=torch.bfloat16) * 3.2)(Dc, H, D) * (Dc ** -0.5)),
            "w_uv": _w((lambda *s: torch.randn(*s, dtype=torch.bfloat16) * 0.2)(Dc, H, D) * (Dc ** -0.5)),
            "w_o": _w((lambda *s: torch.randn(*s, dtype=torch.bfloat16) * 0.2)(H, D, M) * ((H * D) ** -0.5)),
        }
    _A_in = _A_setup()

    def _B_setup():
        B, S, D, F = 1, 2048, 448, 1792
        return {
            "x": _w((lambda *s: torch.randn(*s, dtype=torch.bfloat16) * 0.05)(B, S, D)),
            "w1": _w((lambda *s: torch.randn(*s, dtype=torch.bfloat16) * 0.1)(D, F)),
            "b1": _w((lambda *s: torch.randn(*s, dtype=torch.bfloat16) * 0.8)(F)),
            "w2": _w((lambda *s: torch.randn(*s, dtype=torch.bfloat16) * 0.1)(F, D)),
            "b2": _w((lambda *s: torch.randn(*s, dtype=torch.bfloat16) * 6.4)(D)),
        }
    _B_in = _B_setup()

    return {
        "A_x": _A_in["x"],
        "A_c_kv": _A_in["c_kv"],
        "A_w_q": _A_in["w_q"],
        "A_w_uk": _A_in["w_uk"],
        "A_w_uv": _A_in["w_uv"],
        "A_w_o": _A_in["w_o"],
        "B_x": _B_in["x"],
        "B_w1": _B_in["w1"],
        "B_b1": _B_in["b1"],
        "B_w2": _B_in["w2"],
        "B_b2": _B_in["b2"],
    }

def reference(A_x, A_c_kv, A_w_q, A_w_uk, A_w_uv, A_w_o, B_x, B_w1, B_b1, B_w2, B_b2):
    def _A_ref(x, c_kv, w_q, w_uk, w_uv, w_o):
        B, Sq, M = x.shape[0], x.shape[1], x.shape[2]
        Sk = c_kv.shape[1]
        H, D = w_q.shape[1], w_q.shape[2]
        q = torch.einsum("bqm,mhd->bhqd", x, w_q)
        k = torch.einsum("bkc,chd->bhkd", c_kv, w_uk)
        v = torch.einsum("bkc,chd->bhkd", c_kv, w_uv)
        scores = torch.einsum("bhqd,bhkd->bhqk", q, k) * (D ** -0.5)
        off = Sk - Sq
        qi = torch.arange(Sq)[:, None]
        kj = torch.arange(Sk)[None, :]
        cmask = kj <= (qi + off)
        scores = torch.where(cmask[None, None], scores, -1e9)
        p = _softmax(scores, axis=-1)
        ctx = torch.einsum("bhqk,bhkd->bhqd", p, v)
        return torch.einsum("bhqd,hdm->bqm", ctx, w_o)

    def _B_ref(x, w1, b1, w2, b2):
        h = torch.einsum("bsd,df->bsf", x, w1) + b1
        zero = torch.zeros_like(h)
        r = torch.where(h > zero, h, zero)
        h = r * r
        out = torch.einsum("bsf,fd->bsd", h, w2) + b2
        return out

    x = A_x
    xn = _layernorm(x, axis=-1)
    s = _A_ref(xn, A_c_kv, A_w_q, A_w_uk, A_w_uv, A_w_o)
    g = _silu(_B_ref(xn, B_w1, B_b1, B_w2, B_b2))
    return x + s * g
```

```cpp
#include <hip/hip_runtime.h>
#include <hip/hip_bf16.h>
typedef unsigned short u16;
__device__ __forceinline__ float bf2f(u16 v){ return __uint_as_float(((unsigned)v)<<16); }
__device__ __forceinline__ u16 f2bf(float f){ unsigned u=__float_as_uint(f); u += 0x7FFF + ((u>>16)&1); return (u16)(u>>16); }
__device__ __forceinline__ float rbf(float f){ return bf2f(f2bf(f)); }
#define LN_MIMIC 1
constexpr int S=2048, M=448, DC=1024, H=16, D=256, F=1792;
__global__ void ln_kernel(const u16* x, u16* xn){
  int row=blockIdx.x; int l=threadIdx.x;
  float v[7]; float s=0;
  for(int i=0;i<7;i++){ v[i]=bf2f(x[row*M+l+64*i]); s+=v[i]; }
  for(int o=32;o>0;o>>=1) s+=__shfl_xor(s,o);
  float mean=s/M; float q=0;
  for(int i=0;i<7;i++){ float d=v[i]-mean; q+=d*d; }
  for(int o=32;o>0;o>>=1) q+=__shfl_xor(q,o);
  float var=q/M;
  float mb=rbf(mean), vb=rbf(var); float sd=rbf(sqrtf(rbf(vb+1e-5f)));
  for(int i=0;i<7;i++){ xn[row*M+l+64*i]=f2bf(rbf(v[i]-mb)/sd); }
}
template<int EPI> __global__ void gemm(const u16* A, const u16* B, u16* C, int Mm, int N, int K, const u16* bias, const u16* aux0, const u16* aux1){
  __shared__ float As[16][65], Bs[16][65];
  int tx=threadIdx.x&15, ty=threadIdx.x>>4;
  int bm=blockIdx.y*64, bn=blockIdx.x*64;
  float acc[4][4]={};
  for(int k0=0;k0<K;k0+=16){
    for(int i=threadIdx.x;i<64*16;i+=256){ int r=i>>4,c=i&15; As[c][r]=bf2f(A[(long)(bm+r)*K+k0+c]); }
    for(int i=threadIdx.x;i<64*16;i+=256){ int r=i>>6,c=i&63; Bs[r][c]=bf2f(B[(long)(k0+r)*N+bn+c]); }
    __syncthreads();
    for(int k=0;k<16;k++){
      float a[4],b[4];
      for(int i=0;i<4;i++){a[i]=As[k][ty*4+i]; b[i]=Bs[k][tx*4+i];}
      for(int i=0;i<4;i++)for(int j=0;j<4;j++)acc[i][j]+=a[i]*b[j];
    }
    __syncthreads();
  }
  for(int i=0;i<4;i++)for(int j=0;j<4;j++){
    int r=bm+ty*4+i,c=bn+tx*4+j; float v=acc[i][j];
    if(EPI==1){ v+=bf2f(bias[c]); v=v>0?v:0; v=v*v; }
    if(EPI==2){ v+=bf2f(bias[c]); v=v/(1.f+__expf(-v)); }
    if(EPI==3){ v=bf2f(aux0[(long)r*N+c])+v*bf2f(aux1[(long)r*N+c]); }
    C[(long)r*N+c]=f2bf(v);
  }
}
__global__ void attn(const u16* Q, const u16* Kk, const u16* V, u16* ctx){
  int q=blockIdx.x, h=blockIdx.y, t=threadIdx.x;
  __shared__ float qs[D]; __shared__ float p[S]; __shared__ float red[256];
  qs[t]=bf2f(Q[(long)q*H*D+h*D+t]); __syncthreads();
  float mx=-1e30f;
  for(int k=t;k<=q;k+=256){ float s=0; const u16* kr=Kk+(long)k*H*D+h*D; for(int d=0;d<D;d++) s+=qs[d]*bf2f(kr[d]); s*=0.0625f; p[k]=s; mx=fmaxf(mx,s);}
  red[t]=mx; __syncthreads();
  for(int o=128;o>0;o>>=1){ if(t<o) red[t]=fmaxf(red[t],red[t+o]); __syncthreads(); }
  mx=red[0]; __syncthreads();
  float sum=0;
  for(int k=t;k<=q;k+=256){ float e=__expf(p[k]-mx); p[k]=e; sum+=e; }
  red[t]=sum; __syncthreads();
  for(int o=128;o>0;o>>=1){ if(t<o) red[t]+=red[t+o]; __syncthreads(); }
  sum=red[0];
  float o=0;
  for(int k=0;k<=q;k++) o+=rbf(p[k])*bf2f(V[(long)k*H*D+h*D+t]);
  ctx[(long)q*H*D+h*D+t]=f2bf(o/sum);
}
extern "C" void kernel_launch(void* const* d_in, const int* in_sizes, int n_in, void* d_out, int out_size, void* d_ws, size_t ws_size, hipStream_t stream){
  const u16* x=(const u16*)d_in[0]; const u16* ckv=(const u16*)d_in[1]; const u16* wq=(const u16*)d_in[2]; const u16* wuk=(const u16*)d_in[3];
  const u16* wuv=(const u16*)d_in[4]; const u16* wo=(const u16*)d_in[5]; const u16* w1=(const u16*)d_in[7]; const u16* b1=(const u16*)d_in[8];
  const u16* w2=(const u16*)d_in[9]; const u16* b2=(const u16*)d_in[10];
  u16* ws=(u16*)d_ws; u16* xn=ws; u16* Q=xn+S*M; u16* Kk=Q+S*H*D; u16* V=Kk+S*H*D; u16* ctx=V+S*H*D; u16* Hh=ctx+S*H*D; u16* G=Hh+S*F;
  ln_kernel<<<S,64,0,stream>>>(x,xn);
  gemm<0><<<dim3(H*D/64,S/64),256,0,stream>>>(xn,wq,Q,S,H*D,M,nullptr,nullptr,nullptr);
  gemm<0><<<dim3(H*D/64,S/64),256,0,stream>>>(ckv,wuk,Kk,S,H*D,DC,nullptr,nullptr,nullptr);
  gemm<0><<<dim3(H*D/64,S/64),256,0,stream>>>(ckv,wuv,V,S,H*D,DC,nullptr,nullptr,nullptr);
  gemm<1><<<dim3(F/64,S/64),256,0,stream>>>(xn,w1,Hh,S,F,M,b1,nullptr,nullptr);
  gemm<2><<<dim3(M/64,S/64),256,0,stream>>>(Hh,w2,G,S,M,F,b2,nullptr,nullptr);
  attn<<<dim3(S,H),256,0,stream>>>(Q,Kk,V,ctx);
  gemm<3><<<dim3(M/64,S/64),256,0,stream>>>(ctx,wo,(u16*)d_out,S,M,H*D,nullptr,x,G);
}
```

```cpp
#include <hip/hip_runtime.h>
struct MkArgs { const void* x; const void* ckv; const void* wq; const void* wuk; const void* wuv; const void* wo; const void* w1; const void* b1; const void* w2; const void* b2; void* out; void* ws; };
__global__ __launch_bounds__(256, 1) void mk_kernel(MkArgs a) {
  __shared__ __attribute__((aligned(16))) char lds[163840];
  unsigned wid = __builtin_amdgcn_readfirstlane(threadIdx.x >> 6);
  asm volatile(
R"A(.macro MA d,a,b
v_mfma_f32_16x16x32_bf16 v[\d:\d+3], a[\a:\a+3], a[\b:\b+3], v[\d:\d+3]
.endm
.macro MB d,a,b
v_mfma_f32_16x16x32_bf16 v[\d:\d+3], a[\a:\a+3], v[\b:\b+3], v[\d:\d+3]
.endm
.macro MZ d,a,b
v_mfma_f32_16x16x32_bf16 v[\d:\d+3], a[\a:\a+3], a[\b:\b+3], 0
.endm
.macro TR d,v,o
ds_read_b64_tr_b16 a[\d:\d+1], v\v offset:\o
.endm
.macro RD d,v,o
ds_read_b128 a[\d:\d+3], v\v offset:\o
.endm
.macro WL n
s_waitcnt lgkmcnt(\n)
.endm
.macro PK d,a,b
v_cvt_pk_bf16_f32 v\d, v\a, v\b
.endm
s_load_dwordx16 s[24:39], %0, 0x0
s_load_dwordx8 s[40:47], %0, 0x40
s_mov_b32 s8, %2
s_mov_b32 s9, %1
s_and_b32 s9, %1, 6
s_bfe_u32 s50, %1, 0x10004
s_or_b32 s9, s9, s50
s_and_b32 s50, %1, 1
s_lshl_b32 s50, s50, 3
s_or_b32 s9, s9, s50
s_lshr_b32 s50, %1, 5
s_lshl_b32 s50, s50, 4
s_or_b32 s9, s9, s50
s_and_b32 s50, %1, 8
s_lshl_b32 s50, s50, 4
s_or_b32 s9, s9, s50
v_mbcnt_lo_u32_b32 v100, -1, 0
v_mbcnt_hi_u32_b32 v100, -1, v100
v_lshlrev_b32 v253, 4, v100
s_getpc_b64 s[76:77]
.LCPC:
s_lshr_b32 s50, %1, 3
s_lshl_b32 s50, s50, 2
s_add_u32 s50, s50, s8
s_mul_i32 s50, s50, 1408
s_min_u32 s50, s50, .LCEND-.LCPC-1408
v_min_u32 v101, 0xa0, v253
v_lshl_add_u32 v101, v101, 3, s50
global_load_dword a129, v101, s[76:77]
s_and_b32 s80, s9, 7
s_lshr_b32 s52, s9, 7
s_lshl_b32 s10, s80, 1
s_add_u32 s10, s10, s52
s_bfe_u32 s11, s9, 0x30004
s_lshl_b32 s81, s52, 3
s_add_u32 s81, s81, s11
s_lshl_b32 s56, s8, 10
s_lshl_b32 s5, s9, 2
s_add_u32 s5, s5, s8
s_lshl_b32 s5, s5, 7
s_add_u32 s5, s5, 0x7020000
s_lshl_b32 s50, s9, 3
s_lshl_b32 s51, s8, 1
s_add_u32 s50, s50, s51
v_lshrrev_b32 v101, 5, v100
v_add_u32 v101, s50, v101
v_bfe_u32 v102, v100, 1, 4
v_and_b32 v103, 1, v100
s_movk_i32 s51, 896
v_mul_u32_u24 v110, s51, v101
v_lshl_add_u32 v110, v102, 6, v110
v_lshl_add_u32 v110, v103, 4, v110
v_and_b32 v104, 31, v100
v_lshlrev_b32 v111, 8, v104
v_xor_b32 v104, v101, v100
v_and_b32 v104, 7, v104
v_lshl_add_u32 v111, v104, 4, v111
v_add_u32 v111, 0x14000, v111
v_cmp_gt_u32 vcc, 14, v102
v_mov_b32 v104, 0x7f000000
s_nop 0
v_cndmask_b32 v110, v104, v110, vcc
v_lshrrev_b32 v101, 3, v100
s_lshl_b32 s50, s8, 3
v_add_u32 v101, s50, v101
v_lshlrev_b32 v250, 11, v101
s_and_b32 s50, s8, 1
s_lshl_b32 s50, s50, 2
v_lshrrev_b32 v101, 4, v100
v_add_u32 v101, s50, v101
v_and_b32 v102, 7, v100
v_xor_b32 v101, v101, v102
v_lshl_add_u32 v250, v101, 4, v250
s_lshr_b32 s50, s8, 1
s_lshl_b32 s50, s50, 3
s_and_b32 s51, s8, 1
s_lshl_b32 s51, s51, 1
s_add_u32 s50, s50, s51
v_lshrrev_b32 v101, 5, v100
v_add_u32 v102, s50, v101
s_lshl_b32 s50, s8, 1
v_add_u32 v101, s50, v101
v_bfe_u32 v103, v100, 1, 4
v_xor_b32 v103, v101, v103
v_lshlrev_b32 v103, 5, v103
v_and_b32 v104, 1, v100
v_lshl_add_u32 v103, v104, 4, v103
v_lshl_add_u32 v251, v102, 13, v103
s_bfe_u32 s53, s9, 0x10003
WL 0
s_mov_b32 s16, s24
s_and_b32 s17, s25, 0xffff
s_mov_b32 s18, 1835008
s_mov_b32 s19, 0x20000
buffer_load_dwordx4 v[112:115], v110, s[16:19], 0 offen
buffer_load_dwordx4 v[116:119], v110, s[16:19], 0 offen offset:32
s_mov_b32 s12, s26
s_and_b32 s13, s27, 0xffff
s_mov_b32 s14, 4194304
s_mov_b32 s15, 0x20000
s_mov_b32 s20, s46
s_and_b32 s21, s47, 0xffff
s_mov_b32 s22, 134217728
s_mov_b32 s23, 0x20000
s_cmp_eq_u32 s53, 0
s_cselect_b32 s16, s30, s32
s_cselect_b32 s17, s31, s33
s_and_b32 s17, s17, 0xffff
s_mov_b32 s18, 8388608
s_lshl_b32 s62, s11, 19
s_lshl_b32 s63, s10, 9
s_add_u32 s54, s46, 0x7000000
s_addc_u32 s55, s47, 0
s_load_dwordx2 s[48:49], s[54:55], 0x0
s_add_u32 m0, s56, 98304
s_add_u32 s50, s63, 0x0
buffer_load_dwordx4 v251, s[16:19], s50 offen nt lds
s_add_u32 m0, s56, 102400
s_add_u32 s50, s63, 0x20000
buffer_load_dwordx4 v251, s[16:19], s50 offen nt lds
s_add_u32 m0, s56, 106496
s_add_u32 s50, s63, 0x8000
buffer_load_dwordx4 v251, s[16:19], s50 offen nt lds
s_add_u32 m0, s56, 110592
s_add_u32 s50, s63, 0x28000
buffer_load_dwordx4 v251, s[16:19], s50 offen nt lds
s_add_u32 m0, s56, 114688
s_add_u32 s50, s63, 0x40000
buffer_load_dwordx4 v251, s[16:19], s50 offen nt lds
s_add_u32 m0, s56, 118784
s_add_u32 s50, s63, 0x60000
buffer_load_dwordx4 v251, s[16:19], s50 offen nt lds
s_add_u32 m0, s56, 122880
s_add_u32 s50, s63, 0x48000
buffer_load_dwordx4 v251, s[16:19], s50 offen nt lds
s_add_u32 m0, s56, 126976
s_add_u32 s50, s63, 0x68000
buffer_load_dwordx4 v251, s[16:19], s50 offen nt lds
s_add_u32 m0, s56, 0
s_add_u32 s50, s62, 0x0
buffer_load_dwordx4 v250, s[12:15], s50 offen lds
s_lshr_b32 s50, s8, 1
s_lshl_b32 s50, s50, 14
v_and_b32 v101, 15, v100
v_lshlrev_b32 v248, 7, v101
v_add_u32 v248, s50, v248
v_lshrrev_b32 v101, 1, v101
v_lshrrev_b32 v102, 4, v100
v_xor_b32 v101, v101, v102
v_lshl_add_u32 v248, v101, 4, v248
v_xor_b32 v249, 64, v248
v_lshrrev_b32 v101, 2, v100
v_and_b32 v102, 7, v101
v_lshlrev_b32 v101, 9, v101
v_and_b32 v103, 3, v100
v_lshl_add_u32 v101, v103, 3, v101
s_and_b32 s50, s8, 1
s_lshl_b32 s50, s50, 8
s_add_u32 s50, s50, 98304
v_add_u32 v101, s50, v101
v_xor_b32 v103, 0, v102
v_lshl_add_u32 v240, v103, 5, v101
v_xor_b32 v103, 1, v102
v_lshl_add_u32 v241, v103, 5, v101
v_xor_b32 v103, 2, v102
v_lshl_add_u32 v242, v103, 5, v101
v_xor_b32 v103, 3, v102
v_lshl_add_u32 v243, v103, 5, v101
v_xor_b32 v103, 4, v102
v_lshl_add_u32 v244, v103, 5, v101
v_xor_b32 v103, 5, v102
v_lshl_add_u32 v245, v103, 5, v101
v_xor_b32 v103, 6, v102
v_lshl_add_u32 v246, v103, 5, v101
v_xor_b32 v103, 7, v102
v_lshl_add_u32 v247, v103, 5, v101
s_mov_b32 s71, 0x20000
s_cmp_eq_u32 s53, 0
s_cbranch_scc0 .LTA
s_mov_b32 s68, s42
s_and_b32 s69, s43, 0xffff
s_mov_b32 s70, 896
v_lshlrev_b32 v101, 3, v253
buffer_load_dword a132, v101, s[68:71], 0 offen
s_mov_b32 s68, s28
s_and_b32 s69, s29, 0xffff
s_mov_b32 s70, 3670016
s_lshl_b32 s50, s11, 2
s_add_u32 s50, s50, s8
s_mul_i32 s50, s50, 114688
s_add_u32 s50, s50, s63
s_movk_i32 s51, 0x2000
s_mov_b32 s66, 0x4000
s_movk_i32 s52, 8
s_sub_u32 s67, 7, s11
s_branch .LTD
.LTA:
s_mov_b32 s68, s38
s_and_b32 s69, s39, 0xffff
s_mov_b32 s70, 3584
v_min_u32 v101, 0x30, v253
s_lshl_b32 s50, s80, 9
v_lshl_add_u32 v101, v101, 3, s50
buffer_load_dword a132, v101, s[68:71], 0 offen
s_sub_u32 s67, 15, s81
s_min_u32 s67, s67, s81
s_movk_i32 s51, 512
s_movk_i32 s66, 2048
s_movk_i32 s52, 16
s_cmp_ge_u32 s8, 2
s_cbranch_scc1 .LTA2
s_cmp_eq_u32 s8, 1
s_cbranch_scc1 .LTA1
s_mov_b32 s68, s36
s_and_b32 s69, s37, 0xffff
s_mov_b32 s70, 1605632
s_mul_i32 s51, s81, 100352
s_add_u32 s50, s50, s51
s_movk_i32 s51, 3584
s_mov_b32 s66, 14336
s_branch .LTD
.LTA1:
s_mov_b32 s68, s40
s_and_b32 s69, s41, 0xffff
s_mov_b32 s70, 1605632
s_lshl_b32 s50, s80, 4
s_add_u32 s50, s50, s81
s_mul_i32 s50, s50, 14336
s_branch .LTD
.LTA2:
s_mov_b32 s68, s34
s_and_b32 s69, s35, 0xffff
s_mov_b32 s70, 3670016
s_lshl_b32 s50, s10, 3
s_add_u32 s50, s50, s11
s_lshl_b32 s50, s50, 1
s_add_u32 s50, s50, s8
s_sub_u32 s50, s50, 2
s_mul_i32 s50, s50, 14336
.LTD:
s_lshr_b32 s54, s81, 1
s_cmp_eq_u32 s54, s80
s_cselect_b32 s55, 7, s80
s_cmp_eq_u32 s80, 7
s_cselect_b32 s80, s54, s55
v_lshrrev_b32 v101, 2, v100
v_mul_u32_u24 v252, s51, v101
v_and_b32 v101, 3, v100
v_lshl_add_u32 v252, v101, 7, v252
v_add_u32 v252, s50, v252
v_cmp_gt_u32 vcc, s52, v100
v_mov_b32 v101, 0x7f000000
s_lshr_b32 s67, s67, 1
s_lshl_b32 s67, s67, 10
s_add_u32 s67, s67, 0x7001000
v_cndmask_b32 v252, v101, v252, vcc
s_lshr_b32 s50, s9, 4
s_sub_u32 s51, 15, s50
s_min_u32 s51, s51, s50
s_lshl_b32 s51, s51, 1
s_lshr_b32 s50, s50, 3
s_add_u32 s51, s51, s50
s_lshl_b32 s51, s51, 4
s_and_b32 s50, s9, 15
s_add_u32 s51, s51, s50
s_lshl_b32 s72, s51, 4
s_add_u32 s72, s72, 0x7001000
s_cmp_eq_u32 s8, 0
s_cselect_b32 s72, s72, s5
s_add_u32 s50, s56, 0x14000
v_add_u32 v255, s50, v253
s_lshr_b32 s50, s9, 1
s_mul_i32 s50, s50, 14336
s_and_b32 s51, s9, 1
s_lshl_b32 s51, s51, 7
s_add_u32 s50, s50, s51
s_lshl_b32 s51, s56, 1
s_add_u32 s50, s50, s51
v_and_b32 v109, 0x380, v253
v_and_b32 v108, 0x70, v250
v_lshl_add_u32 v109, v109, 1, v108
v_add_u32 v254, s50, v109
s_mov_b32 s74, 0x2000000
s_mov_b32 s75, 0x2002000
s_cmp_eq_u32 s8, 3
s_cselect_b32 s75, 0x3800000, s75
s_lshr_b32 s50, s9, 2
s_add_u32 s50, s50, s8
s_lshl_b32 s50, s50, 6
s_add_u32 s50, s50, 0x7014000
s_and_b32 s51, s9, 15
s_cselect_b32 s50, 0x7f000000, s50
v_mov_b32 v90, s50
v_mov_b32 v91, 0
s_waitcnt vmcnt(10)
s_add_u32 m0, s56, 4096
s_add_u32 s50, s62, 0x10000
buffer_load_dwordx4 v250, s[12:15], s50 offen lds
v_lshlrev_b32 v224, 16, v112
v_and_b32 v225, 0xffff0000, v112
v_lshlrev_b32 v226, 16, v113
v_and_b32 v227, 0xffff0000, v113
v_lshlrev_b32 v228, 16, v114
v_and_b32 v229, 0xffff0000, v114
v_lshlrev_b32 v230, 16, v115
s_add_u32 m0, s56, 8192
s_add_u32 s50, s62, 0x20000
buffer_load_dwordx4 v250, s[12:15], s50 offen lds
v_and_b32 v231, 0xffff0000, v115
v_lshlrev_b32 v232, 16, v116
v_and_b32 v233, 0xffff0000, v116
v_lshlrev_b32 v234, 16, v117
v_and_b32 v235, 0xffff0000, v117
v_lshlrev_b32 v236, 16, v118
s_add_u32 m0, s56, 12288
s_add_u32 s50, s62, 0x30000
buffer_load_dwordx4 v250, s[12:15], s50 offen lds
v_and_b32 v237, 0xffff0000, v118
v_lshlrev_b32 v238, 16, v119
v_and_b32 v239, 0xffff0000, v119
v_add_f32 v96, v224, v225
v_add_f32 v96, v96, v226
v_add_f32 v96, v96, v227
v_add_f32 v96, v96, v228
s_add_u32 m0, s56, 16384
s_add_u32 s50, s62, 0x40000
buffer_load_dwordx4 v250, s[12:15], s50 offen lds
v_add_f32 v96, v96, v229
v_add_f32 v96, v96, v230
v_add_f32 v96, v96, v231
v_add_f32 v96, v96, v232
v_add_f32 v96, v96, v233
v_add_f32 v96, v96, v234
s_add_u32 m0, s56, 20480
s_add_u32 s50, s62, 0x50000
buffer_load_dwordx4 v250, s[12:15], s50 offen lds
v_add_f32 v96, v96, v235
v_add_f32 v96, v96, v236
v_add_f32 v96, v96, v237
v_add_f32 v96, v96, v238
v_add_f32 v96, v96, v239
s_nop 1
v_add_f32_dpp v96, v96, v96 quad_perm:[1,0,3,2] row_mask:0xf bank_mask:0xf
s_add_u32 m0, s56, 24576
s_add_u32 s50, s62, 0x60000
buffer_load_dwordx4 v250, s[12:15], s50 offen lds
s_nop 1
v_add_f32_dpp v96, v96, v96 quad_perm:[2,3,0,1] row_mask:0xf bank_mask:0xf
s_nop 1
v_add_f32_dpp v96, v96, v96 row_half_mirror row_mask:0xf bank_mask:0xf
s_nop 1
v_add_f32_dpp v96, v96, v96 row_mirror row_mask:0xf bank_mask:0xf
s_add_u32 m0, s56, 28672
s_add_u32 s50, s62, 0x70000
buffer_load_dwordx4 v250, s[12:15], s50 offen lds
v_mov_b32 v97, v96
s_nop 1
v_permlane16_swap_b32 v96, v97
v_add_f32 v96, v96, v97
v_mul_f32 v96, 0x3b124925, v96
v_mov_b32 v98, 0
s_mov_b32 exec_lo, 0x0fffffff
s_mov_b32 exec_hi, 0x0fffffff
v_sub_f32 v99, v224, v96
v_fmac_f32 v98, v99, v99
v_sub_f32 v99, v225, v96
v_fmac_f32 v98, v99, v99
v_sub_f32 v99, v226, v96
v_fmac_f32 v98, v99, v99
v_sub_f32 v99, v227, v96
v_fmac_f32 v98, v99, v99
v_sub_f32 v99, v228, v96
v_fmac_f32 v98, v99, v99
v_sub_f32 v99, v229, v96
v_fmac_f32 v98, v99, v99
v_sub_f32 v99, v230, v96
v_fmac_f32 v98, v99, v99
v_sub_f32 v99, v231, v96
v_fmac_f32 v98, v99, v99
v_sub_f32 v99, v232, v96
v_fmac_f32 v98, v99, v99
v_sub_f32 v99, v233, v96
v_fmac_f32 v98, v99, v99
v_sub_f32 v99, v234, v96
v_fmac_f32 v98, v99, v99
v_sub_f32 v99, v235, v96
v_fmac_f32 v98, v99, v99
v_sub_f32 v99, v236, v96
v_fmac_f32 v98, v99, v99
v_sub_f32 v99, v237, v96
v_fmac_f32 v98, v99, v99
v_sub_f32 v99, v238, v96
v_fmac_f32 v98, v99, v99
v_sub_f32 v99, v239, v96
v_fmac_f32 v98, v99, v99
s_mov_b64 exec, -1
s_add_u32 m0, s56, 131072
s_add_u32 s50, s63, 0x80000
buffer_load_dwordx4 v251, s[16:19], s50 offen nt lds
s_nop 1
v_add_f32_dpp v98, v98, v98 quad_perm:[1,0,3,2] row_mask:0xf bank_mask:0xf
s_nop 1
s_add_u32 m0, s56, 135168
s_add_u32 s50, s63, 0xa0000
buffer_load_dwordx4 v251, s[16:19], s50 offen nt lds
v_add_f32_dpp v98, v98, v98 quad_perm:[2,3,0,1] row_mask:0xf bank_mask:0xf
s_nop 1
v_add_f32_dpp v98, v98, v98 row_half_mirror row_mask:0xf bank_mask:0xf
s_add_u32 m0, s56, 139264
s_add_u32 s50, s63, 0x88000
buffer_load_dwordx4 v251, s[16:19], s50 offen nt lds
s_nop 1
v_add_f32_dpp v98, v98, v98 row_mirror row_mask:0xf bank_mask:0xf
v_mov_b32 v97, v98
s_add_u32 m0, s56, 143360
s_add_u32 s50, s63, 0xa8000
buffer_load_dwordx4 v251, s[16:19], s50 offen nt lds
s_nop 1
v_permlane16_swap_b32 v98, v97
v_add_f32 v98, v98, v97
s_add_u32 m0, s56, 147456
s_add_u32 s50, s63, 0xc0000
buffer_load_dwordx4 v251, s[16:19], s50 offen nt lds
)A"
R"A(v_mul_f32 v98, 0x3b124925, v98
PK 96,96,98
v_and_b32 v98, 0xffff0000, v96
s_add_u32 m0, s56, 151552
s_add_u32 s50, s63, 0xe0000
buffer_load_dwordx4 v251, s[16:19], s50 offen nt lds
v_lshlrev_b32 v96, 16, v96
v_add_f32 v98, 0x3727c5ac, v98
PK 98,98,98
s_add_u32 m0, s56, 155648
s_add_u32 s50, s63, 0xc8000
buffer_load_dwordx4 v251, s[16:19], s50 offen nt lds
v_lshlrev_b32 v98, 16, v98
v_sqrt_f32 v98, v98
s_nop 0
s_add_u32 m0, s56, 159744
s_add_u32 s50, s63, 0xe8000
buffer_load_dwordx4 v251, s[16:19], s50 offen nt lds
PK 98,98,98
v_lshlrev_b32 v98, 16, v98
v_rcp_f32 v98, v98
buffer_store_dword v91, v90, s[20:23], 0 offen sc1
s_nop 0
v_mul_f32 v99, 0x3db8aa3b, v98
s_add_u32 s62, s62, 256
s_add_u32 s64, s56, 65536
s_add_u32 s63, s63, 1048576
s_mov_b32 s57, 0
WL 0
s_add_u32 s48, s48, 1
s_addc_u32 s49, s49, 0
s_cmp_eq_u32 s53, 0
s_cbranch_scc0 .LROLE_A
s_waitcnt vmcnt(9)
s_barrier
RD 96,248,0
RD 100,249,0
TR 0,240,0
TR 2,240,8192
TR 4,240,16384
TR 6,240,24576
TR 8,241,0
TR 10,241,8192
TR 12,241,16384
TR 14,241,24576
TR 16,242,0
TR 18,242,8192
TR 20,242,16384
TR 22,242,24576
TR 24,243,0
TR 26,243,8192
TR 28,243,16384
TR 30,243,24576
TR 64,244,0
TR 66,244,8192
TR 68,244,16384
TR 70,244,24576
WL 12
MZ 128,0,96
TR 72,245,0
TR 74,245,8192
MA 128,4,100
TR 76,245,16384
TR 78,245,24576
MZ 0,12,100
TR 80,246,0
TR 82,246,8192
MA 0,8,96
WL 10
TR 84,246,16384
TR 86,246,24576
MZ 132,16,96
TR 88,247,0
TR 90,247,8192
MA 132,20,100
TR 92,247,16384
TR 94,247,24576
MZ 4,28,100
RD 104,248,2048
RD 108,249,2048
MA 4,24,96
WL 10
v_sub_f32 v224, v224, v96
v_sub_f32 v225, v225, v96
MZ 136,64,96
s_add_u32 m0, s56, 32768
s_add_u32 s50, s62, 0xffffff80
PK 225,224,225
MA 136,68,100
buffer_load_dwordx4 v250, s[12:15], s50 offen lds
MZ 8,76,100
s_add_u32 m0, s56, 36864
RD 112,248,4096
RD 116,249,4096
MA 8,72,96
WL 4
v_lshlrev_b32 v224, 16, v225
v_and_b32 v225, 0xffff0000, v225
MZ 140,80,96
s_add_u32 s50, s62, 0xff80
v_mul_f32 v106, v224, v98
v_mul_f32 v107, v225, v98
MA 140,84,100
buffer_load_dwordx4 v250, s[12:15], s50 offen lds
MZ 12,92,100
s_add_u32 m0, s56, 40960
PK 112,106,107
v_mul_f32 v106, v224, v99
MA 12,88,96
WL 2
v_mul_f32 v107, v225, v99
PK 120,106,107
MZ 144,0,104
s_add_u32 s50, s62, 0x1ff80
v_sub_f32 v226, v226, v96
v_sub_f32 v227, v227, v96
MA 144,4,108
buffer_load_dwordx4 v250, s[12:15], s50 offen lds
MZ 16,12,108
PK 227,226,227
v_lshlrev_b32 v226, 16, v227
MA 16,8,104
WL 0
s_barrier
v_and_b32 v227, 0xffff0000, v227
MZ 148,16,104
s_add_u32 m0, s56, 45056
RD 120,248,6144
RD 124,249,6144
MA 148,20,108
v_mul_f32 v106, v226, v98
v_mul_f32 v107, v227, v98
MZ 20,28,108
s_add_u32 s50, s62, 0x2ff80
RD 96,248,8192
RD 100,249,8192
MA 20,24,104
buffer_load_dwordx4 v250, s[12:15], s50 offen lds
MZ 152,64,104
s_add_u32 m0, s56, 49152
PK 113,106,107
v_mul_f32 v106, v226, v99
MA 152,68,108
s_add_u32 s50, s62, 0x3ff80
v_mul_f32 v107, v227, v99
PK 121,106,107
MZ 24,76,108
v_sub_f32 v228, v228, v96
v_sub_f32 v229, v229, v96
MA 24,72,104
buffer_load_dwordx4 v250, s[12:15], s50 offen lds
MZ 156,80,104
s_add_u32 m0, s56, 53248
PK 229,228,229
v_lshlrev_b32 v228, 16, v229
MA 156,84,108
s_add_u32 s50, s62, 0x4ff80
v_and_b32 v229, 0xffff0000, v229
v_mul_f32 v106, v228, v98
MZ 28,92,108
s_add_u32 s6, s57, 32768
v_mul_f32 v107, v229, v98
PK 116,106,107
MA 28,88,104
v_mul_f32 v106, v228, v99
v_mul_f32 v107, v229, v99
MZ 44,88,112
s_sub_u32 s61, s57, 65536
PK 124,106,107
v_sub_f32 v230, v230, v96
MA 44,92,116
s_min_u32 s6, s6, s61
v_sub_f32 v231, v231, v96
PK 231,230,231
MZ 172,84,116
buffer_load_dwordx4 v250, s[12:15], s50 offen lds
MA 172,80,112
s_add_u32 m0, s56, 57344
s_add_u32 s50, s62, 0x5ff80
v_lshlrev_b32 v230, 16, v231
MZ 40,72,112
buffer_load_dwordx4 v250, s[12:15], s50 offen lds
MA 40,76,116
s_add_u32 m0, s56, 61440
v_and_b32 v231, 0xffff0000, v231
v_mul_f32 v106, v230, v98
MZ 168,68,116
s_sub_u32 s61, s6, s57
RD 104,248,10240
RD 108,249,10240
MA 168,64,112
s_mov_b32 s57, s6
v_mul_f32 v107, v231, v98
PK 117,106,107
MZ 36,24,112
s_add_u32 s50, s62, 0x6ff80
v_mul_f32 v106, v230, v99
v_mul_f32 v107, v231, v99
MA 36,28,116
buffer_load_dwordx4 v250, s[12:15], s50 offen lds
MZ 164,20,116
s_add_u32 m0, s56, 98304
PK 125,106,107
v_sub_f32 v232, v232, v96
MA 164,16,112
s_add_u32 s50, s63, 0x0
v_sub_f32 v233, v233, v96
PK 233,232,233
MZ 32,8,112
v_lshlrev_b32 v232, 16, v233
v_and_b32 v233, 0xffff0000, v233
MA 32,12,116
v_mul_f32 v106, v232, v98
v_mul_f32 v107, v233, v98
MZ 160,4,116
PK 114,106,107
v_mul_f32 v106, v232, v99
MA 160,0,112
WL 4
v_mul_f32 v107, v233, v99
PK 122,106,107
MZ 176,0,120
v_sub_f32 v234, v234, v96
v_sub_f32 v235, v235, v96
MA 176,4,124
buffer_load_dwordx4 v251, s[16:19], s50 offen nt lds
MZ 48,12,124
PK 235,234,235
v_lshlrev_b32 v234, 16, v235
MA 48,8,120
v_and_b32 v235, 0xffff0000, v235
v_mul_f32 v106, v234, v98
MZ 180,16,120
v_mul_f32 v107, v235, v98
PK 115,106,107
MA 180,20,124
v_mul_f32 v106, v234, v99
v_mul_f32 v107, v235, v99
MZ 52,28,124
s_add_u32 m0, s56, 102400
RD 112,248,12288
RD 116,249,12288
s_add_u32 s50, s63, 0x20000
MA 52,24,120
buffer_load_dwordx4 v251, s[16:19], s50 offen nt lds
MZ 184,64,120
s_add_u32 m0, s56, 106496
PK 123,106,107
v_sub_f32 v236, v236, v96
MA 184,68,124
s_add_u32 s50, s63, 0x8000
v_sub_f32 v237, v237, v96
PK 237,236,237
MZ 56,76,124
v_lshlrev_b32 v236, 16, v237
v_and_b32 v237, 0xffff0000, v237
MA 56,72,120
v_mul_f32 v106, v236, v98
v_mul_f32 v107, v237, v98
MZ 188,80,120
PK 118,106,107
v_mul_f32 v106, v236, v99
MA 188,84,124
buffer_load_dwordx4 v251, s[16:19], s50 offen nt lds
MZ 60,92,124
s_add_u32 m0, s56, 110592
v_mul_f32 v107, v237, v99
PK 126,106,107
MA 60,88,120
WL 4
v_sub_f32 v238, v238, v96
v_sub_f32 v239, v239, v96
MZ 76,88,96
s_add_u32 s50, s63, 0x28000
PK 239,238,239
v_lshlrev_b32 v238, 16, v239
MA 76,92,100
v_and_b32 v239, 0xffff0000, v239
v_mul_f32 v106, v238, v98
MZ 204,84,100
v_mul_f32 v107, v239, v98
PK 119,106,107
MA 204,80,96
buffer_load_dwordx4 v251, s[16:19], s50 offen nt lds
MZ 72,72,96
v_mul_f32 v106, v238, v99
v_mul_f32 v107, v239, v99
MA 72,76,100
PK 127,106,107
MZ 200,68,100
s_add_u32 m0, s56, 114688
RD 120,248,14336
RD 124,249,14336
MA 200,64,96
ds_write_b128 v111, v[112:115] offset:0
MZ 68,24,96
ds_write_b128 v111, v[116:119] offset:128
MA 68,28,100
s_add_u32 s50, s63, 0x40000
v_add_u32 v248, s61, v248
v_add_u32 v249, s61, v249
MZ 196,20,100
ds_write_b128 v111, v[120:123] offset:8192
MA 196,16,96
buffer_load_dwordx4 v251, s[16:19], s50 offen nt lds
MZ 64,8,96
s_add_u32 m0, s56, 118784
MA 64,12,100
s_add_u32 s50, s63, 0x60000
MZ 192,4,100
ds_write_b128 v111, v[124:127] offset:8320
MA 192,0,96
WL 8
MZ 208,0,104
MA 208,4,108
buffer_load_dwordx4 v251, s[16:19], s50 offen nt lds
MZ 80,12,108
s_add_u32 m0, s56, 122880
MA 80,8,104
s_add_u32 s50, s63, 0x48000
MZ 212,16,104
MA 212,20,108
MZ 84,28,108
MA 84,24,104
buffer_load_dwordx4 v251, s[16:19], s50 offen nt lds
MZ 216,64,104
MA 216,68,108
MZ 88,76,108
MA 88,72,104
s_waitcnt vmcnt(7) lgkmcnt(0)
s_barrier
MZ 220,80,104
s_add_u32 m0, s56, 126976
RD 96,248,0
RD 100,249,0
MA 220,84,108
RD 144,255,0
RD 148,255,4096
MZ 92,92,108
s_add_u32 s50, s63, 0x68000
TR 32,240,32768
TR 34,240,40960
MA 92,88,104
RD 152,255,8192
RD 156,255,12288
MZ 108,88,112
TR 36,240,49152
TR 38,240,57344
MA 108,92,116
buffer_load_dwordx4 v251, s[16:19], s50 offen nt lds
MZ 236,84,116
s_add_u32 m0, s64, 0
TR 40,241,32768
TR 42,241,40960
MA 236,80,112
MZ 104,72,112
s_add_u32 s50, s62, 0x0
TR 44,241,49152
TR 46,241,57344
MA 104,76,116
buffer_load_dwordx4 v250, s[12:15], s50 offen lds
MZ 232,68,116
s_add_u32 m0, s64, 4096
TR 48,242,32768
TR 50,242,40960
MA 232,64,112
MZ 100,24,112
s_add_u32 s50, s62, 0x10000
TR 52,242,49152
TR 54,242,57344
MA 100,28,116
buffer_load_dwordx4 v250, s[12:15], s50 offen lds
MZ 228,20,116
s_add_u32 m0, s64, 8192
TR 56,243,32768
TR 58,243,40960
MA 228,16,112
MZ 96,8,112
s_add_u32 s50, s62, 0x20000
TR 60,243,49152
TR 62,243,57344
MA 96,12,116
MZ 224,4,116
s_lshl_b32 s59, s11, 17
RD 104,248,2048
RD 108,249,2048
MA 224,0,112
buffer_load_dwordx4 v250, s[12:15], s50 offen lds
v_mfma_f32_16x16x32_bf16 a[248:251], a[64:67], a[120:123], 0
s_add_u32 s59, s59, 1048576
v_mfma_f32_16x16x32_bf16 a[248:251], a[68:71], a[124:127], a[248:251]
s_and_b32 s60, s10, 7
MZ 120,76,124
s_add_u32 m0, s64, 12288
TR 64,244,32768
TR 66,244,40960
MA 120,72,120
v_mfma_f32_16x16x32_bf16 a[252:255], a[80:83], a[120:123], 0
s_add_u32 s50, s62, 0x30000
TR 68,244,49152
TR 70,244,57344
v_mfma_f32_16x16x32_bf16 a[252:255], a[84:87], a[124:127], a[252:255]
MZ 124,92,124
s_lshl_b32 s60, s60, 16
TR 72,245,32768
TR 74,245,40960
MA 124,88,120
buffer_load_dwordx4 v250, s[12:15], s50 offen lds
v_mfma_f32_16x16x32_bf16 a[240:243], a[0:3], a[120:123], 0
s_add_u32 s60, s60, 256
TR 76,245,49152
TR 78,245,57344
v_mfma_f32_16x16x32_bf16 a[240:243], a[4:7], a[124:127], a[240:243]
MZ 112,12,124
TR 80,246,32768
TR 82,246,40960
MA 112,8,120
v_mfma_f32_16x16x32_bf16 a[244:247], a[16:19], a[120:123], 0
TR 84,246,49152
TR 86,246,57344
v_mfma_f32_16x16x32_bf16 a[244:247], a[20:23], a[124:127], a[244:247]
TR 88,247,32768
TR 90,247,40960
MZ 116,28,124
TR 92,247,49152
TR 94,247,57344
MA 116,24,120
WL 15
MA 128,32,96
s_add_u32 s51, s62, s60
MA 128,36,100
s_min_u32 s51, s51, 0x3f0780
MA 0,44,100
buffer_load_dword a234, v250, s[12:15], s51 offen
MA 0,40,96
MA 132,48,96
MA 132,52,100
MA 4,60,100
MA 4,56,96
WL 0
s_barrier
MA 136,64,96
s_add_u32 m0, s64, 16384
RD 112,248,4096
RD 116,249,4096
MA 136,68,100
MA 8,76,100
s_add_u32 s50, s62, 0x40000
RD 120,248,6144
RD 124,249,6144
MA 8,72,96
buffer_load_dwordx4 v250, s[12:15], s50 offen lds
MA 140,80,96
s_add_u32 m0, s64, 20480
MA 140,84,100
s_add_u32 s50, s62, 0x50000
MA 12,92,100
buffer_load_dword a239, v252, s[68:71], 0 offen
MA 12,88,96
buffer_load_dwordx4 v250, s[12:15], s50 offen lds
MA 144,32,104
s_add_u32 m0, s64, 24576
v_add_u32 v252, s66, v252
MA 144,36,108
s_add_u32 s50, s62, 0x60000
MA 16,44,108
MA 16,40,104
buffer_load_dwordx4 v250, s[12:15], s50 offen lds
MA 148,48,104
s_mov_b32 s65, 5
MA 148,52,108
MA 20,60,108
s_add_u32 m0, s64, 28672
RD 96,248,8192
RD 100,249,8192
MA 20,56,104
s_add_u32 s50, s62, 0x70000
MA 152,64,104
buffer_load_dwordx4 v250, s[12:15], s50 offen lds
MA 152,68,108
s_add_u32 m0, s56, 131072
MA 24,76,108
s_add_u32 s50, s63, 0x80000
MA 24,72,104
MA 156,80,104
buffer_load_dwordx4 v251, s[16:19], s50 offen nt lds
MA 156,84,108
s_add_u32 m0, s56, 135168
s_add_u32 s50, s63, 0xa0000
MA 28,92,108
s_add_u32 s6, s57, 32768
MA 28,88,104
WL 4
MA 44,88,112
s_sub_u32 s61, s57, 65536
MA 44,92,116
s_min_u32 s6, s6, s61
MA 172,84,116
buffer_load_dwordx4 v251, s[16:19], s50 offen nt lds
MA 172,80,112
s_add_u32 m0, s56, 139264
MA 40,72,112
s_add_u32 s50, s63, 0x88000
MA 40,76,116
buffer_load_dwordx4 v251, s[16:19], s50 offen nt lds
MA 168,68,116
s_sub_u32 s61, s6, s57
RD 104,248,10240
RD 108,249,10240
MA 168,64,112
s_mov_b32 s57, s6
MA 36,56,112
s_add_u32 m0, s56, 143360
s_add_u32 s50, s63, 0xa8000
MA 36,60,116
buffer_load_dwordx4 v251, s[16:19], s50 offen nt lds
MA 164,52,116
s_add_u32 m0, s56, 147456
MA 164,48,112
s_add_u32 s50, s63, 0xc0000
MA 32,40,112
MA 32,44,116
buffer_load_dwordx4 v251, s[16:19], s50 offen nt lds
MA 160,36,116
s_add_u32 m0, s56, 151552
MA 160,32,112
WL 4
MA 176,32,120
s_add_u32 s50, s63, 0xe0000
MA 176,36,124
buffer_load_dwordx4 v251, s[16:19], s50 offen nt lds
MA 48,44,124
s_add_u32 m0, s56, 155648
MA 48,40,120
s_add_u32 s50, s63, 0xc8000
MA 180,48,120
MA 180,52,124
buffer_load_dwordx4 v251, s[16:19], s50 offen nt lds
MA 52,60,124
s_add_u32 m0, s56, 159744
RD 112,248,12288
RD 116,249,12288
MA 52,56,120
s_add_u32 s50, s63, 0xe8000
MA 184,64,120
MA 184,68,124
buffer_load_dwordx4 v251, s[16:19], s50 offen nt lds
MA 56,76,124
s_add_u32 m0, s56, 0
MA 56,72,120
s_add_u32 s50, s62, 0x80
MA 188,80,120
MA 188,84,124
buffer_load_dwordx4 v250, s[12:15], s50 offen lds
MA 60,92,124
s_add_u32 m0, s56, 4096
MA 60,88,120
WL 4
MA 76,88,96
s_add_u32 s50, s62, 0x10080
MA 76,92,100
buffer_load_dwordx4 v250, s[12:15], s50 offen lds
MA 204,84,100
s_add_u32 m0, s56, 8192
MA 204,80,96
s_add_u32 s50, s62, 0x20080
)A"
R"A(MA 72,72,96
MA 72,76,100
buffer_load_dwordx4 v250, s[12:15], s50 offen lds
MA 200,68,100
s_add_u32 m0, s56, 12288
RD 120,248,14336
RD 124,249,14336
MA 200,64,96
MA 68,56,96
MA 68,60,100
s_add_u32 s50, s62, 0x30080
v_add_u32 v248, s61, v248
v_add_u32 v249, s61, v249
MA 196,52,100
MA 196,48,96
buffer_load_dwordx4 v250, s[12:15], s50 offen lds
MA 64,40,96
s_add_u32 m0, s56, 16384
MA 64,44,100
s_add_u32 s50, s62, 0x40080
MA 192,36,100
MA 192,32,96
WL 4
buffer_load_dwordx4 v250, s[12:15], s50 offen lds
MA 208,32,104
s_add_u32 m0, s56, 20480
MA 208,36,108
s_add_u32 s50, s62, 0x50080
MA 80,44,108
MA 80,40,104
buffer_load_dwordx4 v250, s[12:15], s50 offen lds
MA 212,48,104
s_add_u32 m0, s56, 24576
MA 212,52,108
s_add_u32 s50, s62, 0x60080
MA 84,60,108
MA 84,56,104
buffer_load_dwordx4 v250, s[12:15], s50 offen lds
MA 216,64,104
MA 216,68,108
MA 88,76,108
MA 88,72,104
s_waitcnt vmcnt(15) lgkmcnt(0)
s_barrier
MA 220,80,104
s_add_u32 m0, s56, 28672
RD 96,248,0
RD 100,249,0
s_add_u32 s50, s62, 0x70080
MA 220,84,108
buffer_load_dwordx4 v250, s[12:15], s50 offen lds
MA 92,92,108
s_add_u32 m0, s56, 32768
TR 0,240,0
TR 2,240,8192
MA 92,88,104
MA 108,88,112
s_add_u32 s50, s62, 0x100
TR 4,240,16384
TR 6,240,24576
MA 108,92,116
buffer_load_dwordx4 v250, s[12:15], s50 offen lds
MA 236,84,116
s_add_u32 m0, s56, 36864
TR 8,241,0
TR 10,241,8192
MA 236,80,112
MA 104,72,112
s_add_u32 s50, s62, 0x10100
TR 12,241,16384
TR 14,241,24576
MA 104,76,116
MA 232,68,116
TR 16,242,0
TR 18,242,8192
MA 232,64,112
MA 100,56,112
TR 20,242,16384
TR 22,242,24576
MA 100,60,116
buffer_load_dwordx4 v250, s[12:15], s50 offen lds
MA 228,52,116
s_add_u32 m0, s56, 40960
TR 24,243,0
TR 26,243,8192
MA 228,48,112
MA 96,40,112
s_add_u32 s50, s62, 0x20100
TR 28,243,16384
TR 30,243,24576
MA 96,44,116
MA 224,36,116
RD 104,248,2048
RD 108,249,2048
MA 224,32,112
buffer_load_dwordx4 v250, s[12:15], s50 offen lds
v_mfma_f32_16x16x32_bf16 a[248:251], a[64:67], a[120:123], a[248:251]
v_mfma_f32_16x16x32_bf16 a[248:251], a[68:71], a[124:127], a[248:251]
MA 120,76,124
s_add_u32 m0, s56, 45056
TR 64,244,0
TR 66,244,8192
MA 120,72,120
v_mfma_f32_16x16x32_bf16 a[252:255], a[80:83], a[120:123], a[252:255]
s_add_u32 s50, s62, 0x30100
TR 68,244,16384
TR 70,244,24576
v_mfma_f32_16x16x32_bf16 a[252:255], a[84:87], a[124:127], a[252:255]
MA 124,92,124
TR 72,245,0
TR 74,245,8192
MA 124,88,120
buffer_load_dwordx4 v250, s[12:15], s50 offen lds
v_mfma_f32_16x16x32_bf16 a[240:243], a[32:35], a[120:123], a[240:243]
s_add_u32 s62, s62, 256
TR 76,245,16384
TR 78,245,24576
v_mfma_f32_16x16x32_bf16 a[240:243], a[36:39], a[124:127], a[240:243]
MA 112,44,124
s_add_u32 s64, s56, 32768
TR 80,246,0
TR 82,246,8192
MA 112,40,120
v_mfma_f32_16x16x32_bf16 a[244:247], a[48:51], a[120:123], a[244:247]
s_add_u32 s63, s63, 1048576
TR 84,246,16384
TR 86,246,24576
v_mfma_f32_16x16x32_bf16 a[244:247], a[52:55], a[124:127], a[244:247]
TR 88,247,0
TR 90,247,8192
MA 116,60,124
TR 92,247,16384
TR 94,247,24576
MA 116,56,120
WL 15
; role B: K projection main loop (two 64-deep k-tiles per trip)
.LP1LB:
MA 128,0,96
s_add_u32 m0, s64, 16384
MA 128,4,100
s_add_u32 s50, s62, 0x40000
MA 0,12,100
MA 0,8,96
buffer_load_dwordx4 v250, s[12:15], s50 offen lds
MA 132,16,96
MA 132,20,100
MA 4,28,100
MA 4,24,96
WL 0
s_barrier
MA 136,64,96
s_add_u32 m0, s64, 20480
RD 112,248,4096
RD 116,249,4096
MA 136,68,100
MA 8,76,100
s_add_u32 s50, s62, 0x50000
RD 120,248,6144
RD 124,249,6144
MA 8,72,96
buffer_load_dword a238, v252, s[68:71], 0 offen
MA 140,80,96
v_add_u32 v252, s66, v252
MA 140,84,100
buffer_load_dwordx4 v250, s[12:15], s50 offen lds
MA 12,92,100
s_add_u32 m0, s64, 24576
MA 12,88,96
s_add_u32 s50, s62, 0x60000
MA 144,0,104
s_add_u32 s51, s62, s60
s_min_u32 s51, s51, 0x3f0780
MA 144,4,108
buffer_load_dword a237, v250, s[12:15], s51 offen
MA 16,12,108
MA 16,8,104
buffer_load_dwordx4 v250, s[12:15], s50 offen lds
MA 148,16,104
MA 148,20,108
MA 20,28,108
s_add_u32 m0, s64, 28672
RD 96,248,8192
RD 100,249,8192
MA 20,24,104
s_add_u32 s50, s62, 0x70000
MA 152,64,104
MA 152,68,108
MA 24,76,108
MA 24,72,104
buffer_load_dwordx4 v250, s[12:15], s50 offen lds
MA 156,80,104
s_add_u32 m0, s56, 98304
MA 156,84,108
s_add_u32 s50, s63, 0x0
MA 28,92,108
s_add_u32 s6, s57, 32768
MA 28,88,104
WL 4
MA 44,88,112
s_sub_u32 s61, s57, 65536
MA 44,92,116
s_min_u32 s6, s6, s61
MA 172,84,116
MA 172,80,112
buffer_load_dwordx4 v251, s[16:19], s50 offen nt lds
MA 40,72,112
s_add_u32 m0, s56, 102400
MA 40,76,116
s_add_u32 s50, s63, 0x20000
MA 168,68,116
s_sub_u32 s61, s6, s57
RD 104,248,10240
RD 108,249,10240
MA 168,64,112
s_mov_b32 s57, s6
MA 36,24,112
MA 36,28,116
MA 164,20,116
MA 164,16,112
buffer_load_dwordx4 v251, s[16:19], s50 offen nt lds
MA 32,8,112
s_add_u32 m0, s56, 106496
MA 32,12,116
s_add_u32 s50, s63, 0x8000
MA 160,4,116
MA 160,0,112
WL 4
MA 176,0,120
MA 176,4,124
MA 48,12,124
s_add_u32 s62, s62, 128
MA 48,8,120
buffer_load_dwordx4 v251, s[16:19], s50 offen nt lds
MA 180,16,120
s_add_u32 s7, s64, 32768
MA 180,20,124
s_sub_u32 s64, s64, 65536
MA 52,28,124
s_add_u32 m0, s56, 110592
RD 112,248,12288
RD 116,249,12288
MA 52,24,120
s_add_u32 s50, s63, 0x28000
MA 184,64,120
s_min_u32 s64, s7, s64
MA 184,68,124
MA 56,76,124
MA 56,72,120
buffer_load_dwordx4 v251, s[16:19], s50 offen nt lds
MA 188,80,120
s_add_u32 m0, s56, 114688
MA 188,84,124
s_add_u32 s50, s63, 0x40000
MA 60,92,124
MA 60,88,120
WL 4
MA 76,88,96
MA 76,92,100
MA 204,84,100
MA 204,80,96
buffer_load_dwordx4 v251, s[16:19], s50 offen nt lds
MA 72,72,96
MA 72,76,100
MA 200,68,100
s_add_u32 m0, s56, 118784
RD 120,248,14336
RD 124,249,14336
MA 200,64,96
MA 68,24,96
MA 68,28,100
s_add_u32 s50, s63, 0x60000
v_add_u32 v248, s61, v248
v_add_u32 v249, s61, v249
MA 196,20,100
MA 196,16,96
buffer_load_dwordx4 v251, s[16:19], s50 offen nt lds
MA 64,8,96
s_add_u32 m0, s56, 122880
MA 64,12,100
s_add_u32 s50, s63, 0x48000
MA 192,4,100
MA 192,0,96
WL 4
MA 208,0,104
MA 208,4,108
MA 80,12,108
buffer_load_dwordx4 v251, s[16:19], s50 offen nt lds
MA 80,8,104
s_add_u32 m0, s56, 126976
MA 212,16,104
s_add_u32 s50, s63, 0x68000
MA 212,20,108
MA 84,28,108
MA 84,24,104
buffer_load_dwordx4 v251, s[16:19], s50 offen nt lds
MA 216,64,104
MA 216,68,108
MA 88,76,108
MA 88,72,104
s_waitcnt vmcnt(18) lgkmcnt(0)
s_barrier
MA 220,80,104
s_add_u32 m0, s64, 0
RD 96,248,0
RD 100,249,0
MA 220,84,108
MA 92,92,108
s_add_u32 s50, s62, 0x0
TR 32,240,32768
TR 34,240,40960
MA 92,88,104
MA 108,88,112
TR 36,240,49152
TR 38,240,57344
MA 108,92,116
buffer_load_dwordx4 v250, s[12:15], s50 offen lds
MA 236,84,116
s_add_u32 m0, s64, 4096
TR 40,241,32768
TR 42,241,40960
MA 236,80,112
MA 104,72,112
s_add_u32 s50, s62, 0x10000
TR 44,241,49152
TR 46,241,57344
MA 104,76,116
MA 232,68,116
TR 48,242,32768
TR 50,242,40960
MA 232,64,112
buffer_load_dwordx4 v250, s[12:15], s50 offen lds
MA 100,24,112
s_add_u32 m0, s64, 8192
TR 52,242,49152
TR 54,242,57344
MA 100,28,116
MA 228,20,116
s_add_u32 s50, s62, 0x20000
TR 56,243,32768
TR 58,243,40960
MA 228,16,112
MA 96,8,112
TR 60,243,49152
TR 62,243,57344
MA 96,12,116
buffer_load_dwordx4 v250, s[12:15], s50 offen lds
MA 224,4,116
s_add_u32 m0, s64, 12288
RD 104,248,2048
RD 108,249,2048
MA 224,0,112
v_mfma_f32_16x16x32_bf16 a[248:251], a[64:67], a[120:123], a[248:251]
v_mfma_f32_16x16x32_bf16 a[248:251], a[68:71], a[124:127], a[248:251]
MA 120,76,124
s_add_u32 s50, s62, 0x30000
TR 64,244,32768
TR 66,244,40960
MA 120,72,120
v_mfma_f32_16x16x32_bf16 a[252:255], a[80:83], a[120:123], a[252:255]
TR 68,244,49152
TR 70,244,57344
v_mfma_f32_16x16x32_bf16 a[252:255], a[84:87], a[124:127], a[252:255]
MA 124,92,124
TR 72,245,32768
TR 74,245,40960
MA 124,88,120
buffer_load_dwordx4 v250, s[12:15], s50 offen lds
v_mfma_f32_16x16x32_bf16 a[240:243], a[0:3], a[120:123], a[240:243]
TR 76,245,49152
TR 78,245,57344
v_mfma_f32_16x16x32_bf16 a[240:243], a[4:7], a[124:127], a[240:243]
MA 112,12,124
TR 80,246,32768
TR 82,246,40960
MA 112,8,120
v_mfma_f32_16x16x32_bf16 a[244:247], a[16:19], a[120:123], a[244:247]
TR 84,246,49152
TR 86,246,57344
v_mfma_f32_16x16x32_bf16 a[244:247], a[20:23], a[124:127], a[244:247]
TR 88,247,32768
TR 90,247,40960
MA 116,28,124
TR 92,247,49152
TR 94,247,57344
MA 116,24,120
WL 15
MA 128,32,96
s_add_u32 m0, s64, 16384
MA 128,36,100
s_add_u32 s50, s62, 0x40000
MA 0,44,100
MA 0,40,96
buffer_load_dwordx4 v250, s[12:15], s50 offen lds
MA 132,48,96
MA 132,52,100
MA 4,60,100
MA 4,56,96
WL 0
s_barrier
MA 136,64,96
s_add_u32 m0, s64, 20480
RD 112,248,4096
RD 116,249,4096
MA 136,68,100
MA 8,76,100
s_add_u32 s50, s62, 0x50000
RD 120,248,6144
RD 124,249,6144
MA 8,72,96
MA 140,80,96
MA 140,84,100
buffer_load_dwordx4 v250, s[12:15], s50 offen lds
MA 12,92,100
s_add_u32 m0, s64, 24576
MA 12,88,96
s_add_u32 s50, s62, 0x60000
MA 144,32,104
MA 144,36,108
MA 16,44,108
MA 16,40,104
buffer_load_dwordx4 v250, s[12:15], s50 offen lds
MA 148,48,104
s_sub_u32 s65, s65, 1
MA 148,52,108
MA 20,60,108
s_add_u32 m0, s64, 28672
RD 96,248,8192
RD 100,249,8192
MA 20,56,104
s_add_u32 s50, s62, 0x70000
MA 152,64,104
MA 152,68,108
MA 24,76,108
MA 24,72,104
buffer_load_dwordx4 v250, s[12:15], s50 offen lds
MA 156,80,104
s_add_u32 m0, s56, 131072
MA 156,84,108
s_add_u32 s50, s63, 0x80000
MA 28,92,108
s_add_u32 s6, s57, 32768
MA 28,88,104
WL 4
MA 44,88,112
s_sub_u32 s61, s57, 65536
MA 44,92,116
s_min_u32 s6, s6, s61
MA 172,84,116
MA 172,80,112
buffer_load_dwordx4 v251, s[16:19], s50 offen nt lds
MA 40,72,112
s_add_u32 m0, s56, 135168
MA 40,76,116
s_add_u32 s50, s63, 0xa0000
MA 168,68,116
s_sub_u32 s61, s6, s57
RD 104,248,10240
RD 108,249,10240
MA 168,64,112
s_mov_b32 s57, s6
MA 36,56,112
MA 36,60,116
MA 164,52,116
MA 164,48,112
buffer_load_dwordx4 v251, s[16:19], s50 offen nt lds
MA 32,40,112
s_add_u32 m0, s56, 139264
MA 32,44,116
s_add_u32 s50, s63, 0x88000
MA 160,36,116
MA 160,32,112
WL 4
MA 176,32,120
MA 176,36,124
MA 48,44,124
s_add_u32 s62, s62, 128
MA 48,40,120
buffer_load_dwordx4 v251, s[16:19], s50 offen nt lds
MA 180,48,120
s_add_u32 s7, s64, 32768
MA 180,52,124
s_sub_u32 s64, s64, 65536
MA 52,60,124
s_add_u32 m0, s56, 143360
RD 112,248,12288
RD 116,249,12288
MA 52,56,120
s_add_u32 s50, s63, 0xa8000
MA 184,64,120
s_min_u32 s64, s7, s64
MA 184,68,124
MA 56,76,124
MA 56,72,120
buffer_load_dwordx4 v251, s[16:19], s50 offen nt lds
MA 188,80,120
s_add_u32 m0, s56, 147456
MA 188,84,124
s_add_u32 s50, s63, 0xc0000
MA 60,92,124
MA 60,88,120
WL 4
MA 76,88,96
MA 76,92,100
MA 204,84,100
MA 204,80,96
buffer_load_dwordx4 v251, s[16:19], s50 offen nt lds
MA 72,72,96
MA 72,76,100
MA 200,68,100
s_add_u32 m0, s56, 151552
RD 120,248,14336
RD 124,249,14336
MA 200,64,96
MA 68,56,96
MA 68,60,100
s_add_u32 s50, s63, 0xe0000
v_add_u32 v248, s61, v248
v_add_u32 v249, s61, v249
MA 196,52,100
MA 196,48,96
buffer_load_dwordx4 v251, s[16:19], s50 offen nt lds
MA 64,40,96
s_add_u32 m0, s56, 155648
MA 64,44,100
s_add_u32 s50, s63, 0xc8000
MA 192,36,100
MA 192,32,96
WL 4
MA 208,32,104
MA 208,36,108
MA 80,44,108
buffer_load_dwordx4 v251, s[16:19], s50 offen nt lds
MA 80,40,104
s_add_u32 m0, s56, 159744
MA 212,48,104
s_add_u32 s50, s63, 0xe8000
MA 212,52,108
MA 84,60,108
MA 84,56,104
buffer_load_dwordx4 v251, s[16:19], s50 offen nt lds
MA 216,64,104
MA 216,68,108
MA 88,76,108
MA 88,72,104
s_waitcnt vmcnt(16) lgkmcnt(0)
s_barrier
MA 220,80,104
s_add_u32 m0, s64, 0
RD 96,248,0
RD 100,249,0
MA 220,84,108
MA 92,92,108
s_add_u32 s50, s62, 0x0
TR 0,240,0
TR 2,240,8192
MA 92,88,104
MA 108,88,112
TR 4,240,16384
TR 6,240,24576
MA 108,92,116
buffer_load_dwordx4 v250, s[12:15], s50 offen lds
MA 236,84,116
s_add_u32 m0, s64, 4096
TR 8,241,0
TR 10,241,8192
MA 236,80,112
MA 104,72,112
s_add_u32 s50, s62, 0x10000
TR 12,241,16384
TR 14,241,24576
MA 104,76,116
MA 232,68,116
TR 16,242,0
TR 18,242,8192
MA 232,64,112
buffer_load_dwordx4 v250, s[12:15], s50 offen lds
MA 100,56,112
s_add_u32 m0, s64, 8192
TR 20,242,16384
TR 22,242,24576
MA 100,60,116
MA 228,52,116
s_add_u32 s50, s62, 0x20000
TR 24,243,0
TR 26,243,8192
MA 228,48,112
MA 96,40,112
TR 28,243,16384
TR 30,243,24576
MA 96,44,116
buffer_load_dwordx4 v250, s[12:15], s50 offen lds
MA 224,36,116
)A"
R"A(s_add_u32 m0, s64, 12288
RD 104,248,2048
RD 108,249,2048
MA 224,32,112
v_mfma_f32_16x16x32_bf16 a[248:251], a[64:67], a[120:123], a[248:251]
v_mfma_f32_16x16x32_bf16 a[248:251], a[68:71], a[124:127], a[248:251]
MA 120,76,124
s_add_u32 s50, s62, 0x30000
TR 64,244,0
TR 66,244,8192
MA 120,72,120
v_mfma_f32_16x16x32_bf16 a[252:255], a[80:83], a[120:123], a[252:255]
s_add_u32 s63, s63, 1048576
TR 68,244,16384
TR 70,244,24576
v_mfma_f32_16x16x32_bf16 a[252:255], a[84:87], a[124:127], a[252:255]
MA 124,92,124
TR 72,245,0
TR 74,245,8192
MA 124,88,120
buffer_load_dwordx4 v250, s[12:15], s50 offen lds
v_mfma_f32_16x16x32_bf16 a[240:243], a[32:35], a[120:123], a[240:243]
TR 76,245,16384
TR 78,245,24576
v_mfma_f32_16x16x32_bf16 a[240:243], a[36:39], a[124:127], a[240:243]
MA 112,44,124
TR 80,246,0
TR 82,246,8192
MA 112,40,120
v_mfma_f32_16x16x32_bf16 a[244:247], a[48:51], a[120:123], a[244:247]
TR 84,246,16384
TR 86,246,24576
v_mfma_f32_16x16x32_bf16 a[244:247], a[52:55], a[124:127], a[244:247]
TR 88,247,0
TR 90,247,8192
MA 116,60,124
TR 92,247,16384
TR 94,247,24576
MA 116,56,120
WL 15
s_cmp_eq_u32 s65, 4
s_cbranch_scc1 .LXF4B
s_cmp_eq_u32 s65, 3
s_cbranch_scc1 .LXF3B
s_cmp_eq_u32 s65, 2
s_cbranch_scc1 .LXF2B
s_cmp_eq_u32 s65, 1
s_cbranch_scc1 .LXF1B
s_branch .LXEB
.LXF4B:
buffer_store_dwordx4 a[144:147], v254, s[20:23], s74 offen sc1
buffer_store_dwordx4 a[148:151], v254, s[20:23], s75 offen sc1
s_add_u32 s74, s74, 0x200000
s_add_u32 s75, s75, 0x200000
buffer_store_dwordx4 a[152:155], v254, s[20:23], s74 offen sc1
buffer_store_dwordx4 a[156:159], v254, s[20:23], s75 offen sc1
s_branch .LP1LB
.LXF3B:
v_mov_b32 v254, s48
v_mov_b32 v255, s49
buffer_store_dwordx2 v[254:255], off, s[20:23], s72 sc1
s_branch .LP1LB
.LXF2B:
buffer_load_dwordx2 v[254:255], v253, s[20:23], s67 offen sc1
s_add_u32 s79, s11, 8
s_lshl_b32 s74, s79, 3
s_add_u32 s74, s74, s8
s_mul_i32 s74, s74, 14336
s_add_u32 s74, s74, 0x2200000
s_add_u32 s75, s74, 57344
s_branch .LP1LB
.LXF1B:
v_cmp_ne_u64 vcc, s[48:49], v[254:255]
s_cbranch_vccnz .LPS0
.LPR0:
buffer_load_dwordx4 a[128:131], v253, s[20:23], s74 offen offset:0 sc1
buffer_load_dwordx4 a[132:135], v253, s[20:23], s75 offen offset:0 sc1
buffer_load_dwordx4 a[136:139], v253, s[20:23], s74 offen offset:1024 sc1
buffer_load_dwordx4 a[140:143], v253, s[20:23], s75 offen offset:1024 sc1
s_add_u32 s74, s74, 2048
s_add_u32 s75, s75, 2048
s_branch .LP1LB
; last k-tiles, Q(j), Q(i), first QK step
.LXEB:
MA 128,0,96
s_add_u32 m0, s64, 16384
MA 128,4,100
s_add_u32 s50, s62, 0x40000
MA 0,12,100
MA 0,8,96
buffer_load_dwordx4 v250, s[12:15], s50 offen lds
MA 132,16,96
MA 132,20,100
MA 4,28,100
MA 4,24,96
WL 0
s_barrier
MA 136,64,96
s_add_u32 m0, s64, 20480
RD 112,248,4096
RD 116,249,4096
MA 136,68,100
MA 8,76,100
s_add_u32 s50, s62, 0x50000
RD 120,248,6144
RD 124,249,6144
MA 8,72,96
buffer_load_dword a238, v252, s[68:71], 0 offen
MA 140,80,96
v_add_u32 v252, s66, v252
MA 140,84,100
buffer_load_dwordx4 v250, s[12:15], s50 offen lds
MA 12,92,100
s_add_u32 m0, s64, 24576
MA 12,88,96
s_add_u32 s50, s62, 0x60000
MA 144,0,104
MA 144,4,108
MA 16,12,108
MA 16,8,104
buffer_load_dwordx4 v250, s[12:15], s50 offen lds
MA 148,16,104
MA 148,20,108
MA 20,28,108
s_add_u32 m0, s64, 28672
RD 96,248,8192
RD 100,249,8192
MA 20,24,104
s_add_u32 s50, s62, 0x70000
MA 152,64,104
buffer_load_dwordx4 a[144:147], v253, s[20:23], s74 offen offset:0 sc1
MA 152,68,108
MA 24,76,108
MA 24,72,104
buffer_load_dwordx4 v250, s[12:15], s50 offen lds
MA 156,80,104
s_add_u32 m0, s56, 98304
MA 156,84,108
s_add_u32 s50, s63, 0x0
MA 28,92,108
s_add_u32 s6, s57, 32768
MA 28,88,104
WL 4
MA 44,88,112
s_sub_u32 s61, s57, 65536
MA 44,92,116
s_min_u32 s6, s6, s61
MA 172,84,116
MA 172,80,112
buffer_load_dwordx4 v251, s[16:19], s50 offen nt lds
MA 40,72,112
s_add_u32 m0, s56, 102400
MA 40,76,116
s_add_u32 s50, s63, 0x20000
MA 168,68,116
s_sub_u32 s61, s6, s57
RD 104,248,10240
RD 108,249,10240
MA 168,64,112
s_mov_b32 s57, s6
MA 36,24,112
MA 36,28,116
MA 164,20,116
MA 164,16,112
buffer_load_dwordx4 v251, s[16:19], s50 offen nt lds
MA 32,8,112
s_add_u32 m0, s56, 106496
MA 32,12,116
s_add_u32 s50, s63, 0x8000
MA 160,4,116
MA 160,0,112
WL 4
MA 176,0,120
MA 176,4,124
MA 48,12,124
s_add_u32 s62, s62, 128
MA 48,8,120
buffer_load_dwordx4 v251, s[16:19], s50 offen nt lds
MA 180,16,120
s_add_u32 s7, s64, 32768
MA 180,20,124
s_sub_u32 s64, s64, 65536
MA 52,28,124
s_add_u32 m0, s56, 110592
RD 112,248,12288
RD 116,249,12288
MA 52,24,120
s_add_u32 s50, s63, 0x28000
MA 184,64,120
s_min_u32 s64, s7, s64
MA 184,68,124
buffer_load_dwordx4 a[148:151], v253, s[20:23], s75 offen offset:0 sc1
MA 56,76,124
MA 56,72,120
buffer_load_dwordx4 v251, s[16:19], s50 offen nt lds
MA 188,80,120
s_add_u32 m0, s56, 114688
MA 188,84,124
s_add_u32 s50, s63, 0x40000
MA 60,92,124
MA 60,88,120
WL 4
MA 76,88,96
MA 76,92,100
MA 204,84,100
MA 204,80,96
buffer_load_dwordx4 v251, s[16:19], s50 offen nt lds
MA 72,72,96
MA 72,76,100
MA 200,68,100
s_add_u32 m0, s56, 118784
RD 120,248,14336
RD 124,249,14336
MA 200,64,96
MA 68,24,96
MA 68,28,100
s_add_u32 s50, s63, 0x60000
v_add_u32 v248, s61, v248
v_add_u32 v249, s61, v249
MA 196,20,100
MA 196,16,96
buffer_load_dwordx4 v251, s[16:19], s50 offen nt lds
MA 64,8,96
s_add_u32 m0, s56, 122880
MA 64,12,100
s_add_u32 s50, s63, 0x48000
MA 192,4,100
MA 192,0,96
WL 4
MA 208,0,104
MA 208,4,108
MA 80,12,108
buffer_load_dwordx4 v251, s[16:19], s50 offen nt lds
MA 80,8,104
s_add_u32 m0, s56, 126976
MA 212,16,104
s_add_u32 s50, s63, 0x68000
MA 212,20,108
buffer_load_dwordx4 a[152:155], v253, s[20:23], s74 offen offset:1024 sc1
MA 84,28,108
MA 84,24,104
buffer_load_dwordx4 v251, s[16:19], s50 offen nt lds
MA 216,64,104
MA 216,68,108
MA 88,76,108
MA 88,72,104
s_waitcnt vmcnt(20) lgkmcnt(0)
s_barrier
MA 220,80,104
s_add_u32 m0, s64, 0
RD 96,248,0
RD 100,249,0
MA 220,84,108
MA 92,92,108
s_add_u32 s50, s62, 0x0
TR 32,240,32768
TR 34,240,40960
MA 92,88,104
MA 108,88,112
TR 36,240,49152
TR 38,240,57344
MA 108,92,116
buffer_load_dwordx4 v250, s[12:15], s50 offen lds
MA 236,84,116
s_add_u32 m0, s64, 4096
TR 40,241,32768
TR 42,241,40960
MA 236,80,112
MA 104,72,112
s_add_u32 s50, s62, 0x10000
TR 44,241,49152
TR 46,241,57344
MA 104,76,116
MA 232,68,116
TR 48,242,32768
TR 50,242,40960
MA 232,64,112
buffer_load_dwordx4 v250, s[12:15], s50 offen lds
MA 100,24,112
s_add_u32 m0, s64, 8192
TR 52,242,49152
TR 54,242,57344
MA 100,28,116
MA 228,20,116
s_add_u32 s50, s62, 0x20000
TR 56,243,32768
TR 58,243,40960
MA 228,16,112
MA 96,8,112
TR 60,243,49152
TR 62,243,57344
MA 96,12,116
buffer_load_dwordx4 v250, s[12:15], s50 offen lds
MA 224,4,116
s_add_u32 m0, s64, 12288
RD 104,248,2048
RD 108,249,2048
MA 224,0,112
v_mfma_f32_16x16x32_bf16 a[248:251], a[64:67], a[120:123], a[248:251]
v_mfma_f32_16x16x32_bf16 a[248:251], a[68:71], a[124:127], a[248:251]
buffer_load_dwordx4 a[156:159], v253, s[20:23], s75 offen offset:1024 sc1
MA 120,76,124
s_add_u32 s50, s62, 0x30000
TR 64,244,32768
TR 66,244,40960
MA 120,72,120
v_mfma_f32_16x16x32_bf16 a[252:255], a[80:83], a[120:123], a[252:255]
s_add_u32 s74, s74, 2048
TR 68,244,49152
TR 70,244,57344
v_mfma_f32_16x16x32_bf16 a[252:255], a[84:87], a[124:127], a[252:255]
MA 124,92,124
s_add_u32 s75, s75, 2048
TR 72,245,32768
TR 74,245,40960
MA 124,88,120
buffer_load_dwordx4 v250, s[12:15], s50 offen lds
v_mfma_f32_16x16x32_bf16 a[240:243], a[0:3], a[120:123], a[240:243]
TR 76,245,49152
TR 78,245,57344
v_mfma_f32_16x16x32_bf16 a[240:243], a[4:7], a[124:127], a[240:243]
MA 112,12,124
TR 80,246,32768
TR 82,246,40960
MA 112,8,120
v_mfma_f32_16x16x32_bf16 a[244:247], a[16:19], a[120:123], a[244:247]
TR 84,246,49152
TR 86,246,57344
v_mfma_f32_16x16x32_bf16 a[244:247], a[20:23], a[124:127], a[244:247]
TR 88,247,32768
TR 90,247,40960
MA 116,28,124
TR 92,247,49152
TR 94,247,57344
MA 116,24,120
WL 15
MA 128,32,96
s_add_u32 m0, s64, 16384
MA 128,36,100
s_add_u32 s50, s62, 0x40000
MA 0,44,100
MA 0,40,96
buffer_load_dwordx4 v250, s[12:15], s50 offen lds
MA 132,48,96
MA 132,52,100
MA 4,60,100
MA 4,56,96
WL 0
s_barrier
MA 136,64,96
s_add_u32 m0, s64, 20480
RD 112,248,4096
RD 116,249,4096
MA 136,68,100
MA 8,76,100
s_add_u32 s50, s62, 0x50000
RD 120,248,6144
RD 124,249,6144
MA 8,72,96
buffer_load_dwordx4 a[160:163], v253, s[20:23], s74 offen offset:0 sc1
MA 140,80,96
MA 140,84,100
buffer_load_dwordx4 v250, s[12:15], s50 offen lds
MA 12,92,100
s_add_u32 m0, s64, 24576
MA 12,88,96
s_add_u32 s50, s62, 0x60000
MA 144,32,104
MA 144,36,108
MA 16,44,108
MA 16,40,104
buffer_load_dwordx4 v250, s[12:15], s50 offen lds
MA 148,48,104
MA 148,52,108
MA 20,60,108
s_add_u32 m0, s64, 28672
RD 96,248,8192
RD 100,249,8192
MA 20,56,104
s_add_u32 s50, s62, 0x70000
MA 152,64,104
MA 152,68,108
MA 24,76,108
MA 24,72,104
buffer_load_dwordx4 v250, s[12:15], s50 offen lds
MA 156,80,104
s_add_u32 m0, s56, 131072
MA 156,84,108
s_add_u32 s50, s63, 0x80000
MA 28,92,108
s_add_u32 s6, s57, 32768
MA 28,88,104
WL 4
MA 44,88,112
s_sub_u32 s61, s57, 65536
MA 44,92,116
s_min_u32 s6, s6, s61
MA 172,84,116
MA 172,80,112
buffer_load_dwordx4 v251, s[16:19], s50 offen nt lds
MA 40,72,112
s_add_u32 m0, s56, 135168
MA 40,76,116
s_add_u32 s50, s63, 0xa0000
MA 168,68,116
s_sub_u32 s61, s6, s57
RD 104,248,10240
RD 108,249,10240
MA 168,64,112
s_mov_b32 s57, s6
MA 36,56,112
buffer_load_dwordx4 a[164:167], v253, s[20:23], s75 offen offset:0 sc1
MA 36,60,116
MA 164,52,116
MA 164,48,112
buffer_load_dwordx4 v251, s[16:19], s50 offen nt lds
MA 32,40,112
s_add_u32 m0, s56, 139264
MA 32,44,116
s_add_u32 s50, s63, 0x88000
MA 160,36,116
MA 160,32,112
WL 4
MA 176,32,120
MA 176,36,124
MA 48,44,124
MA 48,40,120
buffer_load_dwordx4 v251, s[16:19], s50 offen nt lds
MA 180,48,120
MA 180,52,124
MA 52,60,124
s_add_u32 m0, s56, 143360
RD 112,248,12288
RD 116,249,12288
MA 52,56,120
s_add_u32 s50, s63, 0xa8000
MA 184,64,120
MA 184,68,124
MA 56,76,124
MA 56,72,120
buffer_load_dwordx4 v251, s[16:19], s50 offen nt lds
MA 188,80,120
s_add_u32 m0, s56, 147456
MA 188,84,124
s_add_u32 s50, s63, 0xc0000
MA 60,92,124
MA 60,88,120
WL 4
MA 76,88,96
buffer_load_dwordx4 a[168:171], v253, s[20:23], s74 offen offset:1024 sc1
MA 76,92,100
MA 204,84,100
MA 204,80,96
buffer_load_dwordx4 v251, s[16:19], s50 offen nt lds
MA 72,72,96
MA 72,76,100
MA 200,68,100
s_add_u32 m0, s56, 151552
RD 120,248,14336
RD 124,249,14336
MA 200,64,96
MA 68,56,96
MA 68,60,100
s_add_u32 s50, s63, 0xe0000
v_add_u32 v248, s61, v248
v_add_u32 v249, s61, v249
MA 196,52,100
MA 196,48,96
buffer_load_dwordx4 v251, s[16:19], s50 offen nt lds
MA 64,40,96
s_add_u32 m0, s56, 155648
MA 64,44,100
s_add_u32 s50, s63, 0xc8000
MA 192,36,100
MA 192,32,96
WL 4
MA 208,32,104
MA 208,36,108
MA 80,44,108
buffer_load_dwordx4 v251, s[16:19], s50 offen nt lds
MA 80,40,104
s_add_u32 m0, s56, 159744
MA 212,48,104
s_add_u32 s50, s63, 0xe8000
MA 212,52,108
buffer_load_dwordx4 a[172:175], v253, s[20:23], s75 offen offset:1024 sc1
MA 84,60,108
s_add_u32 s74, s74, 2048
MA 84,56,104
buffer_load_dwordx4 v251, s[16:19], s50 offen nt lds
MA 216,64,104
s_add_u32 s75, s75, 2048
MA 216,68,108
MA 88,76,108
MA 88,72,104
s_waitcnt vmcnt(21) lgkmcnt(0)
s_barrier
MA 220,80,104
RD 96,248,0
RD 100,249,0
MA 220,84,108
MA 92,92,108
TR 0,240,0
TR 2,240,8192
MA 92,88,104
MA 108,88,112
TR 4,240,16384
TR 6,240,24576
MA 108,92,116
buffer_load_dwordx4 a[176:179], v253, s[20:23], s74 offen offset:0 sc1
MA 236,84,116
TR 8,241,0
TR 10,241,8192
MA 236,80,112
MA 104,72,112
TR 12,241,16384
TR 14,241,24576
MA 104,76,116
MA 232,68,116
TR 16,242,0
TR 18,242,8192
MA 232,64,112
buffer_load_dwordx4 a[180:183], v253, s[20:23], s75 offen offset:0 sc1
MA 100,56,112
TR 20,242,16384
TR 22,242,24576
MA 100,60,116
MA 228,52,116
TR 24,243,0
TR 26,243,8192
MA 228,48,112
MA 96,40,112
TR 28,243,16384
TR 30,243,24576
MA 96,44,116
buffer_load_dwordx4 a[184:187], v253, s[20:23], s74 offen offset:1024 sc1
MA 224,36,116
RD 104,248,2048
RD 108,249,2048
MA 224,32,112
v_mfma_f32_16x16x32_bf16 a[248:251], a[64:67], a[120:123], a[248:251]
v_mfma_f32_16x16x32_bf16 a[248:251], a[68:71], a[124:127], a[248:251]
MA 120,76,124
s_add_u32 s63, s63, 1048576
)A"
R"A(TR 64,244,0
TR 66,244,8192
MA 120,72,120
v_mfma_f32_16x16x32_bf16 a[252:255], a[80:83], a[120:123], a[252:255]
TR 68,244,16384
TR 70,244,24576
v_mfma_f32_16x16x32_bf16 a[252:255], a[84:87], a[124:127], a[252:255]
MA 124,92,124
TR 72,245,0
TR 74,245,8192
MA 124,88,120
buffer_load_dwordx4 a[188:191], v253, s[20:23], s75 offen offset:1024 sc1
v_mfma_f32_16x16x32_bf16 a[240:243], a[32:35], a[120:123], a[240:243]
s_add_u32 s74, s74, 2048
TR 76,245,16384
TR 78,245,24576
v_mfma_f32_16x16x32_bf16 a[240:243], a[36:39], a[124:127], a[240:243]
MA 112,44,124
s_add_u32 s75, s75, 2048
TR 80,246,0
TR 82,246,8192
MA 112,40,120
v_mfma_f32_16x16x32_bf16 a[244:247], a[48:51], a[120:123], a[244:247]
TR 84,246,16384
TR 86,246,24576
v_mfma_f32_16x16x32_bf16 a[244:247], a[52:55], a[124:127], a[244:247]
TR 88,247,0
TR 90,247,8192
MA 116,60,124
TR 92,247,16384
TR 94,247,24576
MA 116,56,120
WL 15
MA 128,0,96
s_lshr_b32 s50, %1, 3
v_bfe_u32 v252, v253, 9, 1
v_lshl_add_u32 v252, s8, 1, v252
MA 128,4,100
s_lshl_b32 s50, s50, 2
v_mul_u32_u24 v252, 8192, v252
v_and_b32 v251, 0x1ff, v251
MA 0,12,100
s_add_u32 s50, s50, s8
v_add_u32 v252, v252, v251
MA 0,8,96
s_mul_i32 s50, s50, 1408
MA 132,16,96
MA 132,20,100
buffer_load_dwordx4 a[192:195], v253, s[20:23], s74 offen offset:0 sc1
MA 4,28,100
MA 4,24,96
WL 0
s_barrier
MA 136,64,96
s_min_u32 s50, s50, .LCEND-.LCPC-1408
RD 112,248,4096
RD 116,249,4096
MA 136,68,100
v_min_u32 v250, 0xa0, v253
v_lshl_add_u32 v250, v250, 3, s50
MA 8,76,100
RD 120,248,6144
RD 124,249,6144
MA 8,72,96
global_load_dword v250, v250, s[76:77]
MA 140,80,96
s_mov_b32 s12, s28
MA 140,84,100
buffer_load_dwordx4 a[196:199], v253, s[20:23], s75 offen offset:0 sc1
MA 12,92,100
s_and_b32 s13, s29, 0xffff
MA 12,88,96
s_mov_b32 s14, 3670016
MA 144,0,104
s_lshl_b32 s59, s10, 9
MA 144,4,108
s_sub_u32 s77, 7, s11
MA 16,12,108
MA 16,8,104
buffer_load_dwordx4 a[200:203], v253, s[20:23], s74 offen offset:1024 sc1
MA 148,16,104
MA 148,20,108
MA 20,28,108
s_add_u32 s58, s56, 32768
RD 96,248,8192
RD 100,249,8192
MA 20,24,104
s_mul_i32 s62, s56, 7
MA 152,64,104
s_lshl_b32 s73, s10, 21
MA 152,68,108
s_lshl_b32 s50, s11, 18
MA 24,76,108
s_add_u32 s73, s73, s50
s_lshr_b32 s50, s8, 1
s_lshl_b32 s50, s50, 17
MA 24,72,104
buffer_load_dwordx4 a[204:207], v253, s[20:23], s75 offen offset:1024 sc1
MA 156,80,104
s_add_u32 s74, s74, 2048
s_add_u32 s73, s73, s50
MA 156,84,108
s_add_u32 s75, s75, 2048
MA 28,92,108
s_add_u32 s6, s57, 32768
MA 28,88,104
WL 4
MA 44,88,112
s_sub_u32 s61, s57, 65536
MA 44,92,116
s_min_u32 s6, s6, s61
MA 172,84,116
s_and_b32 s54, s8, 1
MA 172,80,112
buffer_load_dwordx4 a[208:211], v253, s[20:23], s74 offen offset:0 sc1
MA 40,72,112
s_lshl_b32 s50, s54, 12
MA 40,76,116
s_add_u32 s73, s73, s50
MA 168,68,116
s_sub_u32 s61, s6, s57
RD 104,248,10240
RD 108,249,10240
MA 168,64,112
s_mov_b32 s57, s6
MA 36,24,112
s_lshl_b32 s54, s54, 8
MA 36,28,116
s_sub_u32 s54, 0xffff0000, s54
MA 164,20,116
MA 164,16,112
buffer_load_dwordx4 a[212:215], v253, s[20:23], s75 offen offset:0 sc1
MA 32,8,112
MA 32,12,116
MA 160,4,116
MA 160,0,112
WL 4
MA 176,0,120
MA 176,4,124
MA 48,12,124
MA 48,8,120
buffer_load_dwordx4 a[216:219], v253, s[20:23], s74 offen offset:1024 sc1
MA 180,16,120
MA 180,20,124
MA 52,28,124
RD 112,248,12288
RD 116,249,12288
MA 52,24,120
MA 184,64,120
MA 184,68,124
buffer_load_dwordx4 a[220:223], v253, s[20:23], s75 offen offset:1024 sc1
MA 56,76,124
s_add_u32 s74, s74, 2048
MA 56,72,120
s_add_u32 s75, s75, 2048
MA 188,80,120
MA 188,84,124
MA 60,92,124
s_add_u32 m0, s58, 0
MA 60,88,120
WL 4
MA 76,88,96
MA 76,92,100
buffer_load_dwordx4 v252, s[12:15], s59 offen lds
MA 204,84,100
MA 204,80,96
MA 72,72,96
MA 72,76,100
MA 200,68,100
s_add_u32 s59, s59, 65536
RD 120,248,14336
RD 124,249,14336
s_add_u32 m0, s58, 4096
MA 200,64,96
buffer_load_dwordx4 v252, s[12:15], s59 offen lds
MA 68,24,96
MA 68,28,100
s_add_u32 s59, s59, 65536
v_add_u32 v248, s61, v248
v_add_u32 v249, s61, v249
MA 196,20,100
s_add_u32 m0, s58, 8192
MA 196,16,96
buffer_load_dwordx4 a[224:227], v253, s[20:23], s74 offen offset:0 sc1
MA 64,8,96
MA 64,12,100
buffer_load_dwordx4 v252, s[12:15], s59 offen lds
MA 192,4,100
s_add_u32 s59, s59, 65536
MA 192,0,96
WL 4
MA 208,0,104
s_add_u32 m0, s58, 12288
MA 208,4,108
buffer_load_dwordx4 a[228:231], v253, s[20:23], s75 offen offset:0 sc1
MA 80,12,108
MA 80,8,104
buffer_load_dwordx4 v252, s[12:15], s59 offen lds
MA 212,16,104
s_add_u32 s59, s59, 65536
MA 212,20,108
s_add_u32 m0, s58, 16384
MA 84,28,108
MA 84,24,104
buffer_load_dwordx4 v252, s[12:15], s59 offen lds
MA 216,64,104
MA 216,68,108
MA 88,76,108
MA 88,72,104
s_waitcnt vmcnt(20) lgkmcnt(0)
s_barrier
MA 220,80,104
s_add_u32 s59, s59, 65536
RD 96,248,0
RD 100,249,0
MA 220,84,108
MA 92,92,108
s_add_u32 m0, s58, 20480
TR 32,240,32768
TR 34,240,40960
MA 92,88,104
MA 108,88,112
TR 36,240,49152
TR 38,240,57344
MA 108,92,116
buffer_load_dwordx4 v252, s[12:15], s59 offen lds
MA 236,84,116
s_add_u32 s59, s59, 65536
TR 40,241,32768
TR 42,241,40960
MA 236,80,112
MA 104,72,112
s_add_u32 m0, s58, 24576
TR 44,241,49152
TR 46,241,57344
MA 104,76,116
buffer_load_dwordx4 a[232:235], v253, s[20:23], s74 offen offset:1024 sc1
MA 232,68,116
TR 48,242,32768
TR 50,242,40960
MA 232,64,112
MA 100,24,112
TR 52,242,49152
TR 54,242,57344
MA 100,28,116
buffer_load_dwordx4 v252, s[12:15], s59 offen lds
MA 228,20,116
s_add_u32 s59, s59, 65536
TR 56,243,32768
TR 58,243,40960
MA 228,16,112
MA 96,8,112
s_add_u32 m0, s58, 28672
TR 60,243,49152
TR 62,243,57344
MA 96,12,116
MA 224,4,116
RD 104,248,2048
RD 108,249,2048
MA 224,0,112
buffer_load_dwordx4 v252, s[12:15], s59 offen lds
v_mfma_f32_16x16x32_bf16 a[248:251], a[64:67], a[120:123], a[248:251]
v_mfma_f32_16x16x32_bf16 a[248:251], a[68:71], a[124:127], a[248:251]
buffer_load_dwordx4 a[236:239], v253, s[20:23], s75 offen offset:1024 sc1
MA 120,76,124
s_add_u32 s59, s59, 0x10000
TR 64,244,32768
TR 66,244,40960
MA 120,72,120
s_add_u32 s74, s74, 2048
v_mfma_f32_16x16x32_bf16 a[252:255], a[80:83], a[120:123], a[252:255]
s_add_u32 s7, s58, 32768
TR 68,244,49152
TR 70,244,57344
v_mfma_f32_16x16x32_bf16 a[252:255], a[84:87], a[124:127], a[252:255]
s_add_u32 s75, s75, 2048
MA 124,92,124
s_sub_u32 s58, s58, 131072
TR 72,245,32768
TR 74,245,40960
MA 124,88,120
v_mfma_f32_16x16x32_bf16 a[240:243], a[0:3], a[120:123], a[240:243]
s_min_u32 s58, s7, s58
TR 76,245,49152
TR 78,245,57344
v_mfma_f32_16x16x32_bf16 a[240:243], a[4:7], a[124:127], a[240:243]
MA 112,12,124
TR 80,246,32768
TR 82,246,40960
MA 112,8,120
v_mfma_f32_16x16x32_bf16 a[244:247], a[16:19], a[120:123], a[244:247]
TR 84,246,49152
TR 86,246,57344
v_mfma_f32_16x16x32_bf16 a[244:247], a[20:23], a[124:127], a[244:247]
TR 88,247,32768
TR 90,247,40960
MA 116,28,124
TR 92,247,49152
TR 94,247,57344
MA 116,24,120
WL 15
MA 128,32,96
s_add_u32 m0, s58, 0
v_add_u32 v240, s54, v240
v_add_u32 v241, s54, v241
MA 128,36,100
v_add_u32 v242, s54, v242
v_add_u32 v243, s54, v243
MA 0,44,100
v_add_u32 v244, s54, v244
v_add_u32 v245, s54, v245
MA 0,40,96
v_add_u32 v246, s54, v246
v_add_u32 v247, s54, v247
MA 132,48,96
MA 132,52,100
buffer_load_dwordx4 v252, s[12:15], s59 offen lds
MA 4,60,100
MA 4,56,96
WL 0
s_barrier
MA 136,64,96
s_add_u32 s59, s59, 65536
RD 112,248,4096
RD 116,249,4096
MA 136,68,100
MA 8,76,100
s_add_u32 m0, s58, 4096
RD 120,248,6144
RD 124,249,6144
MA 8,72,96
buffer_load_dwordx4 v252, s[12:15], s59 offen lds
MA 140,80,96
s_add_u32 s59, s59, 65536
MA 140,84,100
s_add_u32 m0, s58, 8192
MA 12,92,100
MA 12,88,96
MA 144,32,104
MA 144,36,108
buffer_load_dwordx4 v252, s[12:15], s59 offen lds
MA 16,44,108
MA 16,40,104
MA 148,48,104
PK 128,128,129
PK 129,130,131
MA 148,52,108
PK 130,0,1
PK 131,2,3
MA 20,60,108
s_add_u32 s59, s59, 65536
RD 96,248,8192
RD 100,249,8192
s_add_u32 m0, s58, 12288
MA 20,56,104
buffer_load_dwordx4 v252, s[12:15], s59 offen lds
MA 152,64,104
s_add_u32 s59, s59, 65536
PK 132,132,133
PK 133,134,135
MA 152,68,108
s_add_u32 m0, s58, 16384
PK 134,4,5
PK 135,6,7
MA 24,76,108
PK 136,136,137
PK 137,138,139
MA 24,72,104
PK 138,8,9
PK 139,10,11
MA 156,80,104
PK 140,140,141
PK 141,142,143
MA 156,84,108
buffer_load_dwordx4 v252, s[12:15], s59 offen lds
MA 28,92,108
s_add_u32 s59, s59, 65536
PK 142,12,13
PK 143,14,15
MA 28,88,104
WL 4
MA 44,88,112
s_add_u32 m0, s58, 20480
MA 44,92,116
MA 172,84,116
MA 172,80,112
buffer_load_dwordx4 v252, s[12:15], s59 offen lds
MA 40,72,112
PK 144,144,145
PK 145,146,147
MA 40,76,116
PK 146,16,17
PK 147,18,19
MA 168,68,116
s_add_u32 s59, s59, 65536
RD 104,248,10240
RD 108,249,10240
MA 168,64,112
s_add_u32 m0, s58, 24576
PK 148,148,149
PK 149,150,151
MA 36,56,112
PK 150,20,21
PK 151,22,23
MA 36,60,116
buffer_load_dwordx4 v252, s[12:15], s59 offen lds
MA 164,52,116
s_add_u32 s59, s59, 65536
PK 152,152,153
PK 153,154,155
MA 164,48,112
s_add_u32 m0, s58, 28672
PK 154,24,25
PK 155,26,27
MA 32,40,112
PK 156,156,157
PK 157,158,159
MA 32,44,116
PK 158,28,29
PK 159,30,31
MA 160,36,116
buffer_load_dwordx4 v252, s[12:15], s59 offen lds
MA 160,32,112
WL 4
MA 176,32,120
s_add_u32 s59, s59, 0x10000
MA 176,36,124
s_add_u32 s7, s58, 32768
MA 48,44,124
MA 48,40,120
MA 180,48,120
s_add_u32 s51, s73, 0
PK 160,160,161
PK 161,162,163
MA 180,52,124
buffer_store_dwordx4 v[128:131], v253, s[20:23], s51 offen offset:0 sc1
MA 52,60,124
s_sub_u32 s58, s58, 131072
RD 112,248,12288
RD 116,249,12288
MA 52,56,120
s_min_u32 s58, s7, s58
PK 162,32,33
PK 163,34,35
MA 184,64,120
s_add_u32 m0, s58, 0
PK 164,164,165
PK 165,166,167
MA 184,68,124
buffer_load_dwordx4 v252, s[12:15], s59 offen lds
MA 56,76,124
s_add_u32 s59, s59, 65536
PK 166,36,37
PK 167,38,39
MA 56,72,120
s_add_u32 m0, s58, 4096
PK 168,168,169
PK 169,170,171
MA 188,80,120
PK 170,40,41
PK 171,42,43
MA 188,84,124
PK 172,172,173
PK 173,174,175
MA 60,92,124
PK 174,44,45
PK 175,46,47
MA 60,88,120
WL 4
MA 76,88,96
buffer_store_dwordx4 v[132:135], v253, s[20:23], s51 offen offset:1024 sc1
MA 76,92,100
buffer_load_dwordx4 v252, s[12:15], s59 offen lds
MA 204,84,100
MA 204,80,96
MA 72,72,96
PK 176,176,177
PK 177,178,179
MA 72,76,100
PK 178,48,49
PK 179,50,51
MA 200,68,100
s_add_u32 s59, s59, 65536
RD 120,248,14336
RD 124,249,14336
MA 200,64,96
s_add_u32 m0, s58, 8192
PK 180,180,181
PK 181,182,183
MA 68,56,96
PK 182,52,53
PK 183,54,55
MA 68,60,100
buffer_load_dwordx4 v252, s[12:15], s59 offen lds
MA 196,52,100
s_add_u32 s59, s59, 65536
PK 184,184,185
PK 185,186,187
MA 196,48,96
s_add_u32 m0, s58, 12288
PK 186,56,57
PK 187,58,59
MA 64,40,96
buffer_store_dwordx4 v[136:139], v253, s[20:23], s51 offen offset:2048 sc1
MA 64,44,100
PK 188,188,189
PK 189,190,191
MA 192,36,100
PK 190,60,61
PK 191,62,63
MA 192,32,96
WL 4
MA 208,32,104
MA 208,36,108
buffer_load_dwordx4 v252, s[12:15], s59 offen lds
MA 80,44,108
s_add_u32 s59, s59, 65536
MA 80,40,104
s_add_u32 m0, s58, 16384
MA 212,48,104
PK 192,192,193
PK 193,194,195
MA 212,52,108
PK 194,64,65
PK 195,66,67
MA 84,60,108
PK 196,196,197
PK 197,198,199
MA 84,56,104
PK 198,68,69
PK 199,70,71
MA 216,64,104
buffer_store_dwordx4 v[140:143], v253, s[20:23], s51 offen offset:3072 sc1
MA 216,68,108
buffer_load_dwordx4 v252, s[12:15], s59 offen lds
MA 88,76,108
s_add_u32 s59, s59, 65536
PK 200,200,201
PK 201,202,203
MA 88,72,104
s_add_u32 m0, s58, 20480
PK 202,72,73
PK 203,74,75
MA 220,80,104
PK 204,204,205
PK 205,206,207
MA 220,84,108
PK 206,76,77
PK 207,78,79
MA 92,92,108
MA 92,88,104
WL 2
MA 108,88,112
MA 108,92,116
buffer_load_dwordx4 v252, s[12:15], s59 offen lds
MA 236,84,116
s_add_u32 s59, s59, 65536
MA 236,80,112
s_add_u32 m0, s58, 24576
MA 104,72,112
s_add_u32 s51, s73, 8192
PK 208,208,209
PK 209,210,211
MA 104,76,116
buffer_store_dwordx4 v[144:147], v253, s[20:23], s51 offen offset:0 sc1
MA 232,68,116
PK 210,80,81
PK 211,82,83
MA 232,64,112
buffer_load_dwordx4 v252, s[12:15], s59 offen lds
MA 100,56,112
s_add_u32 s59, s59, 65536
PK 212,212,213
PK 213,214,215
MA 100,60,116
s_add_u32 m0, s58, 28672
PK 214,84,85
PK 215,86,87
MA 228,52,116
PK 216,216,217
PK 217,218,219
MA 228,48,112
PK 218,88,89
PK 219,90,91
MA 96,40,112
PK 220,220,221
PK 221,222,223
MA 96,44,116
buffer_load_dwordx4 v252, s[12:15], s59 offen lds
)A"
R"A(MA 224,36,116
PK 222,92,93
PK 223,94,95
MA 224,32,112
s_waitcnt vmcnt(22) lgkmcnt(0)
s_barrier
v_mfma_f32_16x16x32_bf16 a[248:251], a[64:67], a[120:123], a[248:251]
s_add_u32 s59, s59, 0x10000
TR 0,240,0
TR 2,240,8192
v_mfma_f32_16x16x32_bf16 a[248:251], a[68:71], a[124:127], a[248:251]
buffer_store_dwordx4 v[148:151], v253, s[20:23], s51 offen offset:1024 sc1
MA 120,76,124
s_add_u32 s7, s58, 32768
TR 4,240,16384
TR 6,240,24576
MA 120,72,120
s_mov_b32 s57, 32768
v_mfma_f32_16x16x32_bf16 a[252:255], a[80:83], a[120:123], a[252:255]
s_sub_u32 s58, s58, 131072
TR 8,241,0
TR 10,241,8192
v_mfma_f32_16x16x32_bf16 a[252:255], a[84:87], a[124:127], a[252:255]
PK 224,224,225
PK 225,226,227
MA 124,92,124
s_min_u32 s58, s7, s58
TR 12,241,16384
TR 14,241,24576
MA 124,88,120
PK 226,96,97
PK 227,98,99
v_mfma_f32_16x16x32_bf16 a[240:243], a[32:35], a[120:123], a[240:243]
TR 16,242,0
TR 18,242,8192
v_mfma_f32_16x16x32_bf16 a[240:243], a[36:39], a[124:127], a[240:243]
PK 228,228,229
PK 229,230,231
MA 112,44,124
TR 20,242,16384
TR 22,242,24576
MA 112,40,120
PK 230,100,101
PK 231,102,103
v_mfma_f32_16x16x32_bf16 a[244:247], a[48:51], a[120:123], a[244:247]
TR 24,243,0
TR 26,243,8192
v_mfma_f32_16x16x32_bf16 a[244:247], a[52:55], a[124:127], a[244:247]
PK 232,232,233
PK 233,234,235
MA 116,60,124
TR 28,243,16384
TR 30,243,24576
MA 116,56,120
WL 12
PK 234,104,105
PK 235,106,107
MZ 0,0,128
s_add_u32 m0, s58, 0
TR 32,244,0
TR 34,244,8192
MA 0,4,136
MZ 4,4,140
TR 36,244,16384
TR 38,244,24576
MA 4,0,132
WL 12
PK 115,114,115
PK 114,112,113
MZ 8,8,128
TR 40,245,0
TR 42,245,8192
v_accvgpr_read_b32 v112, a240
MA 8,12,136
buffer_load_dwordx4 v252, s[12:15], s59 offen lds
MZ 12,12,140
s_add_u32 s59, s59, 65536
TR 44,245,16384
TR 46,245,24576
MA 12,8,132
WL 12
v_accvgpr_read_b32 v113, a241
PK 112,112,113
v_accvgpr_read_b32 v113, a242
MZ 16,16,128
s_add_u32 m0, s58, 4096
TR 48,246,0
TR 50,246,8192
MA 16,20,136
buffer_load_dwordx4 v252, s[12:15], s59 offen lds
MZ 20,20,140
s_add_u32 s59, s59, 65536
TR 52,246,16384
TR 54,246,24576
MA 20,16,132
WL 12
v_accvgpr_read_b32 v254, a243
PK 113,113,254
PK 119,118,119
MZ 24,24,128
s_add_u32 m0, s58, 8192
TR 56,247,0
TR 58,247,8192
MA 24,28,136
PK 118,116,117
v_accvgpr_read_b32 v116, a244
v_accvgpr_read_b32 v117, a245
PK 236,236,237
MZ 28,28,140
TR 60,247,16384
TR 62,247,24576
MA 28,24,132
WL 12
PK 116,116,117
v_accvgpr_read_b32 v117, a246
MZ 32,32,128
TR 0,240,256
TR 2,240,8448
v_accvgpr_read_b32 v254, a247
MA 32,36,136
buffer_load_dwordx4 v252, s[12:15], s59 offen lds
MZ 36,36,140
s_add_u32 s59, s59, 65536
TR 4,240,16640
TR 6,240,24832
MA 36,32,132
WL 12
PK 117,117,254
PK 123,122,123
PK 122,120,121
MZ 40,40,128
s_add_u32 m0, s58, 12288
TR 8,241,256
TR 10,241,8448
MA 40,44,136
buffer_load_dwordx4 v252, s[12:15], s59 offen lds
MZ 44,44,140
s_add_u32 s59, s59, 65536
TR 12,241,16640
TR 14,241,24832
v_accvgpr_read_b32 v120, a248
MA 44,40,132
WL 12
v_accvgpr_read_b32 v121, a249
PK 120,120,121
MZ 48,48,128
s_add_u32 m0, s58, 16384
TR 16,242,256
TR 18,242,8448
MA 48,52,136
v_accvgpr_read_b32 v121, a250
v_accvgpr_read_b32 v254, a251
PK 121,121,254
MZ 52,52,140
s_add_u32 s6, s57, 32768
TR 20,242,16640
TR 22,242,24832
MA 52,48,132
WL 12
PK 127,126,127
PK 126,124,125
v_accvgpr_read_b32 v124, a252
MZ 56,56,128
s_sub_u32 s61, s57, 131072
TR 24,243,256
TR 26,243,8448
MA 56,60,136
buffer_load_dwordx4 v252, s[12:15], s59 offen lds
MZ 60,60,140
s_add_u32 s59, s59, 65536
TR 28,243,16640
TR 30,243,24832
MA 60,56,132
WL 12
v_accvgpr_read_b32 v125, a253
PK 124,124,125
v_accvgpr_read_b32 v125, a254
MZ 64,0,128
s_add_u32 m0, s58, 20480
TR 32,244,256
TR 34,244,8448
MA 64,4,136
buffer_load_dwordx4 v252, s[12:15], s59 offen lds
MZ 68,4,140
s_add_u32 s59, s59, 65536
TR 36,244,16640
TR 38,244,24832
v_accvgpr_read_b32 v254, a255
PK 237,238,239
MA 68,0,132
WL 12
s_add_u32 s51, s73, 90112
PK 125,125,254
MZ 72,8,128
s_add_u32 m0, s58, 24576
TR 40,245,256
TR 42,245,8448
MA 72,12,136
buffer_store_dwordx4 v[112:115], v253, s[20:23], s51 offen offset:0 sc1
MZ 76,12,140
s_min_u32 s6, s6, s61
TR 44,245,16640
TR 46,245,24832
MA 76,8,132
WL 12
PK 238,108,109
PK 239,110,111
MZ 80,16,128
s_sub_u32 s61, s6, s57
TR 48,246,256
TR 50,246,8448
s_mov_b32 s57, s6
MA 80,20,136
buffer_load_dwordx4 v252, s[12:15], s59 offen lds
v_add_u32 v241, s61, v241
MZ 84,20,140
s_add_u32 s59, s59, 65536
TR 52,246,16640
TR 54,246,24832
v_add_u32 v242, s61, v242
MA 84,16,132
WL 12
buffer_store_dwordx4 v[116:119], v253, s[20:23], s51 offen offset:1024 sc1
MZ 88,24,128
s_add_u32 m0, s58, 28672
TR 56,247,256
TR 58,247,8448
MA 88,28,136
buffer_load_dwordx4 v252, s[12:15], s59 offen lds
MZ 92,28,140
s_add_u32 s59, s59, 0x10000
TR 60,247,16640
TR 62,247,24832
MA 92,24,132
s_waitcnt vmcnt(24) lgkmcnt(12)
s_barrier
v_add_u32 v240, s61, v240
MZ 96,32,128
s_add_u32 s7, s58, 32768
TR 0,240,0
TR 2,240,8192
MA 96,36,136
buffer_store_dwordx4 v[120:123], v253, s[20:23], s51 offen offset:2048 sc1
MZ 100,36,140
s_sub_u32 s58, s58, 131072
TR 4,240,16384
TR 6,240,24576
MA 100,32,132
WL 12
buffer_store_dwordx4 v[124:127], v253, s[20:23], s51 offen offset:3072 sc1
MZ 104,40,128
s_min_u32 s58, s7, s58
TR 8,241,0
TR 10,241,8192
MA 104,44,136
v_add_u32 v243, s61, v243
v_add_u32 v244, s61, v244
MZ 108,44,140
s_lshl_b32 s74, s77, 3
TR 12,241,16384
TR 14,241,24576
MA 108,40,132
WL 12
v_add_u32 v245, s61, v245
v_add_u32 v246, s61, v246
MZ 112,48,128
s_add_u32 s74, s74, s8
TR 16,242,0
TR 18,242,8192
MA 112,52,136
v_add_u32 v247, s61, v247
MZ 116,52,140
s_mul_i32 s74, s74, 14336
TR 20,242,16384
TR 22,242,24576
MA 116,48,132
WL 12
MZ 120,56,128
s_add_u32 s74, s74, 0x2200000
TR 24,243,0
TR 26,243,8192
MA 120,60,136
MZ 124,60,140
s_add_u32 s75, s74, 57344
TR 28,243,16384
TR 30,243,24576
MA 124,56,132
WL 12
MA 0,0,144
s_add_u32 m0, s58, 0
TR 32,244,0
TR 34,244,8192
MA 0,4,152
MA 4,4,156
s_add_u32 s51, s73, 8192
TR 36,244,16384
TR 38,244,24576
MA 4,0,148
WL 12
buffer_store_dwordx4 v[152:155], v253, s[20:23], s51 offen offset:2048 sc1
MA 8,8,144
s_add_u32 s6, s57, 32768
TR 40,245,0
TR 42,245,8192
MA 8,12,152
buffer_load_dwordx4 v252, s[12:15], s59 offen lds
MA 12,12,156
s_add_u32 s59, s59, 65536
TR 44,245,16384
TR 46,245,24576
MA 12,8,148
WL 12
s_sub_u32 s61, s57, 131072
MA 16,16,144
s_add_u32 m0, s58, 4096
TR 48,246,0
TR 50,246,8192
MA 16,20,152
buffer_load_dwordx4 v252, s[12:15], s59 offen lds
MA 20,20,156
s_add_u32 s59, s59, 65536
TR 52,246,16384
TR 54,246,24576
MA 20,16,148
WL 12
MA 24,24,144
s_add_u32 m0, s58, 8192
TR 56,247,0
TR 58,247,8192
MA 24,28,152
s_min_u32 s6, s6, s61
MA 28,28,156
s_sub_u32 s61, s6, s57
TR 60,247,16384
TR 62,247,24576
MA 28,24,148
WL 12
MA 32,32,144
s_mov_b32 s57, s6
TR 0,240,256
TR 2,240,8448
MA 32,36,152
buffer_load_dwordx4 v252, s[12:15], s59 offen lds
MA 36,36,156
s_add_u32 s59, s59, 65536
TR 4,240,16640
TR 6,240,24832
MA 36,32,148
WL 12
v_add_u32 v240, s61, v240
MA 40,40,144
s_add_u32 m0, s58, 12288
TR 8,241,256
TR 10,241,8448
MA 40,44,152
buffer_load_dwordx4 v252, s[12:15], s59 offen lds
MA 44,44,156
s_add_u32 s59, s59, 65536
TR 12,241,16640
TR 14,241,24832
MA 44,40,148
WL 12
buffer_store_dwordx4 v[156:159], v253, s[20:23], s51 offen offset:3072 sc1
MA 48,48,144
s_add_u32 m0, s58, 16384
TR 16,242,256
TR 18,242,8448
MA 48,52,152
v_add_u32 v241, s61, v241
MA 52,52,156
TR 20,242,16640
TR 22,242,24832
MA 52,48,148
WL 12
v_add_u32 v242, s61, v242
MA 56,56,144
TR 24,243,256
TR 26,243,8448
MA 56,60,152
buffer_load_dwordx4 v252, s[12:15], s59 offen lds
MA 60,60,156
s_add_u32 s59, s59, 65536
TR 28,243,16640
TR 30,243,24832
MA 60,56,148
WL 12
v_add_u32 v243, s61, v243
MA 64,0,144
s_add_u32 m0, s58, 20480
TR 32,244,256
TR 34,244,8448
MA 64,4,152
buffer_load_dwordx4 v252, s[12:15], s59 offen lds
MA 68,4,156
s_add_u32 s59, s59, 65536
TR 36,244,16640
TR 38,244,24832
MA 68,0,148
WL 12
v_add_u32 v244, s61, v244
MA 72,8,144
s_add_u32 m0, s58, 24576
TR 40,245,256
TR 42,245,8448
MA 72,12,152
MA 76,12,156
TR 44,245,16640
TR 46,245,24832
MA 76,8,148
WL 12
v_add_u32 v245, s61, v245
MA 80,16,144
TR 48,246,256
TR 50,246,8448
MA 80,20,152
buffer_load_dwordx4 v252, s[12:15], s59 offen lds
MA 84,20,156
s_add_u32 s59, s59, 65536
TR 52,246,16640
TR 54,246,24832
MA 84,16,148
WL 12
s_add_u32 s51, s73, 16384
v_add_u32 v246, s61, v246
MA 88,24,144
s_add_u32 m0, s58, 28672
TR 56,247,256
TR 58,247,8448
MA 88,28,152
buffer_load_dwordx4 v252, s[12:15], s59 offen lds
MA 92,28,156
s_add_u32 s59, s59, 0x10000
TR 60,247,16640
TR 62,247,24832
MA 92,24,148
s_waitcnt vmcnt(23) lgkmcnt(12)
s_barrier
v_add_u32 v247, s61, v247
MA 96,32,144
s_add_u32 s7, s58, 32768
TR 0,240,0
TR 2,240,8192
MA 96,36,152
buffer_store_dwordx4 v[160:163], v253, s[20:23], s51 offen offset:0 sc1
MA 100,36,156
s_sub_u32 s58, s58, 131072
TR 4,240,16384
TR 6,240,24576
MA 100,32,148
WL 12
MA 104,40,144
s_min_u32 s58, s7, s58
TR 8,241,0
TR 10,241,8192
MA 104,44,152
MA 108,44,156
TR 12,241,16384
TR 14,241,24576
MA 108,40,148
WL 12
MA 112,48,144
TR 16,242,0
TR 18,242,8192
MA 112,52,152
MA 116,52,156
TR 20,242,16384
TR 22,242,24576
MA 116,48,148
WL 12
MA 120,56,144
TR 24,243,0
TR 26,243,8192
MA 120,60,152
MA 124,60,156
TR 28,243,16384
TR 30,243,24576
MA 124,56,148
WL 12
MA 0,0,160
s_add_u32 m0, s58, 0
TR 32,244,0
TR 34,244,8192
MA 0,4,168
MA 4,4,172
s_add_u32 s6, s57, 32768
TR 36,244,16384
TR 38,244,24576
MA 4,0,164
WL 12
buffer_store_dwordx4 v[164:167], v253, s[20:23], s51 offen offset:1024 sc1
MA 8,8,160
s_sub_u32 s61, s57, 131072
TR 40,245,0
TR 42,245,8192
MA 8,12,168
buffer_load_dwordx4 v252, s[12:15], s59 offen lds
MA 12,12,172
s_add_u32 s59, s59, 65536
TR 44,245,16384
TR 46,245,24576
MA 12,8,164
WL 12
s_min_u32 s6, s6, s61
MA 16,16,160
s_add_u32 m0, s58, 4096
TR 48,246,0
TR 50,246,8192
MA 16,20,168
buffer_load_dwordx4 v252, s[12:15], s59 offen lds
MA 20,20,172
s_add_u32 s59, s59, 65536
TR 52,246,16384
TR 54,246,24576
MA 20,16,164
WL 12
MA 24,24,160
s_add_u32 m0, s58, 8192
TR 56,247,0
TR 58,247,8192
MA 24,28,168
s_sub_u32 s61, s6, s57
MA 28,28,172
s_mov_b32 s57, s6
TR 60,247,16384
TR 62,247,24576
MA 28,24,164
WL 12
MA 32,32,160
TR 0,240,256
TR 2,240,8448
MA 32,36,168
buffer_load_dwordx4 v252, s[12:15], s59 offen lds
MA 36,36,172
s_add_u32 s59, s59, 65536
TR 4,240,16640
TR 6,240,24832
MA 36,32,164
WL 12
buffer_store_dwordx4 v[168:171], v253, s[20:23], s51 offen offset:2048 sc1
MA 40,40,160
s_add_u32 m0, s58, 12288
TR 8,241,256
TR 10,241,8448
MA 40,44,168
buffer_load_dwordx4 v252, s[12:15], s59 offen lds
MA 44,44,172
s_add_u32 s59, s59, 65536
TR 12,241,16640
TR 14,241,24832
MA 44,40,164
WL 12
v_add_u32 v240, s61, v240
v_add_u32 v241, s61, v241
MA 48,48,160
s_add_u32 m0, s58, 16384
TR 16,242,256
TR 18,242,8448
MA 48,52,168
MA 52,52,172
TR 20,242,16640
TR 22,242,24832
MA 52,48,164
WL 12
v_add_u32 v242, s61, v242
MA 56,56,160
TR 24,243,256
TR 26,243,8448
MA 56,60,168
buffer_load_dwordx4 v252, s[12:15], s59 offen lds
MA 60,60,172
s_add_u32 s59, s59, 65536
TR 28,243,16640
TR 30,243,24832
MA 60,56,164
WL 12
v_add_u32 v243, s61, v243
MA 64,0,160
s_add_u32 m0, s58, 20480
TR 32,244,256
TR 34,244,8448
MA 64,4,168
buffer_load_dwordx4 v252, s[12:15], s59 offen lds
MA 68,4,172
s_add_u32 s59, s59, 65536
TR 36,244,16640
TR 38,244,24832
MA 68,0,164
WL 12
buffer_store_dwordx4 v[172:175], v253, s[20:23], s51 offen offset:3072 sc1
MA 72,8,160
s_add_u32 m0, s58, 24576
TR 40,245,256
TR 42,245,8448
MA 72,12,168
v_add_u32 v244, s61, v244
MA 76,12,172
TR 44,245,16640
TR 46,245,24832
MA 76,8,164
WL 12
v_add_u32 v245, s61, v245
MA 80,16,160
TR 48,246,256
TR 50,246,8448
MA 80,20,168
buffer_load_dwordx4 v252, s[12:15], s59 offen lds
MA 84,20,172
s_add_u32 s59, s59, 65536
TR 52,246,16640
TR 54,246,24832
MA 84,16,164
WL 12
v_add_u32 v246, s61, v246
MA 88,24,160
s_add_u32 m0, s58, 28672
TR 56,247,256
TR 58,247,8448
MA 88,28,168
buffer_load_dwordx4 v252, s[12:15], s59 offen lds
MA 92,28,172
s_add_u32 s59, s59, 0x10000
TR 60,247,16640
TR 62,247,24832
MA 92,24,164
s_waitcnt vmcnt(24) lgkmcnt(12)
s_barrier
v_add_u32 v247, s61, v247
MA 96,32,160
s_add_u32 s7, s58, 32768
TR 0,240,0
TR 2,240,8192
MA 96,36,168
s_add_u32 s51, s73, 24576
MA 100,36,172
)A"
R"A(s_sub_u32 s58, s58, 131072
TR 4,240,16384
TR 6,240,24576
MA 100,32,164
WL 12
buffer_store_dwordx4 v[176:179], v253, s[20:23], s51 offen offset:0 sc1
MA 104,40,160
s_min_u32 s58, s7, s58
TR 8,241,0
TR 10,241,8192
MA 104,44,168
MA 108,44,172
TR 12,241,16384
TR 14,241,24576
MA 108,40,164
WL 12
MA 112,48,160
TR 16,242,0
TR 18,242,8192
MA 112,52,168
MA 116,52,172
TR 20,242,16384
TR 22,242,24576
MA 116,48,164
WL 12
MA 120,56,160
TR 24,243,0
TR 26,243,8192
MA 120,60,168
MA 124,60,172
TR 28,243,16384
TR 30,243,24576
MA 124,56,164
WL 12
MA 0,0,176
s_add_u32 m0, s58, 0
TR 32,244,0
TR 34,244,8192
MA 0,4,184
v_bfe_u32 v250, v253, 4, 4
v_bfe_u32 v251, v253, 8, 2
MA 4,4,188
s_add_u32 s6, s57, 32768
TR 36,244,16384
TR 38,244,24576
MA 4,0,180
WL 12
buffer_store_dwordx4 v[180:183], v253, s[20:23], s51 offen offset:1024 sc1
MA 8,8,176
s_sub_u32 s61, s57, 131072
TR 40,245,0
TR 42,245,8192
MA 8,12,184
buffer_load_dwordx4 v252, s[12:15], s59 offen lds
MA 12,12,188
s_add_u32 s59, s59, 65536
TR 44,245,16384
TR 46,245,24576
MA 12,8,180
WL 12
s_min_u32 s6, s6, s61
v_lshlrev_b32 v251, 2, v251
MA 16,16,176
s_add_u32 m0, s58, 4096
TR 48,246,0
TR 50,246,8192
MA 16,20,184
buffer_load_dwordx4 v252, s[12:15], s59 offen lds
MA 20,20,188
s_add_u32 s59, s59, 65536
TR 52,246,16384
TR 54,246,24576
MA 20,16,180
WL 12
v_sub_u32 v250, v250, v251
v_lshrrev_b32 v250, 1, v250
MA 24,24,176
s_add_u32 m0, s58, 8192
TR 56,247,0
TR 58,247,8192
MA 24,28,184
s_sub_u32 s61, s6, s57
v_and_b32 v251, 16, v253
v_mov_b32 v248, 0x3f80
MA 28,28,188
s_mov_b32 s57, s6
TR 60,247,16384
TR 62,247,24576
MA 28,24,180
WL 12
v_lshlrev_b32 v251, v251, v248
v_min_u32 v248, 1, v250
MA 32,32,176
TR 0,240,256
TR 2,240,8448
MA 32,36,184
buffer_load_dwordx4 v252, s[12:15], s59 offen lds
MA 36,36,188
s_add_u32 s59, s59, 65536
TR 4,240,16640
TR 6,240,24832
MA 36,32,180
WL 12
buffer_store_dwordx4 v[184:187], v253, s[20:23], s51 offen offset:2048 sc1
MA 40,40,176
s_add_u32 m0, s58, 12288
TR 8,241,256
TR 10,241,8448
MA 40,44,184
buffer_load_dwordx4 v252, s[12:15], s59 offen lds
MA 44,44,188
s_add_u32 s59, s59, 65536
TR 12,241,16640
TR 14,241,24832
MA 44,40,180
WL 12
v_add_u32 v240, s61, v240
v_add_u32 v241, s61, v241
MA 48,48,176
s_add_u32 m0, s58, 16384
TR 16,242,256
TR 18,242,8448
MA 48,52,184
v_add_u32 v248, -1, v248
v_and_b32 v248, v248, v251
MA 52,52,188
TR 20,242,16640
TR 22,242,24832
MA 52,48,180
WL 12
v_add_u32 v242, s61, v242
v_accvgpr_write_b32 a250, v248
MA 56,56,176
TR 24,243,256
TR 26,243,8448
MA 56,60,184
buffer_load_dwordx4 v252, s[12:15], s59 offen lds
MA 60,60,188
s_add_u32 s59, s59, 65536
TR 28,243,16640
TR 30,243,24832
MA 60,56,180
WL 12
v_add_u32 v243, s61, v243
v_xor_b32 v250, 1, v250
MA 64,0,176
s_add_u32 m0, s58, 20480
TR 32,244,256
TR 34,244,8448
MA 64,4,184
buffer_load_dwordx4 v252, s[12:15], s59 offen lds
MA 68,4,188
s_add_u32 s59, s59, 65536
TR 36,244,16640
TR 38,244,24832
MA 68,0,180
WL 12
buffer_store_dwordx4 v[188:191], v253, s[20:23], s51 offen offset:3072 sc1
MA 72,8,176
s_add_u32 m0, s58, 24576
TR 40,245,256
TR 42,245,8448
MA 72,12,184
v_add_u32 v244, s61, v244
v_min_u32 v248, 1, v250
MA 76,12,188
TR 44,245,16640
TR 46,245,24832
MA 76,8,180
WL 12
v_add_u32 v245, s61, v245
v_add_u32 v248, -1, v248
MA 80,16,176
TR 48,246,256
TR 50,246,8448
MA 80,20,184
buffer_load_dwordx4 v252, s[12:15], s59 offen lds
MA 84,20,188
s_add_u32 s59, s59, 65536
TR 52,246,16640
TR 54,246,24832
MA 84,16,180
WL 12
v_add_u32 v246, s61, v246
v_and_b32 v248, v248, v251
MA 88,24,176
s_add_u32 m0, s58, 28672
TR 56,247,256
TR 58,247,8448
MA 88,28,184
buffer_load_dwordx4 v252, s[12:15], s59 offen lds
MA 92,28,188
s_lshl_b32 s59, s10, 9
TR 60,247,16640
TR 62,247,24832
MA 92,24,180
s_waitcnt vmcnt(24) lgkmcnt(12)
s_barrier
v_add_u32 v247, s61, v247
MA 96,32,176
s_add_u32 s7, s58, 32768
TR 0,240,0
TR 2,240,8192
MA 96,36,184
s_add_u32 s51, s73, 65536
v_accvgpr_write_b32 a251, v248
MA 100,36,188
s_sub_u32 s58, s58, 131072
TR 4,240,16384
TR 6,240,24576
MA 100,32,180
WL 12
buffer_store_dwordx4 v[192:195], v253, s[20:23], s51 offen offset:0 sc1
MA 104,40,176
s_min_u32 s58, s7, s58
TR 8,241,0
TR 10,241,8192
MA 104,44,184
v_accvgpr_write_b32 a248, 0
v_accvgpr_write_b32 a249, 0
MA 108,44,188
TR 12,241,16384
TR 14,241,24576
MA 108,40,180
WL 12
v_accvgpr_write_b32 a252, 0
v_accvgpr_write_b32 a253, 0
MA 112,48,176
TR 16,242,0
TR 18,242,8192
MA 112,52,184
MA 116,52,188
TR 20,242,16384
TR 22,242,24576
MA 116,48,180
WL 12
MA 120,56,176
TR 24,243,0
TR 26,243,8192
MA 120,60,184
MA 124,60,188
TR 28,243,16384
TR 30,243,24576
MA 124,56,180
WL 12
MA 0,0,192
s_add_u32 m0, s58, 0
TR 32,244,0
TR 34,244,8192
MA 0,4,200
MA 4,4,204
s_add_u32 s6, s57, 32768
TR 36,244,16384
TR 38,244,24576
MA 4,0,196
WL 12
buffer_store_dwordx4 v[196:199], v253, s[20:23], s51 offen offset:1024 sc1
MA 8,8,192
s_sub_u32 s61, s57, 131072
TR 40,245,0
TR 42,245,8192
MA 8,12,200
buffer_load_dwordx4 v252, s[12:15], s59 offen lds
MA 12,12,204
s_add_u32 s59, s59, 65536
TR 44,245,16384
TR 46,245,24576
MA 12,8,196
WL 12
s_min_u32 s6, s6, s61
MA 16,16,192
s_add_u32 m0, s58, 4096
TR 48,246,0
TR 50,246,8192
MA 16,20,200
buffer_load_dwordx4 v252, s[12:15], s59 offen lds
MA 20,20,204
s_add_u32 s59, s59, 65536
TR 52,246,16384
TR 54,246,24576
MA 20,16,196
WL 12
buffer_load_dwordx4 a[128:131], v253, s[20:23], s74 offen offset:0 sc1
MA 24,24,192
s_add_u32 m0, s58, 8192
TR 56,247,0
TR 58,247,8192
MA 24,28,200
s_sub_u32 s61, s6, s57
MA 28,28,204
s_mov_b32 s57, s6
TR 60,247,16384
TR 62,247,24576
MA 28,24,196
WL 12
MA 32,32,192
TR 0,240,256
TR 2,240,8448
MA 32,36,200
buffer_load_dwordx4 v252, s[12:15], s59 offen lds
MA 36,36,204
s_add_u32 s59, s59, 65536
TR 4,240,16640
TR 6,240,24832
MA 36,32,196
WL 12
v_add_u32 v240, s61, v240
MA 40,40,192
s_add_u32 m0, s58, 12288
TR 8,241,256
TR 10,241,8448
MA 40,44,200
buffer_load_dwordx4 v252, s[12:15], s59 offen lds
MA 44,44,204
s_add_u32 s59, s59, 65536
TR 12,241,16640
TR 14,241,24832
MA 44,40,196
WL 12
buffer_store_dwordx4 v[200:203], v253, s[20:23], s51 offen offset:2048 sc1
MA 48,48,192
s_add_u32 m0, s58, 16384
TR 16,242,256
TR 18,242,8448
MA 48,52,200
buffer_load_dwordx4 a[132:135], v253, s[20:23], s75 offen offset:0 sc1
MA 52,52,204
TR 20,242,16640
TR 22,242,24832
MA 52,48,196
WL 12
v_add_u32 v241, s61, v241
v_add_u32 v242, s61, v242
MA 56,56,192
TR 24,243,256
TR 26,243,8448
MA 56,60,200
buffer_load_dwordx4 v252, s[12:15], s59 offen lds
MA 60,60,204
s_add_u32 s59, s59, 65536
TR 28,243,16640
TR 30,243,24832
MA 60,56,196
WL 12
v_add_u32 v243, s61, v243
MA 64,0,192
s_add_u32 m0, s58, 20480
TR 32,244,256
TR 34,244,8448
MA 64,4,200
buffer_load_dwordx4 v252, s[12:15], s59 offen lds
MA 68,4,204
s_add_u32 s59, s59, 65536
TR 36,244,16640
TR 38,244,24832
MA 68,0,196
WL 12
v_add_u32 v244, s61, v244
MA 72,8,192
s_add_u32 m0, s58, 24576
TR 40,245,256
TR 42,245,8448
MA 72,12,200
MA 76,12,204
TR 44,245,16640
TR 46,245,24832
MA 76,8,196
WL 12
buffer_load_dwordx4 a[136:139], v253, s[20:23], s74 offen offset:1024 sc1
MA 80,16,192
TR 48,246,256
TR 50,246,8448
MA 80,20,200
buffer_load_dwordx4 v252, s[12:15], s59 offen lds
MA 84,20,204
s_add_u32 s59, s59, 65536
TR 52,246,16640
TR 54,246,24832
MA 84,16,196
WL 12
v_add_u32 v245, s61, v245
v_add_u32 v246, s61, v246
MA 88,24,192
s_add_u32 m0, s58, 28672
TR 56,247,256
TR 58,247,8448
MA 88,28,200
buffer_load_dwordx4 v252, s[12:15], s59 offen lds
MA 92,28,204
s_add_u32 s59, s59, 0x10000
TR 60,247,16640
TR 62,247,24832
MA 92,24,196
s_waitcnt vmcnt(26) lgkmcnt(12)
s_barrier
v_add_u32 v247, s61, v247
MA 96,32,192
s_add_u32 s7, s58, 32768
TR 0,240,0
TR 2,240,8192
MA 96,36,200
buffer_store_dwordx4 v[204:207], v253, s[20:23], s51 offen offset:3072 sc1
MA 100,36,204
s_sub_u32 s58, s58, 131072
TR 4,240,16384
TR 6,240,24576
MA 100,32,196
WL 12
MA 104,40,192
s_min_u32 s58, s7, s58
TR 8,241,0
TR 10,241,8192
MA 104,44,200
MA 108,44,204
TR 12,241,16384
TR 14,241,24576
MA 108,40,196
WL 12
buffer_load_dwordx4 a[140:143], v253, s[20:23], s75 offen offset:1024 sc1
MA 112,48,192
s_add_u32 s74, s74, 2048
TR 16,242,0
TR 18,242,8192
MA 112,52,200
MA 116,52,204
s_add_u32 s75, s75, 2048
TR 20,242,16384
TR 22,242,24576
MA 116,48,196
WL 12
MA 120,56,192
TR 24,243,0
TR 26,243,8192
MA 120,60,200
MA 124,60,204
TR 28,243,16384
TR 30,243,24576
MA 124,56,196
WL 12
MA 0,0,208
s_add_u32 m0, s58, 0
TR 32,244,0
TR 34,244,8192
MA 0,4,216
v_mov_b32 v254, s48
v_mov_b32 v255, s49
MA 4,4,220
s_add_u32 s51, s73, 73728
TR 36,244,16384
TR 38,244,24576
MA 4,0,212
WL 12
buffer_store_dwordx4 v[208:211], v253, s[20:23], s51 offen offset:0 sc1
MA 8,8,208
s_add_u32 s6, s57, 32768
TR 40,245,0
TR 42,245,8192
MA 8,12,216
buffer_load_dwordx4 v252, s[12:15], s59 offen lds
MA 12,12,220
s_add_u32 s59, s59, 65536
TR 44,245,16384
TR 46,245,24576
MA 12,8,212
WL 12
s_sub_u32 s61, s57, 131072
v_and_b32 v250, 0x380, v253
MA 16,16,208
s_add_u32 m0, s58, 4096
TR 48,246,0
TR 50,246,8192
MA 16,20,216
buffer_load_dwordx4 v252, s[12:15], s59 offen lds
MA 20,20,220
s_add_u32 s59, s59, 65536
TR 52,246,16384
TR 54,246,24576
MA 20,16,212
WL 12
buffer_load_dwordx4 a[144:147], v253, s[20:23], s74 offen offset:0 sc1
MA 24,24,208
s_add_u32 m0, s58, 8192
TR 56,247,0
TR 58,247,8192
MA 24,28,216
s_min_u32 s6, s6, s61
MA 28,28,220
s_sub_u32 s61, s6, s57
TR 60,247,16384
TR 62,247,24576
MA 28,24,212
WL 12
MA 32,32,208
s_mov_b32 s57, s6
TR 0,240,256
TR 2,240,8448
MA 32,36,216
buffer_load_dwordx4 v252, s[12:15], s59 offen lds
MA 36,36,220
s_add_u32 s59, s59, 65536
TR 4,240,16640
TR 6,240,24832
MA 36,32,212
WL 12
buffer_store_dwordx4 v[212:215], v253, s[20:23], s51 offen offset:1024 sc1
MA 40,40,208
s_add_u32 m0, s58, 12288
TR 8,241,256
TR 10,241,8448
MA 40,44,216
buffer_load_dwordx4 v252, s[12:15], s59 offen lds
MA 44,44,220
s_add_u32 s59, s59, 65536
TR 12,241,16640
TR 14,241,24832
MA 44,40,212
WL 12
v_add_u32 v240, s61, v240
v_add_u32 v241, s61, v241
MA 48,48,208
s_add_u32 m0, s58, 16384
TR 16,242,256
TR 18,242,8448
MA 48,52,216
buffer_load_dwordx4 a[148:151], v253, s[20:23], s75 offen offset:0 sc1
MA 52,52,220
TR 20,242,16640
TR 22,242,24832
MA 52,48,212
WL 12
v_add_u32 v242, s61, v242
MA 56,56,208
TR 24,243,256
TR 26,243,8448
MA 56,60,216
buffer_load_dwordx4 v252, s[12:15], s59 offen lds
MA 60,60,220
s_add_u32 s59, s59, 65536
TR 28,243,16640
TR 30,243,24832
MA 60,56,212
WL 12
v_add_u32 v243, s61, v243
s_min_u32 s50, s8, 1
MA 64,0,208
s_add_u32 m0, s58, 20480
TR 32,244,256
TR 34,244,8448
MA 64,4,216
buffer_load_dwordx4 v252, s[12:15], s59 offen lds
MA 68,4,220
s_add_u32 s59, s59, 65536
TR 36,244,16640
TR 38,244,24832
MA 68,0,212
WL 12
buffer_store_dwordx4 v[216:219], v253, s[20:23], s51 offen offset:2048 sc1
MA 72,8,208
s_add_u32 m0, s58, 24576
TR 40,245,256
TR 42,245,8448
MA 72,12,216
s_lshl_b32 s50, s50, 30
v_add_u32 v244, s61, v244
v_bfe_u32 v251, v253, 4, 4
MA 76,12,220
s_lshl_b32 s72, s10, 4
TR 44,245,16640
TR 46,245,24832
MA 76,8,212
WL 12
buffer_load_dwordx4 a[152:155], v253, s[20:23], s74 offen offset:1024 sc1
MA 80,16,208
s_add_u32 s72, s72, s11
TR 48,246,256
TR 50,246,8448
MA 80,20,216
buffer_load_dwordx4 v252, s[12:15], s59 offen lds
MA 84,20,220
s_add_u32 s59, s59, 65536
TR 52,246,16640
TR 54,246,24832
MA 84,16,212
WL 12
v_add_u32 v245, s61, v245
v_add_u32 v246, s61, v246
MA 88,24,208
s_add_u32 m0, s58, 28672
TR 56,247,256
TR 58,247,8448
MA 88,28,216
buffer_load_dwordx4 v252, s[12:15], s59 offen lds
MA 92,28,220
s_add_u32 s59, s59, 0x10000
TR 60,247,16640
TR 62,247,24832
MA 92,24,212
s_waitcnt vmcnt(30) lgkmcnt(12)
s_barrier
v_add_u32 v247, s61, v247
MA 96,32,208
s_add_u32 s7, s58, 32768
TR 0,240,0
TR 2,240,8192
MA 96,36,216
s_lshl_b32 s72, s72, 7
v_lshl_add_u32 v251, v251, 3, s50
MA 100,36,220
s_sub_u32 s58, s58, 131072
TR 4,240,16384
TR 6,240,24576
MA 100,32,212
WL 12
buffer_store_dwordx4 v[220:223], v253, s[20:23], s51 offen offset:3072 sc1
MA 104,40,208
s_min_u32 s58, s7, s58
TR 8,241,0
TR 10,241,8192
MA 104,44,216
MA 108,44,220
s_add_u32 s72, s72, 0x6fc0000
TR 12,241,16384
TR 14,241,24576
MA 108,40,212
WL 12
buffer_load_dwordx4 a[156:159], v253, s[20:23], s75 offen offset:1024 sc1
MA 112,48,208
s_add_u32 s74, s74, 2048
)A"
R"A(TR 16,242,0
TR 18,242,8192
MA 112,52,216
MA 116,52,220
s_add_u32 s75, s75, 2048
TR 20,242,16384
TR 22,242,24576
MA 116,48,212
WL 12
MA 120,56,208
TR 24,243,0
TR 26,243,8192
MA 120,60,216
MA 124,60,220
TR 28,243,16384
TR 30,243,24576
MA 124,56,212
WL 12
MA 0,0,224
s_add_u32 m0, s58, 0
TR 32,244,0
TR 34,244,8192
MA 0,4,232
MA 4,4,236
s_add_u32 s51, s73, 81920
TR 36,244,16384
TR 38,244,24576
MA 4,0,228
WL 12
buffer_store_dwordx4 v[224:227], v253, s[20:23], s51 offen offset:0 sc1
MA 8,8,224
s_add_u32 s6, s57, 32768
TR 40,245,0
TR 42,245,8192
MA 8,12,232
buffer_load_dwordx4 v252, s[12:15], s59 offen lds
MA 12,12,236
s_add_u32 s59, s59, 65536
TR 44,245,16384
TR 46,245,24576
MA 12,8,228
WL 12
s_sub_u32 s61, s57, 131072
MA 16,16,224
s_add_u32 m0, s58, 4096
TR 48,246,0
TR 50,246,8192
MA 16,20,232
buffer_load_dwordx4 v252, s[12:15], s59 offen lds
MA 20,20,236
s_add_u32 s59, s59, 65536
TR 52,246,16384
TR 54,246,24576
MA 20,16,228
WL 12
buffer_store_dwordx4 v[228:231], v253, s[20:23], s51 offen offset:1024 sc1
MA 24,24,224
s_add_u32 m0, s58, 8192
TR 56,247,0
TR 58,247,8192
MA 24,28,232
s_min_u32 s6, s6, s61
PK 128,0,1
PK 129,2,3
MA 28,28,236
s_sub_u32 s61, s6, s57
TR 60,247,16384
TR 62,247,24576
MA 28,24,228
WL 12
PK 130,8,9
PK 131,10,11
MA 32,32,224
s_mov_b32 s57, s6
TR 0,240,256
TR 2,240,8448
MA 32,36,232
buffer_load_dwordx4 v252, s[12:15], s59 offen lds
MA 36,36,236
s_add_u32 s59, s59, 65536
TR 4,240,16640
TR 6,240,24832
MA 36,32,228
WL 12
buffer_store_dwordx4 v[232:235], v253, s[20:23], s51 offen offset:2048 sc1
MA 40,40,224
s_add_u32 m0, s58, 12288
TR 8,241,256
TR 10,241,8448
MA 40,44,232
buffer_load_dwordx4 v252, s[12:15], s59 offen lds
MA 44,44,236
s_add_u32 s59, s59, 65536
TR 12,241,16640
TR 14,241,24832
MA 44,40,228
WL 12
v_add_u32 v240, s61, v240
v_add_u32 v241, s61, v241
MA 48,48,224
s_add_u32 m0, s58, 16384
TR 16,242,256
TR 18,242,8448
MA 48,52,232
PK 132,4,5
PK 133,6,7
MA 52,52,236
TR 20,242,16640
TR 22,242,24832
MA 52,48,228
WL 12
buffer_store_dwordx4 v[236:239], v253, s[20:23], s51 offen offset:3072 sc1
MA 56,56,224
TR 24,243,256
TR 26,243,8448
MA 56,60,232
buffer_load_dwordx4 v252, s[12:15], s59 offen lds
MA 60,60,236
s_add_u32 s59, s59, 65536
TR 28,243,16640
TR 30,243,24832
MA 60,56,228
WL 12
v_add_u32 v242, s61, v242
v_add_u32 v243, s61, v243
MA 64,0,224
s_add_u32 m0, s58, 20480
TR 32,244,256
TR 34,244,8448
MA 64,4,232
buffer_load_dwordx4 v252, s[12:15], s59 offen lds
MA 68,4,236
s_add_u32 s59, s59, 65536
TR 36,244,16640
TR 38,244,24832
MA 68,0,228
WL 12
v_add_u32 v244, s61, v244
PK 134,12,13
MA 72,8,224
s_add_u32 m0, s58, 24576
TR 40,245,256
TR 42,245,8448
MA 72,12,232
PK 135,14,15
PK 136,16,17
MA 76,12,236
s_lshl_b32 s73, s10, 3
TR 44,245,16640
TR 46,245,24832
MA 76,8,228
WL 12
v_add_u32 v245, s61, v245
PK 137,18,19
MA 80,16,224
s_add_u32 s73, s73, s11
TR 48,246,256
TR 50,246,8448
MA 80,20,232
buffer_load_dwordx4 v252, s[12:15], s59 offen lds
MA 84,20,236
s_add_u32 s59, s59, 65536
TR 52,246,16640
TR 54,246,24832
MA 84,16,228
WL 12
v_add_u32 v246, s61, v246
s_lshl_b32 s73, s73, 2
MA 88,24,224
s_add_u32 m0, s58, 28672
TR 56,247,256
TR 58,247,8448
MA 88,28,232
buffer_load_dwordx4 v252, s[12:15], s59 offen lds
MA 92,28,236
s_add_u32 s59, s59, 0x10000
TR 60,247,16640
TR 62,247,24832
MA 92,24,228
s_waitcnt vmcnt(30) lgkmcnt(12)
s_barrier
v_add_u32 v247, s61, v247
MA 96,32,224
s_add_u32 s7, s58, 32768
TR 0,240,0
TR 2,240,8192
MA 96,36,232
s_add_u32 s73, s73, s8
PK 138,24,25
PK 139,26,27
MA 100,36,236
s_sub_u32 s58, s58, 131072
TR 4,240,16384
TR 6,240,24576
MA 100,32,228
WL 12
PK 140,20,21
PK 141,22,23
MA 104,40,224
s_min_u32 s58, s7, s58
TR 8,241,0
TR 10,241,8192
MA 104,44,232
PK 142,28,29
PK 143,30,31
MA 108,44,236
s_lshl_b32 s73, s73, 14
TR 12,241,16384
TR 14,241,24576
MA 108,40,228
WL 12
PK 144,32,33
PK 145,34,35
MA 112,48,224
s_add_u32 s73, s73, 0x2400000
TR 16,242,0
TR 18,242,8192
MA 112,52,232
PK 146,40,41
PK 147,42,43
MA 116,52,236
TR 20,242,16384
TR 22,242,24576
MA 116,48,228
WL 12
PK 148,36,37
PK 149,38,39
MA 120,56,224
TR 24,243,0
TR 26,243,8192
MA 120,60,232
PK 150,44,45
PK 151,46,47
MA 124,60,236
TR 28,243,16384
TR 30,243,24576
MA 124,56,228
WL 12
PK 152,48,49
PK 153,50,51
PK 154,56,57
PK 155,58,59
PK 156,52,53
PK 157,54,55
PK 158,60,61
PK 159,62,63
PK 160,64,65
PK 161,66,67
PK 162,72,73
PK 163,74,75
PK 164,68,69
PK 165,70,71
PK 166,76,77
PK 167,78,79
PK 168,80,81
PK 169,82,83
PK 170,88,89
PK 171,90,91
PK 172,84,85
PK 173,86,87
PK 174,92,93
PK 175,94,95
s_waitcnt vmcnt(28)
MZ 0,0,128
s_add_u32 m0, s58, 0
TR 32,244,0
TR 34,244,8192
MA 0,4,136
PK 176,96,97
PK 177,98,99
PK 178,104,105
MZ 4,4,140
s_add_u32 s51, s73, 0
TR 36,244,16384
TR 38,244,24576
MA 4,0,132
WL 12
buffer_store_dwordx4 v[128:131], v253, s[20:23], s51 offen offset:0 sc1
MZ 8,8,128
s_add_u32 s6, s57, 32768
TR 40,245,0
TR 42,245,8192
MA 8,12,136
buffer_load_dwordx4 v252, s[12:15], s59 offen lds
MZ 12,12,140
s_add_u32 s59, s59, 65536
TR 44,245,16384
TR 46,245,24576
MA 12,8,132
WL 12
s_sub_u32 s61, s57, 131072
PK 179,106,107
PK 180,100,101
MZ 16,16,128
s_add_u32 m0, s58, 4096
TR 48,246,0
TR 50,246,8192
MA 16,20,136
buffer_load_dwordx4 v252, s[12:15], s59 offen lds
MZ 20,20,140
s_add_u32 s59, s59, 65536
TR 52,246,16384
TR 54,246,24576
MA 20,16,132
WL 12
buffer_load_dwordx4 a[160:163], v253, s[20:23], s74 offen offset:0 sc1
MZ 24,24,128
s_add_u32 m0, s58, 8192
TR 56,247,0
TR 58,247,8192
MA 24,28,136
s_min_u32 s6, s6, s61
PK 181,102,103
PK 182,108,109
PK 183,110,111
MZ 28,28,140
s_sub_u32 s61, s6, s57
TR 60,247,16384
TR 62,247,24576
PK 184,112,113
MA 28,24,132
WL 12
PK 185,114,115
PK 186,120,121
MZ 32,32,128
s_mov_b32 s57, s6
TR 0,240,256
TR 2,240,8448
PK 187,122,123
MA 32,36,136
buffer_load_dwordx4 v252, s[12:15], s59 offen lds
MZ 36,36,140
s_add_u32 s59, s59, 65536
TR 4,240,16640
TR 6,240,24832
MA 36,32,132
WL 12
v_add_u32 v240, s61, v240
PK 188,116,117
PK 189,118,119
MZ 40,40,128
s_add_u32 m0, s58, 12288
TR 8,241,256
TR 10,241,8448
MA 40,44,136
buffer_load_dwordx4 v252, s[12:15], s59 offen lds
MZ 44,44,140
s_add_u32 s59, s59, 65536
TR 12,241,16640
TR 14,241,24832
PK 190,124,125
MA 44,40,132
WL 12
buffer_store_dwordx4 v[132:135], v253, s[20:23], s51 offen offset:1024 sc1
MZ 48,48,128
s_add_u32 m0, s58, 16384
TR 16,242,256
TR 18,242,8448
MA 48,52,136
buffer_load_dwordx4 a[164:167], v253, s[20:23], s75 offen offset:0 sc1
MZ 52,52,140
TR 20,242,16640
TR 22,242,24832
PK 191,126,127
MA 52,48,132
WL 12
v_add_u32 v241, s61, v241
v_add_u32 v242, s61, v242
MZ 56,56,128
TR 24,243,256
TR 26,243,8448
MA 56,60,136
buffer_load_dwordx4 v252, s[12:15], s59 offen lds
MZ 60,60,140
s_add_u32 s59, s59, 65536
TR 28,243,16640
TR 30,243,24832
MA 60,56,132
WL 12
v_add_u32 v243, s61, v243
v_accvgpr_write_b32 a64, v128
MZ 64,0,128
s_add_u32 m0, s58, 20480
TR 32,244,256
TR 34,244,8448
MA 64,4,136
buffer_load_dwordx4 v252, s[12:15], s59 offen lds
MZ 68,4,140
s_add_u32 s59, s59, 65536
TR 36,244,16640
TR 38,244,24832
MA 68,0,132
WL 12
v_add_u32 v244, s61, v244
v_accvgpr_write_b32 a65, v129
MZ 72,8,128
s_add_u32 m0, s58, 24576
TR 40,245,256
TR 42,245,8448
MA 72,12,136
buffer_store_dwordx4 v[136:139], v253, s[20:23], s51 offen offset:2048 sc1
MZ 76,12,140
TR 44,245,16640
TR 46,245,24832
MA 76,8,132
WL 12
buffer_load_dwordx4 a[168:171], v253, s[20:23], s74 offen offset:1024 sc1
MZ 80,16,128
TR 48,246,256
TR 50,246,8448
MA 80,20,136
buffer_load_dwordx4 v252, s[12:15], s59 offen lds
MZ 84,20,140
s_add_u32 s59, s59, 65536
TR 52,246,16640
TR 54,246,24832
MA 84,16,132
WL 12
v_add_u32 v245, s61, v245
v_add_u32 v246, s61, v246
MZ 88,24,128
s_add_u32 m0, s58, 28672
TR 56,247,256
TR 58,247,8448
MA 88,28,136
buffer_load_dwordx4 v252, s[12:15], s59 offen lds
MZ 92,28,140
s_add_u32 s59, s59, 0x10000
TR 60,247,16640
TR 62,247,24832
MA 92,24,132
s_waitcnt vmcnt(18) lgkmcnt(12)
s_barrier
v_add_u32 v247, s61, v247
MZ 96,32,128
s_add_u32 s7, s58, 32768
TR 0,240,0
TR 2,240,8192
MA 96,36,136
buffer_store_dwordx2 v[254:255], v251, s[20:23], s72 offen offset:0 sc1
MZ 100,36,140
s_sub_u32 s58, s58, 131072
TR 4,240,16384
TR 6,240,24576
MA 100,32,132
WL 12
v_accvgpr_write_b32 a66, v130
v_accvgpr_write_b32 a67, v131
MZ 104,40,128
s_min_u32 s58, s7, s58
TR 8,241,0
TR 10,241,8192
MA 104,44,136
v_accvgpr_write_b32 a68, v132
v_accvgpr_write_b32 a69, v133
MZ 108,44,140
TR 12,241,16384
TR 14,241,24576
MA 108,40,132
WL 12
buffer_store_dwordx4 v[140:143], v253, s[20:23], s51 offen offset:3072 sc1
MZ 112,48,128
TR 16,242,0
TR 18,242,8192
MA 112,52,136
buffer_load_dwordx4 a[172:175], v253, s[20:23], s75 offen offset:1024 sc1
MZ 116,52,140
s_add_u32 s74, s74, 2048
TR 20,242,16384
TR 22,242,24576
MA 116,48,132
WL 12
v_accvgpr_write_b32 a70, v134
v_accvgpr_write_b32 a71, v135
MZ 120,56,128
s_add_u32 s75, s75, 2048
TR 24,243,0
TR 26,243,8192
MA 120,60,136
v_accvgpr_write_b32 a72, v136
v_accvgpr_write_b32 a73, v137
MZ 124,60,140
TR 28,243,16384
TR 30,243,24576
MA 124,56,132
WL 12
v_accvgpr_write_b32 a74, v138
v_accvgpr_write_b32 a75, v139
v_accvgpr_write_b32 a76, v140
v_accvgpr_write_b32 a77, v141
v_accvgpr_write_b32 a78, v142
v_accvgpr_write_b32 a79, v143
MA 0,0,144
s_add_u32 m0, s58, 0
TR 32,244,0
TR 34,244,8192
MA 0,4,152
v_accvgpr_write_b32 a80, v144
v_accvgpr_write_b32 a81, v145
MA 4,4,156
s_add_u32 s51, s73, 4096
TR 36,244,16384
TR 38,244,24576
MA 4,0,148
WL 12
buffer_store_dwordx4 v[144:147], v253, s[20:23], s51 offen offset:0 sc1
MA 8,8,144
s_add_u32 s6, s57, 32768
TR 40,245,0
TR 42,245,8192
MA 8,12,152
buffer_load_dwordx4 v252, s[12:15], s59 offen lds
MA 12,12,156
s_add_u32 s59, s59, 65536
TR 44,245,16384
TR 46,245,24576
MA 12,8,148
WL 12
s_sub_u32 s61, s57, 131072
v_accvgpr_write_b32 a82, v146
MA 16,16,144
s_add_u32 m0, s58, 4096
TR 48,246,0
TR 50,246,8192
MA 16,20,152
buffer_load_dwordx4 v252, s[12:15], s59 offen lds
MA 20,20,156
s_add_u32 s59, s59, 65536
TR 52,246,16384
TR 54,246,24576
MA 20,16,148
WL 12
buffer_load_dwordx4 a[176:179], v253, s[20:23], s74 offen offset:0 sc1
MA 24,24,144
s_add_u32 m0, s58, 8192
TR 56,247,0
TR 58,247,8192
MA 24,28,152
buffer_store_dwordx4 v[148:151], v253, s[20:23], s51 offen offset:1024 sc1
MA 28,28,156
s_min_u32 s6, s6, s61
TR 60,247,16384
TR 62,247,24576
MA 28,24,148
WL 12
v_accvgpr_write_b32 a83, v147
v_accvgpr_write_b32 a84, v148
MA 32,32,144
s_sub_u32 s61, s6, s57
TR 0,240,256
TR 2,240,8448
MA 32,36,152
buffer_load_dwordx4 v252, s[12:15], s59 offen lds
MA 36,36,156
s_add_u32 s59, s59, 65536
TR 4,240,16640
TR 6,240,24832
MA 36,32,148
WL 12
s_mov_b32 s57, s6
v_add_u32 v240, s61, v240
MA 40,40,144
s_add_u32 m0, s58, 12288
TR 8,241,256
TR 10,241,8448
MA 40,44,152
buffer_load_dwordx4 v252, s[12:15], s59 offen lds
MA 44,44,156
s_add_u32 s59, s59, 65536
TR 12,241,16640
TR 14,241,24832
MA 44,40,148
WL 12
buffer_store_dwordx4 v[152:155], v253, s[20:23], s51 offen offset:2048 sc1
MA 48,48,144
s_add_u32 m0, s58, 16384
TR 16,242,256
TR 18,242,8448
MA 48,52,152
buffer_load_dwordx4 a[180:183], v253, s[20:23], s75 offen offset:0 sc1
MA 52,52,156
TR 20,242,16640
TR 22,242,24832
MA 52,48,148
WL 12
v_add_u32 v241, s61, v241
v_add_u32 v242, s61, v242
MA 56,56,144
TR 24,243,256
TR 26,243,8448
MA 56,60,152
buffer_load_dwordx4 v252, s[12:15], s59 offen lds
MA 60,60,156
s_add_u32 s59, s59, 65536
TR 28,243,16640
TR 30,243,24832
MA 60,56,148
WL 12
buffer_store_dwordx4 v[156:159], v253, s[20:23], s51 offen offset:3072 sc1
MA 64,0,144
s_add_u32 m0, s58, 20480
TR 32,244,256
TR 34,244,8448
MA 64,4,152
buffer_load_dwordx4 v252, s[12:15], s59 offen lds
MA 68,4,156
s_add_u32 s59, s59, 65536
TR 36,244,16640
TR 38,244,24832
MA 68,0,148
WL 12
v_add_u32 v243, s61, v243
v_add_u32 v244, s61, v244
MA 72,8,144
s_add_u32 m0, s58, 24576
TR 40,245,256
TR 42,245,8448
MA 72,12,152
v_lshrrev_b32 v216, 6, v253
v_lshlrev_b32 v216, 7, v216
MA 76,12,156
TR 44,245,16640
TR 46,245,24832
MA 76,8,148
WL 12
buffer_load_dwordx4 a[184:187], v253, s[20:23], s74 offen offset:1024 sc1
MA 80,16,144
TR 48,246,256
TR 50,246,8448
MA 80,20,152
buffer_load_dwordx4 v252, s[12:15], s59 offen lds
MA 84,20,156
s_add_u32 s59, s59, 65536
TR 52,246,16640
TR 54,246,24832
MA 84,16,148
WL 12
v_add_u32 v245, s61, v245
)A"
R"A(v_add_u32 v246, s61, v246
MA 88,24,144
s_add_u32 m0, s58, 28672
TR 56,247,256
TR 58,247,8448
MA 88,28,152
buffer_load_dwordx4 v252, s[12:15], s59 offen lds
MA 92,28,156
s_add_u32 s59, s59, 0x10000
TR 60,247,16640
TR 62,247,24832
MA 92,24,148
s_waitcnt vmcnt(32) lgkmcnt(12)
s_barrier
v_add_u32 v247, s61, v247
MA 96,32,144
s_add_u32 s7, s58, 32768
TR 0,240,0
TR 2,240,8192
MA 96,36,152
s_lshl_b32 s53, s10, 11
v_bfe_u32 v217, v253, 4, 2
v_lshl_add_u32 v216, v217, 3, v216
MA 100,36,156
s_sub_u32 s58, s58, 131072
TR 4,240,16384
TR 6,240,24576
MA 100,32,148
WL 12
v_bfe_u32 v217, v253, 7, 2
v_xor_b32 v212, 0, v217
MA 104,40,144
s_min_u32 s58, s7, s58
TR 8,241,0
TR 10,241,8192
MA 104,44,152
s_add_u32 s53, s53, 0x6fc0000
v_lshl_add_u32 v212, v212, 5, v216
v_xor_b32 v213, 1, v217
MA 108,44,156
TR 12,241,16384
TR 14,241,24576
MA 108,40,148
WL 12
buffer_load_dwordx4 a[188:191], v253, s[20:23], s75 offen offset:1024 sc1
MA 112,48,144
s_add_u32 s74, s74, 2048
TR 16,242,0
TR 18,242,8192
MA 112,52,152
v_lshl_add_u32 v213, v213, 5, v216
v_xor_b32 v214, 2, v217
MA 116,52,156
s_add_u32 s75, s75, 2048
TR 20,242,16384
TR 22,242,24576
MA 116,48,148
WL 12
v_lshl_add_u32 v214, v214, 5, v216
v_xor_b32 v215, 3, v217
MA 120,56,144
TR 24,243,0
TR 26,243,8192
MA 120,60,152
v_lshl_add_u32 v215, v215, 5, v216
v_accvgpr_write_b32 a85, v149
MA 124,60,156
TR 28,243,16384
TR 30,243,24576
MA 124,56,148
s_waitcnt vmcnt(16) lgkmcnt(12)
v_accvgpr_write_b32 a86, v150
v_accvgpr_write_b32 a87, v151
v_accvgpr_write_b32 a88, v152
v_accvgpr_write_b32 a89, v153
v_accvgpr_write_b32 a90, v154
v_accvgpr_write_b32 a91, v155
v_accvgpr_write_b32 a92, v156
v_accvgpr_write_b32 a93, v157
v_accvgpr_write_b32 a94, v158
v_accvgpr_write_b32 a95, v159
MA 0,0,160
s_add_u32 m0, s58, 0
TR 32,244,0
TR 34,244,8192
MA 0,4,168
s_add_u32 s6, s57, 32768
s_sub_u32 s61, s57, 131072
MA 4,4,172
s_add_u32 s51, s73, 8192
TR 36,244,16384
TR 38,244,24576
MA 4,0,164
WL 12
buffer_store_dwordx4 v[160:163], v253, s[20:23], s51 offen offset:0 sc1
MA 8,8,160
s_lshl_b32 s72, s10, 3
TR 40,245,0
TR 42,245,8192
MA 8,12,168
buffer_load_dwordx4 v252, s[12:15], s59 offen lds
MA 12,12,172
s_add_u32 s59, s59, 65536
TR 44,245,16384
TR 46,245,24576
MA 12,8,164
WL 12
s_add_u32 s72, s72, s11
s_min_u32 s6, s6, s61
MA 16,16,160
s_add_u32 m0, s58, 4096
TR 48,246,0
TR 50,246,8192
MA 16,20,168
buffer_load_dwordx4 v252, s[12:15], s59 offen lds
MA 20,20,172
s_add_u32 s59, s59, 65536
TR 52,246,16384
TR 54,246,24576
MA 20,16,164
WL 12
buffer_load_dwordx4 a[192:195], v253, s[20:23], s74 offen offset:0 sc1
MA 24,24,160
s_add_u32 m0, s58, 8192
TR 56,247,0
TR 58,247,8192
MA 24,28,168
s_lshl_b32 s72, s72, 8
s_sub_u32 s61, s6, s57
v_accvgpr_write_b32 a96, v160
MA 28,28,172
s_add_u32 s72, s72, 0x6fc8000
TR 60,247,16384
TR 62,247,24576
MA 28,24,164
WL 12
v_accvgpr_write_b32 a97, v161
v_accvgpr_write_b32 a98, v162
MA 32,32,160
s_mov_b32 s57, s6
TR 0,240,256
TR 2,240,8448
MA 32,36,168
buffer_load_dwordx4 v252, s[12:15], s59 offen lds
MA 36,36,172
s_add_u32 s59, s59, 65536
TR 4,240,16640
TR 6,240,24832
MA 36,32,164
WL 12
v_add_u32 v240, s61, v240
v_accvgpr_write_b32 a99, v163
MA 40,40,160
s_add_u32 m0, s58, 12288
TR 8,241,256
TR 10,241,8448
MA 40,44,168
buffer_load_dwordx4 v252, s[12:15], s59 offen lds
MA 44,44,172
s_add_u32 s59, s59, 65536
TR 12,241,16640
TR 14,241,24832
MA 44,40,164
WL 12
buffer_store_dwordx4 v[164:167], v253, s[20:23], s51 offen offset:1024 sc1
MA 48,48,160
s_add_u32 m0, s58, 16384
TR 16,242,256
TR 18,242,8448
MA 48,52,168
buffer_load_dwordx4 a[196:199], v253, s[20:23], s75 offen offset:0 sc1
MA 52,52,172
TR 20,242,16640
TR 22,242,24832
MA 52,48,164
WL 12
v_add_u32 v241, s61, v241
v_add_u32 v242, s61, v242
MA 56,56,160
TR 24,243,256
TR 26,243,8448
MA 56,60,168
buffer_load_dwordx4 v252, s[12:15], s59 offen lds
MA 60,60,172
s_add_u32 s59, s59, 65536
TR 28,243,16640
TR 30,243,24832
MA 60,56,164
WL 12
v_add_u32 v243, s61, v243
v_accvgpr_write_b32 a100, v164
MA 64,0,160
s_add_u32 m0, s58, 20480
TR 32,244,256
TR 34,244,8448
MA 64,4,168
buffer_load_dwordx4 v252, s[12:15], s59 offen lds
MA 68,4,172
s_add_u32 s59, s59, 65536
TR 36,244,16640
TR 38,244,24832
MA 68,0,164
WL 12
v_add_u32 v244, s61, v244
v_accvgpr_write_b32 a101, v165
MA 72,8,160
s_add_u32 m0, s58, 24576
TR 40,245,256
TR 42,245,8448
MA 72,12,168
buffer_store_dwordx4 v[168:171], v253, s[20:23], s51 offen offset:2048 sc1
MA 76,12,172
TR 44,245,16640
TR 46,245,24832
MA 76,8,164
WL 12
buffer_load_dwordx4 a[200:203], v253, s[20:23], s74 offen offset:1024 sc1
MA 80,16,160
TR 48,246,256
TR 50,246,8448
MA 80,20,168
buffer_load_dwordx4 v252, s[12:15], s59 offen lds
MA 84,20,172
s_add_u32 s59, s59, 65536
TR 52,246,16640
TR 54,246,24832
MA 84,16,164
WL 12
v_add_u32 v245, s61, v245
v_add_u32 v246, s61, v246
MA 88,24,160
s_add_u32 m0, s58, 28672
TR 56,247,256
TR 58,247,8448
MA 88,28,168
buffer_load_dwordx4 v252, s[12:15], s59 offen lds
MA 92,28,172
s_add_u32 s59, s59, 0x10000
TR 60,247,16640
TR 62,247,24832
MA 92,24,164
s_waitcnt vmcnt(19) lgkmcnt(12)
s_barrier
v_add_u32 v247, s61, v247
MA 96,32,160
s_add_u32 s7, s58, 32768
TR 0,240,0
TR 2,240,8192
MA 96,36,168
buffer_store_dwordx2 v[254:255], v251, s[20:23], s72 offen offset:0 sc1
MA 100,36,172
s_sub_u32 s58, s58, 131072
TR 4,240,16384
TR 6,240,24576
MA 100,32,164
WL 12
buffer_load_dwordx2 v[248:249], v250, s[20:23], s53 offen sc1
MA 104,40,160
s_min_u32 s58, s7, s58
TR 8,241,0
TR 10,241,8192
MA 104,44,168
v_accvgpr_write_b32 a102, v166
v_accvgpr_write_b32 a103, v167
MA 108,44,172
TR 12,241,16384
TR 14,241,24576
MA 108,40,164
WL 12
buffer_store_dwordx4 v[172:175], v253, s[20:23], s51 offen offset:3072 sc1
MA 112,48,160
TR 16,242,0
TR 18,242,8192
MA 112,52,168
buffer_load_dwordx4 a[204:207], v253, s[20:23], s75 offen offset:1024 sc1
MA 116,52,172
s_add_u32 s74, s74, 2048
TR 20,242,16384
TR 22,242,24576
MA 116,48,164
WL 12
v_accvgpr_write_b32 a104, v168
v_accvgpr_write_b32 a105, v169
MA 120,56,160
s_add_u32 s75, s75, 2048
TR 24,243,0
TR 26,243,8192
MA 120,60,168
v_accvgpr_write_b32 a106, v170
v_accvgpr_write_b32 a107, v171
MA 124,60,172
TR 28,243,16384
TR 30,243,24576
MA 124,56,164
s_waitcnt vmcnt(18) lgkmcnt(12)
v_accvgpr_write_b32 a108, v172
v_accvgpr_write_b32 a109, v173
v_accvgpr_write_b32 a110, v174
v_accvgpr_write_b32 a111, v175
MA 0,0,176
s_add_u32 m0, s58, 0
TR 32,244,0
TR 34,244,8192
MA 0,4,184
v_accvgpr_write_b32 a112, v176
v_accvgpr_write_b32 a113, v177
MA 4,4,188
s_add_u32 s51, s73, 12288
TR 36,244,16384
TR 38,244,24576
MA 4,0,180
WL 12
buffer_store_dwordx4 v[176:179], v253, s[20:23], s51 offen offset:0 sc1
MA 8,8,176
s_add_u32 s6, s57, 32768
TR 40,245,0
TR 42,245,8192
MA 8,12,184
buffer_load_dwordx4 v252, s[12:15], s59 offen lds
MA 12,12,188
s_add_u32 s59, s59, 65536
TR 44,245,16384
TR 46,245,24576
MA 12,8,180
WL 12
s_sub_u32 s61, s57, 131072
v_accvgpr_write_b32 a114, v178
MA 16,16,176
s_add_u32 m0, s58, 4096
TR 48,246,0
TR 50,246,8192
MA 16,20,184
buffer_load_dwordx4 v252, s[12:15], s59 offen lds
MA 20,20,188
s_add_u32 s59, s59, 65536
TR 52,246,16384
TR 54,246,24576
MA 20,16,180
WL 12
buffer_load_dwordx4 a[208:211], v253, s[20:23], s74 offen offset:0 sc1
MA 24,24,176
s_add_u32 m0, s58, 8192
TR 56,247,0
TR 58,247,8192
MA 24,28,184
buffer_store_dwordx4 v[180:183], v253, s[20:23], s51 offen offset:1024 sc1
MA 28,28,188
s_min_u32 s6, s6, s61
TR 60,247,16384
TR 62,247,24576
MA 28,24,180
WL 12
v_accvgpr_write_b32 a115, v179
v_accvgpr_write_b32 a116, v180
MA 32,32,176
s_sub_u32 s61, s6, s57
TR 0,240,256
TR 2,240,8448
MA 32,36,184
buffer_load_dwordx4 v252, s[12:15], s59 offen lds
MA 36,36,188
s_add_u32 s59, s59, 65536
TR 4,240,16640
TR 6,240,24832
MA 36,32,180
WL 12
s_mov_b32 s57, s6
v_add_u32 v240, s61, v240
MA 40,40,176
s_add_u32 m0, s58, 12288
TR 8,241,256
TR 10,241,8448
MA 40,44,184
buffer_load_dwordx4 v252, s[12:15], s59 offen lds
MA 44,44,188
s_add_u32 s59, s59, 65536
TR 12,241,16640
TR 14,241,24832
MA 44,40,180
WL 12
buffer_store_dwordx4 v[184:187], v253, s[20:23], s51 offen offset:2048 sc1
MA 48,48,176
s_add_u32 m0, s58, 16384
TR 16,242,256
TR 18,242,8448
MA 48,52,184
buffer_load_dwordx4 a[212:215], v253, s[20:23], s75 offen offset:0 sc1
MA 52,52,188
TR 20,242,16640
TR 22,242,24832
MA 52,48,180
WL 12
v_add_u32 v241, s61, v241
v_add_u32 v242, s61, v242
MA 56,56,176
TR 24,243,256
TR 26,243,8448
MA 56,60,184
buffer_load_dwordx4 v252, s[12:15], s59 offen lds
MA 60,60,188
s_add_u32 s59, s59, 65536
TR 28,243,16640
TR 30,243,24832
MA 60,56,180
WL 12
buffer_store_dwordx4 v[188:191], v253, s[20:23], s51 offen offset:3072 sc1
MA 64,0,176
s_add_u32 m0, s58, 20480
TR 32,244,256
TR 34,244,8448
MA 64,4,184
buffer_load_dwordx4 v252, s[12:15], s59 offen lds
MA 68,4,188
s_add_u32 s59, s59, 65536
TR 36,244,16640
TR 38,244,24832
MA 68,0,180
WL 12
v_add_u32 v243, s61, v243
v_add_u32 v244, s61, v244
MA 72,8,176
s_add_u32 m0, s58, 24576
TR 40,245,256
TR 42,245,8448
MA 72,12,184
v_accvgpr_write_b32 a117, v181
v_accvgpr_write_b32 a118, v182
MA 76,12,188
TR 44,245,16640
TR 46,245,24832
MA 76,8,180
WL 12
buffer_load_dwordx4 a[216:219], v253, s[20:23], s74 offen offset:1024 sc1
MA 80,16,176
TR 48,246,256
TR 50,246,8448
MA 80,20,184
buffer_load_dwordx4 v252, s[12:15], s59 offen lds
MA 84,20,188
s_add_u32 s59, s59, 65536
TR 52,246,16640
TR 54,246,24832
MA 84,16,180
WL 12
v_add_u32 v245, s61, v245
v_add_u32 v246, s61, v246
MA 88,24,176
s_add_u32 m0, s58, 28672
TR 56,247,256
TR 58,247,8448
MA 88,28,184
buffer_load_dwordx4 v252, s[12:15], s59 offen lds
MA 92,28,188
s_mov_b32 s12, s46
TR 60,247,16640
TR 62,247,24832
MA 92,24,180
WL 12
s_barrier
v_add_u32 v252, s56, v253
MA 96,32,176
s_and_b32 s13, s47, 0xffff
TR 0,240,0
TR 2,240,8192
MA 96,36,184
v_add_u32 v247, s61, v247
v_accvgpr_write_b32 a119, v183
MA 100,36,188
s_mov_b32 s14, 134217728
TR 4,240,16384
TR 6,240,24576
MA 100,32,180
WL 12
v_accvgpr_write_b32 a120, v184
v_accvgpr_write_b32 a121, v185
MA 104,40,176
s_lshl_b32 s59, s10, 21
TR 8,241,0
TR 10,241,8192
MA 104,44,184
v_accvgpr_write_b32 a122, v186
v_accvgpr_write_b32 a123, v187
MA 108,44,188
s_add_u32 s59, s59, 0x100000
TR 12,241,16384
TR 14,241,24576
MA 108,40,180
WL 12
buffer_load_dwordx4 a[220:223], v253, s[20:23], s75 offen offset:1024 sc1
MA 112,48,176
s_lshl_b32 s54, s11, 2
TR 16,242,0
TR 18,242,8192
MA 112,52,184
s_add_u32 s74, s74, 2048
v_accvgpr_write_b32 a124, v188
v_accvgpr_write_b32 a125, v189
MA 116,52,188
s_mov_b32 s55, 0x32222222
TR 20,242,16384
TR 22,242,24576
MA 116,48,180
WL 12
v_accvgpr_write_b32 a126, v190
v_accvgpr_write_b32 a127, v191
MA 120,56,176
s_lshr_b32 s54, s55, s54
TR 24,243,0
TR 26,243,8192
MA 120,60,184
s_add_u32 s75, s75, 2048
v_bfe_u32 v196, v253, 8, 2
v_lshlrev_b32 v196, 3, v196
MA 124,60,188
s_and_b32 s54, s54, 15
TR 28,243,16384
TR 30,243,24576
MA 124,56,180
s_waitcnt vmcnt(16) lgkmcnt(12)
s_sub_u32 s54, s54, 2
s_lshl_b32 s55, s54, 16
s_add_u32 s59, s59, s55
s_add_u32 s7, s58, 32768
s_sub_u32 s58, s58, 131072
s_min_u32 s58, s7, s58
v_cmp_ne_u64 vcc, s[48:49], v[248:249]
s_cbranch_vccnz .LPS1
.LPR1:
s_add_u32 s53, s53, 1024
v_bfe_u32 v194, v253, 4, 4
v_mul_u32_u24 v194, 0x380, v194
v_mul_u32_u24 v197, 5, v196
v_and_or_b32 v194, v197, 48, v194
v_bfe_u32 v197, v253, 4, 5
v_lshlrev_b32 v197, 7, v197
MA 0,0,192
s_add_u32 s63, s59, s62
TR 32,244,0
TR 34,244,8192
MA 0,4,200
v_bfe_u32 v206, v253, 4, 4
v_bfe_u32 v207, v253, 8, 2
MA 4,4,204
s_add_u32 m0, s58, s62
TR 36,244,16384
TR 38,244,24576
MA 4,0,196
WL 12
v_lshlrev_b32 v207, 2, v207
v_sub_u32 v206, v206, v207
MA 8,8,192
s_add_u32 s6, s57, 32768
TR 40,245,0
TR 42,245,8192
MA 8,12,200
buffer_load_dwordx4 v252, s[12:15], s63 offen offset:0 sc1 lds
MA 12,12,204
s_add_u32 s59, s59, 0x10000
TR 44,245,16384
TR 46,245,24576
MA 12,8,196
WL 12
s_sub_u32 s61, s57, 131072
v_lshl_add_u32 v206, s8, 4, v206
MA 16,16,192
s_add_u32 s7, s58, 32768
TR 48,246,0
TR 50,246,8192
MA 16,20,200
buffer_load_dwordx4 v252, s[12:15], s63 offen offset:1024 sc1 lds
MA 20,20,204
s_sub_u32 s58, s58, 131072
TR 52,246,16384
TR 54,246,24576
MA 20,16,196
WL 12
buffer_load_dwordx4 a[224:227], v253, s[20:23], s74 offen offset:0 sc1
MA 24,24,192
)A"
R"A(s_min_u32 s58, s7, s58
TR 56,247,0
TR 58,247,8192
MA 24,28,200
s_min_u32 s6, s6, s61
v_subrev_u32 v176, 0, v206
v_ashrrev_i32 v176, 31, v176
MA 28,28,204
s_sub_u32 s61, s6, s57
TR 60,247,16384
TR 62,247,24576
MA 28,24,196
WL 12
v_and_b32 v176, 0xff800000, v176
v_subrev_u32 v177, 1, v206
MA 32,32,192
s_mov_b32 s57, s6
TR 0,240,256
TR 2,240,8448
MA 32,36,200
buffer_load_dwordx4 v252, s[12:15], s63 offen offset:2048 sc1 lds
MA 36,36,204
TR 4,240,16640
TR 6,240,24832
MA 36,32,196
WL 12
v_add_u32 v240, s61, v240
v_ashrrev_i32 v177, 31, v177
MA 40,40,192
s_lshl_b32 s71, s11, 1
TR 8,241,256
TR 10,241,8448
MA 40,44,200
buffer_load_dwordx4 v252, s[12:15], s63 offen offset:3072 sc1 lds
MA 44,44,204
s_add_u32 s63, s63, 4096
TR 12,241,16640
TR 14,241,24832
MA 44,40,196
WL 12
v_add_u32 v241, s61, v241
v_and_b32 v177, 0xff800000, v177
MA 48,48,192
s_add_u32 m0, m0, 4096
TR 16,242,256
TR 18,242,8448
MA 48,52,200
buffer_load_dwordx4 a[228:231], v253, s[20:23], s75 offen offset:0 sc1
MA 52,52,204
s_add_u32 s71, s71, 2
TR 20,242,16640
TR 22,242,24832
MA 52,48,196
WL 12
v_add_u32 v242, s61, v242
v_subrev_u32 v178, 2, v206
MA 56,56,192
s_sub_u32 s71, s71, s54
TR 24,243,256
TR 26,243,8448
MA 56,60,200
buffer_load_dwordx4 v252, s[12:15], s63 offen offset:0 sc1 lds
MA 60,60,204
s_mov_b32 s70, 0
TR 28,243,16640
TR 30,243,24832
MA 60,56,196
WL 12
v_add_u32 v243, s61, v243
v_ashrrev_i32 v178, 31, v178
MA 64,0,192
s_mov_b32 s66, 0x10000
TR 32,244,256
TR 34,244,8448
MA 64,4,200
buffer_load_dwordx4 v252, s[12:15], s63 offen offset:1024 sc1 lds
MA 68,4,204
s_getpc_b64 s[98:99]
.LCT0:
TR 36,244,16640
TR 38,244,24832
MA 68,0,196
WL 12
v_add_u32 v244, s61, v244
v_and_b32 v178, 0xff800000, v178
MA 72,8,192
s_lshr_b32 s78, s9, 4
TR 40,245,256
TR 42,245,8448
MA 72,12,200
s_cmp_eq_u32 s71, 2
s_cselect_b32 s66, s66, 0x18000
v_subrev_u32 v179, 3, v206
MA 76,12,204
s_lshl_b32 s78, s78, 12
TR 44,245,16640
TR 46,245,24832
MA 76,8,196
WL 12
buffer_load_dwordx4 a[232:235], v253, s[20:23], s74 offen offset:1024 sc1
MA 80,16,192
s_add_u32 s78, s78, s56
TR 48,246,256
TR 50,246,8448
MA 80,20,200
buffer_load_dwordx4 v252, s[12:15], s63 offen offset:2048 sc1 lds
MA 84,20,204
s_min_u32 s78, s78, .LCEND-.LCT0-1024
TR 52,246,16640
TR 54,246,24832
MA 84,16,196
WL 12
v_add_u32 v245, s61, v245
v_add_u32 v246, s61, v246
MA 88,24,192
TR 56,247,256
TR 58,247,8448
MA 88,28,200
buffer_load_dwordx4 v252, s[12:15], s63 offen offset:3072 sc1 lds
MA 92,28,204
TR 60,247,16640
TR 62,247,24832
MA 92,24,196
s_waitcnt vmcnt(16) lgkmcnt(12)
s_barrier
v_add_u32 v247, s61, v247
MA 96,32,192
TR 0,240,0
TR 2,240,8192
MA 96,36,200
buffer_store_dwordx2 v[254:255], v251, s[20:23], s72 offen offset:128 sc1
MA 100,36,204
TR 4,240,16384
TR 6,240,24576
MA 100,32,196
WL 12
v_min_u32 v218, 0x70, v253
v_lshl_add_u32 v218, v218, 3, s78
MA 104,40,192
TR 8,241,0
TR 10,241,8192
MA 104,44,200
global_load_dword v218, v218, s[98:99]
MA 108,44,204
TR 12,241,16384
TR 14,241,24576
MA 108,40,196
WL 12
buffer_load_dwordx4 a[236:239], v253, s[20:23], s75 offen offset:1024 sc1
MA 112,48,192
s_add_u32 s74, s74, 2048
TR 16,242,0
TR 18,242,8192
MA 112,52,200
v_ashrrev_i32 v179, 31, v179
v_and_b32 v179, 0xff800000, v179
MA 116,52,204
s_add_u32 s75, s75, 2048
TR 20,242,16384
TR 22,242,24576
MA 116,48,196
WL 12
buffer_load_dwordx2 v[248:249], v250, s[20:23], s53 offen sc1
MA 120,56,192
TR 24,243,0
TR 26,243,8192
MA 120,60,200
v_subrev_u32 v180, 16, v206
v_ashrrev_i32 v180, 31, v180
MA 124,60,204
TR 28,243,16384
TR 30,243,24576
MA 124,56,196
s_waitcnt vmcnt(15) lgkmcnt(12)
v_and_b32 v180, 0xff800000, v180
v_subrev_u32 v181, 17, v206
v_ashrrev_i32 v181, 31, v181
v_and_b32 v181, 0xff800000, v181
MA 0,0,208
s_add_u32 s63, s59, s62
TR 32,244,0
TR 34,244,8192
MA 0,4,216
v_subrev_u32 v182, 18, v206
v_ashrrev_i32 v182, 31, v182
MA 4,4,220
s_add_u32 m0, s58, s62
TR 36,244,16384
TR 38,244,24576
MA 4,0,212
WL 12
s_lshl_b32 s82, s77, 2
s_add_u32 s82, s82, s8
MA 8,8,208
s_add_u32 s6, s57, 32768
TR 40,245,0
TR 42,245,8192
MA 8,12,216
buffer_load_dwordx4 v252, s[12:15], s63 offen offset:0 sc1 lds
MA 12,12,220
s_add_u32 s59, s59, 0xffff8000
TR 44,245,16384
TR 46,245,24576
MA 12,8,212
WL 12
s_sub_u32 s61, s57, 131072
s_lshl_b32 s82, s82, 6
MA 16,16,208
s_add_u32 s7, s58, 32768
TR 48,246,0
TR 50,246,8192
MA 16,20,216
buffer_load_dwordx4 v252, s[12:15], s63 offen offset:1024 sc1 lds
MA 20,20,220
s_sub_u32 s58, s58, 131072
TR 52,246,16384
TR 54,246,24576
MA 20,16,212
WL 12
s_add_u32 s82, s82, 0x7014000
v_and_b32 v182, 0xff800000, v182
MA 24,24,208
s_min_u32 s58, s7, s58
TR 56,247,0
TR 58,247,8192
MA 24,28,216
s_min_u32 s6, s6, s61
v_subrev_u32 v183, 19, v206
v_ashrrev_i32 v183, 31, v183
MA 28,28,220
s_sub_u32 s61, s6, s57
TR 60,247,16384
TR 62,247,24576
MA 28,24,212
WL 12
v_and_b32 v183, 0xff800000, v183
v_subrev_u32 v184, 32, v206
MA 32,32,208
s_mov_b32 s57, s6
TR 0,240,256
TR 2,240,8448
MA 32,36,216
buffer_load_dwordx4 v252, s[12:15], s63 offen offset:2048 sc1 lds
MA 36,36,220
s_lshl_b32 s55, s77, 10
TR 4,240,16640
TR 6,240,24832
MA 36,32,212
WL 12
v_add_u32 v240, s61, v240
v_ashrrev_i32 v184, 31, v184
MA 40,40,208
s_add_u32 s55, s55, 0x6fd4000
TR 8,241,256
TR 10,241,8448
MA 40,44,216
buffer_load_dwordx4 v252, s[12:15], s63 offen offset:3072 sc1 lds
MA 44,44,220
s_add_u32 s63, s63, 4096
TR 12,241,16640
TR 14,241,24832
MA 44,40,212
WL 12
v_add_u32 v241, s61, v241
v_and_b32 v184, 0xff800000, v184
MA 48,48,208
s_add_u32 m0, m0, 4096
TR 16,242,256
TR 18,242,8448
MA 48,52,216
s_lshl_b32 s73, s10, 3
v_subrev_u32 v185, 33, v206
v_ashrrev_i32 v185, 31, v185
MA 52,52,220
s_add_u32 s73, s73, s11
TR 20,242,16640
TR 22,242,24832
MA 52,48,212
WL 12
v_add_u32 v242, s61, v242
v_and_b32 v185, 0xff800000, v185
MA 56,56,208
s_lshl_b32 s72, s73, 7
TR 24,243,256
TR 26,243,8448
MA 56,60,216
buffer_load_dwordx4 v252, s[12:15], s63 offen offset:0 sc1 lds
MA 60,60,220
s_add_u32 s72, s72, 0x6fd0000
TR 28,243,16640
TR 30,243,24832
MA 60,56,212
WL 12
v_add_u32 v243, s61, v243
v_subrev_u32 v186, 34, v206
MA 64,0,208
TR 32,244,256
TR 34,244,8448
MA 64,4,216
buffer_load_dwordx4 v252, s[12:15], s63 offen offset:1024 sc1 lds
MA 68,4,220
TR 36,244,16640
TR 38,244,24832
MA 68,0,212
WL 12
v_add_u32 v244, s61, v244
v_ashrrev_i32 v186, 31, v186
MA 72,8,208
TR 40,245,256
TR 42,245,8448
MA 72,12,216
s_cmp_eq_u32 s8, 0
s_cselect_b32 s72, s72, 0x7f000000
v_and_b32 v186, 0xff800000, v186
MA 76,12,220
s_lshl_b32 s73, s73, 2
TR 44,245,16640
TR 46,245,24832
MA 76,8,212
WL 12
v_add_u32 v245, s61, v245
v_subrev_u32 v187, 35, v206
MA 80,16,208
s_add_u32 s73, s73, s8
TR 48,246,256
TR 50,246,8448
MA 80,20,216
buffer_load_dwordx4 v252, s[12:15], s63 offen offset:2048 sc1 lds
MA 84,20,220
s_mul_i32 s73, s73, 17408
TR 52,246,16640
TR 54,246,24832
MA 84,16,212
WL 12
v_add_u32 v246, s61, v246
v_ashrrev_i32 v187, 31, v187
MA 88,24,208
s_add_u32 s73, s73, 0x2c00000
TR 56,247,256
TR 58,247,8448
MA 88,28,216
buffer_load_dwordx4 v252, s[12:15], s63 offen offset:3072 sc1 lds
MA 92,28,220
TR 60,247,16640
TR 62,247,24832
MA 92,24,212
WL 12
s_barrier
v_add_u32 v247, s61, v247
MA 96,32,208
TR 0,240,0
TR 2,240,8192
MA 96,36,216
v_and_b32 v187, 0xff800000, v187
v_subrev_u32 v188, 48, v206
MA 100,36,220
TR 4,240,16384
TR 6,240,24576
MA 100,32,212
WL 12
v_ashrrev_i32 v188, 31, v188
v_and_b32 v188, 0xff800000, v188
MA 104,40,208
TR 8,241,0
TR 10,241,8192
MA 104,44,216
v_subrev_u32 v189, 49, v206
v_ashrrev_i32 v189, 31, v189
MA 108,44,220
TR 12,241,16384
TR 14,241,24576
MA 108,40,212
WL 12
v_and_b32 v189, 0xff800000, v189
v_subrev_u32 v190, 50, v206
MA 112,48,208
TR 16,242,0
TR 18,242,8192
MA 112,52,216
v_ashrrev_i32 v190, 31, v190
v_and_b32 v190, 0xff800000, v190
MA 116,52,220
TR 20,242,16384
TR 22,242,24576
MA 116,48,212
WL 12
v_subrev_u32 v191, 51, v206
v_ashrrev_i32 v191, 31, v191
MA 120,56,208
TR 24,243,0
TR 26,243,8192
MA 120,60,216
v_and_b32 v191, 0xff800000, v191
MA 124,60,220
TR 28,243,16384
TR 30,243,24576
MA 124,56,212
s_waitcnt vmcnt(9) lgkmcnt(12)
s_waitcnt vmcnt(8)
v_cmp_ne_u64 vcc, s[48:49], v[248:249]
s_cbranch_vccnz .LPS2
.LPR2:
MA 0,0,224
s_add_u32 s63, s59, s62
TR 32,244,0
TR 34,244,8192
MA 0,4,232
v_and_b32 v198, 0x380, v197
v_bfe_u32 v199, v253, 1, 7
MA 4,4,236
s_add_u32 m0, s58, s62
TR 36,244,16384
TR 38,244,24576
MA 4,0,228
WL 12
v_and_b32 v198, 0x380, v253
v_bfe_u32 v200, v253, 9, 1
MA 8,8,224
s_add_u32 s6, s57, 32768
TR 40,245,0
TR 42,245,8192
MA 8,12,232
buffer_load_dwordx4 v252, s[12:15], s63 offen offset:0 sc1 lds
MA 12,12,236
s_add_u32 s59, s59, s66
TR 44,245,16384
TR 46,245,24576
MA 12,8,228
WL 12
s_sub_u32 s57, s57, 131072
v_lshl_add_u32 v197, v200, 30, v197
MA 16,16,224
s_add_u32 s7, s58, 32768
TR 48,246,0
TR 50,246,8192
MA 16,20,232
buffer_load_dwordx4 v252, s[12:15], s63 offen offset:1024 sc1 lds
MA 20,20,236
s_sub_u32 s58, s58, 131072
TR 52,246,16384
TR 54,246,24576
MA 20,16,228
WL 12
v_mov_b32 v200, 0x7f000000
v_mov_b32 v192, s48
MA 24,24,224
s_min_u32 s58, s7, s58
TR 56,247,0
TR 58,247,8192
MA 24,28,232
s_min_u32 s57, s6, s57
v_mov_b32 v193, s49
v_mov_b32 v204, 1
MA 28,28,236
TR 60,247,16384
TR 62,247,24576
MA 28,24,228
WL 12
v_add_u32 v201, s72, v199
PK 0,0,1
MA 32,32,224
TR 0,240,256
TR 2,240,8448
MA 32,36,232
buffer_load_dwordx4 v252, s[12:15], s63 offen offset:2048 sc1 lds
MA 36,36,236
TR 4,240,16640
TR 6,240,24832
MA 36,32,228
WL 12
PK 1,2,3
PK 2,8,9
MA 40,40,224
TR 8,241,256
TR 10,241,8448
MA 40,44,232
buffer_load_dwordx4 v252, s[12:15], s63 offen offset:3072 sc1 lds
MA 44,44,236
s_add_u32 s63, s63, 4096
TR 12,241,16640
TR 14,241,24832
MA 44,40,228
WL 12
PK 3,10,11
PK 4,4,5
MA 48,48,224
s_add_u32 m0, m0, 4096
TR 16,242,256
TR 18,242,8448
MA 48,52,232
PK 5,6,7
PK 6,12,13
MA 52,52,236
TR 20,242,16640
TR 22,242,24832
MA 52,48,228
WL 12
PK 7,14,15
PK 16,16,17
MA 56,56,224
TR 24,243,256
TR 26,243,8448
MA 56,60,232
buffer_load_dwordx4 v252, s[12:15], s63 offen offset:0 sc1 lds
MA 60,60,236
TR 28,243,16640
TR 30,243,24832
MA 60,56,228
WL 12
PK 17,18,19
PK 18,24,25
MA 64,0,224
TR 32,244,256
TR 34,244,8448
MA 64,4,232
buffer_load_dwordx4 v252, s[12:15], s63 offen offset:1024 sc1 lds
MA 68,4,236
TR 36,244,16640
TR 38,244,24832
MA 68,0,228
WL 12
v_add_u32 v240, s57, v253
PK 19,26,27
MA 72,8,224
TR 40,245,256
TR 42,245,8448
MA 72,12,232
PK 20,20,21
PK 21,22,23
MA 76,12,236
TR 44,245,16640
TR 46,245,24832
MA 76,8,228
WL 12
PK 22,28,29
PK 23,30,31
MA 80,16,224
TR 48,246,256
TR 50,246,8448
MA 80,20,232
buffer_load_dwordx4 v252, s[12:15], s63 offen offset:2048 sc1 lds
MA 84,20,236
TR 52,246,16640
TR 54,246,24832
MA 84,16,228
WL 12
PK 32,32,33
PK 33,34,35
MA 88,24,224
TR 56,247,256
TR 58,247,8448
MA 88,28,232
buffer_load_dwordx4 v252, s[12:15], s63 offen offset:3072 sc1 lds
MA 92,28,236
TR 60,247,16640
TR 62,247,24832
MA 92,24,228
WL 12
s_barrier
PK 34,40,41
MA 96,32,224
RD 0,240,0
PK 35,42,43
MA 96,36,232
PK 36,36,37
PK 37,38,39
MA 100,36,236
RD 4,240,8192
PK 38,44,45
MA 100,32,228
WL 10
PK 39,46,47
PK 48,48,49
MA 104,40,224
RD 8,240,16384
PK 49,50,51
MA 104,44,232
PK 50,56,57
PK 51,58,59
MA 108,44,236
RD 12,240,24576
PK 52,52,53
MA 108,40,228
WL 8
PK 53,54,55
PK 54,60,61
MA 112,48,224
RD 16,240,1024
PK 55,62,63
MA 112,52,232
PK 64,64,65
PK 65,66,67
MA 116,52,236
RD 20,240,9216
PK 66,72,73
MA 116,48,228
WL 6
PK 67,74,75
PK 68,68,69
MA 120,56,224
RD 24,240,17408
PK 69,70,71
MA 120,60,232
PK 70,76,77
PK 71,78,79
MA 124,60,236
RD 28,240,25600
PK 80,80,81
MA 124,56,228
WL 6
PK 81,82,83
PK 82,88,89
PK 83,90,91
PK 84,84,85
PK 85,86,87
PK 86,92,93
PK 87,94,95
MZ 128,0,64
s_add_u32 s63, s59, s62
RD 32,240,2048
v_accvgpr_write_b32 a128, v0
MZ 132,0,68
v_accvgpr_write_b32 a129, v1
v_accvgpr_write_b32 a130, v2
MZ 136,4,64
s_add_u32 m0, s58, s62
RD 36,240,10240
v_accvgpr_write_b32 a131, v3
MZ 140,4,68
WL 6
v_accvgpr_write_b32 a132, v4
v_accvgpr_write_b32 a133, v5
MZ 144,8,64
RD 40,240,18432
v_accvgpr_write_b32 a134, v6
MZ 148,8,68
buffer_load_dwordx4 v252, s[12:15], s63 offen offset:0 sc1 lds
MZ 152,12,64
s_add_u32 s59, s59, 0xffff8000
RD 44,240,26624
s_add_u32 s6, s57, 32768
MZ 156,12,68
WL 6
v_accvgpr_write_b32 a135, v7
v_accvgpr_write_b32 a136, v16
)A"
R"A(MA 128,16,72
s_add_u32 s7, s58, 32768
RD 48,240,3072
v_accvgpr_write_b32 a137, v17
MA 132,16,76
buffer_load_dwordx4 v252, s[12:15], s63 offen offset:1024 sc1 lds
MA 136,20,72
s_sub_u32 s58, s58, 131072
RD 52,240,11264
v_accvgpr_write_b32 a138, v18
MA 140,20,76
WL 6
v_accvgpr_write_b32 a139, v19
v_accvgpr_write_b32 a140, v20
MA 144,24,72
s_min_u32 s58, s7, s58
RD 56,240,19456
v_accvgpr_write_b32 a141, v21
MA 148,24,76
s_sub_u32 s57, s57, 131072
v_accvgpr_write_b32 a142, v22
v_accvgpr_write_b32 a143, v23
MA 152,28,72
s_min_u32 s57, s6, s57
RD 60,240,27648
v_accvgpr_write_b32 a144, v32
MA 156,28,76
WL 6
v_accvgpr_write_b32 a145, v33
v_accvgpr_write_b32 a146, v34
MA 128,32,80
RD 0,240,4096
v_accvgpr_write_b32 a147, v35
MA 132,32,84
buffer_load_dwordx4 v252, s[12:15], s63 offen offset:2048 sc1 lds
MA 136,36,80
RD 4,240,12288
v_accvgpr_write_b32 a148, v36
MA 140,36,84
WL 6
v_accvgpr_write_b32 a149, v37
v_accvgpr_write_b32 a150, v38
MA 144,40,80
RD 8,240,20480
v_accvgpr_write_b32 a151, v39
MA 148,40,84
buffer_load_dwordx4 v252, s[12:15], s63 offen offset:3072 sc1 lds
MA 152,44,80
s_add_u32 s63, s63, 4096
RD 12,240,28672
v_accvgpr_write_b32 a152, v48
MA 156,44,84
WL 6
v_accvgpr_write_b32 a153, v49
v_accvgpr_write_b32 a154, v50
MA 128,48,88
s_add_u32 m0, m0, 4096
RD 16,240,5120
v_accvgpr_write_b32 a155, v51
MA 132,48,92
v_accvgpr_write_b32 a156, v52
v_accvgpr_write_b32 a157, v53
MA 136,52,88
RD 20,240,13312
v_accvgpr_write_b32 a158, v54
MA 140,52,92
WL 6
v_accvgpr_write_b32 a159, v55
v_accvgpr_write_b32 a160, v64
MA 144,56,88
RD 24,240,21504
v_accvgpr_write_b32 a161, v65
MA 148,56,92
buffer_load_dwordx4 v252, s[12:15], s63 offen offset:0 sc1 lds
MA 152,60,88
RD 28,240,29696
v_accvgpr_write_b32 a162, v66
MA 156,60,92
WL 6
v_accvgpr_write_b32 a163, v67
v_accvgpr_write_b32 a164, v68
MA 128,0,96
RD 32,240,6144
v_accvgpr_write_b32 a165, v69
MA 132,0,100
buffer_load_dwordx4 v252, s[12:15], s63 offen offset:1024 sc1 lds
MA 136,4,96
RD 36,240,14336
v_accvgpr_write_b32 a166, v70
MA 140,4,100
WL 6
v_accvgpr_write_b32 a167, v71
v_accvgpr_write_b32 a168, v80
MA 144,8,96
RD 40,240,22528
v_accvgpr_write_b32 a169, v81
MA 148,8,100
v_accvgpr_write_b32 a170, v82
v_accvgpr_write_b32 a171, v83
MA 152,12,96
RD 44,240,30720
v_accvgpr_write_b32 a172, v84
MA 156,12,100
WL 6
v_accvgpr_write_b32 a173, v85
v_accvgpr_write_b32 a174, v86
MA 128,16,104
RD 48,240,7168
v_accvgpr_write_b32 a175, v87
MA 132,16,108
buffer_load_dwordx4 v252, s[12:15], s63 offen offset:2048 sc1 lds
MA 136,20,104
RD 52,240,15360
PK 96,96,97
MA 140,20,108
WL 6
PK 97,98,99
PK 98,104,105
MA 144,24,104
RD 56,240,23552
PK 99,106,107
MA 148,24,108
buffer_load_dwordx4 v252, s[12:15], s63 offen offset:3072 sc1 lds
MA 152,28,104
RD 60,240,31744
v_add_u32 v240, s57, v253
MA 156,28,108
s_waitcnt vmcnt(16) lgkmcnt(6)
s_barrier
v_accvgpr_write_b32 a176, v96
MA 128,32,112
RD 0,240,0
RD 4,240,1024
MA 132,32,116
v_accvgpr_write_b32 a177, v97
v_accvgpr_write_b32 a178, v98
MA 136,36,112
RD 8,240,2048
RD 12,240,3072
MA 140,36,116
WL 8
v_accvgpr_write_b32 a179, v99
PK 100,100,101
MA 144,40,112
RD 16,240,4096
PK 101,102,103
MA 148,40,116
PK 102,108,109
PK 103,110,111
MA 152,44,112
RD 20,240,5120
v_accvgpr_write_b32 a180, v100
MA 156,44,116
WL 8
v_accvgpr_write_b32 a181, v101
v_accvgpr_write_b32 a182, v102
MA 128,48,120
RD 24,240,6144
v_accvgpr_write_b32 a183, v103
MA 132,48,124
PK 112,112,113
PK 113,114,115
MA 136,52,120
RD 28,240,7168
PK 114,120,121
MA 140,52,124
WL 8
PK 115,122,123
v_accvgpr_write_b32 a184, v112
MA 144,56,120
v_accvgpr_write_b32 a185, v113
v_accvgpr_write_b32 a186, v114
MA 148,56,124
v_accvgpr_write_b32 a187, v115
PK 116,116,117
MA 152,60,120
PK 117,118,119
PK 118,124,125
MA 156,60,124
WL 4
PK 119,126,127
v_accvgpr_write_b32 a188, v116
v_accvgpr_write_b32 a189, v117
v_accvgpr_write_b32 a190, v118
v_accvgpr_write_b32 a191, v119
; first tile of a key segment: reference = true row max
.LBSTART:
s_cmp_eq_u32 s71, 2
s_cbranch_scc1 .LMASK0
.LNOMASK0:
v_mov_b64 v[242:243], 0
s_mov_b32 s69, 0
v_max3_f32 v254, v128, v129, v130
v_max_f32 v254, v254, v131
v_max3_f32 v255, v132, v133, v134
v_max_f32 v255, v255, v135
v_max3_f32 v254, v254, v136, v137
v_max3_f32 v254, v254, v138, v139
v_max3_f32 v255, v255, v140, v141
v_max3_f32 v255, v255, v142, v143
v_max3_f32 v254, v254, v144, v145
v_max3_f32 v254, v254, v146, v147
v_max3_f32 v255, v255, v148, v149
v_max3_f32 v255, v255, v150, v151
v_max3_f32 v254, v254, v152, v153
v_max3_f32 v254, v254, v154, v155
v_max3_f32 v255, v255, v156, v157
v_max3_f32 v255, v255, v158, v159
v_mov_b32 v244, v254
v_mov_b32 v248, v255
s_nop 0
v_permlane16_swap_b32 v254, v244
v_permlane16_swap_b32 v255, v248
v_max_f32 v254, v254, v244
v_max_f32 v255, v255, v248
v_mov_b32 v244, v254
v_mov_b32 v248, v255
s_nop 0
v_permlane32_swap_b32 v254, v244
v_permlane32_swap_b32 v255, v248
v_max_f32 v254, v254, v244
v_max_f32 v255, v255, v248
v_sub_f32 v244, 0, v254
v_sub_f32 v245, 0, v254
v_sub_f32 v246, 0, v254
v_sub_f32 v247, 0, v254
v_sub_f32 v128, v128, v254
v_sub_f32 v129, v129, v254
v_sub_f32 v130, v130, v254
v_sub_f32 v131, v131, v254
v_sub_f32 v136, v136, v254
v_sub_f32 v137, v137, v254
v_sub_f32 v138, v138, v254
v_sub_f32 v139, v139, v254
v_sub_f32 v248, 0, v255
v_sub_f32 v249, 0, v255
v_sub_f32 v250, 0, v255
v_sub_f32 v251, 0, v255
v_sub_f32 v132, v132, v255
v_sub_f32 v133, v133, v255
v_sub_f32 v134, v134, v255
v_sub_f32 v135, v135, v255
v_sub_f32 v140, v140, v255
v_sub_f32 v141, v141, v255
v_sub_f32 v142, v142, v255
v_sub_f32 v143, v143, v255
v_exp_f32 v128, v128
v_exp_f32 v129, v129
v_exp_f32 v130, v130
v_exp_f32 v131, v131
v_exp_f32 v132, v132
v_exp_f32 v133, v133
v_exp_f32 v134, v134
v_exp_f32 v135, v135
v_exp_f32 v136, v136
v_exp_f32 v137, v137
v_exp_f32 v138, v138
v_exp_f32 v139, v139
v_exp_f32 v140, v140
v_exp_f32 v141, v141
v_exp_f32 v142, v142
v_exp_f32 v143, v143
v_add_f32 v242, v242, v128
v_add_f32 v242, v242, v129
v_add_f32 v242, v242, v130
v_add_f32 v242, v242, v131
v_add_f32 v243, v243, v132
v_add_f32 v243, v243, v133
v_add_f32 v243, v243, v134
v_add_f32 v243, v243, v135
v_add_f32 v242, v242, v136
v_add_f32 v242, v242, v137
v_add_f32 v242, v242, v138
v_add_f32 v242, v242, v139
v_add_f32 v243, v243, v140
v_add_f32 v243, v243, v141
v_add_f32 v243, v243, v142
v_add_f32 v243, v243, v143
PK 160,128,129
PK 161,130,131
PK 162,136,137
PK 163,138,139
PK 164,132,133
PK 165,134,135
PK 166,140,141
PK 167,142,143
s_cmp_le_u32 s71, 3
s_cbranch_scc1 .LBN3
v_mfma_f32_16x16x32_bf16 v[128:131], a[0:3], a[64:67], v[244:247]
s_add_u32 s63, s59, s62
RD 32,240,8192
RD 36,240,9216
MA 128,4,72
v_add_f32 v144, v144, v244
v_add_f32 v145, v145, v244
MA 128,8,80
s_add_u32 m0, s58, s62
RD 40,240,10240
RD 44,240,11264
v_add_f32 v146, v146, v244
MA 128,12,88
WL 4
v_add_f32 v147, v147, v244
v_add_f32 v148, v148, v248
MA 128,16,96
RD 48,240,12288
v_add_f32 v149, v149, v248
MA 128,20,104
buffer_load_dwordx4 v252, s[12:15], s63 offen offset:0 sc1 lds
MA 128,24,112
s_add_u32 s59, s59, 0x18000
RD 52,240,13312
v_add_f32 v150, v150, v248
MA 128,28,120
v_add_f32 v151, v151, v248
v_add_f32 v152, v152, v244
v_mfma_f32_16x16x32_bf16 v[132:135], a[0:3], a[68:71], v[248:251]
s_add_u32 s7, s58, 32768
RD 56,240,14336
v_add_f32 v153, v153, v244
MA 132,4,76
buffer_load_dwordx4 v252, s[12:15], s63 offen offset:1024 sc1 lds
MA 132,8,84
s_sub_u32 s58, s58, 131072
RD 60,240,15360
v_add_f32 v154, v154, v244
v_add_f32 v155, v155, v244
MA 132,12,92
s_min_u32 s58, s7, s58
v_add_f32 v156, v156, v248
v_add_f32 v157, v157, v248
MA 132,16,100
s_add_u32 s6, s57, 32768
v_add_f32 v158, v158, v248
v_add_f32 v159, v159, v248
MA 132,20,108
v_exp_f32 v144, v144
MA 132,24,116
v_exp_f32 v145, v145
MA 132,28,124
WL 4
v_exp_f32 v146, v146
PK 168,144,145
v_mfma_f32_16x16x32_bf16 v[136:139], a[32:35], a[64:67], v[244:247]
s_sub_u32 s57, s57, 131072
RD 0,240,16384
RD 4,240,17408
MA 136,36,72
buffer_load_dwordx4 v252, s[12:15], s63 offen offset:2048 sc1 lds
MA 136,40,80
s_min_u32 s57, s6, s57
RD 8,240,18432
RD 12,240,19456
MA 136,44,88
WL 4
v_exp_f32 v147, v147
MA 136,48,96
RD 16,240,20480
v_add_f32 v242, v242, v144
PK 169,146,147
MA 136,52,104
buffer_load_dwordx4 v252, s[12:15], s63 offen offset:3072 sc1 lds
MA 136,56,112
s_add_u32 s63, s63, 4096
RD 20,240,21504
v_add_f32 v242, v242, v145
MA 136,60,120
v_exp_f32 v148, v148
v_mfma_f32_16x16x32_bf16 v[140:143], a[32:35], a[68:71], v[248:251]
s_add_u32 m0, m0, 4096
RD 24,240,22528
v_add_f32 v242, v242, v146
MA 140,36,76
v_exp_f32 v149, v149
MA 140,40,84
RD 28,240,23552
v_add_f32 v242, v242, v147
PK 172,148,149
MA 140,44,92
v_exp_f32 v150, v150
MA 140,48,100
v_exp_f32 v151, v151
v_add_f32 v243, v243, v150
MA 140,52,108
buffer_load_dwordx4 v252, s[12:15], s63 offen offset:0 sc1 lds
v_add_f32 v243, v243, v151
MA 140,56,116
v_exp_f32 v152, v152
PK 173,150,151
MA 140,60,124
WL 4
v_exp_f32 v153, v153
v_mfma_f32_16x16x32_bf16 v[144:147], a[0:3], a[64:67], v[244:247]
RD 32,240,24576
RD 36,240,25600
PK 170,152,153
MA 144,4,72
buffer_load_dwordx4 v252, s[12:15], s63 offen offset:1024 sc1 lds
MA 144,8,80
RD 40,240,26624
RD 44,240,27648
MA 144,12,88
WL 4
v_exp_f32 v154, v154
MA 144,16,96
RD 48,240,28672
v_add_f32 v243, v243, v148
MA 144,20,104
v_exp_f32 v155, v155
MA 144,24,112
RD 52,240,29696
v_add_f32 v243, v243, v149
PK 171,154,155
MA 144,28,120
v_exp_f32 v156, v156
v_mfma_f32_16x16x32_bf16 v[148:151], a[0:3], a[68:71], v[248:251]
RD 56,240,30720
v_add_f32 v242, v242, v152
MA 148,4,76
buffer_load_dwordx4 v252, s[12:15], s63 offen offset:2048 sc1 lds
MA 148,8,84
RD 60,240,31744
v_add_f32 v242, v242, v153
MA 148,12,92
v_exp_f32 v157, v157
MA 148,16,100
v_exp_f32 v158, v158
MA 148,20,108
buffer_load_dwordx4 v252, s[12:15], s63 offen offset:3072 sc1 lds
MA 148,24,116
v_add_u32 v240, s57, v253
v_add_f32 v242, v242, v154
MA 148,28,124
s_waitcnt vmcnt(16) lgkmcnt(4)
s_barrier
v_add_f32 v242, v242, v155
v_mfma_f32_16x16x32_bf16 v[152:155], a[32:35], a[64:67], v[244:247]
s_add_u32 s63, s59, s62
RD 0,240,0
v_add_f32 v243, v243, v156
MA 152,36,72
v_exp_f32 v159, v159
MA 152,40,80
s_add_u32 m0, s58, s62
RD 4,240,1024
v_add_f32 v243, v243, v157
MA 152,44,88
WL 2
v_add_f32 v243, v243, v158
v_add_f32 v243, v243, v159
MA 152,48,96
RD 8,240,2048
PK 174,156,157
MA 152,52,104
buffer_load_dwordx4 v252, s[12:15], s63 offen offset:0 sc1 lds
MA 152,56,112
RD 12,240,3072
PK 175,158,159
MA 152,60,120
v_max3_f32 v254, v128, v129, v130
v_max_f32 v254, v254, v131
v_mfma_f32_16x16x32_bf16 v[156:159], a[32:35], a[68:71], v[248:251]
RD 16,240,4096
v_max3_f32 v255, v132, v133, v134
MA 156,36,76
buffer_load_dwordx4 v252, s[12:15], s63 offen offset:1024 sc1 lds
MA 156,40,84
RD 20,240,5120
v_max_f32 v255, v255, v135
MA 156,44,92
v_max3_f32 v254, v254, v136, v137
v_max3_f32 v254, v254, v138, v139
MA 156,48,100
RD 24,240,6144
v_max3_f32 v255, v255, v140, v141
MA 156,52,108
buffer_load_dwordx4 v252, s[12:15], s63 offen offset:2048 sc1 lds
MA 156,56,116
RD 28,240,7168
v_max3_f32 v255, v255, v142, v143
MA 156,60,124
WL 6
v_max3_f32 v254, v254, v144, v145
v_max3_f32 v254, v254, v146, v147
v_max3_f32 v255, v255, v148, v149
v_max3_f32 v255, v255, v150, v151
v_mfma_f32_16x16x32_bf16 v[0:3], a[0:3], v[160:163], 0
RD 32,240,8192
RD 36,240,9216
v_mfma_f32_16x16x32_bf16 v[4:7], a[0:3], v[164:167], 0
buffer_load_dwordx4 v252, s[12:15], s63 offen offset:3072 sc1 lds
v_mfma_f32_16x16x32_bf16 v[12:15], a[4:7], v[164:167], 0
s_add_u32 s63, s63, 4096
RD 40,240,10240
v_max3_f32 v254, v254, v152, v153
v_mfma_f32_16x16x32_bf16 v[8:11], a[4:7], v[160:163], 0
WL 7
v_max3_f32 v254, v254, v154, v155
v_max3_f32 v255, v255, v156, v157
v_mfma_f32_16x16x32_bf16 v[16:19], a[8:11], v[160:163], 0
s_add_u32 m0, m0, 4096
RD 44,240,11264
v_max3_f32 v255, v255, v158, v159
v_mfma_f32_16x16x32_bf16 v[20:23], a[8:11], v[164:167], 0
v_max_f32 v241, v254, v255
v_cmp_lt_f32 vcc, 0x41800000, v241
v_mfma_f32_16x16x32_bf16 v[28:31], a[12:15], v[164:167], 0
s_cbranch_vccnz .LRA0
.LRAR0:
RD 48,240,12288
v_mfma_f32_16x16x32_bf16 v[24:27], a[12:15], v[160:163], 0
WL 7
v_exp_f32 v128, v128
v_mfma_f32_16x16x32_bf16 v[32:35], a[16:19], v[160:163], 0
)A"
R"A(RD 52,240,13312
RD 56,240,14336
v_mfma_f32_16x16x32_bf16 v[36:39], a[16:19], v[164:167], 0
v_exp_f32 v129, v129
v_mfma_f32_16x16x32_bf16 v[44:47], a[20:23], v[164:167], 0
RD 60,240,15360
v_add_f32 v242, v242, v128
v_mfma_f32_16x16x32_bf16 v[40:43], a[20:23], v[160:163], 0
WL 8
v_exp_f32 v130, v130
v_mfma_f32_16x16x32_bf16 v[48:51], a[24:27], v[160:163], 0
RD 0,240,16384
v_add_f32 v242, v242, v129
v_mfma_f32_16x16x32_bf16 v[52:55], a[24:27], v[164:167], 0
buffer_load_dwordx4 v252, s[12:15], s63 offen offset:0 sc1 lds
v_mfma_f32_16x16x32_bf16 v[60:63], a[28:31], v[164:167], 0
s_add_u32 s59, s59, 0xffff8000
RD 4,240,17408
v_add_f32 v242, v242, v130
v_mfma_f32_16x16x32_bf16 v[56:59], a[28:31], v[160:163], 0
WL 8
v_exp_f32 v131, v131
v_mfma_f32_16x16x32_bf16 v[64:67], a[32:35], v[160:163], 0
s_add_u32 s7, s58, 32768
RD 8,240,18432
v_add_f32 v242, v242, v131
v_mfma_f32_16x16x32_bf16 v[68:71], a[32:35], v[164:167], 0
v_exp_f32 v132, v132
v_mfma_f32_16x16x32_bf16 v[76:79], a[36:39], v[164:167], 0
s_sub_u32 s58, s58, 131072
RD 12,240,19456
RD 16,240,20480
v_mfma_f32_16x16x32_bf16 v[72:75], a[36:39], v[160:163], 0
WL 9
v_exp_f32 v133, v133
v_mfma_f32_16x16x32_bf16 v[80:83], a[40:43], v[160:163], 0
s_min_u32 s58, s7, s58
RD 20,240,21504
v_add_f32 v243, v243, v132
v_mfma_f32_16x16x32_bf16 v[84:87], a[40:43], v[164:167], 0
buffer_load_dwordx4 v252, s[12:15], s63 offen offset:1024 sc1 lds
v_mfma_f32_16x16x32_bf16 v[92:95], a[44:47], v[164:167], 0
s_add_u32 s6, s57, 32768
RD 24,240,22528
v_add_f32 v243, v243, v133
v_mfma_f32_16x16x32_bf16 v[88:91], a[44:47], v[160:163], 0
WL 9
v_exp_f32 v134, v134
v_mfma_f32_16x16x32_bf16 v[96:99], a[48:51], v[160:163], 0
s_sub_u32 s57, s57, 131072
RD 28,240,23552
v_add_f32 v243, v243, v134
v_mfma_f32_16x16x32_bf16 v[100:103], a[48:51], v[164:167], 0
v_exp_f32 v135, v135
v_mfma_f32_16x16x32_bf16 v[108:111], a[52:55], v[164:167], 0
s_min_u32 s57, s6, s57
RD 32,240,24576
v_add_f32 v243, v243, v135
v_mfma_f32_16x16x32_bf16 v[104:107], a[52:55], v[160:163], 0
WL 9
v_exp_f32 v136, v136
v_mfma_f32_16x16x32_bf16 v[112:115], a[56:59], v[160:163], 0
RD 36,240,25600
RD 40,240,26624
v_mfma_f32_16x16x32_bf16 v[116:119], a[56:59], v[164:167], 0
buffer_load_dwordx4 v252, s[12:15], s63 offen offset:2048 sc1 lds
v_mfma_f32_16x16x32_bf16 v[124:127], a[60:63], v[164:167], 0
RD 44,240,27648
v_add_f32 v242, v242, v136
v_mfma_f32_16x16x32_bf16 v[120:123], a[60:63], v[160:163], 0
WL 10
v_exp_f32 v137, v137
MB 0,0,168
RD 48,240,28672
PK 160,128,129
MB 4,0,172
v_exp_f32 v138, v138
MB 12,4,172
RD 52,240,29696
PK 161,130,131
MB 8,4,168
WL 10
v_exp_f32 v139, v139
MB 16,8,168
RD 56,240,30720
PK 164,132,133
MB 20,8,172
v_exp_f32 v140, v140
MB 28,12,172
RD 60,240,31744
PK 165,134,135
MB 24,12,168
WL 10
v_exp_f32 v141, v141
MB 32,16,168
v_exp_f32 v142, v142
MB 36,16,172
buffer_load_dwordx4 v252, s[12:15], s63 offen offset:3072 sc1 lds
MB 44,20,172
v_exp_f32 v143, v143
MB 40,20,168
WL 8
v_exp_f32 v144, v144
MB 48,24,168
v_exp_f32 v145, v145
MB 52,24,172
v_exp_f32 v146, v146
MB 60,28,172
v_add_u32 v240, s57, v253
v_add_f32 v242, v242, v137
MB 56,28,168
s_waitcnt vmcnt(8) lgkmcnt(0)
s_barrier
v_add_f32 v242, v242, v138
MB 64,32,168
RD 0,240,0
RD 4,240,1024
MB 68,32,172
v_exp_f32 v147, v147
MB 76,36,172
RD 8,240,2048
RD 12,240,3072
MB 72,36,168
v_add_f32 v242, v242, v139
PK 162,136,137
MB 80,40,168
RD 16,240,4096
PK 163,138,139
MB 84,40,172
v_add_f32 v243, v243, v140
v_add_f32 v243, v243, v141
MB 92,44,172
RD 20,240,5120
v_add_f32 v243, v243, v142
MB 88,44,168
v_add_f32 v243, v243, v143
PK 166,140,141
MB 96,48,168
s_sub_u32 s65, s71, 4
RD 24,240,6144
PK 167,142,143
MB 100,48,172
v_add_f32 v242, v242, v144
v_add_f32 v242, v242, v145
MB 108,52,172
RD 28,240,7168
v_add_f32 v242, v242, v146
MB 104,52,168
v_add_f32 v242, v242, v147
MB 112,56,168
MB 116,56,172
MB 124,60,172
MB 120,60,168
WL 4
s_cmp_lg_u32 s69, 0
s_cbranch_scc1 .LRB0
.LRBR0:
.LBLP:
s_cmp_eq_u32 s65, 0
s_cbranch_scc1 .LBT1
; attention main loop: QK(t+1) || softmax, PV(t) || softmax
.LATLB:
v_mfma_f32_16x16x32_bf16 v[128:131], a[0:3], a[64:67], v[244:247]
s_add_u32 s63, s59, s62
RD 32,240,8192
RD 36,240,9216
MA 128,4,72
buffer_load_dwordx2 v[202:203], v198, s[20:23], s55 offen sc1
MA 128,8,80
s_add_u32 m0, s58, s62
RD 40,240,10240
RD 44,240,11264
MA 128,12,88
WL 4
v_exp_f32 v148, v148
MA 128,16,96
RD 48,240,12288
PK 168,144,145
MA 128,20,104
buffer_load_dwordx4 v252, s[12:15], s63 offen offset:0 sc1 lds
MA 128,24,112
s_add_u32 s59, s59, 0x18000
RD 52,240,13312
PK 169,146,147
MA 128,28,120
v_exp_f32 v149, v149
v_mfma_f32_16x16x32_bf16 v[132:135], a[0:3], a[68:71], v[248:251]
s_add_u32 s7, s58, 32768
RD 56,240,14336
v_add_f32 v243, v243, v148
MA 132,4,76
buffer_load_dwordx4 v252, s[12:15], s63 offen offset:1024 sc1 lds
MA 132,8,84
s_sub_u32 s58, s58, 131072
RD 60,240,15360
v_add_f32 v243, v243, v149
MA 132,12,92
s_min_u32 s58, s7, s58
v_exp_f32 v150, v150
MA 132,16,100
s_add_u32 s6, s57, 32768
v_exp_f32 v151, v151
MA 132,20,108
buffer_load_dwordx4 v252, s[12:15], s63 offen offset:2048 sc1 lds
MA 132,24,116
v_exp_f32 v152, v152
MA 132,28,124
WL 4
v_exp_f32 v153, v153
v_mfma_f32_16x16x32_bf16 v[136:139], a[32:35], a[64:67], v[244:247]
s_sub_u32 s57, s57, 131072
RD 0,240,16384
RD 4,240,17408
MA 136,36,72
buffer_load_dwordx4 v252, s[12:15], s63 offen offset:3072 sc1 lds
MA 136,40,80
s_add_u32 s63, s63, 4096
RD 8,240,18432
RD 12,240,19456
MA 136,44,88
WL 4
v_exp_f32 v154, v154
MA 136,48,96
s_add_u32 m0, m0, 4096
RD 16,240,20480
v_add_f32 v243, v243, v150
MA 136,52,104
buffer_load_dwordx4 v252, s[12:15], s63 offen offset:0 sc1 lds
MA 136,56,112
s_min_u32 s57, s6, s57
RD 20,240,21504
v_add_f32 v243, v243, v151
MA 136,60,120
v_exp_f32 v155, v155
v_mfma_f32_16x16x32_bf16 v[140:143], a[32:35], a[68:71], v[248:251]
RD 24,240,22528
PK 172,148,149
MA 140,36,76
buffer_load_dwordx4 v252, s[12:15], s63 offen offset:1024 sc1 lds
MA 140,40,84
RD 28,240,23552
PK 173,150,151
MA 140,44,92
v_exp_f32 v156, v156
MA 140,48,100
v_exp_f32 v157, v157
MA 140,52,108
buffer_load_dwordx4 v252, s[12:15], s63 offen offset:2048 sc1 lds
MA 140,56,116
v_exp_f32 v158, v158
MA 140,60,124
WL 4
v_exp_f32 v159, v159
v_mfma_f32_16x16x32_bf16 v[144:147], a[0:3], a[64:67], v[244:247]
RD 32,240,24576
RD 36,240,25600
MA 144,4,72
buffer_load_dwordx4 v252, s[12:15], s63 offen offset:3072 sc1 lds
MA 144,8,80
s_add_u32 s63, s59, s62
RD 40,240,26624
RD 44,240,27648
MA 144,12,88
WL 4
v_add_f32 v242, v242, v152
v_add_f32 v242, v242, v153
MA 144,16,96
s_add_u32 m0, s58, s62
RD 48,240,28672
v_add_f32 v242, v242, v154
MA 144,20,104
v_add_f32 v242, v242, v155
PK 170,152,153
MA 144,24,112
RD 52,240,29696
PK 171,154,155
MA 144,28,120
v_add_f32 v243, v243, v156
v_add_f32 v243, v243, v157
v_mfma_f32_16x16x32_bf16 v[148:151], a[0:3], a[68:71], v[248:251]
RD 56,240,30720
v_add_f32 v243, v243, v158
MA 148,4,76
v_add_f32 v243, v243, v159
PK 174,156,157
MA 148,8,84
RD 60,240,31744
PK 175,158,159
MA 148,12,92
v_max3_f32 v254, v128, v129, v130
v_max_f32 v254, v254, v131
MA 148,16,100
v_max3_f32 v255, v132, v133, v134
v_max_f32 v255, v255, v135
MA 148,20,108
v_max3_f32 v254, v254, v136, v137
v_max3_f32 v254, v254, v138, v139
MA 148,24,116
v_add_u32 v240, s57, v253
v_max3_f32 v255, v255, v140, v141
MA 148,28,124
WL 4
v_max3_f32 v255, v255, v142, v143
v_mfma_f32_16x16x32_bf16 v[152:155], a[32:35], a[64:67], v[244:247]
RD 0,240,0
MA 152,36,72
MA 152,40,80
RD 4,240,1024
MA 152,44,88
WL 2
MA 152,48,96
RD 8,240,2048
MA 152,52,104
buffer_load_dwordx4 v252, s[12:15], s63 offen offset:0 sc1 lds
MA 152,56,112
RD 12,240,3072
v_max3_f32 v254, v254, v144, v145
MA 152,60,120
v_max3_f32 v254, v254, v146, v147
v_max3_f32 v255, v255, v148, v149
v_mfma_f32_16x16x32_bf16 v[156:159], a[32:35], a[68:71], v[248:251]
RD 16,240,4096
v_max3_f32 v255, v255, v150, v151
MA 156,36,76
buffer_load_dwordx4 v252, s[12:15], s63 offen offset:1024 sc1 lds
MA 156,40,84
RD 20,240,5120
MA 156,44,92
MA 156,48,100
RD 24,240,6144
MA 156,52,108
buffer_load_dwordx4 v252, s[12:15], s63 offen offset:2048 sc1 lds
MA 156,56,116
RD 28,240,7168
MA 156,60,124
WL 6
MB 0,0,160
RD 32,240,8192
RD 36,240,9216
MB 4,0,164
buffer_load_dwordx4 v252, s[12:15], s63 offen offset:3072 sc1 lds
MB 12,4,164
s_add_u32 s63, s63, 4096
RD 40,240,10240
v_max3_f32 v254, v254, v152, v153
MB 8,4,160
WL 7
v_max3_f32 v254, v254, v154, v155
v_max3_f32 v255, v255, v156, v157
MB 16,8,160
s_add_u32 m0, m0, 4096
RD 44,240,11264
v_max3_f32 v255, v255, v158, v159
MB 20,8,164
v_max_f32 v241, v254, v255
v_cmp_lt_f32 vcc, 0x41800000, v241
MB 28,12,164
s_cbranch_vccnz .LRA1
.LRAR1:
RD 48,240,12288
MB 24,12,160
WL 7
v_exp_f32 v128, v128
MB 32,16,160
RD 52,240,13312
RD 56,240,14336
MB 36,16,164
v_exp_f32 v129, v129
MB 44,20,164
RD 60,240,15360
v_add_f32 v242, v242, v128
MB 40,20,160
WL 8
v_exp_f32 v130, v130
MB 48,24,160
RD 0,240,16384
v_add_f32 v242, v242, v129
MB 52,24,164
buffer_load_dwordx4 v252, s[12:15], s63 offen offset:0 sc1 lds
MB 60,28,164
s_add_u32 s59, s59, 0xffff8000
RD 4,240,17408
v_add_f32 v242, v242, v130
MB 56,28,160
WL 8
v_exp_f32 v131, v131
MB 64,32,160
s_add_u32 s7, s58, 32768
RD 8,240,18432
v_add_f32 v242, v242, v131
MB 68,32,164
v_exp_f32 v132, v132
MB 76,36,164
s_sub_u32 s58, s58, 131072
RD 12,240,19456
RD 16,240,20480
MB 72,36,160
WL 9
v_exp_f32 v133, v133
MB 80,40,160
s_min_u32 s58, s7, s58
RD 20,240,21504
v_add_f32 v243, v243, v132
MB 84,40,164
buffer_load_dwordx4 v252, s[12:15], s63 offen offset:1024 sc1 lds
MB 92,44,164
s_add_u32 s6, s57, 32768
RD 24,240,22528
v_add_f32 v243, v243, v133
MB 88,44,160
WL 9
v_exp_f32 v134, v134
MB 96,48,160
s_sub_u32 s57, s57, 131072
RD 28,240,23552
v_add_f32 v243, v243, v134
MB 100,48,164
v_exp_f32 v135, v135
MB 108,52,164
s_min_u32 s57, s6, s57
RD 32,240,24576
v_add_f32 v243, v243, v135
MB 104,52,160
WL 9
v_exp_f32 v136, v136
MB 112,56,160
RD 36,240,25600
RD 40,240,26624
MB 116,56,164
buffer_load_dwordx4 v252, s[12:15], s63 offen offset:2048 sc1 lds
MB 124,60,164
RD 44,240,27648
v_add_f32 v242, v242, v136
MB 120,60,160
WL 10
v_exp_f32 v137, v137
MB 0,0,168
RD 48,240,28672
PK 160,128,129
MB 4,0,172
v_exp_f32 v138, v138
MB 12,4,172
RD 52,240,29696
PK 161,130,131
MB 8,4,168
WL 10
v_exp_f32 v139, v139
MB 16,8,168
RD 56,240,30720
PK 164,132,133
MB 20,8,172
v_exp_f32 v140, v140
MB 28,12,172
RD 60,240,31744
PK 165,134,135
MB 24,12,168
WL 10
v_exp_f32 v141, v141
MB 32,16,168
v_exp_f32 v142, v142
MB 36,16,172
buffer_load_dwordx4 v252, s[12:15], s63 offen offset:3072 sc1 lds
MB 44,20,172
v_exp_f32 v143, v143
MB 40,20,168
WL 8
v_exp_f32 v144, v144
MB 48,24,168
v_exp_f32 v145, v145
MB 52,24,172
v_exp_f32 v146, v146
MB 60,28,172
v_add_u32 v240, s57, v253
v_add_f32 v242, v242, v137
MB 56,28,168
s_waitcnt vmcnt(8) lgkmcnt(0)
s_barrier
v_add_f32 v242, v242, v138
MB 64,32,168
RD 0,240,0
RD 4,240,1024
MB 68,32,172
v_exp_f32 v147, v147
MB 76,36,172
RD 8,240,2048
RD 12,240,3072
MB 72,36,168
v_add_f32 v242, v242, v139
PK 162,136,137
MB 80,40,168
RD 16,240,4096
PK 163,138,139
MB 84,40,172
v_add_f32 v243, v243, v140
v_add_f32 v243, v243, v141
MB 92,44,172
RD 20,240,5120
v_add_f32 v243, v243, v142
MB 88,44,168
v_add_f32 v243, v243, v143
PK 166,140,141
MB 96,48,168
s_sub_u32 s65, s65, 1
RD 24,240,6144
PK 167,142,143
MB 100,48,172
v_add_f32 v242, v242, v144
v_add_f32 v242, v242, v145
MB 108,52,172
RD 28,240,7168
v_add_f32 v242, v242, v146
MB 104,52,168
v_add_f32 v242, v242, v147
MB 112,56,168
MB 116,56,172
MB 124,60,172
MB 120,60,168
WL 4
s_cmp_lg_u32 s69, 0
s_cbranch_scc1 .LRB1
.LRBR1:
s_cmp_lg_u32 s65, 0
s_cbranch_scc1 .LATLB
; last (diagonal) tiles of a segment
.LBT1:
s_cmp_eq_u32 s70, 0
s_cbranch_scc0 .LBT2
v_mfma_f32_16x16x32_bf16 v[128:131], a[0:3], a[64:67], v[244:247]
s_add_u32 s63, s59, s62
RD 32,240,8192
RD 36,240,9216
MA 128,4,72
v_exp_f32 v148, v148
MA 128,8,80
s_add_u32 m0, s58, s62
RD 40,240,10240
RD 44,240,11264
MA 128,12,88
WL 4
v_exp_f32 v149, v149
MA 128,16,96
RD 48,240,12288
PK 168,144,145
MA 128,20,104
buffer_load_dwordx4 v252, s[12:15], s63 offen offset:0 sc1 lds
MA 128,24,112
s_add_u32 s59, s59, 0x10000
RD 52,240,13312
PK 169,146,147
)A"
R"A(MA 128,28,120
v_exp_f32 v150, v150
v_mfma_f32_16x16x32_bf16 v[132:135], a[0:3], a[68:71], v[248:251]
s_add_u32 s7, s58, 32768
RD 56,240,14336
v_add_f32 v243, v243, v148
MA 132,4,76
buffer_load_dwordx4 v252, s[12:15], s63 offen offset:1024 sc1 lds
MA 132,8,84
s_sub_u32 s58, s58, 131072
RD 60,240,15360
v_add_f32 v243, v243, v149
MA 132,12,92
s_min_u32 s58, s7, s58
v_exp_f32 v151, v151
MA 132,16,100
s_add_u32 s6, s57, 32768
v_exp_f32 v152, v152
MA 132,20,108
v_exp_f32 v153, v153
MA 132,24,116
v_exp_f32 v154, v154
MA 132,28,124
WL 4
v_exp_f32 v155, v155
v_mfma_f32_16x16x32_bf16 v[136:139], a[32:35], a[64:67], v[244:247]
s_sub_u32 s57, s57, 131072
RD 0,240,16384
RD 4,240,17408
MA 136,36,72
buffer_load_dwordx4 v252, s[12:15], s63 offen offset:2048 sc1 lds
MA 136,40,80
s_min_u32 s57, s6, s57
RD 8,240,18432
RD 12,240,19456
MA 136,44,88
WL 4
v_exp_f32 v156, v156
MA 136,48,96
RD 16,240,20480
v_add_f32 v243, v243, v150
MA 136,52,104
buffer_load_dwordx4 v252, s[12:15], s63 offen offset:3072 sc1 lds
MA 136,56,112
s_add_u32 s63, s63, 4096
RD 20,240,21504
v_add_f32 v243, v243, v151
MA 136,60,120
v_exp_f32 v157, v157
v_mfma_f32_16x16x32_bf16 v[140:143], a[32:35], a[68:71], v[248:251]
s_add_u32 m0, m0, 4096
RD 24,240,22528
PK 172,148,149
MA 140,36,76
v_exp_f32 v158, v158
MA 140,40,84
RD 28,240,23552
PK 173,150,151
MA 140,44,92
v_exp_f32 v159, v159
MA 140,48,100
v_add_f32 v242, v242, v152
v_add_f32 v242, v242, v153
MA 140,52,108
buffer_load_dwordx4 v252, s[12:15], s63 offen offset:0 sc1 lds
MA 140,56,116
v_add_f32 v242, v242, v154
v_add_f32 v242, v242, v155
MA 140,60,124
WL 4
PK 170,152,153
PK 171,154,155
v_mfma_f32_16x16x32_bf16 v[144:147], a[0:3], a[64:67], v[244:247]
RD 32,240,24576
RD 36,240,25600
MA 144,4,72
buffer_load_dwordx4 v252, s[12:15], s63 offen offset:1024 sc1 lds
MA 144,8,80
RD 40,240,26624
RD 44,240,27648
MA 144,12,88
WL 4
v_add_f32 v243, v243, v156
v_add_f32 v243, v243, v157
MA 144,16,96
RD 48,240,28672
v_add_f32 v243, v243, v158
MA 144,20,104
v_add_f32 v243, v243, v159
PK 174,156,157
MA 144,24,112
RD 52,240,29696
PK 175,158,159
MA 144,28,120
v_add_f32 v128, v128, v176
v_add_f32 v129, v129, v177
v_mfma_f32_16x16x32_bf16 v[148:151], a[0:3], a[68:71], v[248:251]
RD 56,240,30720
v_add_f32 v130, v130, v178
MA 148,4,76
buffer_load_dwordx4 v252, s[12:15], s63 offen offset:2048 sc1 lds
MA 148,8,84
RD 60,240,31744
v_add_f32 v131, v131, v179
MA 148,12,92
v_max3_f32 v254, v128, v129, v130
v_max_f32 v254, v254, v131
MA 148,16,100
v_max3_f32 v255, v132, v133, v134
v_max_f32 v255, v255, v135
MA 148,20,108
buffer_load_dwordx4 v252, s[12:15], s63 offen offset:3072 sc1 lds
MA 148,24,116
v_add_u32 v240, s57, v253
v_add_f32 v136, v136, v180
MA 148,28,124
WL 4
s_barrier
v_add_f32 v137, v137, v181
v_mfma_f32_16x16x32_bf16 v[152:155], a[32:35], a[64:67], v[244:247]
s_add_u32 s63, s59, s62
RD 0,240,0
v_add_f32 v138, v138, v182
MA 152,36,72
v_add_f32 v139, v139, v183
v_max3_f32 v254, v254, v136, v137
MA 152,40,80
s_add_u32 m0, s58, s62
RD 4,240,1024
v_max3_f32 v254, v254, v138, v139
MA 152,44,88
WL 2
v_max3_f32 v255, v255, v140, v141
v_max3_f32 v255, v255, v142, v143
MA 152,48,96
RD 8,240,2048
MA 152,52,104
buffer_load_dwordx4 v252, s[12:15], s63 offen offset:0 sc1 lds
MA 152,56,112
RD 12,240,3072
v_add_f32 v144, v144, v184
MA 152,60,120
v_add_f32 v145, v145, v185
v_add_f32 v146, v146, v186
v_mfma_f32_16x16x32_bf16 v[156:159], a[32:35], a[68:71], v[248:251]
RD 16,240,4096
v_add_f32 v147, v147, v187
MA 156,36,76
buffer_load_dwordx4 v252, s[12:15], s63 offen offset:1024 sc1 lds
MA 156,40,84
RD 20,240,5120
v_max3_f32 v254, v254, v144, v145
MA 156,44,92
v_max3_f32 v254, v254, v146, v147
v_max3_f32 v255, v255, v148, v149
MA 156,48,100
RD 24,240,6144
v_max3_f32 v255, v255, v150, v151
MA 156,52,108
buffer_load_dwordx4 v252, s[12:15], s63 offen offset:2048 sc1 lds
MA 156,56,116
RD 28,240,7168
MA 156,60,124
WL 6
MB 0,0,160
RD 32,240,8192
MB 4,0,164
buffer_load_dwordx4 v252, s[12:15], s63 offen offset:3072 sc1 lds
MB 12,4,164
s_add_u32 s63, s63, 4096
RD 36,240,9216
v_add_f32 v152, v152, v188
MB 8,4,160
WL 6
v_add_f32 v153, v153, v189
v_add_f32 v154, v154, v190
MB 16,8,160
s_add_u32 m0, m0, 4096
RD 40,240,10240
v_add_f32 v155, v155, v191
MB 20,8,164
v_max3_f32 v254, v254, v152, v153
v_max3_f32 v254, v254, v154, v155
MB 28,12,164
RD 44,240,11264
v_max3_f32 v255, v255, v156, v157
MB 24,12,160
WL 6
v_max3_f32 v255, v255, v158, v159
v_max_f32 v241, v254, v255
MB 32,16,160
RD 48,240,12288
v_cmp_lt_f32 vcc, 0x41800000, v241
MB 36,16,164
s_cbranch_vccnz .LRA2
.LRAR2:
v_exp_f32 v128, v128
MB 44,20,164
RD 52,240,13312
v_add_f32 v242, v242, v128
MB 40,20,160
WL 6
v_exp_f32 v129, v129
MB 48,24,160
RD 56,240,14336
v_add_f32 v242, v242, v129
MB 52,24,164
buffer_load_dwordx4 v252, s[12:15], s63 offen offset:0 sc1 lds
MB 60,28,164
s_add_u32 s7, s58, 32768
RD 60,240,15360
MB 56,28,160
WL 6
v_exp_f32 v130, v130
MB 64,32,160
s_sub_u32 s58, s58, 131072
RD 0,240,16384
v_add_f32 v242, v242, v130
MB 68,32,164
v_exp_f32 v131, v131
MB 76,36,164
s_min_u32 s58, s7, s58
RD 4,240,17408
v_add_f32 v242, v242, v131
MB 72,36,160
WL 6
v_exp_f32 v132, v132
MB 80,40,160
RD 8,240,18432
v_add_f32 v243, v243, v132
MB 84,40,164
buffer_load_dwordx4 v252, s[12:15], s63 offen offset:1024 sc1 lds
MB 92,44,164
s_add_u32 s6, s57, 32768
RD 12,240,19456
MB 88,44,160
WL 6
v_exp_f32 v133, v133
MB 96,48,160
s_sub_u32 s57, s57, 131072
RD 16,240,20480
v_add_f32 v243, v243, v133
MB 100,48,164
v_exp_f32 v134, v134
MB 108,52,164
s_min_u32 s57, s6, s57
RD 20,240,21504
v_add_f32 v243, v243, v134
MB 104,52,160
WL 6
v_exp_f32 v135, v135
MB 112,56,160
RD 24,240,22528
v_add_f32 v243, v243, v135
MB 116,56,164
buffer_load_dwordx4 v252, s[12:15], s63 offen offset:2048 sc1 lds
MB 124,60,164
RD 28,240,23552
MB 120,60,160
WL 6
v_exp_f32 v136, v136
MB 0,0,168
RD 32,240,24576
PK 160,128,129
MB 4,0,172
v_exp_f32 v137, v137
MB 12,4,172
RD 36,240,25600
PK 161,130,131
MB 8,4,168
WL 6
v_exp_f32 v138, v138
MB 16,8,168
RD 40,240,26624
PK 164,132,133
MB 20,8,172
v_exp_f32 v139, v139
MB 28,12,172
RD 44,240,27648
PK 165,134,135
MB 24,12,168
WL 6
v_exp_f32 v140, v140
MB 32,16,168
RD 48,240,28672
v_add_f32 v242, v242, v136
MB 36,16,172
buffer_load_dwordx4 v252, s[12:15], s63 offen offset:3072 sc1 lds
MB 44,20,172
RD 52,240,29696
v_add_f32 v242, v242, v137
MB 40,20,168
WL 6
v_exp_f32 v141, v141
MB 48,24,168
RD 56,240,30720
v_add_f32 v242, v242, v138
MB 52,24,172
v_exp_f32 v142, v142
MB 60,28,172
RD 60,240,31744
v_add_u32 v240, s57, v253
MB 56,28,168
s_waitcnt vmcnt(16) lgkmcnt(6)
s_barrier
v_add_f32 v242, v242, v139
MB 64,32,168
RD 0,240,0
RD 4,240,1024
MB 68,32,172
v_exp_f32 v143, v143
MB 76,36,172
RD 8,240,2048
RD 12,240,3072
MB 72,36,168
WL 8
v_exp_f32 v144, v144
MB 80,40,168
RD 16,240,4096
PK 162,136,137
MB 84,40,172
v_exp_f32 v145, v145
MB 92,44,172
RD 20,240,5120
PK 163,138,139
MB 88,44,168
WL 8
v_exp_f32 v146, v146
MB 96,48,168
RD 24,240,6144
v_add_f32 v243, v243, v140
MB 100,48,172
v_exp_f32 v147, v147
MB 108,52,172
RD 28,240,7168
v_add_f32 v243, v243, v141
MB 104,52,168
WL 8
v_add_f32 v243, v243, v142
v_add_f32 v243, v243, v143
MB 112,56,168
PK 166,140,141
PK 167,142,143
MB 116,56,172
v_add_f32 v242, v242, v144
v_add_f32 v242, v242, v145
MB 124,60,172
v_add_f32 v242, v242, v146
v_add_f32 v242, v242, v147
MB 120,60,168
WL 4
s_cmp_lg_u32 s69, 0
s_cbranch_scc1 .LRB2
.LRBR2:
s_branch .LBLAST
.LBT2:
v_cmp_ne_u64 vcc, s[48:49], v[202:203]
s_cbranch_vccnz .LPS3
.LPR3:
v_add_u32 v195, s50, v253
v_add_u32 v205, s80, v253
v_mfma_f32_16x16x32_bf16 v[128:131], a[0:3], a[64:67], v[244:247]
s_add_u32 s63, s59, s62
RD 32,240,8192
RD 36,240,9216
MA 128,4,72
v_exp_f32 v148, v148
MA 128,8,80
s_add_u32 m0, s58, s62
RD 40,240,10240
RD 44,240,11264
MA 128,12,88
WL 4
v_exp_f32 v149, v149
MA 128,16,96
s_add_u32 s52, s54, 0x0
RD 48,240,12288
PK 168,144,145
MA 128,20,104
buffer_load_dwordx4 v252, s[12:15], s63 offen offset:0 sc1 lds
MA 128,24,112
s_add_u32 s59, s59, 0x10000
RD 52,240,13312
PK 169,146,147
MA 128,28,120
v_exp_f32 v150, v150
v_mfma_f32_16x16x32_bf16 v[132:135], a[0:3], a[68:71], v[248:251]
s_add_u32 s7, s58, 32768
RD 56,240,14336
v_add_f32 v243, v243, v148
MA 132,4,76
buffer_load_dwordx4 v252, s[12:15], s63 offen offset:1024 sc1 lds
MA 132,8,84
s_sub_u32 s58, s58, 131072
RD 60,240,15360
v_add_f32 v243, v243, v149
MA 132,12,92
s_min_u32 s58, s7, s58
v_exp_f32 v151, v151
MA 132,16,100
s_add_u32 s6, s57, 32768
v_exp_f32 v152, v152
MA 132,20,108
buffer_load_dwordx4 a[192:195], v195, s[20:23], s52 offen sc1
MA 132,24,116
v_exp_f32 v153, v153
MA 132,28,124
WL 4
v_exp_f32 v154, v154
v_mfma_f32_16x16x32_bf16 v[136:139], a[32:35], a[64:67], v[244:247]
s_sub_u32 s57, s57, 131072
RD 0,240,16384
RD 4,240,17408
MA 136,36,72
buffer_load_dwordx4 v252, s[12:15], s63 offen offset:2048 sc1 lds
MA 136,40,80
s_min_u32 s57, s6, s57
RD 8,240,18432
RD 12,240,19456
MA 136,44,88
WL 4
v_exp_f32 v155, v155
MA 136,48,96
s_getpc_b64 s[98:99]
.LCT1:
RD 16,240,20480
v_add_f32 v243, v243, v150
MA 136,52,104
buffer_load_dwordx4 v252, s[12:15], s63 offen offset:3072 sc1 lds
MA 136,56,112
s_add_u32 s63, s63, 4096
RD 20,240,21504
v_add_f32 v243, v243, v151
MA 136,60,120
s_lshr_b32 s78, s9, 4
v_exp_f32 v156, v156
v_mfma_f32_16x16x32_bf16 v[140:143], a[32:35], a[68:71], v[248:251]
s_add_u32 m0, m0, 4096
RD 24,240,22528
PK 172,148,149
MA 140,36,76
buffer_load_dwordx4 a[220:223], v205, s[20:23], s52 offen sc1
MA 140,40,84
s_add_u32 s52, s54, 0x1c000
RD 28,240,23552
PK 173,150,151
MA 140,44,92
s_lshl_b32 s78, s78, 12
v_exp_f32 v157, v157
MA 140,48,100
s_add_u32 s78, s78, s56
v_exp_f32 v158, v158
MA 140,52,108
buffer_load_dwordx4 v252, s[12:15], s63 offen offset:0 sc1 lds
MA 140,56,116
s_add_u32 s78, s78, .LDWO-.LCT1
v_exp_f32 v159, v159
MA 140,60,124
WL 4
v_add_f32 v242, v242, v152
v_add_f32 v242, v242, v153
v_mfma_f32_16x16x32_bf16 v[144:147], a[0:3], a[64:67], v[244:247]
s_min_u32 s78, s78, .LCEND-.LCT1-1024
RD 32,240,24576
RD 36,240,25600
v_min_u32 v218, 0x70, v253
MA 144,4,72
buffer_load_dwordx4 v252, s[12:15], s63 offen offset:1024 sc1 lds
v_lshl_add_u32 v218, v218, 3, s78
MA 144,8,80
RD 40,240,26624
RD 44,240,27648
global_load_dword v218, v218, s[98:99]
MA 144,12,88
WL 4
v_add_f32 v242, v242, v154
v_add_f32 v242, v242, v155
MA 144,16,96
RD 48,240,28672
PK 170,152,153
MA 144,20,104
buffer_load_dwordx4 a[196:199], v195, s[20:23], s52 offen sc1
MA 144,24,112
RD 52,240,29696
PK 171,154,155
MA 144,28,120
v_add_f32 v243, v243, v156
v_add_f32 v243, v243, v157
v_mfma_f32_16x16x32_bf16 v[148:151], a[0:3], a[68:71], v[248:251]
RD 56,240,30720
v_add_f32 v243, v243, v158
MA 148,4,76
buffer_load_dwordx4 v252, s[12:15], s63 offen offset:2048 sc1 lds
MA 148,8,84
RD 60,240,31744
v_add_f32 v243, v243, v159
MA 148,12,92
buffer_load_dwordx4 a[224:227], v205, s[20:23], s52 offen sc1
MA 148,16,100
PK 174,156,157
PK 175,158,159
MA 148,20,108
buffer_load_dwordx4 v252, s[12:15], s63 offen offset:3072 sc1 lds
MA 148,24,116
s_add_u32 s52, s54, 0x38000
v_add_u32 v240, s57, v253
v_add_f32 v128, v128, v176
MA 148,28,124
WL 4
s_barrier
v_add_f32 v129, v129, v177
v_mfma_f32_16x16x32_bf16 v[152:155], a[32:35], a[64:67], v[244:247]
s_add_u32 s63, s59, s62
RD 0,240,0
v_add_f32 v130, v130, v178
MA 152,36,72
buffer_load_dwordx4 a[200:203], v195, s[20:23], s52 offen sc1
MA 152,40,80
s_add_u32 m0, s58, s62
RD 4,240,1024
v_add_f32 v131, v131, v179
MA 152,44,88
WL 2
buffer_load_dwordx4 a[228:231], v205, s[20:23], s52 offen sc1
MA 152,48,96
s_add_u32 s52, s54, 0x54000
RD 8,240,2048
v_max3_f32 v254, v128, v129, v130
MA 152,52,104
buffer_load_dwordx4 v252, s[12:15], s63 offen offset:0 sc1 lds
MA 152,56,112
RD 12,240,3072
v_max_f32 v254, v254, v131
MA 152,60,120
v_max3_f32 v255, v132, v133, v134
v_max_f32 v255, v255, v135
v_mfma_f32_16x16x32_bf16 v[156:159], a[32:35], a[68:71], v[248:251]
RD 16,240,4096
v_add_f32 v136, v136, v180
MA 156,36,76
buffer_load_dwordx4 v252, s[12:15], s63 offen offset:1024 sc1 lds
MA 156,40,84
RD 20,240,5120
v_add_f32 v137, v137, v181
MA 156,44,92
buffer_load_dwordx4 a[204:207], v195, s[20:23], s52 offen sc1
MA 156,48,100
RD 24,240,6144
v_add_f32 v138, v138, v182
)A"
R"A(MA 156,52,108
buffer_load_dwordx4 v252, s[12:15], s63 offen offset:2048 sc1 lds
MA 156,56,116
RD 28,240,7168
v_add_f32 v139, v139, v183
MA 156,60,124
WL 6
buffer_load_dwordx4 a[232:235], v205, s[20:23], s52 offen sc1
v_max3_f32 v254, v254, v136, v137
v_max3_f32 v254, v254, v138, v139
v_max3_f32 v255, v255, v140, v141
v_max3_f32 v255, v255, v142, v143
v_add_f32 v144, v144, v184
v_add_f32 v145, v145, v185
v_add_f32 v146, v146, v186
v_add_f32 v147, v147, v187
v_max3_f32 v254, v254, v144, v145
v_max3_f32 v254, v254, v146, v147
v_max3_f32 v255, v255, v148, v149
v_max3_f32 v255, v255, v150, v151
MB 0,0,160
s_add_u32 s52, s54, 0x70000
RD 32,240,8192
MB 4,0,164
buffer_load_dwordx4 v252, s[12:15], s63 offen offset:3072 sc1 lds
MB 12,4,164
s_add_u32 s63, s63, 4096
RD 36,240,9216
v_add_f32 v152, v152, v188
MB 8,4,160
WL 6
v_add_f32 v153, v153, v189
v_add_f32 v154, v154, v190
MB 16,8,160
s_add_u32 m0, m0, 4096
RD 40,240,10240
v_add_f32 v155, v155, v191
MB 20,8,164
v_max3_f32 v254, v254, v152, v153
v_max3_f32 v254, v254, v154, v155
MB 28,12,164
RD 44,240,11264
v_max3_f32 v255, v255, v156, v157
MB 24,12,160
WL 6
v_max3_f32 v255, v255, v158, v159
v_max_f32 v241, v254, v255
MB 32,16,160
RD 48,240,12288
v_cmp_lt_f32 vcc, 0x41800000, v241
MB 36,16,164
s_cbranch_vccnz .LRA3
.LRAR3:
v_exp_f32 v128, v128
MB 44,20,164
RD 52,240,13312
v_add_f32 v242, v242, v128
MB 40,20,160
WL 6
v_exp_f32 v129, v129
MB 48,24,160
RD 56,240,14336
v_add_f32 v242, v242, v129
MB 52,24,164
buffer_load_dwordx4 v252, s[12:15], s63 offen offset:0 sc1 lds
MB 60,28,164
s_add_u32 s7, s58, 32768
RD 60,240,15360
MB 56,28,160
WL 6
v_exp_f32 v130, v130
MB 64,32,160
s_sub_u32 s58, s58, 131072
RD 0,240,16384
v_add_f32 v242, v242, v130
MB 68,32,164
buffer_load_dwordx4 a[208:211], v195, s[20:23], s52 offen sc1
MB 76,36,164
s_min_u32 s58, s7, s58
RD 4,240,17408
MB 72,36,160
WL 6
v_exp_f32 v131, v131
MB 80,40,160
RD 8,240,18432
v_add_f32 v242, v242, v131
MB 84,40,164
buffer_load_dwordx4 v252, s[12:15], s63 offen offset:1024 sc1 lds
MB 92,44,164
s_add_u32 s6, s57, 32768
RD 12,240,19456
MB 88,44,160
WL 6
v_exp_f32 v132, v132
MB 96,48,160
s_sub_u32 s57, s57, 131072
RD 16,240,20480
v_add_f32 v243, v243, v132
MB 100,48,164
buffer_load_dwordx4 a[236:239], v205, s[20:23], s52 offen sc1
MB 108,52,164
s_min_u32 s57, s6, s57
RD 20,240,21504
MB 104,52,160
WL 6
v_exp_f32 v133, v133
MB 112,56,160
s_add_u32 s52, s54, 0x8c000
RD 24,240,22528
v_add_f32 v243, v243, v133
MB 116,56,164
buffer_load_dwordx4 v252, s[12:15], s63 offen offset:2048 sc1 lds
MB 124,60,164
RD 28,240,23552
MB 120,60,160
WL 6
v_exp_f32 v134, v134
MB 0,0,168
RD 32,240,24576
PK 160,128,129
MB 4,0,172
v_exp_f32 v135, v135
MB 12,4,172
RD 36,240,25600
PK 161,130,131
MB 8,4,168
WL 6
buffer_load_dwordx4 a[212:215], v195, s[20:23], s52 offen sc1
MB 16,8,168
RD 40,240,26624
v_add_f32 v243, v243, v134
MB 20,8,172
v_exp_f32 v136, v136
MB 28,12,172
RD 44,240,27648
v_add_f32 v243, v243, v135
MB 24,12,168
WL 6
v_exp_f32 v137, v137
MB 32,16,168
RD 48,240,28672
PK 164,132,133
MB 36,16,172
buffer_load_dwordx4 v252, s[12:15], s63 offen offset:3072 sc1 lds
MB 44,20,172
RD 52,240,29696
PK 165,134,135
MB 40,20,168
WL 6
v_exp_f32 v138, v138
MB 48,24,168
RD 56,240,30720
v_add_f32 v242, v242, v136
MB 52,24,172
buffer_load_dwordx4 a[240:243], v205, s[20:23], s52 offen sc1
MB 60,28,172
s_add_u32 s52, s54, 0xa8000
RD 60,240,31744
v_add_u32 v240, s57, v253
MB 56,28,168
s_waitcnt vmcnt(29) lgkmcnt(6)
s_barrier
v_add_f32 v242, v242, v137
MB 64,32,168
RD 0,240,0
RD 4,240,1024
MB 68,32,172
v_exp_f32 v139, v139
MB 76,36,172
RD 8,240,2048
RD 12,240,3072
MB 72,36,168
WL 8
v_exp_f32 v140, v140
MB 80,40,168
RD 16,240,4096
v_add_f32 v242, v242, v138
MB 84,40,172
v_exp_f32 v141, v141
MB 92,44,172
RD 20,240,5120
v_add_f32 v242, v242, v139
PK 166,140,141
MB 88,44,168
WL 8
buffer_load_dwordx4 a[216:219], v195, s[20:23], s52 offen sc1
MB 96,48,168
RD 24,240,6144
PK 162,136,137
MB 100,48,172
v_exp_f32 v142, v142
MB 108,52,172
RD 28,240,7168
PK 163,138,139
v_add_f32 v243, v243, v142
MB 104,52,168
WL 8
v_exp_f32 v143, v143
MB 112,56,168
v_exp_f32 v144, v144
v_add_f32 v243, v243, v143
MB 116,56,172
v_exp_f32 v145, v145
v_exp_f32 v146, v146
v_exp_f32 v147, v147
MB 124,60,172
v_add_f32 v243, v243, v140
v_add_f32 v243, v243, v141
PK 167,142,143
v_add_f32 v242, v242, v145
v_add_f32 v242, v242, v147
MB 120,60,168
WL 4
buffer_load_dwordx4 a[244:247], v205, s[20:23], s52 offen sc1
v_add_f32 v242, v242, v144
v_add_f32 v242, v242, v146
s_cmp_lg_u32 s69, 0
s_cbranch_scc1 .LRB3
.LRBR3:
.LBLAST:
s_cmp_eq_u32 s70, 0
s_cbranch_scc0 .LBNXW
s_mov_b64 exec, 1
buffer_atomic_add v204, off, s[20:23], s82 sc0
s_mov_b64 exec, -1
s_lshl_b32 s59, s10, 21
s_mov_b32 s60, 4096
s_mov_b32 s66, 0x9000
s_mov_b32 s67, 0xffff1000
s_cmp_eq_u32 s11, 7
s_cselect_b32 s68, s66, 0x11000
s_branch .LBNXD
.LBNXW:
s_cmp_eq_u32 s71, 2
s_cbranch_scc1 .LBG2
.LBG3:
s_mov_b32 s12, s34
s_and_b32 s13, s35, 0xffff
s_mov_b32 s14, 3670016
s_mul_i32 s59, s10, 229376
s_mov_b32 s60, 28672
s_mov_b32 s66, 0xfffcf080
s_mov_b32 s67, 0xfffcf080
s_mov_b32 s68, 0xfffcf080
v_add_u32 v252, s56, v253
v_lshrrev_b32 v252, 7, v252
v_mul_u32_u24 v252, 0x380, v252
v_lshrrev_b32 v241, 3, v253
v_xor_b32 v241, v241, v253
v_and_b32 v241, 0x60, v241
v_add_u32 v252, v252, v241
v_and_b32 v241, 16, v253
v_add_u32 v252, v252, v241
.LBNXD:
v_mfma_f32_16x16x32_bf16 v[132:135], a[0:3], a[68:71], v[248:251]
s_add_u32 m0, s58, 0
RD 32,240,8192
v_exp_f32 v148, v148
MA 132,4,76
RD 36,240,9216
v_exp_f32 v149, v149
v_add_f32 v243, v243, v148
MA 132,8,84
s_add_u32 s6, s57, 32768
RD 40,240,10240
buffer_load_dwordx4 v252, s[12:15], s59 offen sc1 lds
v_add_f32 v243, v243, v149
MA 132,12,92
WL 5
RD 44,240,11264
v_exp_f32 v150, v150
PK 172,148,149
MA 132,16,100
s_add_u32 s59, s59, s60
RD 48,240,12288
s_add_u32 m0, s58, 4096
v_exp_f32 v151, v151
MA 132,20,108
WL 5
RD 52,240,13312
buffer_load_dwordx4 v252, s[12:15], s59 offen sc1 lds
v_add_f32 v243, v243, v150
MA 132,24,116
s_add_u32 s59, s59, s60
RD 56,240,14336
s_add_u32 m0, s58, 8192
v_exp_f32 v156, v156
MA 132,28,124
WL 5
RD 60,240,15360
v_exp_f32 v157, v157
v_add_f32 v243, v243, v151
v_mfma_f32_16x16x32_bf16 v[140:143], a[32:35], a[68:71], v[248:251]
s_sub_u32 s57, s57, 131072
RD 0,240,16384
buffer_load_dwordx4 v252, s[12:15], s59 offen sc1 lds
PK 173,150,151
MA 140,36,76
WL 5
RD 4,240,17408
v_exp_f32 v158, v158
v_add_f32 v243, v243, v156
MA 140,40,84
s_add_u32 s59, s59, s60
RD 8,240,18432
s_add_u32 m0, s58, 12288
v_exp_f32 v159, v159
MA 140,44,92
WL 5
RD 12,240,19456
buffer_load_dwordx4 v252, s[12:15], s59 offen sc1 lds
v_add_f32 v243, v243, v157
MA 140,48,100
s_add_u32 s59, s59, s60
RD 16,240,20480
s_add_u32 m0, s58, 16384
v_add_f32 v243, v243, v158
v_add_f32 v243, v243, v159
MA 140,52,108
WL 5
RD 20,240,21504
PK 174,156,157
PK 175,158,159
MA 140,56,116
s_min_u32 s57, s6, s57
RD 24,240,22528
buffer_load_dwordx4 v252, s[12:15], s59 offen sc1 lds
s_add_u32 s59, s59, s60
MA 140,60,124
WL 5
RD 28,240,23552
s_add_u32 m0, s58, 20480
v_mfma_f32_16x16x32_bf16 v[148:151], a[0:3], a[68:71], v[248:251]
RD 32,240,24576
buffer_load_dwordx4 v252, s[12:15], s59 offen sc1 lds
v_add_f32 v132, v132, v176
MA 148,4,76
WL 5
RD 36,240,25600
v_add_f32 v133, v133, v177
v_add_f32 v134, v134, v178
v_add_f32 v135, v135, v179
MA 148,8,84
s_add_u32 s59, s59, s60
RD 40,240,26624
s_add_u32 m0, s58, 24576
v_max3_f32 v255, v132, v133, v134
v_max_f32 v255, v255, v135
MA 148,12,92
WL 5
RD 44,240,27648
buffer_load_dwordx4 v252, s[12:15], s59 offen sc1 lds
v_add_f32 v140, v140, v180
MA 148,16,100
s_add_u32 s59, s59, s60
RD 48,240,28672
s_add_u32 m0, s58, 28672
v_add_f32 v141, v141, v181
v_add_f32 v142, v142, v182
MA 148,20,108
WL 5
RD 52,240,29696
buffer_load_dwordx4 v252, s[12:15], s59 offen sc1 lds
v_add_f32 v143, v143, v183
MA 148,24,116
s_add_u32 s59, s59, s66
RD 56,240,30720
v_max3_f32 v255, v255, v140, v141
v_max3_f32 v255, v255, v142, v143
MA 148,28,124
s_waitcnt vmcnt(17) lgkmcnt(5)
RD 60,240,31744
s_barrier
v_add_u32 v240, s57, v253
v_mfma_f32_16x16x32_bf16 v[156:159], a[32:35], a[68:71], v[248:251]
s_add_u32 s7, s58, 32768
RD 0,240,0
MA 156,36,76
WL 5
RD 4,240,1024
MA 156,40,84
s_sub_u32 s58, s58, 131072
RD 8,240,2048
MA 156,44,92
WL 5
RD 12,240,3072
v_add_f32 v148, v148, v184
v_add_f32 v149, v149, v185
v_add_f32 v150, v150, v186
MA 156,48,100
s_min_u32 s58, s7, s58
RD 16,240,4096
v_add_f32 v151, v151, v187
v_max3_f32 v255, v255, v148, v149
v_max3_f32 v255, v255, v150, v151
MA 156,52,108
WL 5
RD 20,240,5120
MA 156,56,116
RD 24,240,6144
MA 156,60,124
WL 5
RD 28,240,7168
s_cmp_eq_u32 s71, 2
s_cbranch_scc1 .LBYZ
MB 0,0,160
s_add_u32 m0, s58, 0
RD 32,240,8192
PK 168,144,145
MB 4,0,164
v_exp_f32 v152, v152
MB 12,4,164
RD 36,240,9216
PK 169,146,147
MB 8,4,160
WL 6
v_exp_f32 v153, v153
MB 16,8,160
RD 40,240,10240
v_add_f32 v156, v156, v188
MB 20,8,164
buffer_load_dwordx4 v252, s[12:15], s59 offen sc1 lds
MB 28,12,164
s_add_u32 s59, s59, s60
RD 44,240,11264
v_add_f32 v157, v157, v189
MB 24,12,160
WL 6
v_exp_f32 v154, v154
MB 32,16,160
s_add_u32 m0, s58, 4096
RD 48,240,12288
v_add_f32 v158, v158, v190
MB 36,16,164
buffer_load_dwordx4 v252, s[12:15], s59 offen sc1 lds
MB 44,20,164
s_add_u32 s59, s59, s60
RD 52,240,13312
v_add_f32 v159, v159, v191
MB 40,20,160
WL 6
v_exp_f32 v155, v155
MB 48,24,160
s_add_u32 m0, s58, 8192
RD 56,240,14336
v_max3_f32 v255, v255, v156, v157
MB 52,24,164
v_max3_f32 v255, v255, v158, v159
v_cmp_lt_f32 vcc, 0x41800000, v255
s_cbranch_vccnz .LRA4
.LRAR4:
MB 60,28,164
s_add_u32 s6, s57, 32768
RD 60,240,15360
v_add_f32 v242, v242, v152
MB 56,28,160
WL 6
v_exp_f32 v132, v132
MB 64,32,160
s_sub_u32 s57, s57, 131072
RD 0,240,16384
s_min_u32 s57, s6, s57
MB 68,32,164
buffer_load_dwordx4 v252, s[12:15], s59 offen sc1 lds
MB 76,36,164
s_add_u32 s59, s59, s60
RD 4,240,17408
v_add_f32 v242, v242, v153
MB 72,36,160
WL 6
v_exp_f32 v133, v133
MB 80,40,160
s_add_u32 m0, s58, 12288
RD 8,240,18432
v_add_f32 v242, v242, v154
MB 84,40,164
buffer_load_dwordx4 v252, s[12:15], s59 offen sc1 lds
MB 92,44,164
s_add_u32 s59, s59, s60
RD 12,240,19456
v_add_f32 v242, v242, v155
MB 88,44,160
WL 6
v_exp_f32 v134, v134
MB 96,48,160
s_add_u32 m0, s58, 16384
RD 16,240,20480
PK 170,152,153
MB 100,48,164
v_exp_f32 v135, v135
MB 108,52,164
RD 20,240,21504
PK 171,154,155
MB 104,52,160
WL 6
v_exp_f32 v140, v140
MB 112,56,160
RD 24,240,22528
v_add_f32 v243, v243, v132
MB 116,56,164
buffer_load_dwordx4 v252, s[12:15], s59 offen sc1 lds
MB 124,60,164
s_add_u32 s59, s59, s60
RD 28,240,23552
v_add_f32 v243, v243, v133
MB 120,60,160
WL 6
v_exp_f32 v141, v141
MB 0,0,168
s_add_u32 m0, s58, 20480
RD 32,240,24576
v_add_f32 v243, v243, v134
MB 4,0,172
buffer_load_dwordx4 v252, s[12:15], s59 offen sc1 lds
MB 12,4,172
s_add_u32 s59, s59, s60
RD 36,240,25600
v_add_f32 v243, v243, v135
MB 8,4,168
WL 6
v_exp_f32 v142, v142
MB 16,8,168
s_add_u32 m0, s58, 24576
RD 40,240,26624
PK 164,132,133
MB 20,8,172
v_exp_f32 v143, v143
MB 28,12,172
RD 44,240,27648
PK 165,134,135
MB 24,12,168
WL 6
v_exp_f32 v148, v148
MB 32,16,168
RD 48,240,28672
v_add_f32 v243, v243, v140
MB 36,16,172
buffer_load_dwordx4 v252, s[12:15], s59 offen sc1 lds
MB 44,20,172
s_add_u32 s59, s59, s60
RD 52,240,29696
v_add_f32 v243, v243, v141
MB 40,20,168
WL 6
v_exp_f32 v149, v149
MB 48,24,168
s_add_u32 m0, s58, 28672
RD 56,240,30720
v_add_f32 v243, v243, v142
MB 52,24,172
buffer_load_dwordx4 v252, s[12:15], s59 offen sc1 lds
MB 60,28,172
s_add_u32 s59, s59, s67
RD 60,240,31744
v_add_u32 v240, s57, v253
MB 56,28,168
s_waitcnt vmcnt(17) lgkmcnt(6)
s_barrier
v_add_f32 v243, v243, v143
MB 64,32,168
s_add_u32 s7, s58, 32768
RD 0,240,0
PK 166,140,141
MB 68,32,172
v_exp_f32 v150, v150
MB 76,36,172
s_sub_u32 s58, s58, 131072
RD 4,240,1024
PK 167,142,143
MB 72,36,168
WL 6
v_exp_f32 v151, v151
MB 80,40,168
s_min_u32 s58, s7, s58
RD 8,240,2048
v_add_f32 v243, v243, v148
MB 84,40,172
v_exp_f32 v156, v156
MB 92,44,172
RD 12,240,3072
v_add_f32 v243, v243, v149
MB 88,44,168
WL 6
v_exp_f32 v157, v157
MB 96,48,168
RD 16,240,4096
v_add_f32 v243, v243, v150
MB 100,48,172
v_exp_f32 v158, v158
)A"
R"A(MB 108,52,172
RD 20,240,5120
v_add_f32 v243, v243, v151
MB 104,52,168
WL 6
v_exp_f32 v159, v159
MB 112,56,168
RD 24,240,6144
v_add_f32 v243, v243, v156
MB 116,56,172
v_add_f32 v243, v243, v157
v_add_f32 v243, v243, v158
MB 124,60,172
RD 28,240,7168
v_add_f32 v243, v243, v159
MB 120,60,168
WL 6
s_cmp_lg_u32 s69, 0
s_cbranch_scc1 .LRB4
.LRBR4:
.LBYJ:
s_waitcnt vmcnt(16)
v_readfirstlane_b32 s79, v204
MB 4,0,164
s_add_u32 m0, s58, 0
RD 32,240,8192
PK 172,148,149
PK 173,150,151
MB 12,4,164
WL 5
RD 36,240,9216
PK 174,156,157
PK 175,158,159
MB 20,8,164
s_add_u32 s6, s57, 32768
RD 40,240,10240
buffer_load_dwordx4 v252, s[12:15], s59 offen sc1 lds
MB 28,12,164
WL 5
RD 44,240,11264
PK 0,0,1
PK 1,2,3
MB 36,16,164
s_add_u32 s59, s59, s60
RD 48,240,12288
s_add_u32 m0, s58, 4096
PK 2,8,9
MB 44,20,164
WL 5
RD 52,240,13312
PK 3,10,11
PK 16,16,17
MB 52,24,164
s_sub_u32 s57, s57, 131072
RD 56,240,14336
buffer_load_dwordx4 v252, s[12:15], s59 offen sc1 lds
MB 60,28,164
WL 5
RD 60,240,15360
PK 17,18,19
PK 18,24,25
MB 68,32,164
s_add_u32 s59, s59, s60
RD 0,240,16384
s_add_u32 m0, s58, 8192
PK 19,26,27
MB 76,36,164
WL 5
RD 4,240,17408
PK 32,32,33
PK 33,34,35
MB 84,40,164
s_min_u32 s57, s6, s57
RD 8,240,18432
buffer_load_dwordx4 v252, s[12:15], s59 offen sc1 lds
MB 92,44,164
WL 5
RD 12,240,19456
PK 34,40,41
PK 35,42,43
MB 100,48,164
s_add_u32 s59, s59, s60
RD 16,240,20480
s_add_u32 m0, s58, 12288
PK 48,48,49
MB 108,52,164
WL 5
RD 20,240,21504
PK 49,50,51
PK 50,56,57
MB 116,56,164
RD 24,240,22528
buffer_load_dwordx4 v252, s[12:15], s59 offen sc1 lds
MB 124,60,164
WL 5
RD 28,240,23552
PK 51,58,59
PK 64,64,65
MB 4,0,172
s_add_u32 s59, s59, s60
RD 32,240,24576
s_add_u32 m0, s58, 16384
PK 65,66,67
MB 12,4,172
WL 5
RD 36,240,25600
PK 66,72,73
PK 67,74,75
MB 20,8,172
RD 40,240,26624
buffer_load_dwordx4 v252, s[12:15], s59 offen sc1 lds
MB 28,12,172
WL 5
RD 44,240,27648
PK 80,80,81
PK 81,82,83
MB 36,16,172
s_add_u32 s59, s59, s60
RD 48,240,28672
s_add_u32 m0, s58, 20480
PK 82,88,89
MB 44,20,172
WL 5
RD 52,240,29696
PK 83,90,91
PK 96,96,97
MB 52,24,172
RD 56,240,30720
buffer_load_dwordx4 v252, s[12:15], s59 offen sc1 lds
MB 60,28,172
WL 5
RD 60,240,31744
PK 97,98,99
PK 98,104,105
MB 68,32,172
s_add_u32 s59, s59, s60
s_add_u32 m0, s58, 24576
PK 99,106,107
PK 112,112,113
MB 76,36,172
WL 4
buffer_load_dwordx4 v252, s[12:15], s59 offen sc1 lds
PK 113,114,115
MB 84,40,172
s_add_u32 s59, s59, s60
s_add_u32 m0, s58, 28672
PK 114,120,121
PK 115,122,123
MB 92,44,172
WL 2
buffer_load_dwordx4 v252, s[12:15], s59 offen sc1 lds
v_add_u32 v240, s57, v253
MB 100,48,172
s_add_u32 s59, s59, s68
v_add_u32 v208, s57, v212
v_add_u32 v209, s57, v213
v_add_u32 v210, s57, v214
MB 108,52,172
WL 0
v_add_u32 v211, s57, v215
MB 116,56,172
s_add_u32 s7, s58, 32768
MB 124,60,172
s_sub_u32 s58, s58, 131072
s_min_u32 s58, s7, s58
s_cmp_eq_u32 s70, 0
s_cbranch_scc0 .LBFIN
s_waitcnt vmcnt(16)
s_barrier
RD 0,240,0
RD 4,240,8192
RD 8,240,16384
RD 12,240,24576
RD 16,240,1024
RD 20,240,9216
RD 24,240,17408
RD 28,240,25600
s_mov_b32 s70, 1
s_lshl_b32 s71, s77, 1
s_add_u32 s71, s71, 2
v_mov_b32 v245, v248
v_mov_b32 v246, v242
v_mov_b32 v247, v243
v_accvgpr_mov_b32 a64, a128
s_add_u32 s51, s73, 16384
v_accvgpr_mov_b32 a65, a129
v_accvgpr_mov_b32 a66, a130
v_accvgpr_mov_b32 a67, a131
v_accvgpr_mov_b32 a68, a132
v_accvgpr_mov_b32 a69, a133
v_accvgpr_mov_b32 a70, a134
buffer_store_dwordx4 v[244:247], v253, s[20:23], s51 offen sc1
v_accvgpr_mov_b32 a71, a135
v_accvgpr_mov_b32 a72, a136
v_accvgpr_mov_b32 a73, a137
v_accvgpr_mov_b32 a74, a138
v_accvgpr_mov_b32 a75, a139
v_accvgpr_mov_b32 a76, a140
s_add_u32 s51, s73, 0
v_accvgpr_mov_b32 a77, a141
v_accvgpr_mov_b32 a78, a142
v_accvgpr_mov_b32 a79, a143
PK 4,4,5
PK 5,6,7
PK 6,12,13
buffer_store_dwordx4 v[0:3], v253, s[20:23], s51 offen offset:0 sc1
PK 7,14,15
PK 20,20,21
PK 21,22,23
PK 22,28,29
PK 23,30,31
PK 36,36,37
s_add_u32 s51, s73, 0
PK 37,38,39
PK 38,44,45
PK 39,46,47
PK 52,52,53
PK 53,54,55
PK 54,60,61
buffer_store_dwordx4 v[16:19], v253, s[20:23], s51 offen offset:2048 sc1
PK 55,62,63
PK 68,68,69
PK 69,70,71
PK 70,76,77
PK 71,78,79
PK 84,84,85
s_add_u32 s51, s73, 4096
PK 85,86,87
PK 86,92,93
PK 87,94,95
PK 100,100,101
PK 101,102,103
PK 102,108,109
buffer_store_dwordx4 v[32:35], v253, s[20:23], s51 offen offset:0 sc1
PK 103,110,111
PK 116,116,117
PK 117,118,119
PK 118,124,125
PK 119,126,127
WL 4
MZ 128,0,64
s_add_u32 s63, s59, s62
RD 32,240,2048
v_accvgpr_mov_b32 a80, a144
MZ 132,0,68
s_min_u32 s50, s79, 13
v_accvgpr_mov_b32 a81, a145
v_accvgpr_mov_b32 a82, a146
MZ 136,4,64
s_add_u32 m0, s58, s62
RD 36,240,10240
v_accvgpr_mov_b32 a83, a147
MZ 140,4,68
v_accvgpr_mov_b32 a84, a148
v_accvgpr_mov_b32 a85, a149
MZ 144,8,64
s_mul_i32 s54, s8, 14
RD 40,240,18432
v_accvgpr_mov_b32 a86, a150
MZ 148,8,68
buffer_load_dwordx4 v252, s[12:15], s63 offen offset:0 sc1 lds
s_add_u32 s51, s73, 4096
MZ 152,12,64
s_add_u32 s59, s59, 0xffff8000
RD 44,240,26624
s_add_u32 s6, s57, 32768
MZ 156,12,68
WL 6
buffer_store_dwordx4 v[48:51], v253, s[20:23], s51 offen offset:2048 sc1
MA 128,16,72
s_add_u32 s7, s58, 32768
RD 48,240,3072
v_accvgpr_mov_b32 a87, a151
MA 132,16,76
buffer_load_dwordx4 v252, s[12:15], s63 offen offset:1024 sc1 lds
MA 136,20,72
s_sub_u32 s58, s58, 131072
RD 52,240,11264
v_accvgpr_mov_b32 a88, a152
MA 140,20,76
WL 6
v_accvgpr_mov_b32 a89, a153
v_accvgpr_mov_b32 a90, a154
MA 144,24,72
s_min_u32 s58, s7, s58
RD 56,240,19456
v_accvgpr_mov_b32 a91, a155
MA 148,24,76
s_sub_u32 s57, s57, 131072
v_accvgpr_mov_b32 a92, a156
v_accvgpr_mov_b32 a93, a157
MA 152,28,72
s_min_u32 s57, s6, s57
RD 60,240,27648
v_accvgpr_mov_b32 a94, a158
MA 156,28,76
WL 6
v_accvgpr_mov_b32 a95, a159
v_accvgpr_mov_b32 a96, a160
MA 128,32,80
s_add_u32 s51, s73, 8192
RD 0,240,4096
v_accvgpr_mov_b32 a97, a161
MA 132,32,84
buffer_load_dwordx4 v252, s[12:15], s63 offen offset:2048 sc1 lds
MA 136,36,80
s_add_u32 s54, s54, s50
RD 4,240,12288
v_accvgpr_mov_b32 a98, a162
MA 140,36,84
WL 6
buffer_store_dwordx4 v[64:67], v253, s[20:23], s51 offen offset:0 sc1
MA 144,40,80
s_lshr_b32 s84, s54, 3
RD 8,240,20480
v_accvgpr_mov_b32 a99, a163
MA 148,40,84
buffer_load_dwordx4 v252, s[12:15], s63 offen offset:3072 sc1 lds
MA 152,44,80
s_add_u32 s63, s63, 4096
RD 12,240,28672
v_accvgpr_mov_b32 a100, a164
MA 156,44,84
WL 6
v_accvgpr_mov_b32 a101, a165
v_accvgpr_mov_b32 a102, a166
MA 128,48,88
s_add_u32 m0, m0, 4096
RD 16,240,5120
v_accvgpr_mov_b32 a103, a167
MA 132,48,92
s_lshl_b32 s84, s84, 6
v_accvgpr_mov_b32 a104, a168
v_accvgpr_mov_b32 a105, a169
MA 136,52,88
s_add_u32 s51, s73, 8192
RD 20,240,13312
v_accvgpr_mov_b32 a106, a170
MA 140,52,92
WL 6
buffer_store_dwordx4 v[80:83], v253, s[20:23], s51 offen offset:2048 sc1
MA 144,56,88
s_bfe_u32 s83, s54, 0x20001
RD 24,240,21504
v_accvgpr_mov_b32 a107, a171
MA 148,56,92
buffer_load_dwordx4 v252, s[12:15], s63 offen offset:0 sc1 lds
MA 152,60,88
s_lshr_b32 s82, s54, 4
RD 28,240,29696
v_accvgpr_mov_b32 a108, a172
MA 156,60,92
WL 6
v_accvgpr_mov_b32 a109, a173
v_accvgpr_mov_b32 a110, a174
MA 128,0,96
s_lshl_b32 s82, s82, 2
RD 32,240,6144
v_accvgpr_mov_b32 a111, a175
MA 132,0,100
buffer_load_dwordx4 v252, s[12:15], s63 offen offset:1024 sc1 lds
MA 136,4,96
s_add_u32 s82, s82, s83
RD 36,240,14336
v_accvgpr_mov_b32 a112, a176
MA 140,4,100
WL 6
v_accvgpr_mov_b32 a113, a177
v_accvgpr_mov_b32 a114, a178
MA 144,8,96
s_add_u32 s51, s73, 12288
RD 40,240,22528
v_accvgpr_mov_b32 a115, a179
MA 148,8,100
buffer_store_dwordx4 v[96:99], v253, s[20:23], s51 offen offset:0 sc1
MA 152,12,96
s_lshl_b32 s82, s82, 1
RD 44,240,30720
v_accvgpr_mov_b32 a116, a180
MA 156,12,100
WL 6
v_accvgpr_mov_b32 a117, a181
v_accvgpr_mov_b32 a118, a182
MA 128,16,104
s_bfe_u32 s50, s54, 0x10003
RD 48,240,7168
v_accvgpr_mov_b32 a119, a183
MA 132,16,108
buffer_load_dwordx4 v252, s[12:15], s63 offen offset:2048 sc1 lds
MA 136,20,104
s_add_u32 s82, s82, s50
RD 52,240,15360
v_accvgpr_mov_b32 a120, a184
MA 140,20,108
WL 6
v_accvgpr_mov_b32 a121, a185
v_accvgpr_mov_b32 a122, a186
MA 144,24,104
s_lshl_b32 s82, s82, 1
RD 56,240,23552
v_accvgpr_mov_b32 a123, a187
MA 148,24,108
buffer_load_dwordx4 v252, s[12:15], s63 offen offset:3072 sc1 lds
MA 152,28,104
s_add_u32 s51, s73, 12288
RD 60,240,31744
v_add_u32 v240, s57, v253
MA 156,28,108
s_waitcnt vmcnt(24) lgkmcnt(6)
s_barrier
v_accvgpr_mov_b32 a124, a188
MA 128,32,112
s_and_b32 s50, s54, 1
RD 0,240,0
RD 4,240,1024
MA 132,32,116
buffer_store_dwordx4 v[112:115], v253, s[20:23], s51 offen offset:2048 sc1
MA 136,36,112
s_add_u32 s82, s82, s50
RD 8,240,2048
RD 12,240,3072
MA 140,36,116
WL 8
v_accvgpr_mov_b32 a125, a189
v_accvgpr_mov_b32 a126, a190
MA 144,40,112
s_lshl_b32 s82, s82, 10
RD 16,240,4096
v_accvgpr_mov_b32 a127, a191
MA 148,40,116
MA 152,44,112
s_lshl_b32 s50, s50, 2
RD 20,240,5120
MA 156,44,116
WL 8
MA 128,48,120
s_add_u32 s51, s73, 0
RD 24,240,6144
MA 132,48,124
buffer_store_dwordx4 v[4:7], v253, s[20:23], s51 offen offset:1024 sc1
MA 136,52,120
s_add_u32 s83, s83, s50
RD 28,240,7168
MA 140,52,124
WL 8
MA 144,56,120
s_lshl_b32 s50, s77, 3
MA 148,56,124
s_add_u32 s83, s83, s50
MA 152,60,120
s_mul_i32 s83, s83, 14336
s_add_u32 s83, s83, s84
MA 156,60,124
WL 4
s_cmp_eq_u32 s71, 2
s_cbranch_scc1 .LMASK1
.LNOMASK1:
v_mov_b64 v[242:243], 0
s_add_u32 s51, s73, 0
s_mov_b32 s69, 0
v_max3_f32 v254, v128, v129, v130
v_max_f32 v254, v254, v131
v_max3_f32 v255, v132, v133, v134
v_max_f32 v255, v255, v135
v_max3_f32 v254, v254, v136, v137
v_max3_f32 v254, v254, v138, v139
v_max3_f32 v255, v255, v140, v141
v_max3_f32 v255, v255, v142, v143
v_max3_f32 v254, v254, v144, v145
v_max3_f32 v254, v254, v146, v147
v_max3_f32 v255, v255, v148, v149
buffer_store_dwordx4 v[20:23], v253, s[20:23], s51 offen offset:3072 sc1
v_max3_f32 v255, v255, v150, v151
v_max3_f32 v254, v254, v152, v153
v_max3_f32 v254, v254, v154, v155
v_max3_f32 v255, v255, v156, v157
v_max3_f32 v255, v255, v158, v159
v_mov_b32 v244, v254
v_mov_b32 v248, v255
s_nop 0
v_permlane16_swap_b32 v254, v244
v_permlane16_swap_b32 v255, v248
v_max_f32 v254, v254, v244
v_max_f32 v255, v255, v248
s_add_u32 s51, s73, 4096
v_mov_b32 v244, v254
v_mov_b32 v248, v255
s_nop 0
v_permlane32_swap_b32 v254, v244
v_permlane32_swap_b32 v255, v248
v_max_f32 v254, v254, v244
v_max_f32 v255, v255, v248
v_sub_f32 v244, 0, v254
v_sub_f32 v245, 0, v254
v_sub_f32 v246, 0, v254
v_sub_f32 v247, 0, v254
v_sub_f32 v128, v128, v254
buffer_store_dwordx4 v[36:39], v253, s[20:23], s51 offen offset:1024 sc1
v_sub_f32 v129, v129, v254
v_sub_f32 v130, v130, v254
v_sub_f32 v131, v131, v254
v_sub_f32 v136, v136, v254
v_sub_f32 v137, v137, v254
v_sub_f32 v138, v138, v254
v_sub_f32 v139, v139, v254
v_sub_f32 v248, 0, v255
v_sub_f32 v249, 0, v255
v_sub_f32 v250, 0, v255
v_sub_f32 v251, 0, v255
v_sub_f32 v132, v132, v255
s_add_u32 s51, s73, 4096
v_sub_f32 v133, v133, v255
v_sub_f32 v134, v134, v255
v_sub_f32 v135, v135, v255
v_sub_f32 v140, v140, v255
v_sub_f32 v141, v141, v255
v_sub_f32 v142, v142, v255
v_sub_f32 v143, v143, v255
v_exp_f32 v128, v128
v_exp_f32 v129, v129
v_exp_f32 v130, v130
v_exp_f32 v131, v131
v_exp_f32 v132, v132
buffer_store_dwordx4 v[52:55], v253, s[20:23], s51 offen offset:3072 sc1
v_exp_f32 v133, v133
v_exp_f32 v134, v134
v_exp_f32 v135, v135
v_exp_f32 v136, v136
v_exp_f32 v137, v137
v_exp_f32 v138, v138
v_exp_f32 v139, v139
v_exp_f32 v140, v140
v_exp_f32 v141, v141
v_exp_f32 v142, v142
v_exp_f32 v143, v143
v_add_f32 v242, v242, v128
s_add_u32 s51, s73, 8192
v_add_f32 v242, v242, v129
v_add_f32 v242, v242, v130
v_add_f32 v242, v242, v131
v_add_f32 v243, v243, v132
v_add_f32 v243, v243, v133
v_add_f32 v243, v243, v134
v_add_f32 v243, v243, v135
v_add_f32 v242, v242, v136
v_add_f32 v242, v242, v137
v_add_f32 v242, v242, v138
v_add_f32 v242, v242, v139
v_add_f32 v243, v243, v140
buffer_store_dwordx4 v[68:71], v253, s[20:23], s51 offen offset:1024 sc1
v_add_f32 v243, v243, v141
v_add_f32 v243, v243, v142
v_add_f32 v243, v243, v143
PK 160,128,129
PK 161,130,131
PK 162,136,137
PK 163,138,139
PK 164,132,133
PK 165,134,135
PK 166,140,141
PK 167,142,143
s_cmp_eq_u32 s71, 2
s_cbranch_scc1 .LBN2
)A"
R"A(v_mfma_f32_16x16x32_bf16 v[128:131], a[0:3], a[64:67], v[244:247]
s_add_u32 s63, s59, s62
RD 32,240,8192
RD 36,240,9216
MA 128,4,72
v_add_f32 v144, v144, v244
v_add_f32 v145, v145, v244
MA 128,8,80
s_add_u32 m0, s58, s62
RD 40,240,10240
RD 44,240,11264
v_add_f32 v146, v146, v244
MA 128,12,88
WL 4
v_add_f32 v147, v147, v244
v_add_f32 v148, v148, v248
MA 128,16,96
s_add_u32 s51, s73, 8192
RD 48,240,12288
v_add_f32 v149, v149, v248
MA 128,20,104
buffer_load_dwordx4 v252, s[12:15], s63 offen offset:0 sc1 lds
MA 128,24,112
s_add_u32 s59, s59, 0x18000
RD 52,240,13312
v_add_f32 v150, v150, v248
MA 128,28,120
v_add_f32 v151, v151, v248
v_add_f32 v152, v152, v244
v_mfma_f32_16x16x32_bf16 v[132:135], a[0:3], a[68:71], v[248:251]
s_add_u32 s7, s58, 32768
RD 56,240,14336
v_add_f32 v153, v153, v244
MA 132,4,76
buffer_load_dwordx4 v252, s[12:15], s63 offen offset:1024 sc1 lds
MA 132,8,84
s_sub_u32 s58, s58, 131072
RD 60,240,15360
v_add_f32 v154, v154, v244
v_add_f32 v155, v155, v244
MA 132,12,92
s_min_u32 s58, s7, s58
v_add_f32 v156, v156, v248
v_add_f32 v157, v157, v248
MA 132,16,100
s_add_u32 s6, s57, 32768
v_add_f32 v158, v158, v248
v_add_f32 v159, v159, v248
MA 132,20,108
buffer_store_dwordx4 v[84:87], v253, s[20:23], s51 offen offset:3072 sc1
MA 132,24,116
s_add_u32 s51, s73, 12288
v_exp_f32 v144, v144
MA 132,28,124
WL 4
v_exp_f32 v145, v145
v_mfma_f32_16x16x32_bf16 v[136:139], a[32:35], a[64:67], v[244:247]
s_sub_u32 s57, s57, 131072
RD 0,240,16384
RD 4,240,17408
PK 168,144,145
MA 136,36,72
buffer_load_dwordx4 v252, s[12:15], s63 offen offset:2048 sc1 lds
MA 136,40,80
s_min_u32 s57, s6, s57
RD 8,240,18432
RD 12,240,19456
MA 136,44,88
WL 4
v_exp_f32 v146, v146
MA 136,48,96
s_add_u32 s54, s54, 56
RD 16,240,20480
v_add_f32 v242, v242, v144
MA 136,52,104
buffer_load_dwordx4 v252, s[12:15], s63 offen offset:3072 sc1 lds
MA 136,56,112
s_add_u32 s63, s63, 4096
RD 20,240,21504
v_add_f32 v242, v242, v145
MA 136,60,120
s_lshr_b32 s64, s54, 3
v_exp_f32 v147, v147
v_mfma_f32_16x16x32_bf16 v[140:143], a[32:35], a[68:71], v[248:251]
s_add_u32 m0, m0, 4096
RD 24,240,22528
v_add_f32 v242, v242, v146
PK 169,146,147
MA 140,36,76
buffer_store_dwordx4 v[100:103], v253, s[20:23], s51 offen offset:1024 sc1
MA 140,40,84
s_add_u32 s51, s73, 12288
RD 28,240,23552
v_add_f32 v242, v242, v147
MA 140,44,92
s_lshl_b32 s64, s64, 6
v_exp_f32 v148, v148
MA 140,48,100
s_bfe_u32 s85, s54, 0x20001
v_exp_f32 v149, v149
MA 140,52,108
buffer_load_dwordx4 v252, s[12:15], s63 offen offset:0 sc1 lds
PK 172,148,149
MA 140,56,116
s_lshr_b32 s80, s54, 4
v_exp_f32 v150, v150
MA 140,60,124
WL 4
v_exp_f32 v151, v151
v_add_f32 v243, v243, v150
v_mfma_f32_16x16x32_bf16 v[144:147], a[0:3], a[64:67], v[244:247]
RD 32,240,24576
RD 36,240,25600
v_add_f32 v243, v243, v151
MA 144,4,72
buffer_load_dwordx4 v252, s[12:15], s63 offen offset:1024 sc1 lds
PK 173,150,151
MA 144,8,80
s_lshl_b32 s80, s80, 2
RD 40,240,26624
RD 44,240,27648
MA 144,12,88
WL 4
v_exp_f32 v152, v152
MA 144,16,96
s_add_u32 s80, s80, s85
RD 48,240,28672
v_add_f32 v243, v243, v148
MA 144,20,104
buffer_store_dwordx4 v[116:119], v253, s[20:23], s51 offen offset:3072 sc1
MA 144,24,112
s_lshl_b32 s80, s80, 1
RD 52,240,29696
v_add_f32 v243, v243, v149
MA 144,28,120
s_bfe_u32 s50, s54, 0x10003
v_exp_f32 v153, v153
v_mfma_f32_16x16x32_bf16 v[148:151], a[0:3], a[68:71], v[248:251]
RD 56,240,30720
v_add_f32 v242, v242, v152
PK 170,152,153
MA 148,4,76
buffer_load_dwordx4 v252, s[12:15], s63 offen offset:2048 sc1 lds
MA 148,8,84
s_add_u32 s80, s80, s50
RD 60,240,31744
v_add_f32 v242, v242, v153
MA 148,12,92
s_lshl_b32 s80, s80, 1
v_exp_f32 v154, v154
MA 148,16,100
s_and_b32 s50, s54, 1
v_exp_f32 v155, v155
MA 148,20,108
buffer_load_dwordx4 v252, s[12:15], s63 offen offset:3072 sc1 lds
PK 171,154,155
MA 148,24,116
s_add_u32 s80, s80, s50
v_add_u32 v240, s57, v253
v_add_f32 v242, v242, v154
MA 148,28,124
s_waitcnt vmcnt(33) lgkmcnt(4)
s_barrier
v_add_f32 v242, v242, v155
v_mfma_f32_16x16x32_bf16 v[152:155], a[32:35], a[64:67], v[244:247]
s_add_u32 s63, s59, s62
RD 0,240,0
v_max3_f32 v254, v128, v129, v130
MA 152,36,72
buffer_load_dwordx2 v[202:203], v198, s[20:23], s55 offen sc1
MA 152,40,80
s_add_u32 m0, s58, s62
RD 4,240,1024
v_max_f32 v254, v254, v131
MA 152,44,88
WL 2
v_exp_f32 v156, v156
MA 152,48,96
RD 8,240,2048
v_exp_f32 v157, v157
MA 152,52,104
buffer_load_dwordx4 v252, s[12:15], s63 offen offset:0 sc1 lds
v_exp_f32 v158, v158
v_exp_f32 v159, v159
MA 152,56,112
RD 12,240,3072
v_add_f32 v243, v243, v156
v_add_f32 v243, v243, v159
PK 175,158,159
MA 152,60,120
v_add_f32 v243, v243, v157
v_add_f32 v243, v243, v158
PK 174,156,157
v_mfma_f32_16x16x32_bf16 v[156:159], a[32:35], a[68:71], v[248:251]
s_lshl_b32 s80, s80, 10
RD 16,240,4096
v_max3_f32 v255, v132, v133, v134
MA 156,36,76
buffer_load_dwordx4 v252, s[12:15], s63 offen offset:1024 sc1 lds
MA 156,40,84
s_lshl_b32 s50, s50, 2
RD 20,240,5120
v_max_f32 v255, v255, v135
MA 156,44,92
s_add_u32 s85, s85, s50
v_max3_f32 v254, v254, v136, v137
v_max3_f32 v254, v254, v138, v139
MA 156,48,100
s_lshl_b32 s50, s77, 3
RD 24,240,6144
v_max3_f32 v255, v255, v140, v141
MA 156,52,108
buffer_load_dwordx4 v252, s[12:15], s63 offen offset:2048 sc1 lds
MA 156,56,116
s_add_u32 s85, s85, s50
RD 28,240,7168
v_max3_f32 v255, v255, v142, v143
MA 156,60,124
WL 6
v_max3_f32 v254, v254, v144, v145
v_max3_f32 v254, v254, v146, v147
v_max3_f32 v255, v255, v148, v149
v_max3_f32 v255, v255, v150, v151
s_mul_i32 s85, s85, 14336
s_add_u32 s85, s85, s64
s_mul_i32 s54, s77, 0xe0000
s_add_u32 s54, s54, 0x6000000
s_add_u32 s50, s79, 2
s_lshr_b32 s50, s50, 4
s_min_u32 s50, s50, 1
s_lshl_b32 s50, s50, 30
s_add_u32 s80, s80, s50
s_add_u32 s50, s82, s50
v_mfma_f32_16x16x32_bf16 v[0:3], a[0:3], v[160:163], 0
RD 32,240,8192
RD 36,240,9216
v_mfma_f32_16x16x32_bf16 v[4:7], a[0:3], v[164:167], 0
buffer_load_dwordx4 v252, s[12:15], s63 offen offset:3072 sc1 lds
v_mfma_f32_16x16x32_bf16 v[12:15], a[4:7], v[164:167], 0
s_add_u32 s63, s63, 4096
RD 40,240,10240
v_max3_f32 v254, v254, v152, v153
v_mfma_f32_16x16x32_bf16 v[8:11], a[4:7], v[160:163], 0
WL 7
v_max3_f32 v254, v254, v154, v155
v_max3_f32 v255, v255, v156, v157
v_mfma_f32_16x16x32_bf16 v[16:19], a[8:11], v[160:163], 0
s_add_u32 m0, m0, 4096
RD 44,240,11264
v_max3_f32 v255, v255, v158, v159
v_mfma_f32_16x16x32_bf16 v[20:23], a[8:11], v[164:167], 0
v_max_f32 v241, v254, v255
v_cmp_lt_f32 vcc, 0x41800000, v241
v_mfma_f32_16x16x32_bf16 v[28:31], a[12:15], v[164:167], 0
s_cbranch_vccnz .LRA6
.LRAR6:
RD 48,240,12288
v_mfma_f32_16x16x32_bf16 v[24:27], a[12:15], v[160:163], 0
s_waitcnt vmcnt(7) lgkmcnt(7)
s_barrier
v_mfma_f32_16x16x32_bf16 v[32:35], a[16:19], v[160:163], 0
RD 52,240,13312
RD 56,240,14336
v_mfma_f32_16x16x32_bf16 v[36:39], a[16:19], v[164:167], 0
buffer_store_dwordx2 v[192:193], v201, s[20:23], 0 offen sc1
v_mfma_f32_16x16x32_bf16 v[44:47], a[20:23], v[164:167], 0
RD 60,240,15360
v_mfma_f32_16x16x32_bf16 v[40:43], a[20:23], v[160:163], 0
WL 8
v_exp_f32 v128, v128
v_mfma_f32_16x16x32_bf16 v[48:51], a[24:27], v[160:163], 0
RD 0,240,16384
v_add_f32 v242, v242, v128
v_mfma_f32_16x16x32_bf16 v[52:55], a[24:27], v[164:167], 0
buffer_load_dwordx4 v252, s[12:15], s63 offen offset:0 sc1 lds
v_mfma_f32_16x16x32_bf16 v[60:63], a[28:31], v[164:167], 0
s_add_u32 s59, s59, 0xffff8000
RD 4,240,17408
v_mfma_f32_16x16x32_bf16 v[56:59], a[28:31], v[160:163], 0
WL 8
v_exp_f32 v129, v129
v_mfma_f32_16x16x32_bf16 v[64:67], a[32:35], v[160:163], 0
s_add_u32 s7, s58, 32768
RD 8,240,18432
v_add_f32 v242, v242, v129
v_mfma_f32_16x16x32_bf16 v[68:71], a[32:35], v[164:167], 0
v_exp_f32 v130, v130
v_mfma_f32_16x16x32_bf16 v[76:79], a[36:39], v[164:167], 0
s_sub_u32 s58, s58, 131072
RD 12,240,19456
RD 16,240,20480
v_mfma_f32_16x16x32_bf16 v[72:75], a[36:39], v[160:163], 0
WL 9
v_exp_f32 v131, v131
v_mfma_f32_16x16x32_bf16 v[80:83], a[40:43], v[160:163], 0
s_min_u32 s58, s7, s58
RD 20,240,21504
v_add_f32 v242, v242, v130
v_mfma_f32_16x16x32_bf16 v[84:87], a[40:43], v[164:167], 0
buffer_load_dwordx4 v252, s[12:15], s63 offen offset:1024 sc1 lds
v_mfma_f32_16x16x32_bf16 v[92:95], a[44:47], v[164:167], 0
s_add_u32 s6, s57, 32768
RD 24,240,22528
v_add_f32 v242, v242, v131
v_mfma_f32_16x16x32_bf16 v[88:91], a[44:47], v[160:163], 0
WL 9
v_exp_f32 v132, v132
v_mfma_f32_16x16x32_bf16 v[96:99], a[48:51], v[160:163], 0
s_sub_u32 s57, s57, 131072
RD 28,240,23552
v_add_f32 v243, v243, v132
v_mfma_f32_16x16x32_bf16 v[100:103], a[48:51], v[164:167], 0
v_exp_f32 v133, v133
v_mfma_f32_16x16x32_bf16 v[108:111], a[52:55], v[164:167], 0
s_min_u32 s57, s6, s57
RD 32,240,24576
v_add_f32 v243, v243, v133
v_mfma_f32_16x16x32_bf16 v[104:107], a[52:55], v[160:163], 0
WL 9
v_exp_f32 v134, v134
v_mfma_f32_16x16x32_bf16 v[112:115], a[56:59], v[160:163], 0
RD 36,240,25600
RD 40,240,26624
v_mfma_f32_16x16x32_bf16 v[116:119], a[56:59], v[164:167], 0
buffer_load_dwordx4 v252, s[12:15], s63 offen offset:2048 sc1 lds
v_mfma_f32_16x16x32_bf16 v[124:127], a[60:63], v[164:167], 0
RD 44,240,27648
v_add_f32 v243, v243, v134
v_mfma_f32_16x16x32_bf16 v[120:123], a[60:63], v[160:163], 0
WL 10
v_exp_f32 v135, v135
MB 0,0,168
RD 48,240,28672
PK 160,128,129
MB 4,0,172
v_exp_f32 v136, v136
MB 12,4,172
RD 52,240,29696
PK 161,130,131
MB 8,4,168
WL 10
v_exp_f32 v137, v137
MB 16,8,168
RD 56,240,30720
v_add_f32 v243, v243, v135
MB 20,8,172
v_exp_f32 v138, v138
MB 28,12,172
RD 60,240,31744
PK 164,132,133
MB 24,12,168
WL 10
v_exp_f32 v139, v139
MB 32,16,168
v_exp_f32 v140, v140
MB 36,16,172
buffer_load_dwordx4 v252, s[12:15], s63 offen offset:3072 sc1 lds
MB 44,20,172
v_exp_f32 v141, v141
MB 40,20,168
WL 8
v_exp_f32 v142, v142
MB 48,24,168
v_exp_f32 v143, v143
MB 52,24,172
v_exp_f32 v144, v144
MB 60,28,172
v_add_u32 v240, s57, v253
PK 165,134,135
MB 56,28,168
s_waitcnt vmcnt(9) lgkmcnt(0)
s_barrier
v_add_f32 v242, v242, v136
MB 64,32,168
RD 0,240,0
RD 4,240,1024
MB 68,32,172
v_exp_f32 v145, v145
MB 76,36,172
RD 8,240,2048
RD 12,240,3072
MB 72,36,168
v_exp_f32 v146, v146
MB 80,40,168
RD 16,240,4096
v_add_f32 v242, v242, v137
MB 84,40,172
v_exp_f32 v147, v147
MB 92,44,172
RD 20,240,5120
v_add_f32 v242, v242, v138
MB 88,44,168
v_add_f32 v242, v242, v139
PK 162,136,137
MB 96,48,168
s_sub_u32 s65, s71, 4
RD 24,240,6144
PK 163,138,139
MB 100,48,172
v_add_f32 v243, v243, v140
v_add_f32 v243, v243, v141
MB 108,52,172
RD 28,240,7168
v_add_f32 v243, v243, v142
MB 104,52,168
v_add_f32 v243, v243, v143
PK 166,140,141
MB 112,56,168
PK 167,142,143
v_add_f32 v242, v242, v144
MB 116,56,172
v_add_f32 v242, v242, v145
v_add_f32 v242, v242, v146
MB 124,60,172
v_add_f32 v242, v242, v147
MB 120,60,168
WL 4
s_cmp_lg_u32 s69, 0
s_cbranch_scc1 .LRB6
.LRBR6:
s_branch .LBLP
; pack the context of block i
.LBFIN:
s_waitcnt vmcnt(16)
s_barrier
TR 0,208,0
TR 2,208,2048
TR 4,208,4096
TR 6,208,6144
TR 8,208,8192
TR 10,208,10240
TR 12,208,12288
TR 14,208,14336
TR 16,208,16384
TR 18,208,18432
TR 20,208,20480
TR 22,208,22528
TR 24,208,24576
TR 26,208,26624
TR 28,208,28672
TR 30,208,30720
PK 4,4,5
PK 5,6,7
PK 6,12,13
PK 7,14,15
PK 20,20,21
PK 21,22,23
PK 22,28,29
PK 23,30,31
PK 36,36,37
PK 37,38,39
PK 38,44,45
PK 39,46,47
PK 52,52,53
PK 53,54,55
PK 54,60,61
PK 55,62,63
PK 68,68,69
PK 69,70,71
PK 70,76,77
PK 71,78,79
PK 84,84,85
PK 85,86,87
PK 86,92,93
PK 87,94,95
PK 100,100,101
PK 101,102,103
PK 102,108,109
PK 103,110,111
PK 116,116,117
PK 117,118,119
PK 118,124,125
PK 119,126,127
s_branch .LDWO
.LPS0:
s_mov_b32 s78, 200000
.LPL0:
s_sleep 2
buffer_load_dwordx2 v[254:255], v253, s[20:23], s67 offen sc1
s_waitcnt vmcnt(0)
v_cmp_ne_u64 vcc, s[48:49], v[254:255]
s_sub_u32 s78, s78, 1
s_cbranch_scc1 .LPX0
s_cbranch_vccnz .LPL0
.LPX0:
s_branch .LPR0
.LPS1:
s_mov_b32 s78, 200000
.LPL1:
s_sleep 2
buffer_load_dwordx2 v[248:249], v250, s[20:23], s53 offen sc1
s_waitcnt vmcnt(0)
v_cmp_ne_u64 vcc, s[48:49], v[248:249]
s_sub_u32 s78, s78, 1
s_cbranch_scc1 .LPX1
s_cbranch_vccnz .LPL1
.LPX1:
s_branch .LPR1
.LPS2:
s_mov_b32 s78, 200000
.LPL2:
s_sleep 2
buffer_load_dwordx2 v[248:249], v250, s[20:23], s53 offen sc1
s_waitcnt vmcnt(0)
v_cmp_ne_u64 vcc, s[48:49], v[248:249]
)A"
R"A(s_sub_u32 s78, s78, 1
s_cbranch_scc1 .LPX2
s_cbranch_vccnz .LPL2
.LPX2:
s_branch .LPR2
.LMASK0:
s_nop 3
v_add_f32 v128, v128, v176
v_add_f32 v129, v129, v177
v_add_f32 v130, v130, v178
v_add_f32 v131, v131, v179
v_add_f32 v136, v136, v180
v_add_f32 v137, v137, v181
v_add_f32 v138, v138, v182
v_add_f32 v139, v139, v183
v_add_f32 v144, v144, v184
v_add_f32 v145, v145, v185
v_add_f32 v146, v146, v186
v_add_f32 v147, v147, v187
v_add_f32 v152, v152, v188
v_add_f32 v153, v153, v189
v_add_f32 v154, v154, v190
v_add_f32 v155, v155, v191
s_branch .LNOMASK0
.LBN3:
v_add_f32 v144, v144, v244
v_add_f32 v145, v145, v244
v_add_f32 v146, v146, v244
v_add_f32 v147, v147, v244
v_add_f32 v148, v148, v248
v_add_f32 v149, v149, v248
v_add_f32 v150, v150, v248
v_add_f32 v151, v151, v248
v_add_f32 v152, v152, v244
v_add_f32 v153, v153, v244
v_add_f32 v154, v154, v244
v_add_f32 v155, v155, v244
v_add_f32 v156, v156, v248
v_add_f32 v157, v157, v248
v_add_f32 v158, v158, v248
v_add_f32 v159, v159, v248
v_exp_f32 v144, v144
v_exp_f32 v145, v145
v_exp_f32 v146, v146
v_exp_f32 v147, v147
v_add_f32 v242, v242, v144
v_add_f32 v242, v242, v145
v_add_f32 v242, v242, v146
v_add_f32 v242, v242, v147
s_cmp_eq_u32 s71, 2
s_cbranch_scc1 .LBLAST
v_mov_b64 v[0:1], 0
v_mov_b64 v[2:3], 0
v_mov_b64 v[4:5], 0
v_mov_b64 v[6:7], 0
v_mov_b64 v[8:9], 0
v_mov_b64 v[10:11], 0
v_mov_b64 v[12:13], 0
v_mov_b64 v[14:15], 0
v_mov_b64 v[16:17], 0
v_mov_b64 v[18:19], 0
v_mov_b64 v[20:21], 0
v_mov_b64 v[22:23], 0
v_mov_b64 v[24:25], 0
v_mov_b64 v[26:27], 0
v_mov_b64 v[28:29], 0
v_mov_b64 v[30:31], 0
v_mov_b64 v[32:33], 0
v_mov_b64 v[34:35], 0
v_mov_b64 v[36:37], 0
v_mov_b64 v[38:39], 0
v_mov_b64 v[40:41], 0
v_mov_b64 v[42:43], 0
v_mov_b64 v[44:45], 0
v_mov_b64 v[46:47], 0
v_mov_b64 v[48:49], 0
v_mov_b64 v[50:51], 0
v_mov_b64 v[52:53], 0
v_mov_b64 v[54:55], 0
v_mov_b64 v[56:57], 0
v_mov_b64 v[58:59], 0
v_mov_b64 v[60:61], 0
v_mov_b64 v[62:63], 0
v_mov_b64 v[64:65], 0
v_mov_b64 v[66:67], 0
v_mov_b64 v[68:69], 0
v_mov_b64 v[70:71], 0
v_mov_b64 v[72:73], 0
v_mov_b64 v[74:75], 0
v_mov_b64 v[76:77], 0
v_mov_b64 v[78:79], 0
v_mov_b64 v[80:81], 0
v_mov_b64 v[82:83], 0
v_mov_b64 v[84:85], 0
v_mov_b64 v[86:87], 0
v_mov_b64 v[88:89], 0
v_mov_b64 v[90:91], 0
v_mov_b64 v[92:93], 0
v_mov_b64 v[94:95], 0
v_mov_b64 v[96:97], 0
v_mov_b64 v[98:99], 0
v_mov_b64 v[100:101], 0
v_mov_b64 v[102:103], 0
v_mov_b64 v[104:105], 0
v_mov_b64 v[106:107], 0
v_mov_b64 v[108:109], 0
v_mov_b64 v[110:111], 0
v_mov_b64 v[112:113], 0
v_mov_b64 v[114:115], 0
v_mov_b64 v[116:117], 0
v_mov_b64 v[118:119], 0
v_mov_b64 v[120:121], 0
v_mov_b64 v[122:123], 0
v_mov_b64 v[124:125], 0
v_mov_b64 v[126:127], 0
s_branch .LBT1
; out-of-line: rescale paths (RA*: new reference, RB*: rescale O), bounded polls (PS*)
.LRA0:
v_mov_b32 v241, v254
s_nop 1
v_permlane16_swap_b32 v254, v241
v_max_f32 v254, v254, v241
v_mov_b32 v241, v254
s_nop 1
v_permlane32_swap_b32 v254, v241
v_max_f32 v254, v254, v241
v_max_f32 v254, 0, v254
v_sub_f32 v128, v128, v254
v_sub_f32 v129, v129, v254
v_sub_f32 v130, v130, v254
v_sub_f32 v131, v131, v254
v_sub_f32 v136, v136, v254
v_sub_f32 v137, v137, v254
v_sub_f32 v138, v138, v254
v_sub_f32 v139, v139, v254
v_sub_f32 v144, v144, v254
v_sub_f32 v145, v145, v254
v_sub_f32 v146, v146, v254
v_sub_f32 v147, v147, v254
v_sub_f32 v152, v152, v254
v_sub_f32 v153, v153, v254
v_sub_f32 v154, v154, v254
v_sub_f32 v155, v155, v254
v_sub_f32 v244, v244, v254
v_sub_f32 v245, v245, v254
v_sub_f32 v246, v246, v254
v_sub_f32 v247, v247, v254
v_exp_f32 v254, -v254
s_nop 0
v_mul_f32 v242, v242, v254
v_mov_b32 v241, v255
s_nop 1
v_permlane16_swap_b32 v255, v241
v_max_f32 v255, v255, v241
v_mov_b32 v241, v255
s_nop 1
v_permlane32_swap_b32 v255, v241
v_max_f32 v255, v255, v241
v_max_f32 v255, 0, v255
v_sub_f32 v132, v132, v255
v_sub_f32 v133, v133, v255
v_sub_f32 v134, v134, v255
v_sub_f32 v135, v135, v255
v_sub_f32 v140, v140, v255
v_sub_f32 v141, v141, v255
v_sub_f32 v142, v142, v255
v_sub_f32 v143, v143, v255
v_sub_f32 v148, v148, v255
v_sub_f32 v149, v149, v255
v_sub_f32 v150, v150, v255
v_sub_f32 v151, v151, v255
v_sub_f32 v156, v156, v255
v_sub_f32 v157, v157, v255
v_sub_f32 v158, v158, v255
v_sub_f32 v159, v159, v255
v_sub_f32 v248, v248, v255
v_sub_f32 v249, v249, v255
v_sub_f32 v250, v250, v255
v_sub_f32 v251, v251, v255
v_exp_f32 v255, -v255
s_nop 0
v_mul_f32 v243, v243, v255
s_mov_b32 s69, 1
s_branch .LRAR0
.LRB0:
s_nop 7
v_mul_f32 v0, v0, v254
v_mul_f32 v1, v1, v254
v_mul_f32 v2, v2, v254
v_mul_f32 v3, v3, v254
v_mul_f32 v8, v8, v254
v_mul_f32 v9, v9, v254
v_mul_f32 v10, v10, v254
v_mul_f32 v11, v11, v254
v_mul_f32 v16, v16, v254
v_mul_f32 v17, v17, v254
v_mul_f32 v18, v18, v254
v_mul_f32 v19, v19, v254
v_mul_f32 v24, v24, v254
v_mul_f32 v25, v25, v254
v_mul_f32 v26, v26, v254
v_mul_f32 v27, v27, v254
v_mul_f32 v32, v32, v254
v_mul_f32 v33, v33, v254
v_mul_f32 v34, v34, v254
v_mul_f32 v35, v35, v254
v_mul_f32 v40, v40, v254
v_mul_f32 v41, v41, v254
v_mul_f32 v42, v42, v254
v_mul_f32 v43, v43, v254
v_mul_f32 v48, v48, v254
v_mul_f32 v49, v49, v254
v_mul_f32 v50, v50, v254
v_mul_f32 v51, v51, v254
v_mul_f32 v56, v56, v254
v_mul_f32 v57, v57, v254
v_mul_f32 v58, v58, v254
v_mul_f32 v59, v59, v254
v_mul_f32 v64, v64, v254
v_mul_f32 v65, v65, v254
v_mul_f32 v66, v66, v254
v_mul_f32 v67, v67, v254
v_mul_f32 v72, v72, v254
v_mul_f32 v73, v73, v254
v_mul_f32 v74, v74, v254
v_mul_f32 v75, v75, v254
v_mul_f32 v80, v80, v254
v_mul_f32 v81, v81, v254
v_mul_f32 v82, v82, v254
v_mul_f32 v83, v83, v254
v_mul_f32 v88, v88, v254
v_mul_f32 v89, v89, v254
v_mul_f32 v90, v90, v254
v_mul_f32 v91, v91, v254
v_mul_f32 v96, v96, v254
v_mul_f32 v97, v97, v254
v_mul_f32 v98, v98, v254
v_mul_f32 v99, v99, v254
v_mul_f32 v104, v104, v254
v_mul_f32 v105, v105, v254
v_mul_f32 v106, v106, v254
v_mul_f32 v107, v107, v254
v_mul_f32 v112, v112, v254
v_mul_f32 v113, v113, v254
v_mul_f32 v114, v114, v254
v_mul_f32 v115, v115, v254
v_mul_f32 v120, v120, v254
v_mul_f32 v121, v121, v254
v_mul_f32 v122, v122, v254
v_mul_f32 v123, v123, v254
v_mul_f32 v4, v4, v255
v_mul_f32 v5, v5, v255
v_mul_f32 v6, v6, v255
v_mul_f32 v7, v7, v255
v_mul_f32 v12, v12, v255
v_mul_f32 v13, v13, v255
v_mul_f32 v14, v14, v255
v_mul_f32 v15, v15, v255
v_mul_f32 v20, v20, v255
v_mul_f32 v21, v21, v255
v_mul_f32 v22, v22, v255
v_mul_f32 v23, v23, v255
v_mul_f32 v28, v28, v255
v_mul_f32 v29, v29, v255
v_mul_f32 v30, v30, v255
v_mul_f32 v31, v31, v255
v_mul_f32 v36, v36, v255
v_mul_f32 v37, v37, v255
v_mul_f32 v38, v38, v255
v_mul_f32 v39, v39, v255
v_mul_f32 v44, v44, v255
v_mul_f32 v45, v45, v255
v_mul_f32 v46, v46, v255
v_mul_f32 v47, v47, v255
v_mul_f32 v52, v52, v255
v_mul_f32 v53, v53, v255
v_mul_f32 v54, v54, v255
v_mul_f32 v55, v55, v255
v_mul_f32 v60, v60, v255
v_mul_f32 v61, v61, v255
v_mul_f32 v62, v62, v255
v_mul_f32 v63, v63, v255
v_mul_f32 v68, v68, v255
v_mul_f32 v69, v69, v255
v_mul_f32 v70, v70, v255
v_mul_f32 v71, v71, v255
v_mul_f32 v76, v76, v255
v_mul_f32 v77, v77, v255
v_mul_f32 v78, v78, v255
v_mul_f32 v79, v79, v255
v_mul_f32 v84, v84, v255
v_mul_f32 v85, v85, v255
v_mul_f32 v86, v86, v255
v_mul_f32 v87, v87, v255
v_mul_f32 v92, v92, v255
v_mul_f32 v93, v93, v255
v_mul_f32 v94, v94, v255
v_mul_f32 v95, v95, v255
v_mul_f32 v100, v100, v255
v_mul_f32 v101, v101, v255
v_mul_f32 v102, v102, v255
v_mul_f32 v103, v103, v255
v_mul_f32 v108, v108, v255
v_mul_f32 v109, v109, v255
v_mul_f32 v110, v110, v255
v_mul_f32 v111, v111, v255
v_mul_f32 v116, v116, v255
v_mul_f32 v117, v117, v255
v_mul_f32 v118, v118, v255
v_mul_f32 v119, v119, v255
v_mul_f32 v124, v124, v255
v_mul_f32 v125, v125, v255
v_mul_f32 v126, v126, v255
v_mul_f32 v127, v127, v255
s_mov_b32 s69, 0
s_nop 0
s_branch .LRBR0
.LRA1:
v_mov_b32 v241, v254
s_nop 1
v_permlane16_swap_b32 v254, v241
v_max_f32 v254, v254, v241
v_mov_b32 v241, v254
s_nop 1
v_permlane32_swap_b32 v254, v241
v_max_f32 v254, v254, v241
v_max_f32 v254, 0, v254
v_sub_f32 v128, v128, v254
v_sub_f32 v129, v129, v254
v_sub_f32 v130, v130, v254
v_sub_f32 v131, v131, v254
v_sub_f32 v136, v136, v254
v_sub_f32 v137, v137, v254
v_sub_f32 v138, v138, v254
v_sub_f32 v139, v139, v254
v_sub_f32 v144, v144, v254
v_sub_f32 v145, v145, v254
v_sub_f32 v146, v146, v254
v_sub_f32 v147, v147, v254
v_sub_f32 v152, v152, v254
v_sub_f32 v153, v153, v254
v_sub_f32 v154, v154, v254
v_sub_f32 v155, v155, v254
v_sub_f32 v244, v244, v254
v_sub_f32 v245, v245, v254
v_sub_f32 v246, v246, v254
v_sub_f32 v247, v247, v254
v_exp_f32 v254, -v254
s_nop 0
v_mul_f32 v242, v242, v254
v_mov_b32 v241, v255
s_nop 1
v_permlane16_swap_b32 v255, v241
v_max_f32 v255, v255, v241
v_mov_b32 v241, v255
s_nop 1
v_permlane32_swap_b32 v255, v241
v_max_f32 v255, v255, v241
v_max_f32 v255, 0, v255
v_sub_f32 v132, v132, v255
v_sub_f32 v133, v133, v255
v_sub_f32 v134, v134, v255
v_sub_f32 v135, v135, v255
v_sub_f32 v140, v140, v255
v_sub_f32 v141, v141, v255
v_sub_f32 v142, v142, v255
v_sub_f32 v143, v143, v255
v_sub_f32 v148, v148, v255
v_sub_f32 v149, v149, v255
v_sub_f32 v150, v150, v255
v_sub_f32 v151, v151, v255
v_sub_f32 v156, v156, v255
v_sub_f32 v157, v157, v255
v_sub_f32 v158, v158, v255
v_sub_f32 v159, v159, v255
v_sub_f32 v248, v248, v255
v_sub_f32 v249, v249, v255
v_sub_f32 v250, v250, v255
v_sub_f32 v251, v251, v255
v_exp_f32 v255, -v255
s_nop 0
v_mul_f32 v243, v243, v255
s_mov_b32 s69, 1
s_branch .LRAR1
.LRB1:
s_nop 7
v_mul_f32 v0, v0, v254
v_mul_f32 v1, v1, v254
v_mul_f32 v2, v2, v254
v_mul_f32 v3, v3, v254
v_mul_f32 v8, v8, v254
v_mul_f32 v9, v9, v254
v_mul_f32 v10, v10, v254
v_mul_f32 v11, v11, v254
v_mul_f32 v16, v16, v254
v_mul_f32 v17, v17, v254
v_mul_f32 v18, v18, v254
v_mul_f32 v19, v19, v254
v_mul_f32 v24, v24, v254
v_mul_f32 v25, v25, v254
v_mul_f32 v26, v26, v254
v_mul_f32 v27, v27, v254
v_mul_f32 v32, v32, v254
v_mul_f32 v33, v33, v254
v_mul_f32 v34, v34, v254
v_mul_f32 v35, v35, v254
v_mul_f32 v40, v40, v254
v_mul_f32 v41, v41, v254
v_mul_f32 v42, v42, v254
v_mul_f32 v43, v43, v254
v_mul_f32 v48, v48, v254
v_mul_f32 v49, v49, v254
v_mul_f32 v50, v50, v254
v_mul_f32 v51, v51, v254
v_mul_f32 v56, v56, v254
v_mul_f32 v57, v57, v254
v_mul_f32 v58, v58, v254
v_mul_f32 v59, v59, v254
v_mul_f32 v64, v64, v254
v_mul_f32 v65, v65, v254
v_mul_f32 v66, v66, v254
v_mul_f32 v67, v67, v254
v_mul_f32 v72, v72, v254
v_mul_f32 v73, v73, v254
v_mul_f32 v74, v74, v254
v_mul_f32 v75, v75, v254
v_mul_f32 v80, v80, v254
v_mul_f32 v81, v81, v254
v_mul_f32 v82, v82, v254
v_mul_f32 v83, v83, v254
v_mul_f32 v88, v88, v254
v_mul_f32 v89, v89, v254
v_mul_f32 v90, v90, v254
v_mul_f32 v91, v91, v254
v_mul_f32 v96, v96, v254
v_mul_f32 v97, v97, v254
v_mul_f32 v98, v98, v254
v_mul_f32 v99, v99, v254
v_mul_f32 v104, v104, v254
v_mul_f32 v105, v105, v254
v_mul_f32 v106, v106, v254
v_mul_f32 v107, v107, v254
v_mul_f32 v112, v112, v254
v_mul_f32 v113, v113, v254
v_mul_f32 v114, v114, v254
v_mul_f32 v115, v115, v254
v_mul_f32 v120, v120, v254
v_mul_f32 v121, v121, v254
v_mul_f32 v122, v122, v254
v_mul_f32 v123, v123, v254
v_mul_f32 v4, v4, v255
v_mul_f32 v5, v5, v255
v_mul_f32 v6, v6, v255
v_mul_f32 v7, v7, v255
v_mul_f32 v12, v12, v255
v_mul_f32 v13, v13, v255
v_mul_f32 v14, v14, v255
v_mul_f32 v15, v15, v255
v_mul_f32 v20, v20, v255
v_mul_f32 v21, v21, v255
v_mul_f32 v22, v22, v255
v_mul_f32 v23, v23, v255
v_mul_f32 v28, v28, v255
v_mul_f32 v29, v29, v255
v_mul_f32 v30, v30, v255
v_mul_f32 v31, v31, v255
v_mul_f32 v36, v36, v255
v_mul_f32 v37, v37, v255
v_mul_f32 v38, v38, v255
v_mul_f32 v39, v39, v255
v_mul_f32 v44, v44, v255
v_mul_f32 v45, v45, v255
v_mul_f32 v46, v46, v255
v_mul_f32 v47, v47, v255
v_mul_f32 v52, v52, v255
v_mul_f32 v53, v53, v255
v_mul_f32 v54, v54, v255
v_mul_f32 v55, v55, v255
v_mul_f32 v60, v60, v255
v_mul_f32 v61, v61, v255
v_mul_f32 v62, v62, v255
v_mul_f32 v63, v63, v255
v_mul_f32 v68, v68, v255
v_mul_f32 v69, v69, v255
v_mul_f32 v70, v70, v255
v_mul_f32 v71, v71, v255
v_mul_f32 v76, v76, v255
v_mul_f32 v77, v77, v255
v_mul_f32 v78, v78, v255
v_mul_f32 v79, v79, v255
v_mul_f32 v84, v84, v255
v_mul_f32 v85, v85, v255
v_mul_f32 v86, v86, v255
)A"
R"A(v_mul_f32 v87, v87, v255
v_mul_f32 v92, v92, v255
v_mul_f32 v93, v93, v255
v_mul_f32 v94, v94, v255
v_mul_f32 v95, v95, v255
v_mul_f32 v100, v100, v255
v_mul_f32 v101, v101, v255
v_mul_f32 v102, v102, v255
v_mul_f32 v103, v103, v255
v_mul_f32 v108, v108, v255
v_mul_f32 v109, v109, v255
v_mul_f32 v110, v110, v255
v_mul_f32 v111, v111, v255
v_mul_f32 v116, v116, v255
v_mul_f32 v117, v117, v255
v_mul_f32 v118, v118, v255
v_mul_f32 v119, v119, v255
v_mul_f32 v124, v124, v255
v_mul_f32 v125, v125, v255
v_mul_f32 v126, v126, v255
v_mul_f32 v127, v127, v255
s_mov_b32 s69, 0
s_nop 0
s_branch .LRBR1
.LRA2:
v_mov_b32 v241, v254
s_nop 1
v_permlane16_swap_b32 v254, v241
v_max_f32 v254, v254, v241
v_mov_b32 v241, v254
s_nop 1
v_permlane32_swap_b32 v254, v241
v_max_f32 v254, v254, v241
v_max_f32 v254, 0, v254
v_sub_f32 v128, v128, v254
v_sub_f32 v129, v129, v254
v_sub_f32 v130, v130, v254
v_sub_f32 v131, v131, v254
v_sub_f32 v136, v136, v254
v_sub_f32 v137, v137, v254
v_sub_f32 v138, v138, v254
v_sub_f32 v139, v139, v254
v_sub_f32 v144, v144, v254
v_sub_f32 v145, v145, v254
v_sub_f32 v146, v146, v254
v_sub_f32 v147, v147, v254
v_sub_f32 v152, v152, v254
v_sub_f32 v153, v153, v254
v_sub_f32 v154, v154, v254
v_sub_f32 v155, v155, v254
v_sub_f32 v244, v244, v254
v_sub_f32 v245, v245, v254
v_sub_f32 v246, v246, v254
v_sub_f32 v247, v247, v254
v_exp_f32 v254, -v254
s_nop 0
v_mul_f32 v242, v242, v254
v_mov_b32 v241, v255
s_nop 1
v_permlane16_swap_b32 v255, v241
v_max_f32 v255, v255, v241
v_mov_b32 v241, v255
s_nop 1
v_permlane32_swap_b32 v255, v241
v_max_f32 v255, v255, v241
v_max_f32 v255, 0, v255
v_sub_f32 v132, v132, v255
v_sub_f32 v133, v133, v255
v_sub_f32 v134, v134, v255
v_sub_f32 v135, v135, v255
v_sub_f32 v140, v140, v255
v_sub_f32 v141, v141, v255
v_sub_f32 v142, v142, v255
v_sub_f32 v143, v143, v255
v_sub_f32 v148, v148, v255
v_sub_f32 v149, v149, v255
v_sub_f32 v150, v150, v255
v_sub_f32 v151, v151, v255
v_sub_f32 v156, v156, v255
v_sub_f32 v157, v157, v255
v_sub_f32 v158, v158, v255
v_sub_f32 v159, v159, v255
v_sub_f32 v248, v248, v255
v_sub_f32 v249, v249, v255
v_sub_f32 v250, v250, v255
v_sub_f32 v251, v251, v255
v_exp_f32 v255, -v255
s_nop 0
v_mul_f32 v243, v243, v255
s_mov_b32 s69, 1
s_branch .LRAR2
.LRB2:
s_nop 7
v_mul_f32 v0, v0, v254
v_mul_f32 v1, v1, v254
v_mul_f32 v2, v2, v254
v_mul_f32 v3, v3, v254
v_mul_f32 v8, v8, v254
v_mul_f32 v9, v9, v254
v_mul_f32 v10, v10, v254
v_mul_f32 v11, v11, v254
v_mul_f32 v16, v16, v254
v_mul_f32 v17, v17, v254
v_mul_f32 v18, v18, v254
v_mul_f32 v19, v19, v254
v_mul_f32 v24, v24, v254
v_mul_f32 v25, v25, v254
v_mul_f32 v26, v26, v254
v_mul_f32 v27, v27, v254
v_mul_f32 v32, v32, v254
v_mul_f32 v33, v33, v254
v_mul_f32 v34, v34, v254
v_mul_f32 v35, v35, v254
v_mul_f32 v40, v40, v254
v_mul_f32 v41, v41, v254
v_mul_f32 v42, v42, v254
v_mul_f32 v43, v43, v254
v_mul_f32 v48, v48, v254
v_mul_f32 v49, v49, v254
v_mul_f32 v50, v50, v254
v_mul_f32 v51, v51, v254
v_mul_f32 v56, v56, v254
v_mul_f32 v57, v57, v254
v_mul_f32 v58, v58, v254
v_mul_f32 v59, v59, v254
v_mul_f32 v64, v64, v254
v_mul_f32 v65, v65, v254
v_mul_f32 v66, v66, v254
v_mul_f32 v67, v67, v254
v_mul_f32 v72, v72, v254
v_mul_f32 v73, v73, v254
v_mul_f32 v74, v74, v254
v_mul_f32 v75, v75, v254
v_mul_f32 v80, v80, v254
v_mul_f32 v81, v81, v254
v_mul_f32 v82, v82, v254
v_mul_f32 v83, v83, v254
v_mul_f32 v88, v88, v254
v_mul_f32 v89, v89, v254
v_mul_f32 v90, v90, v254
v_mul_f32 v91, v91, v254
v_mul_f32 v96, v96, v254
v_mul_f32 v97, v97, v254
v_mul_f32 v98, v98, v254
v_mul_f32 v99, v99, v254
v_mul_f32 v104, v104, v254
v_mul_f32 v105, v105, v254
v_mul_f32 v106, v106, v254
v_mul_f32 v107, v107, v254
v_mul_f32 v112, v112, v254
v_mul_f32 v113, v113, v254
v_mul_f32 v114, v114, v254
v_mul_f32 v115, v115, v254
v_mul_f32 v120, v120, v254
v_mul_f32 v121, v121, v254
v_mul_f32 v122, v122, v254
v_mul_f32 v123, v123, v254
v_mul_f32 v4, v4, v255
v_mul_f32 v5, v5, v255
v_mul_f32 v6, v6, v255
v_mul_f32 v7, v7, v255
v_mul_f32 v12, v12, v255
v_mul_f32 v13, v13, v255
v_mul_f32 v14, v14, v255
v_mul_f32 v15, v15, v255
v_mul_f32 v20, v20, v255
v_mul_f32 v21, v21, v255
v_mul_f32 v22, v22, v255
v_mul_f32 v23, v23, v255
v_mul_f32 v28, v28, v255
v_mul_f32 v29, v29, v255
v_mul_f32 v30, v30, v255
v_mul_f32 v31, v31, v255
v_mul_f32 v36, v36, v255
v_mul_f32 v37, v37, v255
v_mul_f32 v38, v38, v255
v_mul_f32 v39, v39, v255
v_mul_f32 v44, v44, v255
v_mul_f32 v45, v45, v255
v_mul_f32 v46, v46, v255
v_mul_f32 v47, v47, v255
v_mul_f32 v52, v52, v255
v_mul_f32 v53, v53, v255
v_mul_f32 v54, v54, v255
v_mul_f32 v55, v55, v255
v_mul_f32 v60, v60, v255
v_mul_f32 v61, v61, v255
v_mul_f32 v62, v62, v255
v_mul_f32 v63, v63, v255
v_mul_f32 v68, v68, v255
v_mul_f32 v69, v69, v255
v_mul_f32 v70, v70, v255
v_mul_f32 v71, v71, v255
v_mul_f32 v76, v76, v255
v_mul_f32 v77, v77, v255
v_mul_f32 v78, v78, v255
v_mul_f32 v79, v79, v255
v_mul_f32 v84, v84, v255
v_mul_f32 v85, v85, v255
v_mul_f32 v86, v86, v255
v_mul_f32 v87, v87, v255
v_mul_f32 v92, v92, v255
v_mul_f32 v93, v93, v255
v_mul_f32 v94, v94, v255
v_mul_f32 v95, v95, v255
v_mul_f32 v100, v100, v255
v_mul_f32 v101, v101, v255
v_mul_f32 v102, v102, v255
v_mul_f32 v103, v103, v255
v_mul_f32 v108, v108, v255
v_mul_f32 v109, v109, v255
v_mul_f32 v110, v110, v255
v_mul_f32 v111, v111, v255
v_mul_f32 v116, v116, v255
v_mul_f32 v117, v117, v255
v_mul_f32 v118, v118, v255
v_mul_f32 v119, v119, v255
v_mul_f32 v124, v124, v255
v_mul_f32 v125, v125, v255
v_mul_f32 v126, v126, v255
v_mul_f32 v127, v127, v255
s_mov_b32 s69, 0
s_nop 0
s_branch .LRBR2
.LPS3:
s_mov_b32 s78, 200000
.LPL3:
s_sleep 2
buffer_load_dwordx2 v[202:203], v198, s[20:23], s55 offen sc1
s_waitcnt vmcnt(0)
v_cmp_ne_u64 vcc, s[48:49], v[202:203]
s_sub_u32 s78, s78, 1
s_cbranch_scc1 .LPX3
s_cbranch_vccnz .LPL3
.LPX3:
s_branch .LPR3
.LRA3:
v_mov_b32 v241, v254
s_nop 1
v_permlane16_swap_b32 v254, v241
v_max_f32 v254, v254, v241
v_mov_b32 v241, v254
s_nop 1
v_permlane32_swap_b32 v254, v241
v_max_f32 v254, v254, v241
v_max_f32 v254, 0, v254
v_sub_f32 v128, v128, v254
v_sub_f32 v129, v129, v254
v_sub_f32 v130, v130, v254
v_sub_f32 v131, v131, v254
v_sub_f32 v136, v136, v254
v_sub_f32 v137, v137, v254
v_sub_f32 v138, v138, v254
v_sub_f32 v139, v139, v254
v_sub_f32 v144, v144, v254
v_sub_f32 v145, v145, v254
v_sub_f32 v146, v146, v254
v_sub_f32 v147, v147, v254
v_sub_f32 v152, v152, v254
v_sub_f32 v153, v153, v254
v_sub_f32 v154, v154, v254
v_sub_f32 v155, v155, v254
v_sub_f32 v244, v244, v254
v_sub_f32 v245, v245, v254
v_sub_f32 v246, v246, v254
v_sub_f32 v247, v247, v254
v_exp_f32 v254, -v254
s_nop 0
v_mul_f32 v242, v242, v254
v_mov_b32 v241, v255
s_nop 1
v_permlane16_swap_b32 v255, v241
v_max_f32 v255, v255, v241
v_mov_b32 v241, v255
s_nop 1
v_permlane32_swap_b32 v255, v241
v_max_f32 v255, v255, v241
v_max_f32 v255, 0, v255
v_sub_f32 v132, v132, v255
v_sub_f32 v133, v133, v255
v_sub_f32 v134, v134, v255
v_sub_f32 v135, v135, v255
v_sub_f32 v140, v140, v255
v_sub_f32 v141, v141, v255
v_sub_f32 v142, v142, v255
v_sub_f32 v143, v143, v255
v_sub_f32 v148, v148, v255
v_sub_f32 v149, v149, v255
v_sub_f32 v150, v150, v255
v_sub_f32 v151, v151, v255
v_sub_f32 v156, v156, v255
v_sub_f32 v157, v157, v255
v_sub_f32 v158, v158, v255
v_sub_f32 v159, v159, v255
v_sub_f32 v248, v248, v255
v_sub_f32 v249, v249, v255
v_sub_f32 v250, v250, v255
v_sub_f32 v251, v251, v255
v_exp_f32 v255, -v255
s_nop 0
v_mul_f32 v243, v243, v255
s_mov_b32 s69, 1
s_branch .LRAR3
.LRB3:
s_nop 7
v_mul_f32 v0, v0, v254
v_mul_f32 v1, v1, v254
v_mul_f32 v2, v2, v254
v_mul_f32 v3, v3, v254
v_mul_f32 v8, v8, v254
v_mul_f32 v9, v9, v254
v_mul_f32 v10, v10, v254
v_mul_f32 v11, v11, v254
v_mul_f32 v16, v16, v254
v_mul_f32 v17, v17, v254
v_mul_f32 v18, v18, v254
v_mul_f32 v19, v19, v254
v_mul_f32 v24, v24, v254
v_mul_f32 v25, v25, v254
v_mul_f32 v26, v26, v254
v_mul_f32 v27, v27, v254
v_mul_f32 v32, v32, v254
v_mul_f32 v33, v33, v254
v_mul_f32 v34, v34, v254
v_mul_f32 v35, v35, v254
v_mul_f32 v40, v40, v254
v_mul_f32 v41, v41, v254
v_mul_f32 v42, v42, v254
v_mul_f32 v43, v43, v254
v_mul_f32 v48, v48, v254
v_mul_f32 v49, v49, v254
v_mul_f32 v50, v50, v254
v_mul_f32 v51, v51, v254
v_mul_f32 v56, v56, v254
v_mul_f32 v57, v57, v254
v_mul_f32 v58, v58, v254
v_mul_f32 v59, v59, v254
v_mul_f32 v64, v64, v254
v_mul_f32 v65, v65, v254
v_mul_f32 v66, v66, v254
v_mul_f32 v67, v67, v254
v_mul_f32 v72, v72, v254
v_mul_f32 v73, v73, v254
v_mul_f32 v74, v74, v254
v_mul_f32 v75, v75, v254
v_mul_f32 v80, v80, v254
v_mul_f32 v81, v81, v254
v_mul_f32 v82, v82, v254
v_mul_f32 v83, v83, v254
v_mul_f32 v88, v88, v254
v_mul_f32 v89, v89, v254
v_mul_f32 v90, v90, v254
v_mul_f32 v91, v91, v254
v_mul_f32 v96, v96, v254
v_mul_f32 v97, v97, v254
v_mul_f32 v98, v98, v254
v_mul_f32 v99, v99, v254
v_mul_f32 v104, v104, v254
v_mul_f32 v105, v105, v254
v_mul_f32 v106, v106, v254
v_mul_f32 v107, v107, v254
v_mul_f32 v112, v112, v254
v_mul_f32 v113, v113, v254
v_mul_f32 v114, v114, v254
v_mul_f32 v115, v115, v254
v_mul_f32 v120, v120, v254
v_mul_f32 v121, v121, v254
v_mul_f32 v122, v122, v254
v_mul_f32 v123, v123, v254
v_mul_f32 v4, v4, v255
v_mul_f32 v5, v5, v255
v_mul_f32 v6, v6, v255
v_mul_f32 v7, v7, v255
v_mul_f32 v12, v12, v255
v_mul_f32 v13, v13, v255
v_mul_f32 v14, v14, v255
v_mul_f32 v15, v15, v255
v_mul_f32 v20, v20, v255
v_mul_f32 v21, v21, v255
v_mul_f32 v22, v22, v255
v_mul_f32 v23, v23, v255
v_mul_f32 v28, v28, v255
v_mul_f32 v29, v29, v255
v_mul_f32 v30, v30, v255
v_mul_f32 v31, v31, v255
v_mul_f32 v36, v36, v255
v_mul_f32 v37, v37, v255
v_mul_f32 v38, v38, v255
v_mul_f32 v39, v39, v255
v_mul_f32 v44, v44, v255
v_mul_f32 v45, v45, v255
v_mul_f32 v46, v46, v255
v_mul_f32 v47, v47, v255
v_mul_f32 v52, v52, v255
v_mul_f32 v53, v53, v255
v_mul_f32 v54, v54, v255
v_mul_f32 v55, v55, v255
v_mul_f32 v60, v60, v255
v_mul_f32 v61, v61, v255
v_mul_f32 v62, v62, v255
v_mul_f32 v63, v63, v255
v_mul_f32 v68, v68, v255
v_mul_f32 v69, v69, v255
v_mul_f32 v70, v70, v255
v_mul_f32 v71, v71, v255
v_mul_f32 v76, v76, v255
v_mul_f32 v77, v77, v255
v_mul_f32 v78, v78, v255
v_mul_f32 v79, v79, v255
v_mul_f32 v84, v84, v255
v_mul_f32 v85, v85, v255
v_mul_f32 v86, v86, v255
v_mul_f32 v87, v87, v255
v_mul_f32 v92, v92, v255
v_mul_f32 v93, v93, v255
v_mul_f32 v94, v94, v255
v_mul_f32 v95, v95, v255
v_mul_f32 v100, v100, v255
v_mul_f32 v101, v101, v255
v_mul_f32 v102, v102, v255
v_mul_f32 v103, v103, v255
v_mul_f32 v108, v108, v255
v_mul_f32 v109, v109, v255
v_mul_f32 v110, v110, v255
v_mul_f32 v111, v111, v255
v_mul_f32 v116, v116, v255
v_mul_f32 v117, v117, v255
v_mul_f32 v118, v118, v255
v_mul_f32 v119, v119, v255
v_mul_f32 v124, v124, v255
v_mul_f32 v125, v125, v255
v_mul_f32 v126, v126, v255
v_mul_f32 v127, v127, v255
s_mov_b32 s69, 0
s_nop 0
s_branch .LRBR3
.LPS4:
s_mov_b32 s78, 200000
.LPL4:
s_sleep 2
buffer_load_dwordx2 v[202:203], v198, s[20:23], s55 offen sc1
s_waitcnt vmcnt(0)
v_cmp_ne_u64 vcc, s[48:49], v[202:203]
s_sub_u32 s78, s78, 1
s_cbranch_scc1 .LPX4
s_cbranch_vccnz .LPL4
.LPX4:
s_branch .LPR4
.LBG2:
buffer_load_dwordx2 v[202:203], v198, s[20:23], s55 offen sc1
s_waitcnt vmcnt(0)
v_cmp_ne_u64 vcc, s[48:49], v[202:203]
s_cbranch_vccnz .LPS4
.LPR4:
v_add_u32 v195, s50, v253
v_add_u32 v205, s80, v253
s_add_u32 s52, s54, 0x0
buffer_load_dwordx4 a[192:195], v195, s[20:23], s52 offen sc1
buffer_load_dwordx4 a[220:223], v205, s[20:23], s52 offen sc1
s_add_u32 s52, s54, 0x1c000
buffer_load_dwordx4 a[196:199], v195, s[20:23], s52 offen sc1
buffer_load_dwordx4 a[224:227], v205, s[20:23], s52 offen sc1
s_add_u32 s52, s54, 0x38000
buffer_load_dwordx4 a[200:203], v195, s[20:23], s52 offen sc1
buffer_load_dwordx4 a[228:231], v205, s[20:23], s52 offen sc1
s_add_u32 s52, s54, 0x54000
buffer_load_dwordx4 a[204:207], v195, s[20:23], s52 offen sc1
buffer_load_dwordx4 a[232:235], v205, s[20:23], s52 offen sc1
s_add_u32 s52, s54, 0x70000
buffer_load_dwordx4 a[208:211], v195, s[20:23], s52 offen sc1
buffer_load_dwordx4 a[236:239], v205, s[20:23], s52 offen sc1
s_add_u32 s52, s54, 0x8c000
buffer_load_dwordx4 a[212:215], v195, s[20:23], s52 offen sc1
)A"
R"A(buffer_load_dwordx4 a[240:243], v205, s[20:23], s52 offen sc1
s_add_u32 s52, s54, 0xa8000
buffer_load_dwordx4 a[216:219], v195, s[20:23], s52 offen sc1
buffer_load_dwordx4 a[244:247], v205, s[20:23], s52 offen sc1
s_branch .LBG3
.LRA4:
v_mov_b32 v241, v255
s_nop 1
v_permlane16_swap_b32 v255, v241
v_max_f32 v255, v255, v241
v_mov_b32 v241, v255
s_nop 1
v_permlane32_swap_b32 v255, v241
v_max_f32 v255, v255, v241
v_max_f32 v255, 0, v255
v_sub_f32 v132, v132, v255
v_sub_f32 v133, v133, v255
v_sub_f32 v134, v134, v255
v_sub_f32 v135, v135, v255
v_sub_f32 v140, v140, v255
v_sub_f32 v141, v141, v255
v_sub_f32 v142, v142, v255
v_sub_f32 v143, v143, v255
v_sub_f32 v148, v148, v255
v_sub_f32 v149, v149, v255
v_sub_f32 v150, v150, v255
v_sub_f32 v151, v151, v255
v_sub_f32 v156, v156, v255
v_sub_f32 v157, v157, v255
v_sub_f32 v158, v158, v255
v_sub_f32 v159, v159, v255
v_sub_f32 v248, v248, v255
v_sub_f32 v249, v249, v255
v_sub_f32 v250, v250, v255
v_sub_f32 v251, v251, v255
v_exp_f32 v255, -v255
s_nop 0
v_mul_f32 v243, v243, v255
s_mov_b32 s69, 1
s_branch .LRAR4
.LRB4:
s_nop 7
v_mul_f32 v4, v4, v255
v_mul_f32 v5, v5, v255
v_mul_f32 v6, v6, v255
v_mul_f32 v7, v7, v255
v_mul_f32 v12, v12, v255
v_mul_f32 v13, v13, v255
v_mul_f32 v14, v14, v255
v_mul_f32 v15, v15, v255
v_mul_f32 v20, v20, v255
v_mul_f32 v21, v21, v255
v_mul_f32 v22, v22, v255
v_mul_f32 v23, v23, v255
v_mul_f32 v28, v28, v255
v_mul_f32 v29, v29, v255
v_mul_f32 v30, v30, v255
v_mul_f32 v31, v31, v255
v_mul_f32 v36, v36, v255
v_mul_f32 v37, v37, v255
v_mul_f32 v38, v38, v255
v_mul_f32 v39, v39, v255
v_mul_f32 v44, v44, v255
v_mul_f32 v45, v45, v255
v_mul_f32 v46, v46, v255
v_mul_f32 v47, v47, v255
v_mul_f32 v52, v52, v255
v_mul_f32 v53, v53, v255
v_mul_f32 v54, v54, v255
v_mul_f32 v55, v55, v255
v_mul_f32 v60, v60, v255
v_mul_f32 v61, v61, v255
v_mul_f32 v62, v62, v255
v_mul_f32 v63, v63, v255
v_mul_f32 v68, v68, v255
v_mul_f32 v69, v69, v255
v_mul_f32 v70, v70, v255
v_mul_f32 v71, v71, v255
v_mul_f32 v76, v76, v255
v_mul_f32 v77, v77, v255
v_mul_f32 v78, v78, v255
v_mul_f32 v79, v79, v255
v_mul_f32 v84, v84, v255
v_mul_f32 v85, v85, v255
v_mul_f32 v86, v86, v255
v_mul_f32 v87, v87, v255
v_mul_f32 v92, v92, v255
v_mul_f32 v93, v93, v255
v_mul_f32 v94, v94, v255
v_mul_f32 v95, v95, v255
v_mul_f32 v100, v100, v255
v_mul_f32 v101, v101, v255
v_mul_f32 v102, v102, v255
v_mul_f32 v103, v103, v255
v_mul_f32 v108, v108, v255
v_mul_f32 v109, v109, v255
v_mul_f32 v110, v110, v255
v_mul_f32 v111, v111, v255
v_mul_f32 v116, v116, v255
v_mul_f32 v117, v117, v255
v_mul_f32 v118, v118, v255
v_mul_f32 v119, v119, v255
v_mul_f32 v124, v124, v255
v_mul_f32 v125, v125, v255
v_mul_f32 v126, v126, v255
v_mul_f32 v127, v127, v255
s_mov_b32 s69, 0
s_nop 0
s_branch .LRBR4
.LRA5:
v_mov_b32 v241, v255
s_nop 1
v_permlane16_swap_b32 v255, v241
v_max_f32 v255, v255, v241
v_mov_b32 v241, v255
s_nop 1
v_permlane32_swap_b32 v255, v241
v_max_f32 v255, v255, v241
v_max_f32 v255, 0, v255
v_sub_f32 v132, v132, v255
v_sub_f32 v133, v133, v255
v_sub_f32 v134, v134, v255
v_sub_f32 v135, v135, v255
v_sub_f32 v140, v140, v255
v_sub_f32 v141, v141, v255
v_sub_f32 v142, v142, v255
v_sub_f32 v143, v143, v255
v_sub_f32 v148, v148, v255
v_sub_f32 v149, v149, v255
v_sub_f32 v150, v150, v255
v_sub_f32 v151, v151, v255
v_sub_f32 v156, v156, v255
v_sub_f32 v157, v157, v255
v_sub_f32 v158, v158, v255
v_sub_f32 v159, v159, v255
v_sub_f32 v248, v248, v255
v_sub_f32 v249, v249, v255
v_sub_f32 v250, v250, v255
v_sub_f32 v251, v251, v255
v_exp_f32 v255, -v255
s_nop 0
v_mul_f32 v243, v243, v255
s_mov_b32 s69, 1
s_branch .LRAR5
.LRB5:
s_nop 7
v_mul_f32 v4, v4, v255
v_mul_f32 v5, v5, v255
v_mul_f32 v6, v6, v255
v_mul_f32 v7, v7, v255
v_mul_f32 v12, v12, v255
v_mul_f32 v13, v13, v255
v_mul_f32 v14, v14, v255
v_mul_f32 v15, v15, v255
v_mul_f32 v20, v20, v255
v_mul_f32 v21, v21, v255
v_mul_f32 v22, v22, v255
v_mul_f32 v23, v23, v255
v_mul_f32 v28, v28, v255
v_mul_f32 v29, v29, v255
v_mul_f32 v30, v30, v255
v_mul_f32 v31, v31, v255
v_mul_f32 v36, v36, v255
v_mul_f32 v37, v37, v255
v_mul_f32 v38, v38, v255
v_mul_f32 v39, v39, v255
v_mul_f32 v44, v44, v255
v_mul_f32 v45, v45, v255
v_mul_f32 v46, v46, v255
v_mul_f32 v47, v47, v255
v_mul_f32 v52, v52, v255
v_mul_f32 v53, v53, v255
v_mul_f32 v54, v54, v255
v_mul_f32 v55, v55, v255
v_mul_f32 v60, v60, v255
v_mul_f32 v61, v61, v255
v_mul_f32 v62, v62, v255
v_mul_f32 v63, v63, v255
v_mul_f32 v68, v68, v255
v_mul_f32 v69, v69, v255
v_mul_f32 v70, v70, v255
v_mul_f32 v71, v71, v255
v_mul_f32 v76, v76, v255
v_mul_f32 v77, v77, v255
v_mul_f32 v78, v78, v255
v_mul_f32 v79, v79, v255
v_mul_f32 v84, v84, v255
v_mul_f32 v85, v85, v255
v_mul_f32 v86, v86, v255
v_mul_f32 v87, v87, v255
v_mul_f32 v92, v92, v255
v_mul_f32 v93, v93, v255
v_mul_f32 v94, v94, v255
v_mul_f32 v95, v95, v255
v_mul_f32 v100, v100, v255
v_mul_f32 v101, v101, v255
v_mul_f32 v102, v102, v255
v_mul_f32 v103, v103, v255
v_mul_f32 v108, v108, v255
v_mul_f32 v109, v109, v255
v_mul_f32 v110, v110, v255
v_mul_f32 v111, v111, v255
v_mul_f32 v116, v116, v255
v_mul_f32 v117, v117, v255
v_mul_f32 v118, v118, v255
v_mul_f32 v119, v119, v255
v_mul_f32 v124, v124, v255
v_mul_f32 v125, v125, v255
v_mul_f32 v126, v126, v255
v_mul_f32 v127, v127, v255
s_mov_b32 s69, 0
s_nop 0
s_branch .LRBR5
.LBYZ:
v_mfma_f32_16x16x32_bf16 v[0:3], a[0:3], v[160:163], 0
s_add_u32 m0, s58, 0
RD 32,240,8192
PK 168,144,145
v_mfma_f32_16x16x32_bf16 v[4:7], a[0:3], v[164:167], 0
v_exp_f32 v152, v152
v_mfma_f32_16x16x32_bf16 v[12:15], a[4:7], v[164:167], 0
RD 36,240,9216
PK 169,146,147
v_mfma_f32_16x16x32_bf16 v[8:11], a[4:7], v[160:163], 0
WL 6
v_exp_f32 v153, v153
v_mfma_f32_16x16x32_bf16 v[16:19], a[8:11], v[160:163], 0
RD 40,240,10240
v_add_f32 v156, v156, v188
v_mfma_f32_16x16x32_bf16 v[20:23], a[8:11], v[164:167], 0
buffer_load_dwordx4 v252, s[12:15], s59 offen sc1 lds
v_mfma_f32_16x16x32_bf16 v[28:31], a[12:15], v[164:167], 0
s_add_u32 s59, s59, s60
RD 44,240,11264
v_add_f32 v157, v157, v189
v_mfma_f32_16x16x32_bf16 v[24:27], a[12:15], v[160:163], 0
WL 6
v_exp_f32 v154, v154
v_mfma_f32_16x16x32_bf16 v[32:35], a[16:19], v[160:163], 0
s_add_u32 m0, s58, 4096
RD 48,240,12288
v_add_f32 v158, v158, v190
v_mfma_f32_16x16x32_bf16 v[36:39], a[16:19], v[164:167], 0
buffer_load_dwordx4 v252, s[12:15], s59 offen sc1 lds
v_mfma_f32_16x16x32_bf16 v[44:47], a[20:23], v[164:167], 0
s_add_u32 s59, s59, s60
RD 52,240,13312
v_add_f32 v159, v159, v191
v_mfma_f32_16x16x32_bf16 v[40:43], a[20:23], v[160:163], 0
WL 6
v_exp_f32 v155, v155
v_mfma_f32_16x16x32_bf16 v[48:51], a[24:27], v[160:163], 0
s_add_u32 m0, s58, 8192
RD 56,240,14336
v_max3_f32 v255, v255, v156, v157
v_mfma_f32_16x16x32_bf16 v[52:55], a[24:27], v[164:167], 0
v_max3_f32 v255, v255, v158, v159
v_cmp_lt_f32 vcc, 0x41800000, v255
s_cbranch_vccnz .LRA5
.LRAR5:
v_mfma_f32_16x16x32_bf16 v[60:63], a[28:31], v[164:167], 0
s_add_u32 s6, s57, 32768
RD 60,240,15360
v_add_f32 v242, v242, v152
v_mfma_f32_16x16x32_bf16 v[56:59], a[28:31], v[160:163], 0
WL 6
v_exp_f32 v132, v132
v_mfma_f32_16x16x32_bf16 v[64:67], a[32:35], v[160:163], 0
s_sub_u32 s57, s57, 131072
RD 0,240,16384
s_min_u32 s57, s6, s57
v_mfma_f32_16x16x32_bf16 v[68:71], a[32:35], v[164:167], 0
buffer_load_dwordx4 v252, s[12:15], s59 offen sc1 lds
v_mfma_f32_16x16x32_bf16 v[76:79], a[36:39], v[164:167], 0
s_add_u32 s59, s59, s60
RD 4,240,17408
v_add_f32 v242, v242, v153
v_mfma_f32_16x16x32_bf16 v[72:75], a[36:39], v[160:163], 0
WL 6
v_exp_f32 v133, v133
v_mfma_f32_16x16x32_bf16 v[80:83], a[40:43], v[160:163], 0
s_add_u32 m0, s58, 12288
RD 8,240,18432
v_add_f32 v242, v242, v154
v_mfma_f32_16x16x32_bf16 v[84:87], a[40:43], v[164:167], 0
buffer_load_dwordx4 v252, s[12:15], s59 offen sc1 lds
v_mfma_f32_16x16x32_bf16 v[92:95], a[44:47], v[164:167], 0
s_add_u32 s59, s59, s60
RD 12,240,19456
v_add_f32 v242, v242, v155
v_mfma_f32_16x16x32_bf16 v[88:91], a[44:47], v[160:163], 0
WL 6
v_exp_f32 v134, v134
v_mfma_f32_16x16x32_bf16 v[96:99], a[48:51], v[160:163], 0
s_add_u32 m0, s58, 16384
RD 16,240,20480
PK 170,152,153
v_mfma_f32_16x16x32_bf16 v[100:103], a[48:51], v[164:167], 0
v_exp_f32 v135, v135
v_mfma_f32_16x16x32_bf16 v[108:111], a[52:55], v[164:167], 0
RD 20,240,21504
PK 171,154,155
v_mfma_f32_16x16x32_bf16 v[104:107], a[52:55], v[160:163], 0
WL 6
v_exp_f32 v140, v140
v_mfma_f32_16x16x32_bf16 v[112:115], a[56:59], v[160:163], 0
RD 24,240,22528
v_add_f32 v243, v243, v132
v_mfma_f32_16x16x32_bf16 v[116:119], a[56:59], v[164:167], 0
buffer_load_dwordx4 v252, s[12:15], s59 offen sc1 lds
v_mfma_f32_16x16x32_bf16 v[124:127], a[60:63], v[164:167], 0
s_add_u32 s59, s59, s60
RD 28,240,23552
v_add_f32 v243, v243, v133
v_mfma_f32_16x16x32_bf16 v[120:123], a[60:63], v[160:163], 0
WL 6
v_exp_f32 v141, v141
MB 0,0,168
s_add_u32 m0, s58, 20480
RD 32,240,24576
v_add_f32 v243, v243, v134
MB 4,0,172
buffer_load_dwordx4 v252, s[12:15], s59 offen sc1 lds
MB 12,4,172
s_add_u32 s59, s59, s60
RD 36,240,25600
v_add_f32 v243, v243, v135
MB 8,4,168
WL 6
v_exp_f32 v142, v142
MB 16,8,168
s_add_u32 m0, s58, 24576
RD 40,240,26624
PK 164,132,133
MB 20,8,172
v_exp_f32 v143, v143
MB 28,12,172
RD 44,240,27648
PK 165,134,135
MB 24,12,168
WL 6
v_exp_f32 v148, v148
MB 32,16,168
RD 48,240,28672
v_add_f32 v243, v243, v140
MB 36,16,172
buffer_load_dwordx4 v252, s[12:15], s59 offen sc1 lds
MB 44,20,172
s_add_u32 s59, s59, s60
RD 52,240,29696
v_add_f32 v243, v243, v141
MB 40,20,168
WL 6
v_exp_f32 v149, v149
MB 48,24,168
s_add_u32 m0, s58, 28672
RD 56,240,30720
v_add_f32 v243, v243, v142
MB 52,24,172
buffer_load_dwordx4 v252, s[12:15], s59 offen sc1 lds
MB 60,28,172
s_add_u32 s59, s59, s67
RD 60,240,31744
v_add_u32 v240, s57, v253
MB 56,28,168
s_waitcnt vmcnt(17) lgkmcnt(6)
s_barrier
v_add_f32 v243, v243, v143
MB 64,32,168
s_add_u32 s7, s58, 32768
RD 0,240,0
PK 166,140,141
MB 68,32,172
v_exp_f32 v150, v150
MB 76,36,172
s_sub_u32 s58, s58, 131072
RD 4,240,1024
PK 167,142,143
MB 72,36,168
WL 6
v_exp_f32 v151, v151
MB 80,40,168
s_min_u32 s58, s7, s58
RD 8,240,2048
v_add_f32 v243, v243, v148
MB 84,40,172
v_exp_f32 v156, v156
MB 92,44,172
RD 12,240,3072
v_add_f32 v243, v243, v149
MB 88,44,168
WL 6
v_exp_f32 v157, v157
MB 96,48,168
RD 16,240,4096
v_add_f32 v243, v243, v150
MB 100,48,172
v_exp_f32 v158, v158
MB 108,52,172
RD 20,240,5120
v_add_f32 v243, v243, v151
MB 104,52,168
WL 6
v_exp_f32 v159, v159
MB 112,56,168
RD 24,240,6144
v_add_f32 v243, v243, v156
MB 116,56,172
v_add_f32 v243, v243, v157
v_add_f32 v243, v243, v158
MB 124,60,172
RD 28,240,7168
v_add_f32 v243, v243, v159
MB 120,60,168
WL 6
s_cmp_lg_u32 s69, 0
s_cbranch_scc1 .LRB5
.LRBR5:
s_branch .LBYJ
.LMASK1:
s_nop 3
v_add_f32 v128, v128, v176
v_add_f32 v129, v129, v177
v_add_f32 v130, v130, v178
v_add_f32 v131, v131, v179
v_add_f32 v136, v136, v180
v_add_f32 v137, v137, v181
v_add_f32 v138, v138, v182
v_add_f32 v139, v139, v183
v_add_f32 v144, v144, v184
v_add_f32 v145, v145, v185
v_add_f32 v146, v146, v186
v_add_f32 v147, v147, v187
v_add_f32 v152, v152, v188
v_add_f32 v153, v153, v189
v_add_f32 v154, v154, v190
v_add_f32 v155, v155, v191
s_branch .LNOMASK1
.LBN2:
v_add_f32 v144, v144, v244
s_add_u32 s51, s73, 8192
v_add_f32 v145, v145, v244
v_add_f32 v146, v146, v244
v_add_f32 v147, v147, v244
v_add_f32 v148, v148, v248
v_add_f32 v149, v149, v248
v_add_f32 v150, v150, v248
v_add_f32 v151, v151, v248
v_add_f32 v152, v152, v244
v_add_f32 v153, v153, v244
buffer_store_dwordx4 v[84:87], v253, s[20:23], s51 offen offset:3072 sc1
v_add_f32 v154, v154, v244
v_add_f32 v155, v155, v244
v_add_f32 v156, v156, v248
v_add_f32 v157, v157, v248
v_add_f32 v158, v158, v248
v_add_f32 v159, v159, v248
v_exp_f32 v144, v144
v_exp_f32 v145, v145
v_exp_f32 v146, v146
s_add_u32 s51, s73, 12288
v_exp_f32 v147, v147
v_add_f32 v242, v242, v144
v_add_f32 v242, v242, v145
v_add_f32 v242, v242, v146
v_add_f32 v242, v242, v147
s_add_u32 s54, s54, 56
s_lshr_b32 s64, s54, 3
s_lshl_b32 s64, s64, 6
buffer_store_dwordx4 v[100:103], v253, s[20:23], s51 offen offset:1024 sc1
s_bfe_u32 s85, s54, 0x20001
s_lshr_b32 s80, s54, 4
s_lshl_b32 s80, s80, 2
)A"
R"A(s_add_u32 s80, s80, s85
s_lshl_b32 s80, s80, 1
s_bfe_u32 s50, s54, 0x10003
s_add_u32 s80, s80, s50
s_lshl_b32 s80, s80, 1
s_and_b32 s50, s54, 1
s_add_u32 s51, s73, 12288
s_add_u32 s80, s80, s50
s_lshl_b32 s80, s80, 10
s_lshl_b32 s50, s50, 2
s_add_u32 s85, s85, s50
s_lshl_b32 s50, s77, 3
s_add_u32 s85, s85, s50
s_mul_i32 s85, s85, 14336
s_add_u32 s85, s85, s64
s_mul_i32 s54, s77, 0xe0000
buffer_store_dwordx4 v[116:119], v253, s[20:23], s51 offen offset:3072 sc1
s_add_u32 s54, s54, 0x6000000
s_add_u32 s50, s79, 2
s_lshr_b32 s50, s50, 4
s_min_u32 s50, s50, 1
s_lshl_b32 s50, s50, 30
s_add_u32 s80, s80, s50
s_add_u32 s50, s82, s50
s_waitcnt vmcnt(0)
s_barrier
buffer_store_dwordx2 v[192:193], v201, s[20:23], 0 offen sc1
s_branch .LBLAST
.LRA6:
v_mov_b32 v241, v254
s_nop 1
v_permlane16_swap_b32 v254, v241
v_max_f32 v254, v254, v241
v_mov_b32 v241, v254
s_nop 1
v_permlane32_swap_b32 v254, v241
v_max_f32 v254, v254, v241
v_max_f32 v254, 0, v254
v_sub_f32 v128, v128, v254
v_sub_f32 v129, v129, v254
v_sub_f32 v130, v130, v254
v_sub_f32 v131, v131, v254
v_sub_f32 v136, v136, v254
v_sub_f32 v137, v137, v254
v_sub_f32 v138, v138, v254
v_sub_f32 v139, v139, v254
v_sub_f32 v144, v144, v254
v_sub_f32 v145, v145, v254
v_sub_f32 v146, v146, v254
v_sub_f32 v147, v147, v254
v_sub_f32 v152, v152, v254
v_sub_f32 v153, v153, v254
v_sub_f32 v154, v154, v254
v_sub_f32 v155, v155, v254
v_sub_f32 v244, v244, v254
v_sub_f32 v245, v245, v254
v_sub_f32 v246, v246, v254
v_sub_f32 v247, v247, v254
v_exp_f32 v254, -v254
s_nop 0
v_mul_f32 v242, v242, v254
v_mov_b32 v241, v255
s_nop 1
v_permlane16_swap_b32 v255, v241
v_max_f32 v255, v255, v241
v_mov_b32 v241, v255
s_nop 1
v_permlane32_swap_b32 v255, v241
v_max_f32 v255, v255, v241
v_max_f32 v255, 0, v255
v_sub_f32 v132, v132, v255
v_sub_f32 v133, v133, v255
v_sub_f32 v134, v134, v255
v_sub_f32 v135, v135, v255
v_sub_f32 v140, v140, v255
v_sub_f32 v141, v141, v255
v_sub_f32 v142, v142, v255
v_sub_f32 v143, v143, v255
v_sub_f32 v148, v148, v255
v_sub_f32 v149, v149, v255
v_sub_f32 v150, v150, v255
v_sub_f32 v151, v151, v255
v_sub_f32 v156, v156, v255
v_sub_f32 v157, v157, v255
v_sub_f32 v158, v158, v255
v_sub_f32 v159, v159, v255
v_sub_f32 v248, v248, v255
v_sub_f32 v249, v249, v255
v_sub_f32 v250, v250, v255
v_sub_f32 v251, v251, v255
v_exp_f32 v255, -v255
s_nop 0
v_mul_f32 v243, v243, v255
s_mov_b32 s69, 1
s_branch .LRAR6
.LRB6:
s_nop 7
v_mul_f32 v0, v0, v254
v_mul_f32 v1, v1, v254
v_mul_f32 v2, v2, v254
v_mul_f32 v3, v3, v254
v_mul_f32 v8, v8, v254
v_mul_f32 v9, v9, v254
v_mul_f32 v10, v10, v254
v_mul_f32 v11, v11, v254
v_mul_f32 v16, v16, v254
v_mul_f32 v17, v17, v254
v_mul_f32 v18, v18, v254
v_mul_f32 v19, v19, v254
v_mul_f32 v24, v24, v254
v_mul_f32 v25, v25, v254
v_mul_f32 v26, v26, v254
v_mul_f32 v27, v27, v254
v_mul_f32 v32, v32, v254
v_mul_f32 v33, v33, v254
v_mul_f32 v34, v34, v254
v_mul_f32 v35, v35, v254
v_mul_f32 v40, v40, v254
v_mul_f32 v41, v41, v254
v_mul_f32 v42, v42, v254
v_mul_f32 v43, v43, v254
v_mul_f32 v48, v48, v254
v_mul_f32 v49, v49, v254
v_mul_f32 v50, v50, v254
v_mul_f32 v51, v51, v254
v_mul_f32 v56, v56, v254
v_mul_f32 v57, v57, v254
v_mul_f32 v58, v58, v254
v_mul_f32 v59, v59, v254
v_mul_f32 v64, v64, v254
v_mul_f32 v65, v65, v254
v_mul_f32 v66, v66, v254
v_mul_f32 v67, v67, v254
v_mul_f32 v72, v72, v254
v_mul_f32 v73, v73, v254
v_mul_f32 v74, v74, v254
v_mul_f32 v75, v75, v254
v_mul_f32 v80, v80, v254
v_mul_f32 v81, v81, v254
v_mul_f32 v82, v82, v254
v_mul_f32 v83, v83, v254
v_mul_f32 v88, v88, v254
v_mul_f32 v89, v89, v254
v_mul_f32 v90, v90, v254
v_mul_f32 v91, v91, v254
v_mul_f32 v96, v96, v254
v_mul_f32 v97, v97, v254
v_mul_f32 v98, v98, v254
v_mul_f32 v99, v99, v254
v_mul_f32 v104, v104, v254
v_mul_f32 v105, v105, v254
v_mul_f32 v106, v106, v254
v_mul_f32 v107, v107, v254
v_mul_f32 v112, v112, v254
v_mul_f32 v113, v113, v254
v_mul_f32 v114, v114, v254
v_mul_f32 v115, v115, v254
v_mul_f32 v120, v120, v254
v_mul_f32 v121, v121, v254
v_mul_f32 v122, v122, v254
v_mul_f32 v123, v123, v254
v_mul_f32 v4, v4, v255
v_mul_f32 v5, v5, v255
v_mul_f32 v6, v6, v255
v_mul_f32 v7, v7, v255
v_mul_f32 v12, v12, v255
v_mul_f32 v13, v13, v255
v_mul_f32 v14, v14, v255
v_mul_f32 v15, v15, v255
v_mul_f32 v20, v20, v255
v_mul_f32 v21, v21, v255
v_mul_f32 v22, v22, v255
v_mul_f32 v23, v23, v255
v_mul_f32 v28, v28, v255
v_mul_f32 v29, v29, v255
v_mul_f32 v30, v30, v255
v_mul_f32 v31, v31, v255
v_mul_f32 v36, v36, v255
v_mul_f32 v37, v37, v255
v_mul_f32 v38, v38, v255
v_mul_f32 v39, v39, v255
v_mul_f32 v44, v44, v255
v_mul_f32 v45, v45, v255
v_mul_f32 v46, v46, v255
v_mul_f32 v47, v47, v255
v_mul_f32 v52, v52, v255
v_mul_f32 v53, v53, v255
v_mul_f32 v54, v54, v255
v_mul_f32 v55, v55, v255
v_mul_f32 v60, v60, v255
v_mul_f32 v61, v61, v255
v_mul_f32 v62, v62, v255
v_mul_f32 v63, v63, v255
v_mul_f32 v68, v68, v255
v_mul_f32 v69, v69, v255
v_mul_f32 v70, v70, v255
v_mul_f32 v71, v71, v255
v_mul_f32 v76, v76, v255
v_mul_f32 v77, v77, v255
v_mul_f32 v78, v78, v255
v_mul_f32 v79, v79, v255
v_mul_f32 v84, v84, v255
v_mul_f32 v85, v85, v255
v_mul_f32 v86, v86, v255
v_mul_f32 v87, v87, v255
v_mul_f32 v92, v92, v255
v_mul_f32 v93, v93, v255
v_mul_f32 v94, v94, v255
v_mul_f32 v95, v95, v255
v_mul_f32 v100, v100, v255
v_mul_f32 v101, v101, v255
v_mul_f32 v102, v102, v255
v_mul_f32 v103, v103, v255
v_mul_f32 v108, v108, v255
v_mul_f32 v109, v109, v255
v_mul_f32 v110, v110, v255
v_mul_f32 v111, v111, v255
v_mul_f32 v116, v116, v255
v_mul_f32 v117, v117, v255
v_mul_f32 v118, v118, v255
v_mul_f32 v119, v119, v255
v_mul_f32 v124, v124, v255
v_mul_f32 v125, v125, v255
v_mul_f32 v126, v126, v255
v_mul_f32 v127, v127, v255
s_mov_b32 s69, 0
s_nop 0
s_branch .LRBR6
; role A
.LROLE_A:
s_waitcnt vmcnt(9)
s_barrier
RD 96,248,0
RD 100,249,0
TR 0,240,0
TR 2,240,8192
TR 4,240,16384
TR 6,240,24576
TR 8,241,0
TR 10,241,8192
TR 12,241,16384
TR 14,241,24576
TR 16,242,0
TR 18,242,8192
TR 20,242,16384
TR 22,242,24576
TR 24,243,0
TR 26,243,8192
TR 28,243,16384
TR 30,243,24576
TR 64,244,0
TR 66,244,8192
TR 68,244,16384
TR 70,244,24576
WL 12
MZ 128,96,0
TR 72,245,0
TR 74,245,8192
MA 128,100,4
TR 76,245,16384
TR 78,245,24576
MZ 132,100,12
TR 80,246,0
TR 82,246,8192
MA 132,96,8
WL 10
TR 84,246,16384
TR 86,246,24576
MZ 136,96,16
TR 88,247,0
TR 90,247,8192
MA 136,100,20
TR 92,247,16384
TR 94,247,24576
MZ 140,100,28
RD 104,248,2048
RD 108,249,2048
MA 140,96,24
WL 10
v_sub_f32 v224, v224, v96
v_sub_f32 v225, v225, v96
MZ 144,96,64
s_add_u32 m0, s56, 32768
s_add_u32 s50, s62, 0xffffff80
PK 225,224,225
MA 144,100,68
buffer_load_dwordx4 v250, s[12:15], s50 offen lds
MZ 148,100,76
s_add_u32 m0, s56, 36864
RD 112,248,4096
RD 116,249,4096
MA 148,96,72
WL 4
v_lshlrev_b32 v224, 16, v225
v_and_b32 v225, 0xffff0000, v225
MZ 152,96,80
s_add_u32 s50, s62, 0xff80
v_mul_f32 v106, v224, v98
v_mul_f32 v107, v225, v98
MA 152,100,84
buffer_load_dwordx4 v250, s[12:15], s50 offen lds
MZ 156,100,92
s_add_u32 m0, s56, 40960
PK 112,106,107
v_mul_f32 v106, v224, v99
MA 156,96,88
WL 2
v_mul_f32 v107, v225, v99
PK 120,106,107
MZ 0,104,0
s_add_u32 s50, s62, 0x1ff80
v_sub_f32 v226, v226, v96
v_sub_f32 v227, v227, v96
MA 0,108,4
buffer_load_dwordx4 v250, s[12:15], s50 offen lds
MZ 4,108,12
PK 227,226,227
v_lshlrev_b32 v226, 16, v227
MA 4,104,8
WL 0
s_barrier
v_and_b32 v227, 0xffff0000, v227
MZ 8,104,16
s_add_u32 m0, s56, 45056
RD 120,248,6144
RD 124,249,6144
MA 8,108,20
v_mul_f32 v106, v226, v98
v_mul_f32 v107, v227, v98
MZ 12,108,28
s_add_u32 s50, s62, 0x2ff80
RD 96,248,8192
RD 100,249,8192
MA 12,104,24
buffer_load_dwordx4 v250, s[12:15], s50 offen lds
MZ 16,104,64
s_add_u32 m0, s56, 49152
PK 113,106,107
v_mul_f32 v106, v226, v99
MA 16,108,68
s_add_u32 s50, s62, 0x3ff80
v_mul_f32 v107, v227, v99
PK 121,106,107
MZ 20,108,76
v_sub_f32 v228, v228, v96
v_sub_f32 v229, v229, v96
MA 20,104,72
buffer_load_dwordx4 v250, s[12:15], s50 offen lds
MZ 24,104,80
s_add_u32 m0, s56, 53248
PK 229,228,229
v_lshlrev_b32 v228, 16, v229
MA 24,108,84
s_add_u32 s50, s62, 0x4ff80
v_and_b32 v229, 0xffff0000, v229
v_mul_f32 v106, v228, v98
MZ 28,108,92
s_add_u32 s6, s57, 32768
v_mul_f32 v107, v229, v98
PK 116,106,107
MA 28,104,88
v_mul_f32 v106, v228, v99
v_mul_f32 v107, v229, v99
MZ 188,112,88
s_sub_u32 s61, s57, 65536
PK 124,106,107
v_sub_f32 v230, v230, v96
MA 188,116,92
s_min_u32 s6, s6, s61
v_sub_f32 v231, v231, v96
PK 231,230,231
MZ 184,116,84
buffer_load_dwordx4 v250, s[12:15], s50 offen lds
MA 184,112,80
s_add_u32 m0, s56, 57344
s_add_u32 s50, s62, 0x5ff80
v_lshlrev_b32 v230, 16, v231
MZ 180,112,72
buffer_load_dwordx4 v250, s[12:15], s50 offen lds
MA 180,116,76
s_add_u32 m0, s56, 61440
v_and_b32 v231, 0xffff0000, v231
v_mul_f32 v106, v230, v98
MZ 176,116,68
s_sub_u32 s61, s6, s57
RD 104,248,10240
RD 108,249,10240
MA 176,112,64
s_mov_b32 s57, s6
v_mul_f32 v107, v231, v98
PK 117,106,107
MZ 172,112,24
s_add_u32 s50, s62, 0x6ff80
v_mul_f32 v106, v230, v99
v_mul_f32 v107, v231, v99
MA 172,116,28
buffer_load_dwordx4 v250, s[12:15], s50 offen lds
MZ 168,116,20
s_add_u32 m0, s56, 98304
PK 125,106,107
v_sub_f32 v232, v232, v96
MA 168,112,16
s_add_u32 s50, s63, 0x0
v_sub_f32 v233, v233, v96
PK 233,232,233
MZ 164,112,8
v_lshlrev_b32 v232, 16, v233
v_and_b32 v233, 0xffff0000, v233
MA 164,116,12
v_mul_f32 v106, v232, v98
v_mul_f32 v107, v233, v98
MZ 160,116,4
PK 114,106,107
v_mul_f32 v106, v232, v99
MA 160,112,0
WL 4
v_mul_f32 v107, v233, v99
PK 122,106,107
MZ 32,120,0
v_sub_f32 v234, v234, v96
v_sub_f32 v235, v235, v96
MA 32,124,4
buffer_load_dwordx4 v251, s[16:19], s50 offen nt lds
MZ 36,124,12
PK 235,234,235
v_lshlrev_b32 v234, 16, v235
MA 36,120,8
v_and_b32 v235, 0xffff0000, v235
v_mul_f32 v106, v234, v98
MZ 40,120,16
v_mul_f32 v107, v235, v98
PK 115,106,107
MA 40,124,20
v_mul_f32 v106, v234, v99
v_mul_f32 v107, v235, v99
MZ 44,124,28
s_add_u32 m0, s56, 102400
RD 112,248,12288
RD 116,249,12288
s_add_u32 s50, s63, 0x20000
MA 44,120,24
buffer_load_dwordx4 v251, s[16:19], s50 offen nt lds
MZ 48,120,64
s_add_u32 m0, s56, 106496
PK 123,106,107
v_sub_f32 v236, v236, v96
MA 48,124,68
s_add_u32 s50, s63, 0x8000
v_sub_f32 v237, v237, v96
PK 237,236,237
MZ 52,124,76
v_lshlrev_b32 v236, 16, v237
v_and_b32 v237, 0xffff0000, v237
MA 52,120,72
v_mul_f32 v106, v236, v98
v_mul_f32 v107, v237, v98
MZ 56,120,80
PK 118,106,107
v_mul_f32 v106, v236, v99
MA 56,124,84
buffer_load_dwordx4 v251, s[16:19], s50 offen nt lds
MZ 60,124,92
s_add_u32 m0, s56, 110592
v_mul_f32 v107, v237, v99
PK 126,106,107
MA 60,120,88
WL 4
v_sub_f32 v238, v238, v96
v_sub_f32 v239, v239, v96
MZ 220,96,88
s_add_u32 s50, s63, 0x28000
PK 239,238,239
v_lshlrev_b32 v238, 16, v239
MA 220,100,92
v_and_b32 v239, 0xffff0000, v239
v_mul_f32 v106, v238, v98
MZ 216,100,84
v_mul_f32 v107, v239, v98
PK 119,106,107
MA 216,96,80
buffer_load_dwordx4 v251, s[16:19], s50 offen nt lds
MZ 212,96,72
v_mul_f32 v106, v238, v99
v_mul_f32 v107, v239, v99
MA 212,100,76
PK 127,106,107
MZ 208,100,68
s_add_u32 m0, s56, 114688
RD 120,248,14336
RD 124,249,14336
MA 208,96,64
ds_write_b128 v111, v[112:115] offset:0
MZ 204,96,24
ds_write_b128 v111, v[116:119] offset:128
MA 204,100,28
s_add_u32 s50, s63, 0x40000
v_add_u32 v248, s61, v248
v_add_u32 v249, s61, v249
MZ 200,100,20
ds_write_b128 v111, v[120:123] offset:8192
MA 200,96,16
buffer_load_dwordx4 v251, s[16:19], s50 offen nt lds
MZ 196,96,8
s_add_u32 m0, s56, 118784
MA 196,100,12
s_add_u32 s50, s63, 0x60000
MZ 192,100,4
ds_write_b128 v111, v[124:127] offset:8320
MA 192,96,0
WL 8
MZ 64,104,0
MA 64,108,4
buffer_load_dwordx4 v251, s[16:19], s50 offen nt lds
MZ 68,108,12
s_add_u32 m0, s56, 122880
MA 68,104,8
s_add_u32 s50, s63, 0x48000
MZ 72,104,16
MA 72,108,20
MZ 76,108,28
MA 76,104,24
buffer_load_dwordx4 v251, s[16:19], s50 offen nt lds
MZ 80,104,64
MA 80,108,68
MZ 84,108,76
MA 84,104,72
s_waitcnt vmcnt(7) lgkmcnt(0)
s_barrier
MZ 88,104,80
s_add_u32 m0, s56, 126976
RD 96,248,0
RD 100,249,0
MA 88,108,84
RD 144,255,0
RD 148,255,4096
MZ 92,108,92
s_add_u32 s50, s63, 0x68000
TR 32,240,32768
TR 34,240,40960
MA 92,104,88
RD 152,255,8192
RD 156,255,12288
v_mfma_f32_16x16x32_bf16 a[252:255], a[112:115], a[88:91], 0
)A"
R"A(TR 36,240,49152
TR 38,240,57344
v_mfma_f32_16x16x32_bf16 a[252:255], a[116:119], a[92:95], a[252:255]
buffer_load_dwordx4 v251, s[16:19], s50 offen nt lds
v_mfma_f32_16x16x32_bf16 a[248:251], a[116:119], a[84:87], 0
s_add_u32 m0, s64, 0
TR 40,241,32768
TR 42,241,40960
v_mfma_f32_16x16x32_bf16 a[248:251], a[112:115], a[80:83], a[248:251]
v_mfma_f32_16x16x32_bf16 a[244:247], a[112:115], a[72:75], 0
s_add_u32 s50, s62, 0x0
TR 44,241,49152
TR 46,241,57344
v_mfma_f32_16x16x32_bf16 a[244:247], a[116:119], a[76:79], a[244:247]
buffer_load_dwordx4 v250, s[12:15], s50 offen lds
v_mfma_f32_16x16x32_bf16 a[240:243], a[116:119], a[68:71], 0
s_add_u32 m0, s64, 4096
TR 48,242,32768
TR 50,242,40960
v_mfma_f32_16x16x32_bf16 a[240:243], a[112:115], a[64:67], a[240:243]
MZ 236,112,24
s_add_u32 s50, s62, 0x10000
TR 52,242,49152
TR 54,242,57344
MA 236,116,28
buffer_load_dwordx4 v250, s[12:15], s50 offen lds
MZ 232,116,20
s_add_u32 m0, s64, 8192
TR 56,243,32768
TR 58,243,40960
MA 232,112,16
MZ 228,112,8
s_add_u32 s50, s62, 0x20000
TR 60,243,49152
TR 62,243,57344
MA 228,116,12
MZ 224,116,4
s_lshl_b32 s59, s11, 17
RD 104,248,2048
RD 108,249,2048
MA 224,112,0
buffer_load_dwordx4 v250, s[12:15], s50 offen lds
MZ 112,120,64
s_add_u32 s59, s59, 1048576
MA 112,124,68
s_and_b32 s60, s10, 7
MZ 116,124,76
s_add_u32 m0, s64, 12288
TR 64,244,32768
TR 66,244,40960
MA 116,120,72
MZ 120,120,80
s_add_u32 s50, s62, 0x30000
TR 68,244,49152
TR 70,244,57344
MA 120,124,84
MZ 124,124,92
s_lshl_b32 s60, s60, 16
TR 72,245,32768
TR 74,245,40960
MA 124,120,88
buffer_load_dwordx4 v250, s[12:15], s50 offen lds
MZ 96,120,0
s_add_u32 s60, s60, 384
TR 76,245,49152
TR 78,245,57344
MA 96,124,4
MZ 100,124,12
TR 80,246,32768
TR 82,246,40960
MA 100,120,8
MZ 104,120,16
TR 84,246,49152
TR 86,246,57344
MA 104,124,20
TR 88,247,32768
TR 90,247,40960
MZ 108,124,28
TR 92,247,49152
TR 94,247,57344
MA 108,120,24
WL 15
MA 128,96,32
s_add_u32 s51, s62, s60
MA 128,100,36
s_min_u32 s51, s51, 0x3f0780
MA 132,100,44
buffer_load_dword a234, v250, s[12:15], s51 offen
MA 132,96,40
MA 136,96,48
MA 136,100,52
MA 140,100,60
MA 140,96,56
WL 0
s_barrier
MA 144,96,64
s_add_u32 m0, s64, 16384
RD 112,248,4096
RD 116,249,4096
MA 144,100,68
MA 148,100,76
s_add_u32 s50, s62, 0x40000
RD 120,248,6144
RD 124,249,6144
MA 148,96,72
buffer_load_dwordx4 v250, s[12:15], s50 offen lds
MA 152,96,80
s_add_u32 m0, s64, 20480
MA 152,100,84
s_add_u32 s50, s62, 0x50000
MA 156,100,92
buffer_load_dword a239, v252, s[68:71], 0 offen
MA 156,96,88
buffer_load_dwordx4 v250, s[12:15], s50 offen lds
MA 0,104,32
s_add_u32 m0, s64, 24576
v_add_u32 v252, s66, v252
MA 0,108,36
s_add_u32 s50, s62, 0x60000
MA 4,108,44
MA 4,104,40
buffer_load_dwordx4 v250, s[12:15], s50 offen lds
MA 8,104,48
s_mov_b32 s65, 5
MA 8,108,52
MA 12,108,60
s_add_u32 m0, s64, 28672
RD 96,248,8192
RD 100,249,8192
MA 12,104,56
s_add_u32 s50, s62, 0x70000
MA 16,104,64
buffer_load_dwordx4 v250, s[12:15], s50 offen lds
MA 16,108,68
s_add_u32 m0, s56, 131072
MA 20,108,76
s_add_u32 s50, s63, 0x80000
MA 20,104,72
MA 24,104,80
buffer_load_dwordx4 v251, s[16:19], s50 offen nt lds
MA 24,108,84
s_add_u32 m0, s56, 135168
s_add_u32 s50, s63, 0xa0000
MA 28,108,92
s_add_u32 s6, s57, 32768
MA 28,104,88
WL 4
MA 188,112,88
s_sub_u32 s61, s57, 65536
MA 188,116,92
s_min_u32 s6, s6, s61
MA 184,116,84
buffer_load_dwordx4 v251, s[16:19], s50 offen nt lds
MA 184,112,80
s_add_u32 m0, s56, 139264
MA 180,112,72
s_add_u32 s50, s63, 0x88000
MA 180,116,76
buffer_load_dwordx4 v251, s[16:19], s50 offen nt lds
MA 176,116,68
s_sub_u32 s61, s6, s57
RD 104,248,10240
RD 108,249,10240
MA 176,112,64
s_mov_b32 s57, s6
MA 172,112,56
s_add_u32 m0, s56, 143360
s_add_u32 s50, s63, 0xa8000
MA 172,116,60
buffer_load_dwordx4 v251, s[16:19], s50 offen nt lds
MA 168,116,52
s_add_u32 m0, s56, 147456
MA 168,112,48
s_add_u32 s50, s63, 0xc0000
MA 164,112,40
MA 164,116,44
buffer_load_dwordx4 v251, s[16:19], s50 offen nt lds
MA 160,116,36
s_add_u32 m0, s56, 151552
MA 160,112,32
WL 4
MA 32,120,32
s_add_u32 s50, s63, 0xe0000
MA 32,124,36
buffer_load_dwordx4 v251, s[16:19], s50 offen nt lds
MA 36,124,44
s_add_u32 m0, s56, 155648
MA 36,120,40
s_add_u32 s50, s63, 0xc8000
MA 40,120,48
MA 40,124,52
buffer_load_dwordx4 v251, s[16:19], s50 offen nt lds
MA 44,124,60
s_add_u32 m0, s56, 159744
RD 112,248,12288
RD 116,249,12288
MA 44,120,56
s_add_u32 s50, s63, 0xe8000
MA 48,120,64
MA 48,124,68
buffer_load_dwordx4 v251, s[16:19], s50 offen nt lds
MA 52,124,76
s_add_u32 m0, s56, 0
MA 52,120,72
s_add_u32 s50, s62, 0x80
MA 56,120,80
MA 56,124,84
buffer_load_dwordx4 v250, s[12:15], s50 offen lds
MA 60,124,92
s_add_u32 m0, s56, 4096
MA 60,120,88
WL 4
MA 220,96,88
s_add_u32 s50, s62, 0x10080
MA 220,100,92
buffer_load_dwordx4 v250, s[12:15], s50 offen lds
MA 216,100,84
s_add_u32 m0, s56, 8192
MA 216,96,80
s_add_u32 s50, s62, 0x20080
MA 212,96,72
MA 212,100,76
buffer_load_dwordx4 v250, s[12:15], s50 offen lds
MA 208,100,68
s_add_u32 m0, s56, 12288
RD 120,248,14336
RD 124,249,14336
MA 208,96,64
MA 204,96,56
MA 204,100,60
s_add_u32 s50, s62, 0x30080
v_add_u32 v248, s61, v248
v_add_u32 v249, s61, v249
MA 200,100,52
MA 200,96,48
buffer_load_dwordx4 v250, s[12:15], s50 offen lds
MA 196,96,40
s_add_u32 m0, s56, 16384
MA 196,100,44
s_add_u32 s50, s62, 0x40080
MA 192,100,36
MA 192,96,32
WL 4
buffer_load_dwordx4 v250, s[12:15], s50 offen lds
MA 64,104,32
s_add_u32 m0, s56, 20480
MA 64,108,36
s_add_u32 s50, s62, 0x50080
MA 68,108,44
MA 68,104,40
buffer_load_dwordx4 v250, s[12:15], s50 offen lds
MA 72,104,48
s_add_u32 m0, s56, 24576
MA 72,108,52
s_add_u32 s50, s62, 0x60080
MA 76,108,60
MA 76,104,56
buffer_load_dwordx4 v250, s[12:15], s50 offen lds
MA 80,104,64
MA 80,108,68
MA 84,108,76
MA 84,104,72
s_waitcnt vmcnt(15) lgkmcnt(0)
s_barrier
MA 88,104,80
s_add_u32 m0, s56, 28672
RD 96,248,0
RD 100,249,0
s_add_u32 s50, s62, 0x70080
MA 88,108,84
buffer_load_dwordx4 v250, s[12:15], s50 offen lds
MA 92,108,92
s_add_u32 m0, s56, 32768
TR 0,240,0
TR 2,240,8192
MA 92,104,88
v_mfma_f32_16x16x32_bf16 a[252:255], a[112:115], a[88:91], a[252:255]
s_add_u32 s50, s62, 0x100
TR 4,240,16384
TR 6,240,24576
v_mfma_f32_16x16x32_bf16 a[252:255], a[116:119], a[92:95], a[252:255]
buffer_load_dwordx4 v250, s[12:15], s50 offen lds
v_mfma_f32_16x16x32_bf16 a[248:251], a[116:119], a[84:87], a[248:251]
s_add_u32 m0, s56, 36864
TR 8,241,0
TR 10,241,8192
v_mfma_f32_16x16x32_bf16 a[248:251], a[112:115], a[80:83], a[248:251]
v_mfma_f32_16x16x32_bf16 a[244:247], a[112:115], a[72:75], a[244:247]
s_add_u32 s50, s62, 0x10100
TR 12,241,16384
TR 14,241,24576
v_mfma_f32_16x16x32_bf16 a[244:247], a[116:119], a[76:79], a[244:247]
v_mfma_f32_16x16x32_bf16 a[240:243], a[116:119], a[68:71], a[240:243]
TR 16,242,0
TR 18,242,8192
v_mfma_f32_16x16x32_bf16 a[240:243], a[112:115], a[64:67], a[240:243]
MA 236,112,56
TR 20,242,16384
TR 22,242,24576
MA 236,116,60
buffer_load_dwordx4 v250, s[12:15], s50 offen lds
MA 232,116,52
s_add_u32 m0, s56, 40960
TR 24,243,0
TR 26,243,8192
MA 232,112,48
MA 228,112,40
s_add_u32 s50, s62, 0x20100
TR 28,243,16384
TR 30,243,24576
MA 228,116,44
MA 224,116,36
RD 104,248,2048
RD 108,249,2048
MA 224,112,32
buffer_load_dwordx4 v250, s[12:15], s50 offen lds
MA 112,120,64
MA 112,124,68
MA 116,124,76
s_add_u32 m0, s56, 45056
TR 64,244,0
TR 66,244,8192
MA 116,120,72
MA 120,120,80
s_add_u32 s50, s62, 0x30100
TR 68,244,16384
TR 70,244,24576
MA 120,124,84
MA 124,124,92
TR 72,245,0
TR 74,245,8192
MA 124,120,88
buffer_load_dwordx4 v250, s[12:15], s50 offen lds
MA 96,120,32
s_add_u32 s62, s62, 256
TR 76,245,16384
TR 78,245,24576
MA 96,124,36
MA 100,124,44
s_add_u32 s64, s56, 32768
TR 80,246,0
TR 82,246,8192
MA 100,120,40
MA 104,120,48
s_add_u32 s63, s63, 1048576
TR 84,246,16384
TR 86,246,24576
MA 104,124,52
TR 88,247,0
TR 90,247,8192
MA 108,124,60
TR 92,247,16384
TR 94,247,24576
MA 108,120,56
WL 15
; V projection main loop
.LP1LA:
MA 128,96,0
s_add_u32 m0, s64, 16384
MA 128,100,4
s_add_u32 s50, s62, 0x40000
MA 132,100,12
MA 132,96,8
buffer_load_dwordx4 v250, s[12:15], s50 offen lds
MA 136,96,16
MA 136,100,20
MA 140,100,28
MA 140,96,24
WL 0
s_barrier
MA 144,96,64
s_add_u32 m0, s64, 20480
RD 112,248,4096
RD 116,249,4096
MA 144,100,68
MA 148,100,76
s_add_u32 s50, s62, 0x50000
RD 120,248,6144
RD 124,249,6144
MA 148,96,72
buffer_load_dword a238, v252, s[68:71], 0 offen
MA 152,96,80
v_add_u32 v252, s66, v252
MA 152,100,84
buffer_load_dwordx4 v250, s[12:15], s50 offen lds
MA 156,100,92
s_add_u32 m0, s64, 24576
MA 156,96,88
s_add_u32 s50, s62, 0x60000
MA 0,104,0
s_add_u32 s51, s62, s60
s_min_u32 s51, s51, 0x3f0780
MA 0,108,4
buffer_load_dword a237, v250, s[12:15], s51 offen
MA 4,108,12
MA 4,104,8
buffer_load_dwordx4 v250, s[12:15], s50 offen lds
MA 8,104,16
MA 8,108,20
MA 12,108,28
s_add_u32 m0, s64, 28672
RD 96,248,8192
RD 100,249,8192
MA 12,104,24
s_add_u32 s50, s62, 0x70000
MA 16,104,64
MA 16,108,68
MA 20,108,76
MA 20,104,72
buffer_load_dwordx4 v250, s[12:15], s50 offen lds
MA 24,104,80
s_add_u32 m0, s56, 98304
MA 24,108,84
s_add_u32 s50, s63, 0x0
MA 28,108,92
s_add_u32 s6, s57, 32768
MA 28,104,88
WL 4
MA 188,112,88
s_sub_u32 s61, s57, 65536
MA 188,116,92
s_min_u32 s6, s6, s61
MA 184,116,84
MA 184,112,80
buffer_load_dwordx4 v251, s[16:19], s50 offen nt lds
MA 180,112,72
s_add_u32 m0, s56, 102400
MA 180,116,76
s_add_u32 s50, s63, 0x20000
MA 176,116,68
s_sub_u32 s61, s6, s57
RD 104,248,10240
RD 108,249,10240
MA 176,112,64
s_mov_b32 s57, s6
MA 172,112,24
MA 172,116,28
MA 168,116,20
MA 168,112,16
buffer_load_dwordx4 v251, s[16:19], s50 offen nt lds
MA 164,112,8
s_add_u32 m0, s56, 106496
MA 164,116,12
s_add_u32 s50, s63, 0x8000
MA 160,116,4
MA 160,112,0
WL 4
MA 32,120,0
MA 32,124,4
MA 36,124,12
s_add_u32 s62, s62, 128
MA 36,120,8
buffer_load_dwordx4 v251, s[16:19], s50 offen nt lds
MA 40,120,16
s_add_u32 s7, s64, 32768
MA 40,124,20
s_sub_u32 s64, s64, 65536
MA 44,124,28
s_add_u32 m0, s56, 110592
RD 112,248,12288
RD 116,249,12288
MA 44,120,24
s_add_u32 s50, s63, 0x28000
MA 48,120,64
s_min_u32 s64, s7, s64
MA 48,124,68
MA 52,124,76
MA 52,120,72
buffer_load_dwordx4 v251, s[16:19], s50 offen nt lds
MA 56,120,80
s_add_u32 m0, s56, 114688
MA 56,124,84
s_add_u32 s50, s63, 0x40000
MA 60,124,92
MA 60,120,88
WL 4
MA 220,96,88
MA 220,100,92
MA 216,100,84
MA 216,96,80
buffer_load_dwordx4 v251, s[16:19], s50 offen nt lds
MA 212,96,72
MA 212,100,76
MA 208,100,68
s_add_u32 m0, s56, 118784
RD 120,248,14336
RD 124,249,14336
MA 208,96,64
MA 204,96,24
MA 204,100,28
s_add_u32 s50, s63, 0x60000
v_add_u32 v248, s61, v248
v_add_u32 v249, s61, v249
MA 200,100,20
MA 200,96,16
buffer_load_dwordx4 v251, s[16:19], s50 offen nt lds
MA 196,96,8
s_add_u32 m0, s56, 122880
MA 196,100,12
s_add_u32 s50, s63, 0x48000
MA 192,100,4
MA 192,96,0
WL 4
MA 64,104,0
MA 64,108,4
MA 68,108,12
buffer_load_dwordx4 v251, s[16:19], s50 offen nt lds
MA 68,104,8
s_add_u32 m0, s56, 126976
MA 72,104,16
s_add_u32 s50, s63, 0x68000
MA 72,108,20
MA 76,108,28
MA 76,104,24
buffer_load_dwordx4 v251, s[16:19], s50 offen nt lds
MA 80,104,64
MA 80,108,68
MA 84,108,76
MA 84,104,72
s_waitcnt vmcnt(18) lgkmcnt(0)
s_barrier
MA 88,104,80
s_add_u32 m0, s64, 0
RD 96,248,0
RD 100,249,0
MA 88,108,84
MA 92,108,92
s_add_u32 s50, s62, 0x0
TR 32,240,32768
TR 34,240,40960
MA 92,104,88
v_mfma_f32_16x16x32_bf16 a[252:255], a[112:115], a[88:91], a[252:255]
TR 36,240,49152
TR 38,240,57344
v_mfma_f32_16x16x32_bf16 a[252:255], a[116:119], a[92:95], a[252:255]
buffer_load_dwordx4 v250, s[12:15], s50 offen lds
v_mfma_f32_16x16x32_bf16 a[248:251], a[116:119], a[84:87], a[248:251]
s_add_u32 m0, s64, 4096
TR 40,241,32768
TR 42,241,40960
v_mfma_f32_16x16x32_bf16 a[248:251], a[112:115], a[80:83], a[248:251]
v_mfma_f32_16x16x32_bf16 a[244:247], a[112:115], a[72:75], a[244:247]
s_add_u32 s50, s62, 0x10000
TR 44,241,49152
TR 46,241,57344
v_mfma_f32_16x16x32_bf16 a[244:247], a[116:119], a[76:79], a[244:247]
v_mfma_f32_16x16x32_bf16 a[240:243], a[116:119], a[68:71], a[240:243]
TR 48,242,32768
TR 50,242,40960
v_mfma_f32_16x16x32_bf16 a[240:243], a[112:115], a[64:67], a[240:243]
buffer_load_dwordx4 v250, s[12:15], s50 offen lds
MA 236,112,24
)A"
R"A(s_add_u32 m0, s64, 8192
TR 52,242,49152
TR 54,242,57344
MA 236,116,28
MA 232,116,20
s_add_u32 s50, s62, 0x20000
TR 56,243,32768
TR 58,243,40960
MA 232,112,16
MA 228,112,8
TR 60,243,49152
TR 62,243,57344
MA 228,116,12
buffer_load_dwordx4 v250, s[12:15], s50 offen lds
MA 224,116,4
s_add_u32 m0, s64, 12288
RD 104,248,2048
RD 108,249,2048
MA 224,112,0
MA 112,120,64
MA 112,124,68
MA 116,124,76
s_add_u32 s50, s62, 0x30000
TR 64,244,32768
TR 66,244,40960
MA 116,120,72
MA 120,120,80
TR 68,244,49152
TR 70,244,57344
MA 120,124,84
MA 124,124,92
TR 72,245,32768
TR 74,245,40960
MA 124,120,88
buffer_load_dwordx4 v250, s[12:15], s50 offen lds
MA 96,120,0
TR 76,245,49152
TR 78,245,57344
MA 96,124,4
MA 100,124,12
TR 80,246,32768
TR 82,246,40960
MA 100,120,8
MA 104,120,16
TR 84,246,49152
TR 86,246,57344
MA 104,124,20
TR 88,247,32768
TR 90,247,40960
MA 108,124,28
TR 92,247,49152
TR 94,247,57344
MA 108,120,24
WL 15
MA 128,96,32
s_add_u32 m0, s64, 16384
MA 128,100,36
s_add_u32 s50, s62, 0x40000
MA 132,100,44
MA 132,96,40
buffer_load_dwordx4 v250, s[12:15], s50 offen lds
MA 136,96,48
MA 136,100,52
MA 140,100,60
MA 140,96,56
WL 0
s_barrier
MA 144,96,64
s_add_u32 m0, s64, 20480
RD 112,248,4096
RD 116,249,4096
MA 144,100,68
MA 148,100,76
s_add_u32 s50, s62, 0x50000
RD 120,248,6144
RD 124,249,6144
MA 148,96,72
MA 152,96,80
MA 152,100,84
buffer_load_dwordx4 v250, s[12:15], s50 offen lds
MA 156,100,92
s_add_u32 m0, s64, 24576
MA 156,96,88
s_add_u32 s50, s62, 0x60000
MA 0,104,32
MA 0,108,36
MA 4,108,44
MA 4,104,40
buffer_load_dwordx4 v250, s[12:15], s50 offen lds
MA 8,104,48
s_sub_u32 s65, s65, 1
MA 8,108,52
MA 12,108,60
s_add_u32 m0, s64, 28672
RD 96,248,8192
RD 100,249,8192
MA 12,104,56
s_add_u32 s50, s62, 0x70000
MA 16,104,64
MA 16,108,68
MA 20,108,76
MA 20,104,72
buffer_load_dwordx4 v250, s[12:15], s50 offen lds
MA 24,104,80
s_add_u32 m0, s56, 131072
MA 24,108,84
s_add_u32 s50, s63, 0x80000
MA 28,108,92
s_add_u32 s6, s57, 32768
MA 28,104,88
WL 4
MA 188,112,88
s_sub_u32 s61, s57, 65536
MA 188,116,92
s_min_u32 s6, s6, s61
MA 184,116,84
MA 184,112,80
buffer_load_dwordx4 v251, s[16:19], s50 offen nt lds
MA 180,112,72
s_add_u32 m0, s56, 135168
MA 180,116,76
s_add_u32 s50, s63, 0xa0000
MA 176,116,68
s_sub_u32 s61, s6, s57
RD 104,248,10240
RD 108,249,10240
MA 176,112,64
s_mov_b32 s57, s6
MA 172,112,56
MA 172,116,60
MA 168,116,52
MA 168,112,48
buffer_load_dwordx4 v251, s[16:19], s50 offen nt lds
MA 164,112,40
s_add_u32 m0, s56, 139264
MA 164,116,44
s_add_u32 s50, s63, 0x88000
MA 160,116,36
MA 160,112,32
WL 4
MA 32,120,32
MA 32,124,36
MA 36,124,44
s_add_u32 s62, s62, 128
MA 36,120,40
buffer_load_dwordx4 v251, s[16:19], s50 offen nt lds
MA 40,120,48
s_add_u32 s7, s64, 32768
MA 40,124,52
s_sub_u32 s64, s64, 65536
MA 44,124,60
s_add_u32 m0, s56, 143360
RD 112,248,12288
RD 116,249,12288
MA 44,120,56
s_add_u32 s50, s63, 0xa8000
MA 48,120,64
s_min_u32 s64, s7, s64
MA 48,124,68
MA 52,124,76
MA 52,120,72
buffer_load_dwordx4 v251, s[16:19], s50 offen nt lds
MA 56,120,80
s_add_u32 m0, s56, 147456
MA 56,124,84
s_add_u32 s50, s63, 0xc0000
MA 60,124,92
MA 60,120,88
WL 4
MA 220,96,88
MA 220,100,92
MA 216,100,84
MA 216,96,80
buffer_load_dwordx4 v251, s[16:19], s50 offen nt lds
MA 212,96,72
MA 212,100,76
MA 208,100,68
s_add_u32 m0, s56, 151552
RD 120,248,14336
RD 124,249,14336
MA 208,96,64
MA 204,96,56
MA 204,100,60
s_add_u32 s50, s63, 0xe0000
v_add_u32 v248, s61, v248
v_add_u32 v249, s61, v249
MA 200,100,52
MA 200,96,48
buffer_load_dwordx4 v251, s[16:19], s50 offen nt lds
MA 196,96,40
s_add_u32 m0, s56, 155648
MA 196,100,44
s_add_u32 s50, s63, 0xc8000
MA 192,100,36
MA 192,96,32
WL 4
MA 64,104,32
MA 64,108,36
MA 68,108,44
buffer_load_dwordx4 v251, s[16:19], s50 offen nt lds
MA 68,104,40
s_add_u32 m0, s56, 159744
MA 72,104,48
s_add_u32 s50, s63, 0xe8000
MA 72,108,52
MA 76,108,60
MA 76,104,56
buffer_load_dwordx4 v251, s[16:19], s50 offen nt lds
MA 80,104,64
MA 80,108,68
MA 84,108,76
MA 84,104,72
s_waitcnt vmcnt(16) lgkmcnt(0)
s_barrier
MA 88,104,80
s_add_u32 m0, s64, 0
RD 96,248,0
RD 100,249,0
MA 88,108,84
MA 92,108,92
s_add_u32 s50, s62, 0x0
TR 0,240,0
TR 2,240,8192
MA 92,104,88
v_mfma_f32_16x16x32_bf16 a[252:255], a[112:115], a[88:91], a[252:255]
TR 4,240,16384
TR 6,240,24576
v_mfma_f32_16x16x32_bf16 a[252:255], a[116:119], a[92:95], a[252:255]
buffer_load_dwordx4 v250, s[12:15], s50 offen lds
v_mfma_f32_16x16x32_bf16 a[248:251], a[116:119], a[84:87], a[248:251]
s_add_u32 m0, s64, 4096
TR 8,241,0
TR 10,241,8192
v_mfma_f32_16x16x32_bf16 a[248:251], a[112:115], a[80:83], a[248:251]
v_mfma_f32_16x16x32_bf16 a[244:247], a[112:115], a[72:75], a[244:247]
s_add_u32 s50, s62, 0x10000
TR 12,241,16384
TR 14,241,24576
v_mfma_f32_16x16x32_bf16 a[244:247], a[116:119], a[76:79], a[244:247]
v_mfma_f32_16x16x32_bf16 a[240:243], a[116:119], a[68:71], a[240:243]
TR 16,242,0
TR 18,242,8192
v_mfma_f32_16x16x32_bf16 a[240:243], a[112:115], a[64:67], a[240:243]
buffer_load_dwordx4 v250, s[12:15], s50 offen lds
MA 236,112,56
s_add_u32 m0, s64, 8192
TR 20,242,16384
TR 22,242,24576
MA 236,116,60
MA 232,116,52
s_add_u32 s50, s62, 0x20000
TR 24,243,0
TR 26,243,8192
MA 232,112,48
MA 228,112,40
TR 28,243,16384
TR 30,243,24576
MA 228,116,44
buffer_load_dwordx4 v250, s[12:15], s50 offen lds
MA 224,116,36
s_add_u32 m0, s64, 12288
RD 104,248,2048
RD 108,249,2048
MA 224,112,32
MA 112,120,64
MA 112,124,68
MA 116,124,76
s_add_u32 s50, s62, 0x30000
TR 64,244,0
TR 66,244,8192
MA 116,120,72
MA 120,120,80
s_add_u32 s63, s63, 1048576
TR 68,244,16384
TR 70,244,24576
MA 120,124,84
MA 124,124,92
TR 72,245,0
TR 74,245,8192
MA 124,120,88
buffer_load_dwordx4 v250, s[12:15], s50 offen lds
MA 96,120,32
TR 76,245,16384
TR 78,245,24576
MA 96,124,36
MA 100,124,44
TR 80,246,0
TR 82,246,8192
MA 100,120,40
MA 104,120,48
TR 84,246,16384
TR 86,246,24576
MA 104,124,52
TR 88,247,0
TR 90,247,8192
MA 108,124,60
TR 92,247,16384
TR 94,247,24576
MA 108,120,56
WL 15
s_cmp_eq_u32 s65, 4
s_cbranch_scc1 .LXF4A
s_cmp_eq_u32 s65, 3
s_cbranch_scc1 .LXF3A
s_cmp_eq_u32 s65, 2
s_cbranch_scc1 .LXF2A
s_cmp_eq_u32 s65, 1
s_cbranch_scc1 .LXF1A
s_branch .LXEA
.LXF4A:
buffer_store_dwordx4 a[144:147], v254, s[20:23], s74 offen sc1
buffer_store_dwordx4 a[148:151], v254, s[20:23], s75 offen sc1
s_add_u32 s74, s74, 0x200000
s_add_u32 s75, s75, 0x200000
buffer_store_dwordx4 a[152:155], v254, s[20:23], s74 offen sc1
buffer_store_dwordx4 a[156:159], v254, s[20:23], s75 offen sc1
s_branch .LP1LA
.LXF3A:
v_mov_b32 v254, s48
v_mov_b32 v255, s49
buffer_store_dwordx2 v[254:255], off, s[20:23], s72 sc1
s_branch .LP1LA
.LXF2A:
buffer_load_dwordx2 v[254:255], v253, s[20:23], s67 offen sc1
s_lshl_b32 s74, s81, 3
s_add_u32 s74, s74, s8
s_mul_i32 s74, s74, 14336
s_add_u32 s74, s74, 0x2000000
s_add_u32 s75, s74, 57344
s_branch .LP1LA
.LXF1A:
v_cmp_ne_u64 vcc, s[48:49], v[254:255]
s_cbranch_vccnz .LPS5
.LPR5:
buffer_load_dwordx4 a[128:131], v253, s[20:23], s74 offen offset:0 sc1
buffer_load_dwordx4 a[132:135], v253, s[20:23], s75 offen offset:0 sc1
buffer_load_dwordx4 a[136:139], v253, s[20:23], s74 offen offset:1024 sc1
buffer_load_dwordx4 a[140:143], v253, s[20:23], s75 offen offset:1024 sc1
s_add_u32 s74, s74, 2048
s_add_u32 s75, s75, 2048
s_branch .LP1LA
; last k-tiles, MLP up / activation / down, attention
.LXEA:
MA 128,96,0
s_add_u32 m0, s64, 16384
MA 128,100,4
s_add_u32 s50, s62, 0x40000
MA 132,100,12
MA 132,96,8
buffer_load_dwordx4 v250, s[12:15], s50 offen lds
MA 136,96,16
MA 136,100,20
MA 140,100,28
MA 140,96,24
WL 0
s_barrier
MA 144,96,64
s_add_u32 m0, s64, 20480
RD 112,248,4096
RD 116,249,4096
MA 144,100,68
MA 148,100,76
s_add_u32 s50, s62, 0x50000
RD 120,248,6144
RD 124,249,6144
MA 148,96,72
buffer_load_dword a238, v252, s[68:71], 0 offen
MA 152,96,80
v_add_u32 v252, s66, v252
MA 152,100,84
buffer_load_dwordx4 v250, s[12:15], s50 offen lds
MA 156,100,92
s_add_u32 m0, s64, 24576
MA 156,96,88
s_add_u32 s50, s62, 0x60000
MA 0,104,0
MA 0,108,4
MA 4,108,12
MA 4,104,8
buffer_load_dwordx4 v250, s[12:15], s50 offen lds
MA 8,104,16
MA 8,108,20
MA 12,108,28
s_add_u32 m0, s64, 28672
RD 96,248,8192
RD 100,249,8192
MA 12,104,24
s_add_u32 s50, s62, 0x70000
MA 16,104,64
buffer_load_dwordx4 a[144:147], v253, s[20:23], s74 offen offset:0 sc1
MA 16,108,68
MA 20,108,76
MA 20,104,72
buffer_load_dwordx4 v250, s[12:15], s50 offen lds
MA 24,104,80
s_add_u32 m0, s56, 98304
MA 24,108,84
s_add_u32 s50, s63, 0x0
MA 28,108,92
s_add_u32 s6, s57, 32768
MA 28,104,88
WL 4
MA 188,112,88
s_sub_u32 s61, s57, 65536
MA 188,116,92
s_min_u32 s6, s6, s61
MA 184,116,84
MA 184,112,80
buffer_load_dwordx4 v251, s[16:19], s50 offen nt lds
MA 180,112,72
s_add_u32 m0, s56, 102400
MA 180,116,76
s_add_u32 s50, s63, 0x20000
MA 176,116,68
s_sub_u32 s61, s6, s57
RD 104,248,10240
RD 108,249,10240
MA 176,112,64
s_mov_b32 s57, s6
MA 172,112,24
MA 172,116,28
MA 168,116,20
MA 168,112,16
buffer_load_dwordx4 v251, s[16:19], s50 offen nt lds
MA 164,112,8
s_add_u32 m0, s56, 106496
MA 164,116,12
s_add_u32 s50, s63, 0x8000
MA 160,116,4
MA 160,112,0
WL 4
MA 32,120,0
MA 32,124,4
MA 36,124,12
s_add_u32 s62, s62, 128
MA 36,120,8
buffer_load_dwordx4 v251, s[16:19], s50 offen nt lds
MA 40,120,16
s_add_u32 s7, s64, 32768
MA 40,124,20
s_sub_u32 s64, s64, 65536
MA 44,124,28
s_add_u32 m0, s56, 110592
RD 112,248,12288
RD 116,249,12288
MA 44,120,24
s_add_u32 s50, s63, 0x28000
MA 48,120,64
s_min_u32 s64, s7, s64
MA 48,124,68
buffer_load_dwordx4 a[148:151], v253, s[20:23], s75 offen offset:0 sc1
MA 52,124,76
MA 52,120,72
buffer_load_dwordx4 v251, s[16:19], s50 offen nt lds
MA 56,120,80
s_add_u32 m0, s56, 114688
MA 56,124,84
s_add_u32 s50, s63, 0x40000
MA 60,124,92
MA 60,120,88
WL 4
MA 220,96,88
MA 220,100,92
MA 216,100,84
MA 216,96,80
buffer_load_dwordx4 v251, s[16:19], s50 offen nt lds
MA 212,96,72
MA 212,100,76
MA 208,100,68
s_add_u32 m0, s56, 118784
RD 120,248,14336
RD 124,249,14336
MA 208,96,64
MA 204,96,24
MA 204,100,28
s_add_u32 s50, s63, 0x60000
v_add_u32 v248, s61, v248
v_add_u32 v249, s61, v249
MA 200,100,20
MA 200,96,16
buffer_load_dwordx4 v251, s[16:19], s50 offen nt lds
MA 196,96,8
s_add_u32 m0, s56, 122880
MA 196,100,12
s_add_u32 s50, s63, 0x48000
MA 192,100,4
MA 192,96,0
WL 4
MA 64,104,0
MA 64,108,4
MA 68,108,12
buffer_load_dwordx4 v251, s[16:19], s50 offen nt lds
MA 68,104,8
s_add_u32 m0, s56, 126976
MA 72,104,16
s_add_u32 s50, s63, 0x68000
MA 72,108,20
buffer_load_dwordx4 a[152:155], v253, s[20:23], s74 offen offset:1024 sc1
MA 76,108,28
MA 76,104,24
buffer_load_dwordx4 v251, s[16:19], s50 offen nt lds
MA 80,104,64
MA 80,108,68
MA 84,108,76
MA 84,104,72
s_waitcnt vmcnt(20) lgkmcnt(0)
s_barrier
MA 88,104,80
s_add_u32 m0, s64, 0
RD 96,248,0
RD 100,249,0
MA 88,108,84
MA 92,108,92
s_add_u32 s50, s62, 0x0
TR 32,240,32768
TR 34,240,40960
MA 92,104,88
v_mfma_f32_16x16x32_bf16 a[252:255], a[112:115], a[88:91], a[252:255]
TR 36,240,49152
TR 38,240,57344
v_mfma_f32_16x16x32_bf16 a[252:255], a[116:119], a[92:95], a[252:255]
buffer_load_dwordx4 v250, s[12:15], s50 offen lds
v_mfma_f32_16x16x32_bf16 a[248:251], a[116:119], a[84:87], a[248:251]
s_add_u32 m0, s64, 4096
TR 40,241,32768
TR 42,241,40960
v_mfma_f32_16x16x32_bf16 a[248:251], a[112:115], a[80:83], a[248:251]
v_mfma_f32_16x16x32_bf16 a[244:247], a[112:115], a[72:75], a[244:247]
s_add_u32 s50, s62, 0x10000
TR 44,241,49152
TR 46,241,57344
v_mfma_f32_16x16x32_bf16 a[244:247], a[116:119], a[76:79], a[244:247]
v_mfma_f32_16x16x32_bf16 a[240:243], a[116:119], a[68:71], a[240:243]
TR 48,242,32768
TR 50,242,40960
v_mfma_f32_16x16x32_bf16 a[240:243], a[112:115], a[64:67], a[240:243]
buffer_load_dwordx4 v250, s[12:15], s50 offen lds
MA 236,112,24
s_add_u32 m0, s64, 8192
TR 52,242,49152
TR 54,242,57344
MA 236,116,28
MA 232,116,20
s_add_u32 s50, s62, 0x20000
TR 56,243,32768
TR 58,243,40960
MA 232,112,16
MA 228,112,8
TR 60,243,49152
TR 62,243,57344
MA 228,116,12
buffer_load_dwordx4 v250, s[12:15], s50 offen lds
MA 224,116,4
s_add_u32 m0, s64, 12288
RD 104,248,2048
RD 108,249,2048
MA 224,112,0
MA 112,120,64
MA 112,124,68
buffer_load_dwordx4 a[156:159], v253, s[20:23], s75 offen offset:1024 sc1
MA 116,124,76
s_add_u32 s50, s62, 0x30000
)A"
R"A(TR 64,244,32768
TR 66,244,40960
MA 116,120,72
MA 120,120,80
s_add_u32 s74, s74, 2048
TR 68,244,49152
TR 70,244,57344
MA 120,124,84
MA 124,124,92
s_add_u32 s75, s75, 2048
TR 72,245,32768
TR 74,245,40960
MA 124,120,88
buffer_load_dwordx4 v250, s[12:15], s50 offen lds
MA 96,120,0
TR 76,245,49152
TR 78,245,57344
MA 96,124,4
MA 100,124,12
TR 80,246,32768
TR 82,246,40960
MA 100,120,8
MA 104,120,16
TR 84,246,49152
TR 86,246,57344
MA 104,124,20
TR 88,247,32768
TR 90,247,40960
MA 108,124,28
TR 92,247,49152
TR 94,247,57344
MA 108,120,24
WL 15
MA 128,96,32
s_add_u32 m0, s64, 16384
MA 128,100,36
s_add_u32 s50, s62, 0x40000
MA 132,100,44
MA 132,96,40
buffer_load_dwordx4 v250, s[12:15], s50 offen lds
MA 136,96,48
MA 136,100,52
MA 140,100,60
MA 140,96,56
WL 0
s_barrier
MA 144,96,64
s_add_u32 m0, s64, 20480
RD 112,248,4096
RD 116,249,4096
MA 144,100,68
MA 148,100,76
s_add_u32 s50, s62, 0x50000
RD 120,248,6144
RD 124,249,6144
MA 148,96,72
s_sub_u32 s67, 7, s11
MA 152,96,80
s_lshr_b32 s67, s67, 1
MA 152,100,84
buffer_load_dwordx4 v250, s[12:15], s50 offen lds
MA 156,100,92
s_add_u32 m0, s64, 24576
MA 156,96,88
s_add_u32 s50, s62, 0x60000
MA 0,104,32
s_lshl_b32 s67, s67, 10
MA 0,108,36
s_add_u32 s67, s67, 0x7001000
MA 4,108,44
buffer_load_dwordx4 a[160:163], v253, s[20:23], s74 offen offset:0 sc1
MA 4,104,40
buffer_load_dwordx4 v250, s[12:15], s50 offen lds
MA 8,104,48
buffer_load_dwordx2 v[254:255], v253, s[20:23], s67 offen sc1
MA 8,108,52
MA 12,108,60
s_add_u32 m0, s64, 28672
RD 96,248,8192
RD 100,249,8192
MA 12,104,56
s_add_u32 s50, s62, 0x70000
MA 16,104,64
MA 16,108,68
MA 20,108,76
MA 20,104,72
buffer_load_dwordx4 v250, s[12:15], s50 offen lds
MA 24,104,80
s_add_u32 m0, s56, 131072
MA 24,108,84
s_add_u32 s50, s63, 0x80000
MA 28,108,92
s_add_u32 s6, s57, 32768
MA 28,104,88
WL 4
MA 188,112,88
s_sub_u32 s61, s57, 65536
MA 188,116,92
s_min_u32 s6, s6, s61
MA 184,116,84
MA 184,112,80
buffer_load_dwordx4 v251, s[16:19], s50 offen nt lds
MA 180,112,72
s_add_u32 m0, s56, 135168
MA 180,116,76
s_add_u32 s50, s63, 0xa0000
MA 176,116,68
s_sub_u32 s61, s6, s57
RD 104,248,10240
RD 108,249,10240
MA 176,112,64
s_mov_b32 s57, s6
MA 172,112,56
buffer_load_dwordx4 a[164:167], v253, s[20:23], s75 offen offset:0 sc1
MA 172,116,60
MA 168,116,52
MA 168,112,48
buffer_load_dwordx4 v251, s[16:19], s50 offen nt lds
MA 164,112,40
s_add_u32 m0, s56, 139264
MA 164,116,44
s_add_u32 s50, s63, 0x88000
MA 160,116,36
MA 160,112,32
WL 4
MA 32,120,32
MA 32,124,36
MA 36,124,44
MA 36,120,40
buffer_load_dwordx4 v251, s[16:19], s50 offen nt lds
MA 40,120,48
MA 40,124,52
MA 44,124,60
s_add_u32 m0, s56, 143360
RD 112,248,12288
RD 116,249,12288
MA 44,120,56
s_add_u32 s50, s63, 0xa8000
MA 48,120,64
MA 48,124,68
MA 52,124,76
MA 52,120,72
buffer_load_dwordx4 v251, s[16:19], s50 offen nt lds
MA 56,120,80
s_add_u32 m0, s56, 147456
MA 56,124,84
s_add_u32 s50, s63, 0xc0000
MA 60,124,92
MA 60,120,88
WL 4
MA 220,96,88
buffer_load_dwordx4 a[168:171], v253, s[20:23], s74 offen offset:1024 sc1
MA 220,100,92
MA 216,100,84
MA 216,96,80
buffer_load_dwordx4 v251, s[16:19], s50 offen nt lds
MA 212,96,72
MA 212,100,76
MA 208,100,68
s_add_u32 m0, s56, 151552
RD 120,248,14336
RD 124,249,14336
MA 208,96,64
MA 204,96,56
MA 204,100,60
s_add_u32 s50, s63, 0xe0000
v_add_u32 v248, s61, v248
v_add_u32 v249, s61, v249
MA 200,100,52
MA 200,96,48
buffer_load_dwordx4 v251, s[16:19], s50 offen nt lds
MA 196,96,40
s_add_u32 m0, s56, 155648
MA 196,100,44
s_add_u32 s50, s63, 0xc8000
MA 192,100,36
MA 192,96,32
WL 4
MA 64,104,32
MA 64,108,36
MA 68,108,44
buffer_load_dwordx4 v251, s[16:19], s50 offen nt lds
MA 68,104,40
s_add_u32 m0, s56, 159744
MA 72,104,48
s_add_u32 s50, s63, 0xe8000
MA 72,108,52
buffer_load_dwordx4 a[172:175], v253, s[20:23], s75 offen offset:1024 sc1
MA 76,108,60
s_add_u32 s74, s74, 2048
MA 76,104,56
buffer_load_dwordx4 v251, s[16:19], s50 offen nt lds
MA 80,104,64
s_add_u32 s75, s75, 2048
MA 80,108,68
s_waitcnt vmcnt(12)
v_cmp_ne_u64 vcc, s[48:49], v[254:255]
s_cbranch_vccnz .LPS6
.LPR6:
MA 84,108,76
MA 84,104,72
WL 0
s_barrier
MA 88,104,80
RD 96,248,0
RD 100,249,0
MA 88,108,84
MA 92,108,92
TR 0,240,0
TR 2,240,8192
MA 92,104,88
v_mfma_f32_16x16x32_bf16 a[252:255], a[112:115], a[88:91], a[252:255]
TR 4,240,16384
TR 6,240,24576
v_mfma_f32_16x16x32_bf16 a[252:255], a[116:119], a[92:95], a[252:255]
buffer_load_dwordx4 a[176:179], v253, s[20:23], s74 offen offset:0 sc1
v_mfma_f32_16x16x32_bf16 a[248:251], a[116:119], a[84:87], a[248:251]
TR 8,241,0
TR 10,241,8192
v_mfma_f32_16x16x32_bf16 a[248:251], a[112:115], a[80:83], a[248:251]
v_mfma_f32_16x16x32_bf16 a[244:247], a[112:115], a[72:75], a[244:247]
TR 12,241,16384
TR 14,241,24576
v_mfma_f32_16x16x32_bf16 a[244:247], a[116:119], a[76:79], a[244:247]
v_mfma_f32_16x16x32_bf16 a[240:243], a[116:119], a[68:71], a[240:243]
TR 16,242,0
TR 18,242,8192
v_mfma_f32_16x16x32_bf16 a[240:243], a[112:115], a[64:67], a[240:243]
buffer_load_dwordx4 a[180:183], v253, s[20:23], s75 offen offset:0 sc1
MA 236,112,56
TR 20,242,16384
TR 22,242,24576
MA 236,116,60
MA 232,116,52
TR 24,243,0
TR 26,243,8192
MA 232,112,48
MA 228,112,40
TR 28,243,16384
TR 30,243,24576
MA 228,116,44
buffer_load_dwordx4 a[184:187], v253, s[20:23], s74 offen offset:1024 sc1
MA 224,116,36
RD 104,248,2048
RD 108,249,2048
MA 224,112,32
MA 112,120,64
MA 112,124,68
MA 116,124,76
s_add_u32 s63, s63, 1048576
TR 64,244,0
TR 66,244,8192
MA 116,120,72
MA 120,120,80
TR 68,244,16384
TR 70,244,24576
MA 120,124,84
MA 124,124,92
TR 72,245,0
TR 74,245,8192
MA 124,120,88
buffer_load_dwordx4 a[188:191], v253, s[20:23], s75 offen offset:1024 sc1
MA 96,120,32
s_add_u32 s74, s74, 2048
TR 76,245,16384
TR 78,245,24576
MA 96,124,36
MA 100,124,44
s_add_u32 s75, s75, 2048
TR 80,246,0
TR 82,246,8192
MA 100,120,40
MA 104,120,48
TR 84,246,16384
TR 86,246,24576
MA 104,124,52
TR 88,247,0
TR 90,247,8192
MA 108,124,60
TR 92,247,16384
TR 94,247,24576
MA 108,120,56
WL 15
MA 128,96,0
s_lshr_b32 s50, %1, 3
v_bfe_u32 v252, v253, 9, 1
v_lshl_add_u32 v252, s8, 1, v252
MA 128,100,4
s_lshl_b32 s50, s50, 2
v_mul_u32_u24 v252, 3584, v252
v_and_b32 v251, 0x1ff, v251
MA 132,100,12
s_add_u32 s50, s50, s8
v_add_u32 v252, v252, v251
MA 132,96,8
s_mul_i32 s50, s50, 1408
MA 136,96,16
MA 136,100,20
buffer_load_dwordx4 a[192:195], v253, s[20:23], s74 offen offset:0 sc1
MA 140,100,28
MA 140,96,24
WL 0
s_barrier
MA 144,96,64
s_min_u32 s50, s50, .LCEND-.LCPC-1408
RD 112,248,4096
RD 116,249,4096
MA 144,100,68
v_min_u32 v250, 0xa0, v253
v_lshl_add_u32 v250, v250, 3, s50
MA 148,100,76
RD 120,248,6144
RD 124,249,6144
MA 148,96,72
global_load_dword v250, v250, s[76:77]
MA 152,96,80
s_mov_b32 s12, s36
MA 152,100,84
buffer_load_dwordx4 a[196:199], v253, s[20:23], s75 offen offset:0 sc1
MA 156,100,92
s_and_b32 s13, s37, 0xffff
MA 156,96,88
s_mov_b32 s14, 1605632
MA 0,104,0
s_cmp_eq_u32 s80, 7
s_cselect_b32 s14, 0, s14
MA 0,108,4
s_lshl_b32 s59, s80, 9
MA 4,108,12
MA 4,104,8
buffer_load_dwordx4 a[200:203], v253, s[20:23], s74 offen offset:1024 sc1
MA 8,104,16
MA 8,108,20
MA 12,108,28
s_add_u32 s77, s11, 8
RD 96,248,8192
RD 100,249,8192
MA 12,104,24
s_add_u32 s58, s56, 32768
MA 16,104,64
s_mul_i32 s62, s56, 7
MA 16,108,68
s_lshl_b32 s73, s10, 21
MA 20,108,76
s_lshl_b32 s50, s11, 18
s_add_u32 s73, s73, s50
s_lshr_b32 s50, s8, 1
MA 20,104,72
buffer_load_dwordx4 a[204:207], v253, s[20:23], s75 offen offset:1024 sc1
MA 24,104,80
s_add_u32 s74, s74, 2048
s_lshl_b32 s50, s50, 17
MA 24,108,84
s_add_u32 s75, s75, 2048
MA 28,108,92
s_add_u32 s6, s57, 32768
MA 28,104,88
WL 4
MA 188,112,88
s_sub_u32 s61, s57, 65536
MA 188,116,92
s_min_u32 s6, s6, s61
MA 184,116,84
s_add_u32 s73, s73, s50
MA 184,112,80
buffer_load_dwordx4 a[208:211], v253, s[20:23], s74 offen offset:0 sc1
MA 180,112,72
s_and_b32 s54, s8, 1
MA 180,116,76
s_lshl_b32 s50, s54, 13
MA 176,116,68
s_sub_u32 s61, s6, s57
RD 104,248,10240
RD 108,249,10240
MA 176,112,64
s_mov_b32 s57, s6
MA 172,112,24
s_add_u32 s73, s73, s50
MA 172,116,28
s_add_u32 s73, s73, 32768
MA 168,116,20
s_lshl_b32 s54, s54, 8
MA 168,112,16
buffer_load_dwordx4 a[212:215], v253, s[20:23], s75 offen offset:0 sc1
MA 164,112,8
s_sub_u32 s54, 0xffff0000, s54
MA 164,116,12
MA 160,116,4
MA 160,112,0
WL 4
MA 32,120,0
MA 32,124,4
MA 36,124,12
MA 36,120,8
buffer_load_dwordx4 a[216:219], v253, s[20:23], s74 offen offset:1024 sc1
MA 40,120,16
MA 40,124,20
MA 44,124,28
RD 112,248,12288
RD 116,249,12288
MA 44,120,24
MA 48,120,64
MA 48,124,68
buffer_load_dwordx4 a[220:223], v253, s[20:23], s75 offen offset:1024 sc1
MA 52,124,76
s_add_u32 s74, s74, 2048
MA 52,120,72
s_add_u32 s75, s75, 2048
MA 56,120,80
MA 56,124,84
MA 60,124,92
s_add_u32 m0, s58, 0
MA 60,120,88
WL 4
MA 220,96,88
MA 220,100,92
buffer_load_dwordx4 v252, s[12:15], s59 offen lds
MA 216,100,84
MA 216,96,80
MA 212,96,72
MA 212,100,76
MA 208,100,68
s_add_u32 s59, s59, 28672
RD 120,248,14336
RD 124,249,14336
s_add_u32 m0, s58, 4096
MA 208,96,64
buffer_load_dwordx4 v252, s[12:15], s59 offen lds
MA 204,96,24
MA 204,100,28
s_add_u32 s59, s59, 28672
v_add_u32 v248, s61, v248
v_add_u32 v249, s61, v249
MA 200,100,20
s_add_u32 m0, s58, 8192
MA 200,96,16
buffer_load_dwordx4 a[224:227], v253, s[20:23], s74 offen offset:0 sc1
MA 196,96,8
MA 196,100,12
buffer_load_dwordx4 v252, s[12:15], s59 offen lds
MA 192,100,4
s_add_u32 s59, s59, 28672
MA 192,96,0
WL 4
MA 64,104,0
s_add_u32 m0, s58, 12288
MA 64,108,4
buffer_load_dwordx4 a[228:231], v253, s[20:23], s75 offen offset:0 sc1
MA 68,108,12
MA 68,104,8
buffer_load_dwordx4 v252, s[12:15], s59 offen lds
MA 72,104,16
s_add_u32 s59, s59, 28672
MA 72,108,20
s_add_u32 m0, s58, 16384
MA 76,108,28
MA 76,104,24
buffer_load_dwordx4 v252, s[12:15], s59 offen lds
MA 80,104,64
MA 80,108,68
MA 84,108,76
MA 84,104,72
s_waitcnt vmcnt(20) lgkmcnt(0)
s_barrier
MA 88,104,80
s_add_u32 s59, s59, 28672
RD 96,248,0
RD 100,249,0
MA 88,108,84
MA 92,108,92
s_add_u32 m0, s58, 20480
TR 32,240,32768
TR 34,240,40960
MA 92,104,88
v_mfma_f32_16x16x32_bf16 a[252:255], a[112:115], a[88:91], a[252:255]
TR 36,240,49152
TR 38,240,57344
v_mfma_f32_16x16x32_bf16 a[252:255], a[116:119], a[92:95], a[252:255]
buffer_load_dwordx4 v252, s[12:15], s59 offen lds
v_mfma_f32_16x16x32_bf16 a[248:251], a[116:119], a[84:87], a[248:251]
s_add_u32 s59, s59, 28672
TR 40,241,32768
TR 42,241,40960
v_mfma_f32_16x16x32_bf16 a[248:251], a[112:115], a[80:83], a[248:251]
v_mfma_f32_16x16x32_bf16 a[244:247], a[112:115], a[72:75], a[244:247]
s_add_u32 m0, s58, 24576
TR 44,241,49152
TR 46,241,57344
v_mfma_f32_16x16x32_bf16 a[244:247], a[116:119], a[76:79], a[244:247]
buffer_load_dwordx4 a[232:235], v253, s[20:23], s74 offen offset:1024 sc1
v_mfma_f32_16x16x32_bf16 a[240:243], a[116:119], a[68:71], a[240:243]
TR 48,242,32768
TR 50,242,40960
v_mfma_f32_16x16x32_bf16 a[240:243], a[112:115], a[64:67], a[240:243]
MA 236,112,24
TR 52,242,49152
TR 54,242,57344
MA 236,116,28
buffer_load_dwordx4 v252, s[12:15], s59 offen lds
MA 232,116,20
s_add_u32 s59, s59, 28672
TR 56,243,32768
TR 58,243,40960
MA 232,112,16
MA 228,112,8
s_add_u32 m0, s58, 28672
TR 60,243,49152
TR 62,243,57344
MA 228,116,12
MA 224,116,4
RD 104,248,2048
RD 108,249,2048
MA 224,112,0
buffer_load_dwordx4 v252, s[12:15], s59 offen lds
MA 112,120,64
MA 112,124,68
buffer_load_dwordx4 a[236:239], v253, s[20:23], s75 offen offset:1024 sc1
MA 116,124,76
s_add_u32 s59, s59, 0x7000
TR 64,244,32768
TR 66,244,40960
MA 116,120,72
s_add_u32 s74, s74, 2048
MA 120,120,80
s_add_u32 s7, s58, 32768
TR 68,244,49152
TR 70,244,57344
MA 120,124,84
s_add_u32 s75, s75, 2048
MA 124,124,92
s_sub_u32 s58, s58, 131072
TR 72,245,32768
TR 74,245,40960
MA 124,120,88
MA 96,120,0
s_min_u32 s58, s7, s58
TR 76,245,49152
TR 78,245,57344
MA 96,124,4
MA 100,124,12
TR 80,246,32768
TR 82,246,40960
MA 100,120,8
MA 104,120,16
TR 84,246,49152
TR 86,246,57344
MA 104,124,20
TR 88,247,32768
TR 90,247,40960
MA 108,124,28
TR 92,247,49152
TR 94,247,57344
MA 108,120,24
WL 15
MA 128,96,32
s_add_u32 m0, s58, 0
v_add_u32 v240, s54, v240
v_add_u32 v241, s54, v241
MA 128,100,36
v_add_u32 v242, s54, v242
v_add_u32 v243, s54, v243
MA 132,100,44
v_add_u32 v244, s54, v244
v_add_u32 v245, s54, v245
)A"
R"A(MA 132,96,40
v_add_u32 v246, s54, v246
v_add_u32 v247, s54, v247
MA 136,96,48
MA 136,100,52
buffer_load_dwordx4 v252, s[12:15], s59 offen lds
MA 140,100,60
MA 140,96,56
WL 0
s_barrier
MA 144,96,64
s_add_u32 s59, s59, 28672
RD 112,248,4096
RD 116,249,4096
MA 144,100,68
MA 148,100,76
s_add_u32 m0, s58, 4096
RD 120,248,6144
RD 124,249,6144
MA 148,96,72
buffer_load_dwordx4 v252, s[12:15], s59 offen lds
MA 152,96,80
s_add_u32 s59, s59, 28672
MA 152,100,84
s_add_u32 m0, s58, 8192
MA 156,100,92
MA 156,96,88
MA 0,104,32
MA 0,108,36
buffer_load_dwordx4 v252, s[12:15], s59 offen lds
MA 4,108,44
MA 4,104,40
MA 8,104,48
MA 8,108,52
MA 12,108,60
s_add_u32 s59, s59, 28672
RD 96,248,8192
RD 100,249,8192
s_add_u32 m0, s58, 12288
MA 12,104,56
buffer_load_dwordx4 v252, s[12:15], s59 offen lds
MA 16,104,64
s_add_u32 s59, s59, 28672
PK 128,128,129
PK 129,130,131
MA 16,108,68
s_add_u32 m0, s58, 16384
PK 130,0,1
PK 131,2,3
MA 20,108,76
PK 132,132,133
PK 133,134,135
MA 20,104,72
PK 134,4,5
PK 135,6,7
MA 24,104,80
PK 136,136,137
PK 137,138,139
MA 24,108,84
buffer_load_dwordx4 v252, s[12:15], s59 offen lds
MA 28,108,92
s_add_u32 s59, s59, 28672
PK 138,8,9
PK 139,10,11
MA 28,104,88
WL 4
PK 140,140,141
PK 141,142,143
MA 188,112,88
s_add_u32 m0, s58, 20480
PK 142,12,13
PK 143,14,15
MA 188,116,92
PK 144,144,145
PK 145,146,147
MA 184,116,84
PK 146,16,17
PK 147,18,19
MA 184,112,80
buffer_load_dwordx4 v252, s[12:15], s59 offen lds
MA 180,112,72
PK 148,148,149
PK 149,150,151
MA 180,116,76
PK 150,20,21
PK 151,22,23
MA 176,116,68
s_add_u32 s59, s59, 28672
RD 104,248,10240
RD 108,249,10240
MA 176,112,64
s_add_u32 m0, s58, 24576
PK 152,152,153
PK 153,154,155
MA 172,112,56
PK 154,24,25
PK 155,26,27
MA 172,116,60
buffer_load_dwordx4 v252, s[12:15], s59 offen lds
MA 168,116,52
s_add_u32 s59, s59, 28672
PK 156,156,157
PK 157,158,159
MA 168,112,48
s_add_u32 m0, s58, 28672
PK 158,28,29
PK 159,30,31
MA 164,112,40
MA 164,116,44
MA 160,116,36
buffer_load_dwordx4 v252, s[12:15], s59 offen lds
MA 160,112,32
WL 4
MA 32,120,32
s_add_u32 s59, s59, 0x7000
MA 32,124,36
s_add_u32 s7, s58, 32768
MA 36,124,44
MA 36,120,40
MA 40,120,48
s_add_u32 s51, s73, 0
MA 40,124,52
buffer_store_dwordx4 v[128:131], v253, s[20:23], s51 offen offset:0 sc1
MA 44,124,60
s_sub_u32 s58, s58, 131072
RD 112,248,12288
RD 116,249,12288
MA 44,120,56
s_min_u32 s58, s7, s58
PK 160,160,161
PK 161,162,163
MA 48,120,64
s_add_u32 m0, s58, 0
PK 162,32,33
PK 163,34,35
MA 48,124,68
buffer_load_dwordx4 v252, s[12:15], s59 offen lds
MA 52,124,76
s_add_u32 s59, s59, 28672
PK 164,164,165
PK 165,166,167
MA 52,120,72
s_add_u32 m0, s58, 4096
PK 166,36,37
PK 167,38,39
MA 56,120,80
PK 168,168,169
PK 169,170,171
MA 56,124,84
PK 170,40,41
PK 171,42,43
MA 60,124,92
PK 172,172,173
PK 173,174,175
MA 60,120,88
WL 4
PK 174,44,45
PK 175,46,47
MA 220,96,88
PK 176,176,177
PK 177,178,179
MA 220,100,92
buffer_load_dwordx4 v252, s[12:15], s59 offen lds
MA 216,100,84
PK 178,48,49
PK 179,50,51
MA 216,96,80
PK 180,180,181
PK 181,182,183
MA 212,96,72
PK 182,52,53
PK 183,54,55
MA 212,100,76
PK 184,184,185
PK 185,186,187
MA 208,100,68
s_add_u32 s59, s59, 28672
RD 120,248,14336
RD 124,249,14336
MA 208,96,64
s_add_u32 m0, s58, 8192
PK 186,56,57
PK 187,58,59
MA 204,96,56
buffer_store_dwordx4 v[132:135], v253, s[20:23], s51 offen offset:1024 sc1
MA 204,100,60
buffer_load_dwordx4 v252, s[12:15], s59 offen lds
MA 200,100,52
s_add_u32 s59, s59, 28672
PK 188,188,189
PK 189,190,191
MA 200,96,48
s_add_u32 m0, s58, 12288
PK 190,60,61
PK 191,62,63
MA 196,96,40
MA 196,100,44
MA 192,100,36
MA 192,96,32
WL 4
MA 64,104,32
MA 64,108,36
buffer_load_dwordx4 v252, s[12:15], s59 offen lds
MA 68,108,44
s_add_u32 s59, s59, 28672
MA 68,104,40
s_add_u32 m0, s58, 16384
MA 72,104,48
MA 72,108,52
MA 76,108,60
PK 192,192,193
PK 193,194,195
MA 76,104,56
PK 194,64,65
PK 195,66,67
MA 80,104,64
buffer_store_dwordx4 v[136:139], v253, s[20:23], s51 offen offset:2048 sc1
MA 80,108,68
buffer_load_dwordx4 v252, s[12:15], s59 offen lds
MA 84,108,76
s_add_u32 s59, s59, 28672
PK 196,196,197
PK 197,198,199
MA 84,104,72
s_add_u32 m0, s58, 20480
PK 198,68,69
PK 199,70,71
MA 88,104,80
PK 200,200,201
PK 201,202,203
MA 88,108,84
PK 202,72,73
PK 203,74,75
MA 92,108,92
PK 204,204,205
PK 205,206,207
MA 92,104,88
WL 2
PK 206,76,77
PK 207,78,79
v_mfma_f32_16x16x32_bf16 a[252:255], a[112:115], a[88:91], a[252:255]
PK 208,208,209
PK 209,210,211
v_mfma_f32_16x16x32_bf16 a[252:255], a[116:119], a[92:95], a[252:255]
buffer_load_dwordx4 v252, s[12:15], s59 offen lds
v_mfma_f32_16x16x32_bf16 a[248:251], a[116:119], a[84:87], a[248:251]
s_add_u32 s59, s59, 28672
PK 210,80,81
PK 211,82,83
v_mfma_f32_16x16x32_bf16 a[248:251], a[112:115], a[80:83], a[248:251]
s_add_u32 m0, s58, 24576
PK 212,212,213
PK 213,214,215
v_mfma_f32_16x16x32_bf16 a[244:247], a[112:115], a[72:75], a[244:247]
PK 214,84,85
PK 215,86,87
v_mfma_f32_16x16x32_bf16 a[244:247], a[116:119], a[76:79], a[244:247]
PK 216,216,217
PK 217,218,219
v_mfma_f32_16x16x32_bf16 a[240:243], a[116:119], a[68:71], a[240:243]
PK 218,88,89
PK 219,90,91
v_mfma_f32_16x16x32_bf16 a[240:243], a[112:115], a[64:67], a[240:243]
buffer_load_dwordx4 v252, s[12:15], s59 offen lds
MA 236,112,56
s_add_u32 s59, s59, 28672
PK 220,220,221
PK 221,222,223
MA 236,116,60
s_add_u32 m0, s58, 28672
PK 222,92,93
PK 223,94,95
MA 232,116,52
buffer_store_dwordx4 v[140:143], v253, s[20:23], s51 offen offset:3072 sc1
MA 232,112,48
MA 228,112,40
MA 228,116,44
buffer_load_dwordx4 v252, s[12:15], s59 offen lds
MA 224,116,36
MA 224,112,32
s_waitcnt vmcnt(21) lgkmcnt(0)
s_barrier
MA 112,120,64
s_add_u32 s59, s59, 0x7000
TR 0,240,0
TR 2,240,8192
MA 112,124,68
MA 116,124,76
s_add_u32 s7, s58, 32768
TR 4,240,16384
TR 6,240,24576
MA 116,120,72
s_mov_b32 s57, 32768
MA 120,120,80
s_sub_u32 s58, s58, 131072
TR 8,241,0
TR 10,241,8192
MA 120,124,84
MA 124,124,92
s_min_u32 s58, s7, s58
TR 12,241,16384
TR 14,241,24576
MA 124,120,88
PK 115,114,115
PK 114,112,113
MA 96,120,32
TR 16,242,0
TR 18,242,8192
MA 96,124,36
v_accvgpr_read_b32 v112, a240
v_accvgpr_read_b32 v113, a241
MA 100,124,44
TR 20,242,16384
TR 22,242,24576
MA 100,120,40
PK 112,112,113
v_accvgpr_read_b32 v113, a242
MA 104,120,48
TR 24,243,0
TR 26,243,8192
MA 104,124,52
v_accvgpr_read_b32 v254, a243
PK 113,113,254
MA 108,124,60
TR 28,243,16384
TR 30,243,24576
MA 108,120,56
WL 12
PK 119,118,119
PK 118,116,117
MZ 0,0,128
s_add_u32 m0, s58, 0
TR 32,244,0
TR 34,244,8192
MA 0,4,136
MZ 4,4,140
TR 36,244,16384
TR 38,244,24576
MA 4,0,132
WL 12
v_accvgpr_read_b32 v116, a244
v_accvgpr_read_b32 v117, a245
MZ 8,8,128
TR 40,245,0
TR 42,245,8192
PK 224,224,225
MA 8,12,136
buffer_load_dwordx4 v252, s[12:15], s59 offen lds
MZ 12,12,140
s_add_u32 s59, s59, 28672
TR 44,245,16384
TR 46,245,24576
MA 12,8,132
WL 12
PK 116,116,117
v_accvgpr_read_b32 v117, a246
PK 225,226,227
MZ 16,16,128
s_add_u32 m0, s58, 4096
TR 48,246,0
TR 50,246,8192
MA 16,20,136
buffer_load_dwordx4 v252, s[12:15], s59 offen lds
MZ 20,20,140
s_add_u32 s59, s59, 28672
TR 52,246,16384
TR 54,246,24576
MA 20,16,132
WL 12
v_accvgpr_read_b32 v254, a247
PK 117,117,254
PK 226,96,97
MZ 24,24,128
s_add_u32 m0, s58, 8192
TR 56,247,0
TR 58,247,8192
MA 24,28,136
PK 123,122,123
PK 122,120,121
PK 227,98,99
MZ 28,28,140
TR 60,247,16384
TR 62,247,24576
MA 28,24,132
WL 12
v_accvgpr_read_b32 v120, a248
v_accvgpr_read_b32 v121, a249
PK 228,228,229
MZ 32,32,128
TR 0,240,256
TR 2,240,8448
MA 32,36,136
buffer_load_dwordx4 v252, s[12:15], s59 offen lds
MZ 36,36,140
s_add_u32 s59, s59, 28672
TR 4,240,16640
TR 6,240,24832
MA 36,32,132
WL 12
PK 120,120,121
v_accvgpr_read_b32 v121, a250
v_accvgpr_read_b32 v254, a251
PK 229,230,231
MZ 40,40,128
s_add_u32 m0, s58, 12288
TR 8,241,256
TR 10,241,8448
MA 40,44,136
buffer_load_dwordx4 v252, s[12:15], s59 offen lds
MZ 44,44,140
s_add_u32 s59, s59, 28672
TR 12,241,16640
TR 14,241,24832
PK 230,100,101
MA 44,40,132
WL 12
PK 121,121,254
PK 127,126,127
MZ 48,48,128
s_add_u32 m0, s58, 16384
TR 16,242,256
TR 18,242,8448
MA 48,52,136
PK 126,124,125
v_accvgpr_read_b32 v124, a252
PK 231,102,103
MZ 52,52,140
s_add_u32 s6, s57, 32768
TR 20,242,16640
TR 22,242,24832
MA 52,48,132
WL 12
v_accvgpr_read_b32 v125, a253
PK 124,124,125
PK 232,232,233
MZ 56,56,128
s_sub_u32 s61, s57, 131072
TR 24,243,256
TR 26,243,8448
MA 56,60,136
buffer_load_dwordx4 v252, s[12:15], s59 offen lds
MZ 60,60,140
s_add_u32 s59, s59, 28672
TR 28,243,16640
TR 30,243,24832
MA 60,56,132
WL 12
v_accvgpr_read_b32 v125, a254
v_accvgpr_read_b32 v254, a255
PK 233,234,235
MZ 64,0,128
s_add_u32 m0, s58, 20480
TR 32,244,256
TR 34,244,8448
MA 64,4,136
buffer_load_dwordx4 v252, s[12:15], s59 offen lds
MZ 68,4,140
s_add_u32 s59, s59, 28672
TR 36,244,16640
TR 38,244,24832
PK 234,104,105
MA 68,0,132
WL 12
s_add_u32 s51, s73, 86016
PK 125,125,254
MZ 72,8,128
s_add_u32 m0, s58, 24576
TR 40,245,256
TR 42,245,8448
PK 235,106,107
MA 72,12,136
buffer_store_dwordx4 v[112:115], v253, s[20:23], s51 offen offset:0 sc1
MZ 76,12,140
s_min_u32 s6, s6, s61
TR 44,245,16640
TR 46,245,24832
s_sub_u32 s61, s6, s57
MA 76,8,132
WL 12
PK 236,236,237
PK 237,238,239
s_mov_b32 s57, s6
MZ 80,16,128
TR 48,246,256
TR 50,246,8448
PK 238,108,109
MA 80,20,136
buffer_load_dwordx4 v252, s[12:15], s59 offen lds
v_add_u32 v241, s61, v241
MZ 84,20,140
s_add_u32 s59, s59, 28672
TR 52,246,16640
TR 54,246,24832
v_add_u32 v242, s61, v242
MA 84,16,132
WL 12
buffer_store_dwordx4 v[116:119], v253, s[20:23], s51 offen offset:1024 sc1
MZ 88,24,128
s_add_u32 m0, s58, 28672
TR 56,247,256
TR 58,247,8448
PK 239,110,111
MA 88,28,136
buffer_load_dwordx4 v252, s[12:15], s59 offen lds
MZ 92,28,140
s_add_u32 s59, s59, 0x7000
TR 60,247,16640
TR 62,247,24832
MA 92,24,132
s_waitcnt vmcnt(22) lgkmcnt(12)
s_barrier
v_add_u32 v240, s61, v240
MZ 96,32,128
s_add_u32 s7, s58, 32768
TR 0,240,0
TR 2,240,8192
MA 96,36,136
buffer_store_dwordx4 v[120:123], v253, s[20:23], s51 offen offset:2048 sc1
MZ 100,36,140
s_sub_u32 s58, s58, 131072
TR 4,240,16384
TR 6,240,24576
MA 100,32,132
WL 12
buffer_store_dwordx4 v[124:127], v253, s[20:23], s51 offen offset:3072 sc1
MZ 104,40,128
s_min_u32 s58, s7, s58
TR 8,241,0
TR 10,241,8192
MA 104,44,136
s_mov_b32 s16, s38
v_add_u32 v243, s61, v243
v_add_u32 v244, s61, v244
MZ 108,44,140
s_and_b32 s17, s39, 0xffff
TR 12,241,16384
TR 14,241,24576
MA 108,40,132
WL 12
v_add_u32 v245, s61, v245
v_add_u32 v246, s61, v246
MZ 112,48,128
s_mov_b32 s18, 3584
TR 16,242,0
TR 18,242,8192
MA 112,52,136
s_cmp_eq_u32 s80, 7
s_cselect_b32 s18, 0, s18
v_add_u32 v247, s61, v247
MZ 116,52,140
s_lshl_b32 s53, s80, 9
TR 20,242,16384
TR 22,242,24576
MA 116,48,132
WL 12
v_bfe_u32 v249, v253, 8, 2
v_lshlrev_b32 v249, 3, v249
MZ 120,56,128
TR 24,243,0
TR 26,243,8192
MA 120,60,136
MZ 124,60,140
TR 28,243,16384
TR 30,243,24576
MA 124,56,132
WL 12
MA 0,0,144
s_add_u32 m0, s58, 0
TR 32,244,0
TR 34,244,8192
MA 0,4,152
MA 4,4,156
s_add_u32 s51, s73, 4096
TR 36,244,16384
TR 38,244,24576
MA 4,0,148
WL 12
buffer_store_dwordx4 v[144:147], v253, s[20:23], s51 offen offset:0 sc1
MA 8,8,144
s_add_u32 s6, s57, 32768
TR 40,245,0
TR 42,245,8192
MA 8,12,152
buffer_load_dwordx4 v252, s[12:15], s59 offen lds
MA 12,12,156
s_add_u32 s59, s59, 28672
TR 44,245,16384
TR 46,245,24576
MA 12,8,148
WL 12
s_sub_u32 s61, s57, 131072
MA 16,16,144
s_add_u32 m0, s58, 4096
TR 48,246,0
TR 50,246,8192
MA 16,20,152
buffer_load_dwordx4 v252, s[12:15], s59 offen lds
MA 20,20,156
s_add_u32 s59, s59, 28672
TR 52,246,16384
TR 54,246,24576
MA 20,16,148
WL 12
buffer_load_dwordx2 a[64:65], v249, s[16:19], s53 offen offset:0
MA 24,24,144
s_add_u32 m0, s58, 8192
TR 56,247,0
TR 58,247,8192
MA 24,28,152
s_min_u32 s6, s6, s61
MA 28,28,156
s_sub_u32 s61, s6, s57
TR 60,247,16384
TR 62,247,24576
MA 28,24,148
WL 12
MA 32,32,144
s_mov_b32 s57, s6
TR 0,240,256
TR 2,240,8448
MA 32,36,152
buffer_load_dwordx4 v252, s[12:15], s59 offen lds
MA 36,36,156
s_add_u32 s59, s59, 28672
TR 4,240,16640
TR 6,240,24832
MA 36,32,148
WL 12
v_add_u32 v240, s61, v240
MA 40,40,144
s_add_u32 m0, s58, 12288
TR 8,241,256
TR 10,241,8448
MA 40,44,152
buffer_load_dwordx4 v252, s[12:15], s59 offen lds
MA 44,44,156
s_add_u32 s59, s59, 28672
)A"
R"A(TR 12,241,16640
TR 14,241,24832
MA 44,40,148
WL 12
buffer_store_dwordx4 v[148:151], v253, s[20:23], s51 offen offset:1024 sc1
MA 48,48,144
s_add_u32 m0, s58, 16384
TR 16,242,256
TR 18,242,8448
MA 48,52,152
v_add_u32 v241, s61, v241
MA 52,52,156
TR 20,242,16640
TR 22,242,24832
MA 52,48,148
WL 12
v_add_u32 v242, s61, v242
MA 56,56,144
TR 24,243,256
TR 26,243,8448
MA 56,60,152
buffer_load_dwordx4 v252, s[12:15], s59 offen lds
MA 60,60,156
s_add_u32 s59, s59, 28672
TR 28,243,16640
TR 30,243,24832
MA 60,56,148
WL 12
v_add_u32 v243, s61, v243
MA 64,0,144
s_add_u32 m0, s58, 20480
TR 32,244,256
TR 34,244,8448
MA 64,4,152
buffer_load_dwordx4 v252, s[12:15], s59 offen lds
MA 68,4,156
s_add_u32 s59, s59, 28672
TR 36,244,16640
TR 38,244,24832
MA 68,0,148
WL 12
v_add_u32 v244, s61, v244
MA 72,8,144
s_add_u32 m0, s58, 24576
TR 40,245,256
TR 42,245,8448
MA 72,12,152
buffer_load_dwordx2 a[66:67], v249, s[16:19], s53 offen offset:32
MA 76,12,156
TR 44,245,16640
TR 46,245,24832
MA 76,8,148
WL 12
v_add_u32 v245, s61, v245
MA 80,16,144
TR 48,246,256
TR 50,246,8448
MA 80,20,152
buffer_load_dwordx4 v252, s[12:15], s59 offen lds
MA 84,20,156
s_add_u32 s59, s59, 28672
TR 52,246,16640
TR 54,246,24832
MA 84,16,148
WL 12
v_add_u32 v246, s61, v246
MA 88,24,144
s_add_u32 m0, s58, 28672
TR 56,247,256
TR 58,247,8448
MA 88,28,152
buffer_load_dwordx4 v252, s[12:15], s59 offen lds
MA 92,28,156
s_add_u32 s59, s59, 0x7000
TR 60,247,16640
TR 62,247,24832
MA 92,24,148
s_waitcnt vmcnt(24) lgkmcnt(12)
s_barrier
v_add_u32 v247, s61, v247
MA 96,32,144
s_add_u32 s7, s58, 32768
TR 0,240,0
TR 2,240,8192
MA 96,36,152
buffer_store_dwordx4 v[152:155], v253, s[20:23], s51 offen offset:2048 sc1
MA 100,36,156
s_sub_u32 s58, s58, 131072
TR 4,240,16384
TR 6,240,24576
MA 100,32,148
WL 12
buffer_load_dwordx2 a[68:69], v249, s[16:19], s53 offen offset:64
MA 104,40,144
s_min_u32 s58, s7, s58
TR 8,241,0
TR 10,241,8192
MA 104,44,152
MA 108,44,156
TR 12,241,16384
TR 14,241,24576
MA 108,40,148
WL 12
MA 112,48,144
TR 16,242,0
TR 18,242,8192
MA 112,52,152
MA 116,52,156
TR 20,242,16384
TR 22,242,24576
MA 116,48,148
WL 12
MA 120,56,144
TR 24,243,0
TR 26,243,8192
MA 120,60,152
buffer_load_dwordx2 a[70:71], v249, s[16:19], s53 offen offset:96
MA 124,60,156
TR 28,243,16384
TR 30,243,24576
MA 124,56,148
WL 12
MA 0,0,160
s_add_u32 m0, s58, 0
TR 32,244,0
TR 34,244,8192
MA 0,4,168
MA 4,4,172
s_add_u32 s6, s57, 32768
TR 36,244,16384
TR 38,244,24576
MA 4,0,164
WL 12
buffer_store_dwordx4 v[156:159], v253, s[20:23], s51 offen offset:3072 sc1
MA 8,8,160
s_sub_u32 s61, s57, 131072
TR 40,245,0
TR 42,245,8192
MA 8,12,168
buffer_load_dwordx4 v252, s[12:15], s59 offen lds
MA 12,12,172
s_add_u32 s59, s59, 28672
TR 44,245,16384
TR 46,245,24576
MA 12,8,164
WL 12
s_min_u32 s6, s6, s61
MA 16,16,160
s_add_u32 m0, s58, 4096
TR 48,246,0
TR 50,246,8192
MA 16,20,168
buffer_load_dwordx4 v252, s[12:15], s59 offen lds
MA 20,20,172
s_add_u32 s59, s59, 28672
TR 52,246,16384
TR 54,246,24576
MA 20,16,164
WL 12
buffer_load_dwordx2 a[72:73], v249, s[16:19], s53 offen offset:128
MA 24,24,160
s_add_u32 m0, s58, 8192
TR 56,247,0
TR 58,247,8192
MA 24,28,168
s_sub_u32 s61, s6, s57
MA 28,28,172
s_mov_b32 s57, s6
TR 60,247,16384
TR 62,247,24576
MA 28,24,164
WL 12
MA 32,32,160
TR 0,240,256
TR 2,240,8448
MA 32,36,168
buffer_load_dwordx4 v252, s[12:15], s59 offen lds
MA 36,36,172
s_add_u32 s59, s59, 28672
TR 4,240,16640
TR 6,240,24832
MA 36,32,164
WL 12
v_add_u32 v240, s61, v240
MA 40,40,160
s_add_u32 m0, s58, 12288
TR 8,241,256
TR 10,241,8448
MA 40,44,168
buffer_load_dwordx4 v252, s[12:15], s59 offen lds
MA 44,44,172
s_add_u32 s59, s59, 28672
TR 12,241,16640
TR 14,241,24832
MA 44,40,164
WL 12
s_add_u32 s51, s73, 16384
v_add_u32 v241, s61, v241
MA 48,48,160
s_add_u32 m0, s58, 16384
TR 16,242,256
TR 18,242,8448
MA 48,52,168
buffer_store_dwordx4 v[160:163], v253, s[20:23], s51 offen offset:0 sc1
MA 52,52,172
TR 20,242,16640
TR 22,242,24832
MA 52,48,164
WL 12
v_add_u32 v242, s61, v242
MA 56,56,160
TR 24,243,256
TR 26,243,8448
MA 56,60,168
buffer_load_dwordx4 v252, s[12:15], s59 offen lds
MA 60,60,172
s_add_u32 s59, s59, 28672
TR 28,243,16640
TR 30,243,24832
MA 60,56,164
WL 12
v_add_u32 v243, s61, v243
MA 64,0,160
s_add_u32 m0, s58, 20480
TR 32,244,256
TR 34,244,8448
MA 64,4,168
buffer_load_dwordx4 v252, s[12:15], s59 offen lds
MA 68,4,172
s_add_u32 s59, s59, 28672
TR 36,244,16640
TR 38,244,24832
MA 68,0,164
WL 12
v_add_u32 v244, s61, v244
MA 72,8,160
s_add_u32 m0, s58, 24576
TR 40,245,256
TR 42,245,8448
MA 72,12,168
buffer_load_dwordx2 a[74:75], v249, s[16:19], s53 offen offset:160
MA 76,12,172
TR 44,245,16640
TR 46,245,24832
MA 76,8,164
WL 12
v_add_u32 v245, s61, v245
MA 80,16,160
TR 48,246,256
TR 50,246,8448
MA 80,20,168
buffer_load_dwordx4 v252, s[12:15], s59 offen lds
MA 84,20,172
s_add_u32 s59, s59, 28672
TR 52,246,16640
TR 54,246,24832
MA 84,16,164
WL 12
v_add_u32 v246, s61, v246
MA 88,24,160
s_add_u32 m0, s58, 28672
TR 56,247,256
TR 58,247,8448
MA 88,28,168
buffer_load_dwordx4 v252, s[12:15], s59 offen lds
MA 92,28,172
s_add_u32 s59, s59, 0x7000
TR 60,247,16640
TR 62,247,24832
MA 92,24,164
s_waitcnt vmcnt(29) lgkmcnt(12)
s_barrier
v_add_u32 v247, s61, v247
MA 96,32,160
s_add_u32 s7, s58, 32768
TR 0,240,0
TR 2,240,8192
MA 96,36,168
buffer_store_dwordx4 v[164:167], v253, s[20:23], s51 offen offset:1024 sc1
MA 100,36,172
s_sub_u32 s58, s58, 131072
TR 4,240,16384
TR 6,240,24576
MA 100,32,164
WL 12
buffer_load_dwordx2 a[76:77], v249, s[16:19], s53 offen offset:192
MA 104,40,160
s_min_u32 s58, s7, s58
TR 8,241,0
TR 10,241,8192
MA 104,44,168
MA 108,44,172
TR 12,241,16384
TR 14,241,24576
MA 108,40,164
WL 12
MA 112,48,160
TR 16,242,0
TR 18,242,8192
MA 112,52,168
MA 116,52,172
TR 20,242,16384
TR 22,242,24576
MA 116,48,164
WL 12
MA 120,56,160
TR 24,243,0
TR 26,243,8192
MA 120,60,168
buffer_load_dwordx2 a[78:79], v249, s[16:19], s53 offen offset:224
MA 124,60,172
TR 28,243,16384
TR 30,243,24576
MA 124,56,164
WL 12
MA 0,0,176
s_add_u32 m0, s58, 0
TR 32,244,0
TR 34,244,8192
MA 0,4,184
MA 4,4,188
s_add_u32 s6, s57, 32768
TR 36,244,16384
TR 38,244,24576
MA 4,0,180
WL 12
buffer_store_dwordx4 v[168:171], v253, s[20:23], s51 offen offset:2048 sc1
MA 8,8,176
s_sub_u32 s61, s57, 131072
TR 40,245,0
TR 42,245,8192
MA 8,12,184
buffer_load_dwordx4 v252, s[12:15], s59 offen lds
MA 12,12,188
s_add_u32 s59, s59, 28672
TR 44,245,16384
TR 46,245,24576
MA 12,8,180
WL 12
s_min_u32 s6, s6, s61
MA 16,16,176
s_add_u32 m0, s58, 4096
TR 48,246,0
TR 50,246,8192
MA 16,20,184
buffer_load_dwordx4 v252, s[12:15], s59 offen lds
MA 20,20,188
s_add_u32 s59, s59, 28672
TR 52,246,16384
TR 54,246,24576
MA 20,16,180
WL 12
buffer_load_dwordx2 a[80:81], v249, s[16:19], s53 offen offset:256
MA 24,24,176
s_add_u32 m0, s58, 8192
TR 56,247,0
TR 58,247,8192
MA 24,28,184
s_sub_u32 s61, s6, s57
MA 28,28,188
s_mov_b32 s57, s6
TR 60,247,16384
TR 62,247,24576
MA 28,24,180
WL 12
MA 32,32,176
TR 0,240,256
TR 2,240,8448
MA 32,36,184
buffer_load_dwordx4 v252, s[12:15], s59 offen lds
MA 36,36,188
s_add_u32 s59, s59, 28672
TR 4,240,16640
TR 6,240,24832
MA 36,32,180
WL 12
v_add_u32 v240, s61, v240
MA 40,40,176
s_add_u32 m0, s58, 12288
TR 8,241,256
TR 10,241,8448
MA 40,44,184
buffer_load_dwordx4 v252, s[12:15], s59 offen lds
MA 44,44,188
s_add_u32 s59, s59, 28672
TR 12,241,16640
TR 14,241,24832
MA 44,40,180
WL 12
buffer_store_dwordx4 v[172:175], v253, s[20:23], s51 offen offset:3072 sc1
MA 48,48,176
s_add_u32 m0, s58, 16384
TR 16,242,256
TR 18,242,8448
MA 48,52,184
v_add_u32 v241, s61, v241
MA 52,52,188
TR 20,242,16640
TR 22,242,24832
MA 52,48,180
WL 12
v_add_u32 v242, s61, v242
MA 56,56,176
TR 24,243,256
TR 26,243,8448
MA 56,60,184
buffer_load_dwordx4 v252, s[12:15], s59 offen lds
MA 60,60,188
s_add_u32 s59, s59, 28672
TR 28,243,16640
TR 30,243,24832
MA 60,56,180
WL 12
v_add_u32 v243, s61, v243
MA 64,0,176
s_add_u32 m0, s58, 20480
TR 32,244,256
TR 34,244,8448
MA 64,4,184
buffer_load_dwordx4 v252, s[12:15], s59 offen lds
MA 68,4,188
s_add_u32 s59, s59, 28672
TR 36,244,16640
TR 38,244,24832
MA 68,0,180
WL 12
v_add_u32 v244, s61, v244
MA 72,8,176
s_add_u32 m0, s58, 24576
TR 40,245,256
TR 42,245,8448
MA 72,12,184
buffer_load_dwordx2 a[82:83], v249, s[16:19], s53 offen offset:288
MA 76,12,188
TR 44,245,16640
TR 46,245,24832
MA 76,8,180
WL 12
v_add_u32 v245, s61, v245
MA 80,16,176
TR 48,246,256
TR 50,246,8448
MA 80,20,184
buffer_load_dwordx4 v252, s[12:15], s59 offen lds
MA 84,20,188
s_add_u32 s59, s59, 28672
TR 52,246,16640
TR 54,246,24832
MA 84,16,180
WL 12
s_add_u32 s51, s73, 20480
v_add_u32 v246, s61, v246
MA 88,24,176
s_add_u32 m0, s58, 28672
TR 56,247,256
TR 58,247,8448
MA 88,28,184
buffer_load_dwordx4 v252, s[12:15], s59 offen lds
MA 92,28,188
s_mov_b32 s12, s40
TR 60,247,16640
TR 62,247,24832
MA 92,24,180
s_waitcnt vmcnt(30) lgkmcnt(12)
s_barrier
v_add_u32 v247, s61, v247
MA 96,32,176
s_and_b32 s13, s41, 0xffff
TR 0,240,0
TR 2,240,8192
MA 96,36,184
buffer_store_dwordx4 v[176:179], v253, s[20:23], s51 offen offset:0 sc1
MA 100,36,188
s_mov_b32 s14, 1605632
TR 4,240,16384
TR 6,240,24576
MA 100,32,180
WL 12
v_add_u32 v252, s56, v253
v_lshrrev_b32 v252, 7, v252
MA 104,40,176
TR 8,241,0
TR 10,241,8192
MA 104,44,184
s_cmp_eq_u32 s80, 7
s_cselect_b32 s14, 0, s14
v_mul_u32_u24 v252, 0x380, v252
MA 108,44,188
s_mul_i32 s59, s80, 229376
TR 12,241,16384
TR 14,241,24576
MA 108,40,180
WL 12
v_lshrrev_b32 v250, 3, v253
v_xor_b32 v250, v250, v253
MA 112,48,176
s_add_u32 s7, s58, 32768
TR 16,242,0
TR 18,242,8192
MA 112,52,184
v_and_b32 v250, 0x60, v250
v_add_u32 v252, v252, v250
MA 116,52,188
s_sub_u32 s58, s58, 131072
TR 20,242,16384
TR 22,242,24576
MA 116,48,180
WL 12
v_and_b32 v250, 16, v253
v_add_u32 v252, v252, v250
MA 120,56,176
s_min_u32 s58, s7, s58
TR 24,243,0
TR 26,243,8192
MA 120,60,184
buffer_load_dwordx2 a[84:85], v249, s[16:19], s53 offen offset:320
MA 124,60,188
TR 28,243,16384
TR 30,243,24576
MA 124,56,180
WL 12
buffer_load_dwordx2 a[86:87], v249, s[16:19], s53 offen offset:352
MA 0,0,192
s_add_u32 m0, s58, 0
TR 32,244,0
TR 34,244,8192
MA 0,4,200
MA 4,4,204
s_add_u32 s6, s57, 32768
TR 36,244,16384
TR 38,244,24576
MA 4,0,196
WL 12
buffer_store_dwordx4 v[180:183], v253, s[20:23], s51 offen offset:1024 sc1
MA 8,8,192
s_sub_u32 s61, s57, 131072
TR 40,245,0
TR 42,245,8192
MA 8,12,200
buffer_load_dwordx4 v252, s[12:15], s59 offen lds
MA 12,12,204
s_add_u32 s59, s59, 28672
TR 44,245,16384
TR 46,245,24576
MA 12,8,196
WL 12
s_min_u32 s6, s6, s61
MA 16,16,192
s_add_u32 m0, s58, 4096
TR 48,246,0
TR 50,246,8192
MA 16,20,200
buffer_load_dwordx4 v252, s[12:15], s59 offen lds
MA 20,20,204
s_add_u32 s59, s59, 28672
TR 52,246,16384
TR 54,246,24576
MA 20,16,196
WL 12
buffer_load_dwordx2 a[88:89], v249, s[16:19], s53 offen offset:384
MA 24,24,192
s_add_u32 m0, s58, 8192
TR 56,247,0
TR 58,247,8192
MA 24,28,200
s_sub_u32 s61, s6, s57
MA 28,28,204
s_mov_b32 s57, s6
TR 60,247,16384
TR 62,247,24576
MA 28,24,196
WL 12
MA 32,32,192
TR 0,240,256
TR 2,240,8448
MA 32,36,200
buffer_load_dwordx4 v252, s[12:15], s59 offen lds
MA 36,36,204
s_add_u32 s59, s59, 28672
TR 4,240,16640
TR 6,240,24832
MA 36,32,196
WL 12
v_add_u32 v240, s61, v240
MA 40,40,192
s_add_u32 m0, s58, 12288
TR 8,241,256
TR 10,241,8448
MA 40,44,200
buffer_load_dwordx4 v252, s[12:15], s59 offen lds
MA 44,44,204
s_add_u32 s59, s59, 28672
TR 12,241,16640
TR 14,241,24832
MA 44,40,196
WL 12
buffer_store_dwordx4 v[184:187], v253, s[20:23], s51 offen offset:2048 sc1
MA 48,48,192
s_add_u32 m0, s58, 16384
TR 16,242,256
TR 18,242,8448
MA 48,52,200
v_add_u32 v241, s61, v241
MA 52,52,204
TR 20,242,16640
TR 22,242,24832
MA 52,48,196
WL 12
v_add_u32 v242, s61, v242
MA 56,56,192
TR 24,243,256
TR 26,243,8448
MA 56,60,200
buffer_load_dwordx4 v252, s[12:15], s59 offen lds
MA 60,60,204
s_add_u32 s59, s59, 28672
TR 28,243,16640
TR 30,243,24832
MA 60,56,196
WL 12
v_add_u32 v243, s61, v243
MA 64,0,192
s_add_u32 m0, s58, 20480
TR 32,244,256
TR 34,244,8448
MA 64,4,200
buffer_load_dwordx4 v252, s[12:15], s59 offen lds
MA 68,4,204
s_add_u32 s59, s59, 28672
TR 36,244,16640
TR 38,244,24832
MA 68,0,196
WL 12
v_add_u32 v244, s61, v244
)A"
R"A(MA 72,8,192
s_add_u32 m0, s58, 24576
TR 40,245,256
TR 42,245,8448
MA 72,12,200
buffer_load_dwordx2 a[90:91], v249, s[16:19], s53 offen offset:416
MA 76,12,204
TR 44,245,16640
TR 46,245,24832
MA 76,8,196
WL 12
v_add_u32 v245, s61, v245
MA 80,16,192
TR 48,246,256
TR 50,246,8448
MA 80,20,200
buffer_load_dwordx4 v252, s[12:15], s59 offen lds
MA 84,20,204
s_add_u32 s59, s59, 28672
TR 52,246,16640
TR 54,246,24832
MA 84,16,196
WL 12
v_add_u32 v246, s61, v246
MA 88,24,192
s_add_u32 m0, s58, 28672
TR 56,247,256
TR 58,247,8448
MA 88,28,200
buffer_load_dwordx4 v252, s[12:15], s59 offen lds
MA 92,28,204
s_add_u32 s59, s59, 0xfffcf080
TR 60,247,16640
TR 62,247,24832
MA 92,24,196
s_waitcnt vmcnt(30) lgkmcnt(12)
s_barrier
v_add_u32 v247, s61, v247
MA 96,32,192
s_add_u32 s7, s58, 32768
TR 0,240,0
TR 2,240,8192
MA 96,36,200
buffer_store_dwordx4 v[188:191], v253, s[20:23], s51 offen offset:3072 sc1
MA 100,36,204
s_sub_u32 s58, s58, 131072
TR 4,240,16384
TR 6,240,24576
MA 100,32,196
WL 12
buffer_load_dwordx2 a[92:93], v249, s[16:19], s53 offen offset:448
MA 104,40,192
s_min_u32 s58, s7, s58
TR 8,241,0
TR 10,241,8192
MA 104,44,200
MA 108,44,204
TR 12,241,16384
TR 14,241,24576
MA 108,40,196
WL 12
MA 112,48,192
TR 16,242,0
TR 18,242,8192
MA 112,52,200
MA 116,52,204
TR 20,242,16384
TR 22,242,24576
MA 116,48,196
WL 12
MA 120,56,192
TR 24,243,0
TR 26,243,8192
MA 120,60,200
buffer_load_dwordx2 a[94:95], v249, s[16:19], s53 offen offset:480
MA 124,60,204
TR 28,243,16384
TR 30,243,24576
MA 124,56,196
WL 12
MA 0,0,208
s_add_u32 m0, s58, 0
TR 32,244,0
TR 34,244,8192
MA 0,4,216
v_bfe_u32 v250, v253, 4, 4
v_bfe_u32 v251, v253, 8, 2
MA 4,4,220
s_add_u32 s51, s73, 65536
TR 36,244,16384
TR 38,244,24576
MA 4,0,212
WL 12
buffer_store_dwordx4 v[192:195], v253, s[20:23], s51 offen offset:0 sc1
MA 8,8,208
s_add_u32 s6, s57, 32768
TR 40,245,0
TR 42,245,8192
MA 8,12,216
buffer_load_dwordx4 v252, s[12:15], s59 offen lds
MA 12,12,220
s_add_u32 s59, s59, 28672
TR 44,245,16384
TR 46,245,24576
MA 12,8,212
WL 12
s_sub_u32 s61, s57, 131072
v_lshlrev_b32 v251, 2, v251
MA 16,16,208
s_add_u32 m0, s58, 4096
TR 48,246,0
TR 50,246,8192
MA 16,20,216
buffer_load_dwordx4 v252, s[12:15], s59 offen lds
MA 20,20,220
s_add_u32 s59, s59, 28672
TR 52,246,16384
TR 54,246,24576
MA 20,16,212
WL 12
v_sub_u32 v250, v250, v251
v_lshrrev_b32 v250, 1, v250
MA 24,24,208
s_add_u32 m0, s58, 8192
TR 56,247,0
TR 58,247,8192
MA 24,28,216
s_min_u32 s6, s6, s61
v_and_b32 v251, 16, v253
v_mov_b32 v248, 0x3f80
MA 28,28,220
s_sub_u32 s61, s6, s57
TR 60,247,16384
TR 62,247,24576
MA 28,24,212
WL 12
v_lshlrev_b32 v251, v251, v248
v_min_u32 v248, 1, v250
MA 32,32,208
s_mov_b32 s57, s6
TR 0,240,256
TR 2,240,8448
MA 32,36,216
buffer_load_dwordx4 v252, s[12:15], s59 offen lds
MA 36,36,220
s_add_u32 s59, s59, 28672
TR 4,240,16640
TR 6,240,24832
MA 36,32,212
WL 12
v_add_u32 v240, s61, v240
v_add_u32 v248, -1, v248
MA 40,40,208
s_add_u32 m0, s58, 12288
TR 8,241,256
TR 10,241,8448
MA 40,44,216
buffer_load_dwordx4 v252, s[12:15], s59 offen lds
MA 44,44,220
s_add_u32 s59, s59, 28672
TR 12,241,16640
TR 14,241,24832
MA 44,40,212
WL 12
buffer_store_dwordx4 v[196:199], v253, s[20:23], s51 offen offset:1024 sc1
MA 48,48,208
s_add_u32 m0, s58, 16384
TR 16,242,256
TR 18,242,8448
MA 48,52,216
v_add_u32 v241, s61, v241
v_and_b32 v248, v248, v251
MA 52,52,220
TR 20,242,16640
TR 22,242,24832
MA 52,48,212
WL 12
v_add_u32 v242, s61, v242
v_accvgpr_write_b32 a250, v248
MA 56,56,208
TR 24,243,256
TR 26,243,8448
MA 56,60,216
buffer_load_dwordx4 v252, s[12:15], s59 offen lds
MA 60,60,220
s_add_u32 s59, s59, 28672
TR 28,243,16640
TR 30,243,24832
MA 60,56,212
WL 12
v_add_u32 v243, s61, v243
v_xor_b32 v250, 1, v250
MA 64,0,208
s_add_u32 m0, s58, 20480
TR 32,244,256
TR 34,244,8448
MA 64,4,216
buffer_load_dwordx4 v252, s[12:15], s59 offen lds
MA 68,4,220
s_add_u32 s59, s59, 28672
TR 36,244,16640
TR 38,244,24832
MA 68,0,212
WL 12
v_add_u32 v244, s61, v244
v_min_u32 v248, 1, v250
MA 72,8,208
s_add_u32 m0, s58, 24576
TR 40,245,256
TR 42,245,8448
MA 72,12,216
v_add_u32 v248, -1, v248
v_and_b32 v248, v248, v251
MA 76,12,220
TR 44,245,16640
TR 46,245,24832
MA 76,8,212
WL 12
v_add_u32 v245, s61, v245
v_accvgpr_write_b32 a251, v248
MA 80,16,208
TR 48,246,256
TR 50,246,8448
MA 80,20,216
buffer_load_dwordx4 v252, s[12:15], s59 offen lds
MA 84,20,220
s_add_u32 s59, s59, 28672
TR 52,246,16640
TR 54,246,24832
MA 84,16,212
WL 12
v_add_u32 v246, s61, v246
v_accvgpr_write_b32 a248, 0
MA 88,24,208
s_add_u32 m0, s58, 28672
TR 56,247,256
TR 58,247,8448
MA 88,28,216
buffer_load_dwordx4 v252, s[12:15], s59 offen lds
MA 92,28,220
s_add_u32 s59, s59, 0xfffcf080
TR 60,247,16640
TR 62,247,24832
MA 92,24,212
s_waitcnt vmcnt(28) lgkmcnt(12)
s_barrier
v_add_u32 v247, s61, v247
MA 96,32,208
s_add_u32 s7, s58, 32768
TR 0,240,0
TR 2,240,8192
MA 96,36,216
buffer_store_dwordx4 v[200:203], v253, s[20:23], s51 offen offset:2048 sc1
MA 100,36,220
s_sub_u32 s58, s58, 131072
TR 4,240,16384
TR 6,240,24576
MA 100,32,212
WL 12
v_accvgpr_write_b32 a249, 0
v_accvgpr_write_b32 a252, 0
MA 104,40,208
s_min_u32 s58, s7, s58
TR 8,241,0
TR 10,241,8192
MA 104,44,216
v_accvgpr_write_b32 a253, 0
MA 108,44,220
TR 12,241,16384
TR 14,241,24576
MA 108,40,212
WL 12
MA 112,48,208
TR 16,242,0
TR 18,242,8192
MA 112,52,216
MA 116,52,220
TR 20,242,16384
TR 22,242,24576
MA 116,48,212
WL 12
MA 120,56,208
TR 24,243,0
TR 26,243,8192
MA 120,60,216
MA 124,60,220
TR 28,243,16384
TR 30,243,24576
MA 124,56,212
WL 12
s_waitcnt vmcnt(11)
MA 0,0,224
s_add_u32 m0, s58, 0
TR 32,244,0
TR 34,244,8192
MA 0,4,232
MA 4,4,236
TR 36,244,16384
TR 38,244,24576
MA 4,0,228
buffer_load_dwordx4 v252, s[12:15], s59 offen lds
MA 4,250,64
buffer_store_dwordx4 v[204:207], v253, s[20:23], s51 offen offset:3072 sc1
MA 0,250,64
WL 12
MA 8,8,224
s_add_u32 s59, s59, 28672
TR 40,245,0
TR 42,245,8192
s_add_u32 m0, s58, 4096
MA 8,12,232
buffer_load_dwordx4 v252, s[12:15], s59 offen lds
MA 12,12,236
s_add_u32 s59, s59, 28672
TR 44,245,16384
TR 46,245,24576
MA 12,8,228
s_add_u32 m0, s58, 8192
MA 12,248,64
buffer_load_dwordx4 v252, s[12:15], s59 offen lds
MA 8,248,64
WL 12
MA 16,16,224
s_add_u32 s59, s59, 28672
TR 48,246,0
TR 50,246,8192
MA 16,20,232
MA 20,20,236
s_add_u32 m0, s58, 12288
TR 52,246,16384
TR 54,246,24576
MA 20,16,228
buffer_load_dwordx4 v252, s[12:15], s59 offen lds
MA 20,250,68
v_mul_f32 v0, v0, abs(v0)
v_mul_f32 v1, v1, abs(v1)
MA 16,250,68
WL 12
v_mul_f32 v2, v2, abs(v2)
v_mul_f32 v3, v3, abs(v3)
MA 24,24,224
s_add_u32 s59, s59, 28672
TR 56,247,0
TR 58,247,8192
MA 24,28,232
v_mul_f32 v8, v8, abs(v8)
v_mul_f32 v9, v9, abs(v9)
MA 28,28,236
s_add_u32 m0, s58, 16384
TR 60,247,16384
TR 62,247,24576
MA 28,24,228
buffer_load_dwordx4 v252, s[12:15], s59 offen lds
MA 28,248,68
s_add_u32 s51, s73, 69632
v_mul_f32 v10, v10, abs(v10)
MA 24,248,68
WL 12
v_mul_f32 v11, v11, abs(v11)
PK 0,0,1
MA 32,32,224
s_add_u32 s59, s59, 28672
TR 0,240,256
TR 2,240,8448
MA 32,36,232
buffer_store_dwordx4 v[208:211], v253, s[20:23], s51 offen offset:0 sc1
MA 36,36,236
s_add_u32 m0, s58, 20480
TR 4,240,16640
TR 6,240,24832
MA 36,32,228
PK 1,2,3
PK 2,8,9
MA 36,250,72
PK 3,10,11
v_pk_max_i16 v0, v0, 0
MA 32,250,72
WL 12
v_pk_max_i16 v1, v1, 0
v_pk_max_i16 v2, v2, 0
MA 40,40,224
TR 8,241,256
TR 10,241,8448
MA 40,44,232
v_pk_max_i16 v3, v3, 0
v_mul_f32 v4, v4, abs(v4)
MA 44,44,236
TR 12,241,16640
TR 14,241,24832
MA 44,40,228
buffer_load_dwordx4 v252, s[12:15], s59 offen lds
MA 44,248,72
v_mul_f32 v5, v5, abs(v5)
v_mul_f32 v6, v6, abs(v6)
MA 40,248,72
WL 12
v_mul_f32 v7, v7, abs(v7)
v_mul_f32 v12, v12, abs(v12)
MA 48,48,224
s_add_u32 s59, s59, 28672
TR 16,242,256
TR 18,242,8448
MA 48,52,232
v_mul_f32 v13, v13, abs(v13)
v_mul_f32 v14, v14, abs(v14)
MA 52,52,236
s_add_u32 m0, s58, 24576
TR 20,242,16640
TR 22,242,24832
MA 52,48,228
v_mul_f32 v15, v15, abs(v15)
PK 4,4,5
MA 52,250,76
PK 5,6,7
PK 6,12,13
MA 48,250,76
WL 12
PK 7,14,15
v_pk_max_i16 v4, v4, 0
MA 56,56,224
TR 24,243,256
TR 26,243,8448
MA 56,60,232
v_pk_max_i16 v5, v5, 0
v_pk_max_i16 v6, v6, 0
MA 60,60,236
TR 28,243,16640
TR 30,243,24832
MA 60,56,228
buffer_load_dwordx4 v252, s[12:15], s59 offen lds
MA 60,248,76
buffer_store_dwordx4 v[212:215], v253, s[20:23], s51 offen offset:1024 sc1
MA 56,248,76
WL 12
v_pk_max_i16 v7, v7, 0
v_mul_f32 v16, v16, abs(v16)
MA 64,0,224
s_add_u32 s59, s59, 28672
TR 32,244,256
TR 34,244,8448
MA 64,4,232
v_mul_f32 v17, v17, abs(v17)
v_mul_f32 v18, v18, abs(v18)
MA 68,4,236
s_add_u32 m0, s58, 28672
TR 36,244,16640
TR 38,244,24832
MA 68,0,228
v_mul_f32 v19, v19, abs(v19)
v_mul_f32 v24, v24, abs(v24)
MA 68,250,80
v_mul_f32 v25, v25, abs(v25)
v_mul_f32 v26, v26, abs(v26)
MA 64,250,80
WL 12
v_mul_f32 v27, v27, abs(v27)
PK 16,16,17
MA 72,8,224
TR 40,245,256
TR 42,245,8448
MA 72,12,232
PK 17,18,19
PK 18,24,25
MA 76,12,236
TR 44,245,16640
TR 46,245,24832
MA 76,8,228
buffer_load_dwordx4 v252, s[12:15], s59 offen lds
MA 76,248,80
PK 19,26,27
v_pk_max_i16 v16, v16, 0
MA 72,248,80
WL 12
v_pk_max_i16 v17, v17, 0
v_pk_max_i16 v18, v18, 0
MA 80,16,224
s_add_u32 s59, s59, 0xfffcf080
TR 48,246,256
TR 50,246,8448
MA 80,20,232
s_add_u32 s6, s57, 32768
v_pk_max_i16 v19, v19, 0
v_mul_f32 v20, v20, abs(v20)
MA 84,20,236
s_add_u32 s7, s58, 32768
TR 52,246,16640
TR 54,246,24832
MA 84,16,228
s_sub_u32 s57, s57, 131072
v_mul_f32 v21, v21, abs(v21)
v_mul_f32 v22, v22, abs(v22)
MA 84,250,84
s_min_u32 s57, s6, s57
v_mul_f32 v23, v23, abs(v23)
v_mul_f32 v28, v28, abs(v28)
MA 80,250,84
WL 12
v_mul_f32 v29, v29, abs(v29)
v_mul_f32 v30, v30, abs(v30)
MA 88,24,224
s_sub_u32 s58, s58, 131072
TR 56,247,256
TR 58,247,8448
MA 88,28,232
v_mul_f32 v31, v31, abs(v31)
PK 20,20,21
MA 92,28,236
s_min_u32 s58, s7, s58
TR 60,247,16640
TR 62,247,24832
MA 92,24,228
PK 21,22,23
PK 22,28,29
MA 92,248,84
PK 23,30,31
v_pk_max_i16 v20, v20, 0
MA 88,248,84
WL 12
v_pk_max_i16 v21, v21, 0
v_pk_max_i16 v22, v22, 0
MA 96,32,224
v_pk_max_i16 v23, v23, 0
v_mul_f32 v32, v32, abs(v32)
MA 96,36,232
v_mul_f32 v33, v33, abs(v33)
v_mul_f32 v34, v34, abs(v34)
MA 100,36,236
v_mul_f32 v35, v35, abs(v35)
v_mul_f32 v40, v40, abs(v40)
MA 100,32,228
v_mul_f32 v41, v41, abs(v41)
v_mul_f32 v42, v42, abs(v42)
MA 100,250,88
v_mul_f32 v43, v43, abs(v43)
PK 32,32,33
MA 96,250,88
WL 8
PK 33,34,35
PK 34,40,41
MA 104,40,224
PK 35,42,43
v_pk_max_i16 v32, v32, 0
MA 104,44,232
v_pk_max_i16 v33, v33, 0
v_pk_max_i16 v34, v34, 0
MA 108,44,236
v_pk_max_i16 v35, v35, 0
v_mul_f32 v36, v36, abs(v36)
MA 108,40,228
v_mul_f32 v37, v37, abs(v37)
v_mul_f32 v38, v38, abs(v38)
MA 108,248,88
v_mul_f32 v39, v39, abs(v39)
v_mul_f32 v44, v44, abs(v44)
MA 104,248,88
WL 4
v_mul_f32 v45, v45, abs(v45)
v_mul_f32 v46, v46, abs(v46)
MA 112,48,224
v_mul_f32 v47, v47, abs(v47)
PK 36,36,37
MA 112,52,232
PK 37,38,39
PK 38,44,45
MA 116,52,236
PK 39,46,47
v_pk_max_i16 v36, v36, 0
MA 116,48,228
v_pk_max_i16 v37, v37, 0
v_pk_max_i16 v38, v38, 0
MA 116,250,92
v_pk_max_i16 v39, v39, 0
v_mul_f32 v48, v48, abs(v48)
MA 112,250,92
WL 0
v_mul_f32 v49, v49, abs(v49)
v_mul_f32 v50, v50, abs(v50)
MA 120,56,224
v_mul_f32 v51, v51, abs(v51)
v_mul_f32 v56, v56, abs(v56)
MA 120,60,232
v_lshrrev_b32 v254, 6, v253
v_lshlrev_b32 v254, 7, v254
MA 124,60,236
v_bfe_u32 v255, v253, 4, 2
v_lshl_add_u32 v254, v255, 3, v254
MA 124,56,228
v_add_u32 v254, s57, v254
v_bfe_u32 v255, v253, 7, 2
MA 124,248,92
v_xor_b32 v208, 0, v255
v_lshl_add_u32 v208, v208, 5, v254
MA 120,248,92
v_xor_b32 v209, 1, v255
v_lshl_add_u32 v209, v209, 5, v254
v_xor_b32 v210, 2, v255
v_lshl_add_u32 v210, v210, 5, v254
v_xor_b32 v211, 3, v255
v_lshl_add_u32 v211, v211, 5, v254
s_barrier
TR 0,208,0
TR 2,208,2048
TR 4,208,4096
TR 6,208,6144
TR 8,208,8192
TR 10,208,10240
TR 12,208,12288
TR 14,208,14336
TR 16,208,16384
TR 18,208,18432
TR 20,208,20480
TR 22,208,22528
TR 24,208,24576
TR 26,208,26624
TR 28,208,28672
TR 30,208,30720
v_mul_f32 v57, v57, abs(v57)
v_mul_f32 v58, v58, abs(v58)
v_mul_f32 v59, v59, abs(v59)
PK 48,48,49
PK 49,50,51
PK 50,56,57
PK 51,58,59
v_pk_max_i16 v48, v48, 0
v_pk_max_i16 v49, v49, 0
v_pk_max_i16 v50, v50, 0
v_pk_max_i16 v51, v51, 0
v_mul_f32 v52, v52, abs(v52)
v_mul_f32 v53, v53, abs(v53)
)A"
R"A(v_mul_f32 v54, v54, abs(v54)
v_mul_f32 v55, v55, abs(v55)
v_mul_f32 v60, v60, abs(v60)
v_mul_f32 v61, v61, abs(v61)
v_mul_f32 v62, v62, abs(v62)
v_mul_f32 v63, v63, abs(v63)
PK 52,52,53
PK 53,54,55
PK 54,60,61
PK 55,62,63
v_pk_max_i16 v52, v52, 0
v_pk_max_i16 v53, v53, 0
v_pk_max_i16 v54, v54, 0
v_pk_max_i16 v55, v55, 0
v_mul_f32 v64, v64, abs(v64)
v_mul_f32 v65, v65, abs(v65)
v_mul_f32 v66, v66, abs(v66)
v_mul_f32 v67, v67, abs(v67)
v_mul_f32 v72, v72, abs(v72)
v_mul_f32 v73, v73, abs(v73)
v_mul_f32 v74, v74, abs(v74)
v_mul_f32 v75, v75, abs(v75)
PK 64,64,65
PK 65,66,67
PK 66,72,73
PK 67,74,75
v_pk_max_i16 v64, v64, 0
v_pk_max_i16 v65, v65, 0
v_pk_max_i16 v66, v66, 0
v_pk_max_i16 v67, v67, 0
v_mul_f32 v68, v68, abs(v68)
v_mul_f32 v69, v69, abs(v69)
v_mul_f32 v70, v70, abs(v70)
v_mul_f32 v71, v71, abs(v71)
v_mul_f32 v76, v76, abs(v76)
v_mul_f32 v77, v77, abs(v77)
v_mul_f32 v78, v78, abs(v78)
v_mul_f32 v79, v79, abs(v79)
PK 68,68,69
PK 69,70,71
PK 70,76,77
PK 71,78,79
v_pk_max_i16 v68, v68, 0
v_pk_max_i16 v69, v69, 0
v_pk_max_i16 v70, v70, 0
v_pk_max_i16 v71, v71, 0
v_mul_f32 v80, v80, abs(v80)
v_mul_f32 v81, v81, abs(v81)
v_mul_f32 v82, v82, abs(v82)
v_mul_f32 v83, v83, abs(v83)
v_mul_f32 v88, v88, abs(v88)
v_mul_f32 v89, v89, abs(v89)
v_mul_f32 v90, v90, abs(v90)
v_mul_f32 v91, v91, abs(v91)
PK 80,80,81
PK 81,82,83
PK 82,88,89
PK 83,90,91
v_pk_max_i16 v80, v80, 0
v_pk_max_i16 v81, v81, 0
v_pk_max_i16 v82, v82, 0
v_pk_max_i16 v83, v83, 0
v_mul_f32 v84, v84, abs(v84)
v_mul_f32 v85, v85, abs(v85)
v_mul_f32 v86, v86, abs(v86)
v_mul_f32 v87, v87, abs(v87)
v_mul_f32 v92, v92, abs(v92)
v_mul_f32 v93, v93, abs(v93)
v_mul_f32 v94, v94, abs(v94)
v_mul_f32 v95, v95, abs(v95)
PK 84,84,85
PK 85,86,87
PK 86,92,93
PK 87,94,95
v_pk_max_i16 v84, v84, 0
v_pk_max_i16 v85, v85, 0
v_pk_max_i16 v86, v86, 0
v_pk_max_i16 v87, v87, 0
v_mul_f32 v96, v96, abs(v96)
v_mul_f32 v97, v97, abs(v97)
v_mul_f32 v98, v98, abs(v98)
v_mul_f32 v99, v99, abs(v99)
v_mul_f32 v104, v104, abs(v104)
v_mul_f32 v105, v105, abs(v105)
v_mul_f32 v106, v106, abs(v106)
v_mul_f32 v107, v107, abs(v107)
PK 96,96,97
PK 97,98,99
PK 98,104,105
PK 99,106,107
v_pk_max_i16 v96, v96, 0
v_pk_max_i16 v97, v97, 0
v_pk_max_i16 v98, v98, 0
v_pk_max_i16 v99, v99, 0
v_mul_f32 v100, v100, abs(v100)
v_mul_f32 v101, v101, abs(v101)
v_mul_f32 v102, v102, abs(v102)
v_mul_f32 v103, v103, abs(v103)
v_mul_f32 v108, v108, abs(v108)
v_mul_f32 v109, v109, abs(v109)
v_mul_f32 v110, v110, abs(v110)
v_mul_f32 v111, v111, abs(v111)
PK 100,100,101
PK 101,102,103
PK 102,108,109
PK 103,110,111
v_pk_max_i16 v100, v100, 0
v_pk_max_i16 v101, v101, 0
v_pk_max_i16 v102, v102, 0
v_pk_max_i16 v103, v103, 0
v_mul_f32 v112, v112, abs(v112)
v_mul_f32 v113, v113, abs(v113)
v_mul_f32 v114, v114, abs(v114)
v_mul_f32 v115, v115, abs(v115)
v_mul_f32 v120, v120, abs(v120)
v_mul_f32 v121, v121, abs(v121)
v_mul_f32 v122, v122, abs(v122)
v_mul_f32 v123, v123, abs(v123)
PK 112,112,113
PK 113,114,115
PK 114,120,121
PK 115,122,123
v_pk_max_i16 v112, v112, 0
v_pk_max_i16 v113, v113, 0
v_pk_max_i16 v114, v114, 0
v_pk_max_i16 v115, v115, 0
v_mul_f32 v116, v116, abs(v116)
v_mul_f32 v117, v117, abs(v117)
v_mul_f32 v118, v118, abs(v118)
v_mul_f32 v119, v119, abs(v119)
v_mul_f32 v124, v124, abs(v124)
v_mul_f32 v125, v125, abs(v125)
v_mul_f32 v126, v126, abs(v126)
v_mul_f32 v127, v127, abs(v127)
PK 116,116,117
PK 117,118,119
PK 118,124,125
PK 119,126,127
v_pk_max_i16 v116, v116, 0
v_pk_max_i16 v117, v117, 0
v_pk_max_i16 v118, v118, 0
v_pk_max_i16 v119, v119, 0
WL 8
v_mfma_f32_16x16x32_bf16 v[128:131], a[0:3], v[0:3], 0
s_add_u32 m0, s58, 0
TR 32,209,0
TR 34,209,2048
MB 128,4,16
v_and_b32 v250, 0x380, v253
v_mov_b32 v254, s48
MB 128,8,32
s_lshl_b32 s53, s10, 11
TR 36,209,4096
TR 38,209,6144
MB 128,12,48
WL 4
TR 40,209,8192
TR 42,209,10240
MB 128,16,64
s_add_u32 s53, s53, 0x6fc0000
TR 44,209,12288
TR 46,209,14336
MB 128,20,80
buffer_load_dwordx4 v252, s[12:15], s59 offen lds
MB 128,24,96
s_add_u32 s59, s59, 28672
TR 48,209,16384
TR 50,209,18432
MB 128,28,112
s_add_u32 m0, s58, 4096
TR 52,209,20480
TR 54,209,22528
v_mfma_f32_16x16x32_bf16 v[132:135], a[0:3], v[4:7], 0
s_lshl_b32 s74, s10, 3
TR 56,209,24576
TR 58,209,26624
MB 132,4,20
buffer_load_dwordx4 v252, s[12:15], s59 offen lds
MB 132,8,36
s_add_u32 s59, s59, 28672
TR 60,209,28672
TR 62,209,30720
MB 132,12,52
s_add_u32 m0, s58, 8192
v_mov_b32 v255, s49
MB 132,16,68
buffer_store_dwordx4 v[216:219], v253, s[20:23], s51 offen offset:2048 sc1
MB 132,20,84
s_add_u32 s6, s57, 32768
MB 132,24,100
s_add_u32 s74, s74, s11
s_lshl_b32 s54, s74, 8
s_add_u32 s54, s54, 0x6fc8000
MB 132,28,116
WL 8
s_add_u32 s55, s54, 128
s_lshl_b32 s74, s74, 2
v_mfma_f32_16x16x32_bf16 v[136:139], a[32:35], v[0:3], 0
s_sub_u32 s61, s57, 131072
TR 0,210,0
TR 2,210,2048
MB 136,36,16
buffer_load_dwordx4 v252, s[12:15], s59 offen lds
MB 136,40,32
s_add_u32 s59, s59, 28672
TR 4,210,4096
TR 6,210,6144
MB 136,44,48
WL 4
TR 8,210,8192
TR 10,210,10240
MB 136,48,64
s_add_u32 m0, s58, 12288
TR 12,210,12288
TR 14,210,14336
MB 136,52,80
buffer_load_dwordx4 v252, s[12:15], s59 offen lds
MB 136,56,96
s_add_u32 s59, s59, 28672
TR 16,210,16384
TR 18,210,18432
MB 136,60,112
s_add_u32 m0, s58, 16384
TR 20,210,20480
TR 22,210,22528
PK 160,128,129
v_mfma_f32_16x16x32_bf16 v[140:143], a[32:35], v[4:7], 0
s_min_u32 s6, s6, s61
TR 24,210,24576
TR 26,210,26624
MB 140,36,20
buffer_store_dwordx4 v[220:223], v253, s[20:23], s51 offen offset:3072 sc1
MB 140,40,36
s_sub_u32 s61, s6, s57
TR 28,210,28672
TR 30,210,30720
MB 140,44,52
s_mov_b32 s57, s6
v_add_u32 v208, s61, v208
v_add_u32 v209, s61, v209
MB 140,48,68
s_add_u32 s74, s74, s8
PK 161,130,131
PK 164,132,133
PK 165,134,135
MB 140,52,84
buffer_load_dwordx4 v252, s[12:15], s59 offen lds
MB 140,56,100
s_lshl_b32 s75, s74, 14
v_add_u32 v210, s61, v210
v_accvgpr_write_b32 a128, v160
MB 140,60,116
WL 8
v_accvgpr_write_b32 a129, v161
v_accvgpr_write_b32 a132, v164
v_mfma_f32_16x16x32_bf16 v[144:147], a[0:3], v[0:3], 0
s_add_u32 s59, s59, 28672
TR 32,211,0
TR 34,211,2048
s_add_u32 m0, s58, 20480
MB 144,4,16
buffer_load_dwordx4 v252, s[12:15], s59 offen lds
MB 144,8,32
s_add_u32 s59, s59, 28672
TR 36,211,4096
TR 38,211,6144
v_accvgpr_write_b32 a133, v165
MB 144,12,48
WL 4
TR 40,211,8192
TR 42,211,10240
MB 144,16,64
s_add_u32 m0, s58, 24576
TR 44,211,12288
TR 46,211,14336
MB 144,20,80
s_add_u32 s75, s75, 0x2400000
PK 162,136,137
PK 163,138,139
MB 144,24,96
TR 48,211,16384
TR 50,211,18432
MB 144,28,112
TR 52,211,20480
TR 54,211,22528
v_mfma_f32_16x16x32_bf16 v[148:151], a[0:3], v[4:7], 0
TR 56,211,24576
TR 58,211,26624
MB 148,4,20
buffer_load_dwordx4 v252, s[12:15], s59 offen lds
MB 148,8,36
s_add_u32 s59, s59, 28672
TR 60,211,28672
TR 62,211,30720
MB 148,12,52
s_add_u32 m0, s58, 28672
PK 166,140,141
PK 167,142,143
MB 148,16,68
s_add_u32 s51, s73, 81920
v_accvgpr_write_b32 a130, v162
v_accvgpr_write_b32 a131, v163
MB 148,20,84
buffer_load_dwordx4 v252, s[12:15], s59 offen lds
MB 148,24,100
buffer_store_dwordx4 v[224:227], v253, s[20:23], s51 offen offset:0 sc1
MB 148,28,116
s_waitcnt vmcnt(23) lgkmcnt(8)
s_barrier
v_add_u32 v211, s61, v211
v_mfma_f32_16x16x32_bf16 v[152:155], a[32:35], v[0:3], 0
s_add_u32 s59, s59, 0xfffcf080
TR 0,208,0
TR 2,208,2048
MB 152,36,16
v_accvgpr_write_b32 a134, v166
v_accvgpr_write_b32 a135, v167
MB 152,40,32
s_add_u32 s7, s58, 32768
TR 4,208,4096
TR 6,208,6144
MB 152,44,48
WL 4
TR 8,208,8192
TR 10,208,10240
MB 152,48,64
s_sub_u32 s58, s58, 131072
TR 12,208,12288
TR 14,208,14336
MB 152,52,80
PK 168,144,145
PK 169,146,147
MB 152,56,96
s_min_u32 s58, s7, s58
TR 16,208,16384
TR 18,208,18432
MB 152,60,112
TR 20,208,20480
TR 22,208,22528
v_mfma_f32_16x16x32_bf16 v[156:159], a[32:35], v[4:7], 0
TR 24,208,24576
TR 26,208,26624
MB 156,36,20
PK 172,148,149
PK 173,150,151
MB 156,40,36
TR 28,208,28672
TR 30,208,30720
MB 156,44,52
v_accvgpr_write_b32 a136, v168
v_accvgpr_write_b32 a137, v169
MB 156,48,68
v_accvgpr_write_b32 a140, v172
v_accvgpr_write_b32 a141, v173
MB 156,52,84
MB 156,56,100
MB 156,60,116
WL 8
v_mfma_f32_16x16x32_bf16 v[128:131], a[0:3], v[0:3], 0
s_add_u32 m0, s58, 0
TR 32,209,0
TR 34,209,2048
MB 128,4,16
MB 128,8,32
TR 36,209,4096
TR 38,209,6144
MB 128,12,48
WL 4
TR 40,209,8192
TR 42,209,10240
MB 128,16,64
TR 44,209,12288
TR 46,209,14336
MB 128,20,80
buffer_load_dwordx4 v252, s[12:15], s59 offen lds
MB 128,24,96
s_add_u32 s59, s59, 28672
TR 48,209,16384
TR 50,209,18432
MB 128,28,112
s_add_u32 m0, s58, 4096
TR 52,209,20480
TR 54,209,22528
PK 170,152,153
v_mfma_f32_16x16x32_bf16 v[132:135], a[0:3], v[4:7], 0
TR 56,209,24576
TR 58,209,26624
MB 132,4,20
buffer_load_dwordx4 v252, s[12:15], s59 offen lds
MB 132,8,36
s_add_u32 s59, s59, 28672
TR 60,209,28672
TR 62,209,30720
MB 132,12,52
s_add_u32 m0, s58, 8192
PK 171,154,155
PK 174,156,157
MB 132,16,68
buffer_store_dwordx4 v[228:231], v253, s[20:23], s51 offen offset:1024 sc1
MB 132,20,84
buffer_store_dwordx4 v[232:235], v253, s[20:23], s51 offen offset:2048 sc1
MB 132,24,100
s_add_u32 s6, s57, 32768
PK 175,158,159
v_accvgpr_write_b32 a138, v170
MB 132,28,116
WL 8
v_accvgpr_write_b32 a139, v171
v_accvgpr_write_b32 a142, v174
v_mfma_f32_16x16x32_bf16 v[136:139], a[32:35], v[0:3], 0
s_sub_u32 s61, s57, 131072
TR 0,210,0
TR 2,210,2048
v_accvgpr_write_b32 a143, v175
MB 136,36,16
buffer_load_dwordx4 v252, s[12:15], s59 offen lds
MB 136,40,32
s_add_u32 s59, s59, 28672
TR 4,210,4096
TR 6,210,6144
MB 136,44,48
WL 4
TR 8,210,8192
TR 10,210,10240
MB 136,48,64
s_add_u32 m0, s58, 12288
TR 12,210,12288
TR 14,210,14336
MB 136,52,80
buffer_load_dwordx4 v252, s[12:15], s59 offen lds
MB 136,56,96
s_add_u32 s59, s59, 28672
TR 16,210,16384
TR 18,210,18432
MB 136,60,112
s_add_u32 m0, s58, 16384
TR 20,210,20480
TR 22,210,22528
PK 160,128,129
v_mfma_f32_16x16x32_bf16 v[140:143], a[32:35], v[4:7], 0
s_min_u32 s6, s6, s61
TR 24,210,24576
TR 26,210,26624
MB 140,36,20
buffer_store_dwordx4 v[236:239], v253, s[20:23], s51 offen offset:3072 sc1
MB 140,40,36
s_sub_u32 s61, s6, s57
TR 28,210,28672
TR 30,210,30720
MB 140,44,52
s_mov_b32 s57, s6
v_add_u32 v208, s61, v208
v_add_u32 v209, s61, v209
MB 140,48,68
PK 161,130,131
PK 164,132,133
PK 165,134,135
MB 140,52,84
buffer_load_dwordx4 v252, s[12:15], s59 offen lds
MB 140,56,100
v_add_u32 v210, s61, v210
v_accvgpr_write_b32 a144, v160
MB 140,60,116
WL 8
v_accvgpr_write_b32 a145, v161
v_accvgpr_write_b32 a148, v164
v_mfma_f32_16x16x32_bf16 v[144:147], a[0:3], v[0:3], 0
s_add_u32 s59, s59, 28672
TR 32,211,0
TR 34,211,2048
s_add_u32 m0, s58, 20480
MB 144,4,16
buffer_load_dwordx4 v252, s[12:15], s59 offen lds
MB 144,8,32
s_add_u32 s59, s59, 28672
TR 36,211,4096
TR 38,211,6144
v_accvgpr_write_b32 a149, v165
MB 144,12,48
WL 4
TR 40,211,8192
TR 42,211,10240
MB 144,16,64
s_add_u32 m0, s58, 24576
TR 44,211,12288
TR 46,211,14336
MB 144,20,80
PK 162,136,137
PK 163,138,139
MB 144,24,96
TR 48,211,16384
TR 50,211,18432
MB 144,28,112
TR 52,211,20480
TR 54,211,22528
v_mfma_f32_16x16x32_bf16 v[148:151], a[0:3], v[4:7], 0
TR 56,211,24576
TR 58,211,26624
MB 148,4,20
buffer_load_dwordx4 v252, s[12:15], s59 offen lds
MB 148,8,36
s_add_u32 s59, s59, 28672
TR 60,211,28672
TR 62,211,30720
MB 148,12,52
s_add_u32 m0, s58, 28672
PK 166,140,141
PK 167,142,143
MB 148,16,68
v_accvgpr_write_b32 a146, v162
v_accvgpr_write_b32 a147, v163
MB 148,20,84
buffer_load_dwordx4 v252, s[12:15], s59 offen lds
MB 148,24,100
s_lshl_b32 s72, s11, 7
v_add_u32 v211, s61, v211
v_accvgpr_write_b32 a150, v166
MB 148,28,116
s_waitcnt vmcnt(22) lgkmcnt(8)
s_barrier
v_accvgpr_write_b32 a151, v167
v_mfma_f32_16x16x32_bf16 v[152:155], a[32:35], v[0:3], 0
s_add_u32 s59, s59, 0xfffcf080
TR 0,208,0
TR 2,208,2048
MB 152,36,16
s_add_u32 s72, s72, s53
MB 152,40,32
s_add_u32 s7, s58, 32768
TR 4,208,4096
TR 6,208,6144
MB 152,44,48
WL 4
TR 8,208,8192
TR 10,208,10240
MB 152,48,64
s_sub_u32 s58, s58, 131072
TR 12,208,12288
TR 14,208,14336
MB 152,52,80
s_add_u32 s72, s72, 1024
PK 168,144,145
PK 169,146,147
MB 152,56,96
s_min_u32 s58, s7, s58
TR 16,208,16384
TR 18,208,18432
MB 152,60,112
s_min_u32 s50, s8, 1
TR 20,208,20480
)A"
R"A(TR 22,208,22528
v_mfma_f32_16x16x32_bf16 v[156:159], a[32:35], v[4:7], 0
s_lshl_b32 s50, s50, 30
TR 24,208,24576
TR 26,208,26624
MB 156,36,20
PK 172,148,149
PK 173,150,151
MB 156,40,36
TR 28,208,28672
TR 30,208,30720
MB 156,44,52
v_accvgpr_write_b32 a152, v168
v_accvgpr_write_b32 a153, v169
MB 156,48,68
v_accvgpr_write_b32 a156, v172
v_accvgpr_write_b32 a157, v173
MB 156,52,84
MB 156,56,100
MB 156,60,116
WL 8
v_mfma_f32_16x16x32_bf16 v[128:131], a[0:3], v[0:3], 0
s_add_u32 m0, s58, 0
TR 32,209,0
TR 34,209,2048
MB 128,4,16
v_bfe_u32 v251, v253, 4, 4
v_lshl_add_u32 v251, v251, 3, s50
MB 128,8,32
TR 36,209,4096
TR 38,209,6144
MB 128,12,48
WL 4
TR 40,209,8192
TR 42,209,10240
MB 128,16,64
TR 44,209,12288
TR 46,209,14336
MB 128,20,80
buffer_load_dwordx4 v252, s[12:15], s59 offen lds
MB 128,24,96
s_add_u32 s59, s59, 28672
TR 48,209,16384
TR 50,209,18432
MB 128,28,112
s_add_u32 m0, s58, 4096
TR 52,209,20480
TR 54,209,22528
v_mfma_f32_16x16x32_bf16 v[132:135], a[0:3], v[4:7], 0
TR 56,209,24576
TR 58,209,26624
MB 132,4,20
buffer_load_dwordx4 v252, s[12:15], s59 offen lds
MB 132,8,36
s_add_u32 s59, s59, 28672
TR 60,209,28672
TR 62,209,30720
MB 132,12,52
s_add_u32 m0, s58, 8192
PK 170,152,153
PK 171,154,155
MB 132,16,68
s_add_u32 s6, s57, 32768
PK 174,156,157
PK 175,158,159
MB 132,20,84
v_accvgpr_write_b32 a154, v170
v_accvgpr_write_b32 a155, v171
MB 132,24,100
v_accvgpr_write_b32 a158, v174
v_accvgpr_write_b32 a159, v175
MB 132,28,116
WL 8
v_mfma_f32_16x16x32_bf16 v[136:139], a[32:35], v[0:3], 0
s_sub_u32 s61, s57, 131072
TR 0,210,0
TR 2,210,2048
MB 136,36,16
buffer_load_dwordx4 v252, s[12:15], s59 offen lds
MB 136,40,32
s_add_u32 s59, s59, 28672
TR 4,210,4096
TR 6,210,6144
MB 136,44,48
WL 4
TR 8,210,8192
TR 10,210,10240
MB 136,48,64
s_add_u32 m0, s58, 12288
TR 12,210,12288
TR 14,210,14336
MB 136,52,80
buffer_load_dwordx4 v252, s[12:15], s59 offen lds
MB 136,56,96
s_add_u32 s59, s59, 28672
TR 16,210,16384
TR 18,210,18432
MB 136,60,112
s_add_u32 m0, s58, 16384
TR 20,210,20480
TR 22,210,22528
v_mfma_f32_16x16x32_bf16 v[140:143], a[32:35], v[4:7], 0
s_min_u32 s6, s6, s61
TR 24,210,24576
TR 26,210,26624
MB 140,36,20
PK 160,128,129
PK 161,130,131
MB 140,40,36
s_sub_u32 s61, s6, s57
TR 28,210,28672
TR 30,210,30720
MB 140,44,52
s_mov_b32 s57, s6
v_add_u32 v208, s61, v208
v_add_u32 v209, s61, v209
PK 164,132,133
MB 140,48,68
PK 165,134,135
v_accvgpr_write_b32 a160, v160
MB 140,52,84
buffer_load_dwordx4 v252, s[12:15], s59 offen lds
MB 140,56,100
v_add_u32 v210, s61, v210
v_accvgpr_write_b32 a161, v161
MB 140,60,116
WL 8
v_accvgpr_write_b32 a164, v164
v_accvgpr_write_b32 a165, v165
v_mfma_f32_16x16x32_bf16 v[144:147], a[0:3], v[0:3], 0
s_add_u32 s59, s59, 28672
TR 32,211,0
TR 34,211,2048
s_add_u32 m0, s58, 20480
MB 144,4,16
buffer_load_dwordx4 v252, s[12:15], s59 offen lds
MB 144,8,32
s_add_u32 s59, s59, 28672
TR 36,211,4096
TR 38,211,6144
MB 144,12,48
WL 4
TR 40,211,8192
TR 42,211,10240
MB 144,16,64
s_add_u32 m0, s58, 24576
TR 44,211,12288
TR 46,211,14336
MB 144,20,80
PK 162,136,137
PK 163,138,139
MB 144,24,96
TR 48,211,16384
TR 50,211,18432
MB 144,28,112
TR 52,211,20480
TR 54,211,22528
v_mfma_f32_16x16x32_bf16 v[148:151], a[0:3], v[4:7], 0
TR 56,211,24576
TR 58,211,26624
MB 148,4,20
buffer_load_dwordx4 v252, s[12:15], s59 offen lds
MB 148,8,36
s_add_u32 s59, s59, 28672
TR 60,211,28672
TR 62,211,30720
MB 148,12,52
s_add_u32 m0, s58, 28672
PK 166,140,141
PK 167,142,143
MB 148,16,68
v_accvgpr_write_b32 a162, v162
v_accvgpr_write_b32 a163, v163
MB 148,20,84
buffer_load_dwordx4 v252, s[12:15], s59 offen lds
MB 148,24,100
v_add_u32 v211, s61, v211
v_accvgpr_write_b32 a166, v166
MB 148,28,116
s_waitcnt vmcnt(12) lgkmcnt(8)
s_barrier
v_accvgpr_write_b32 a167, v167
v_mfma_f32_16x16x32_bf16 v[152:155], a[32:35], v[0:3], 0
s_add_u32 s59, s59, 0xfffcf080
TR 0,208,0
TR 2,208,2048
MB 152,36,16
buffer_store_dwordx2 v[254:255], v251, s[20:23], s72 offen offset:0 sc1
MB 152,40,32
s_add_u32 s7, s58, 32768
TR 4,208,4096
TR 6,208,6144
MB 152,44,48
WL 4
TR 8,208,8192
TR 10,208,10240
MB 152,48,64
s_sub_u32 s58, s58, 131072
TR 12,208,12288
TR 14,208,14336
MB 152,52,80
PK 168,144,145
PK 169,146,147
MB 152,56,96
s_min_u32 s58, s7, s58
TR 16,208,16384
TR 18,208,18432
MB 152,60,112
TR 20,208,20480
TR 22,208,22528
v_mfma_f32_16x16x32_bf16 v[156:159], a[32:35], v[4:7], 0
TR 24,208,24576
TR 26,208,26624
MB 156,36,20
buffer_load_dwordx2 v[248:249], v250, s[20:23], s53 offen sc1
MB 156,40,36
TR 28,208,28672
TR 30,208,30720
MB 156,44,52
PK 172,148,149
PK 173,150,151
MB 156,48,68
v_accvgpr_write_b32 a168, v168
v_accvgpr_write_b32 a169, v169
MB 156,52,84
v_accvgpr_write_b32 a172, v172
v_accvgpr_write_b32 a173, v173
MB 156,56,100
MB 156,60,116
WL 8
v_mfma_f32_16x16x32_bf16 v[128:131], a[0:3], v[0:3], 0
s_add_u32 m0, s58, 0
TR 32,209,0
TR 34,209,2048
MB 128,4,16
MB 128,8,32
TR 36,209,4096
TR 38,209,6144
MB 128,12,48
WL 4
TR 40,209,8192
TR 42,209,10240
MB 128,16,64
TR 44,209,12288
TR 46,209,14336
MB 128,20,80
buffer_load_dwordx4 v252, s[12:15], s59 offen lds
MB 128,24,96
s_add_u32 s59, s59, 28672
TR 48,209,16384
TR 50,209,18432
MB 128,28,112
s_add_u32 m0, s58, 4096
TR 52,209,20480
TR 54,209,22528
v_mfma_f32_16x16x32_bf16 v[132:135], a[0:3], v[4:7], 0
TR 56,209,24576
TR 58,209,26624
MB 132,4,20
buffer_load_dwordx4 v252, s[12:15], s59 offen lds
MB 132,8,36
s_add_u32 s59, s59, 28672
TR 60,209,28672
TR 62,209,30720
MB 132,12,52
s_add_u32 m0, s58, 8192
PK 170,152,153
PK 171,154,155
MB 132,16,68
s_add_u32 s6, s57, 32768
PK 174,156,157
PK 175,158,159
MB 132,20,84
v_accvgpr_write_b32 a170, v170
v_accvgpr_write_b32 a171, v171
MB 132,24,100
v_accvgpr_write_b32 a174, v174
v_accvgpr_write_b32 a175, v175
MB 132,28,116
WL 8
v_mfma_f32_16x16x32_bf16 v[136:139], a[32:35], v[0:3], 0
s_sub_u32 s61, s57, 131072
TR 0,210,0
TR 2,210,2048
MB 136,36,16
buffer_load_dwordx4 v252, s[12:15], s59 offen lds
MB 136,40,32
s_add_u32 s59, s59, 28672
TR 4,210,4096
TR 6,210,6144
MB 136,44,48
WL 4
TR 8,210,8192
TR 10,210,10240
MB 136,48,64
s_add_u32 m0, s58, 12288
TR 12,210,12288
TR 14,210,14336
MB 136,52,80
buffer_load_dwordx4 v252, s[12:15], s59 offen lds
MB 136,56,96
s_add_u32 s59, s59, 28672
TR 16,210,16384
TR 18,210,18432
MB 136,60,112
s_add_u32 m0, s58, 16384
TR 20,210,20480
TR 22,210,22528
v_mfma_f32_16x16x32_bf16 v[140:143], a[32:35], v[4:7], 0
s_min_u32 s6, s6, s61
TR 24,210,24576
TR 26,210,26624
MB 140,36,20
PK 160,128,129
PK 161,130,131
MB 140,40,36
s_sub_u32 s61, s6, s57
TR 28,210,28672
TR 30,210,30720
MB 140,44,52
s_mov_b32 s57, s6
v_add_u32 v208, s61, v208
v_add_u32 v209, s61, v209
PK 164,132,133
MB 140,48,68
PK 165,134,135
v_accvgpr_write_b32 a176, v160
MB 140,52,84
buffer_load_dwordx4 v252, s[12:15], s59 offen lds
MB 140,56,100
v_add_u32 v210, s61, v210
v_accvgpr_write_b32 a177, v161
MB 140,60,116
WL 8
v_accvgpr_write_b32 a180, v164
v_accvgpr_write_b32 a181, v165
v_mfma_f32_16x16x32_bf16 v[144:147], a[0:3], v[0:3], 0
s_add_u32 s59, s59, 28672
TR 32,211,0
TR 34,211,2048
s_add_u32 m0, s58, 20480
MB 144,4,16
buffer_load_dwordx4 v252, s[12:15], s59 offen lds
MB 144,8,32
s_add_u32 s59, s59, 28672
TR 36,211,4096
TR 38,211,6144
MB 144,12,48
WL 4
TR 40,211,8192
TR 42,211,10240
MB 144,16,64
s_add_u32 m0, s58, 24576
TR 44,211,12288
TR 46,211,14336
MB 144,20,80
PK 162,136,137
PK 163,138,139
MB 144,24,96
TR 48,211,16384
TR 50,211,18432
MB 144,28,112
TR 52,211,20480
TR 54,211,22528
v_mfma_f32_16x16x32_bf16 v[148:151], a[0:3], v[4:7], 0
TR 56,211,24576
TR 58,211,26624
MB 148,4,20
buffer_load_dwordx4 v252, s[12:15], s59 offen lds
MB 148,8,36
s_add_u32 s59, s59, 28672
TR 60,211,28672
TR 62,211,30720
MB 148,12,52
s_add_u32 m0, s58, 28672
PK 166,140,141
PK 167,142,143
MB 148,16,68
v_accvgpr_write_b32 a178, v162
v_accvgpr_write_b32 a179, v163
MB 148,20,84
buffer_load_dwordx4 v252, s[12:15], s59 offen lds
MB 148,24,100
v_add_u32 v252, s56, v253
v_add_u32 v211, s61, v211
MB 148,28,116
s_waitcnt vmcnt(18) lgkmcnt(8)
s_barrier
v_accvgpr_write_b32 a182, v166
v_mfma_f32_16x16x32_bf16 v[152:155], a[32:35], v[0:3], 0
s_mov_b32 s12, s46
TR 0,208,0
TR 2,208,2048
MB 152,36,16
v_accvgpr_write_b32 a183, v167
MB 152,40,32
s_and_b32 s13, s47, 0xffff
TR 4,208,4096
TR 6,208,6144
MB 152,44,48
WL 4
TR 8,208,8192
TR 10,208,10240
MB 152,48,64
s_mov_b32 s14, 134217728
TR 12,208,12288
TR 14,208,14336
MB 152,52,80
PK 168,144,145
PK 169,146,147
MB 152,56,96
s_lshl_b32 s59, s10, 21
TR 16,208,16384
TR 18,208,18432
MB 152,60,112
s_add_u32 s7, s58, 32768
TR 20,208,20480
TR 22,208,22528
v_mfma_f32_16x16x32_bf16 v[156:159], a[32:35], v[4:7], 0
s_sub_u32 s58, s58, 131072
TR 24,208,24576
TR 26,208,26624
MB 156,36,20
PK 172,148,149
PK 173,150,151
MB 156,40,36
s_min_u32 s58, s7, s58
TR 28,208,28672
TR 30,208,30720
MB 156,44,52
v_accvgpr_write_b32 a184, v168
v_accvgpr_write_b32 a185, v169
MB 156,48,68
v_accvgpr_write_b32 a188, v172
v_accvgpr_write_b32 a189, v173
MB 156,52,84
MB 156,56,100
MB 156,60,116
WL 8
s_waitcnt vmcnt(8)
v_cmp_ne_u64 vcc, s[48:49], v[248:249]
s_cbranch_vccnz .LPS7
.LPR7:
s_add_u32 s53, s53, 1024
v_mfma_f32_16x16x32_bf16 v[128:131], a[0:3], v[0:3], 0
s_add_u32 s63, s59, s62
TR 32,209,0
TR 34,209,2048
MB 128,4,16
buffer_load_dwordx2 v[176:177], off, s[20:23], s54 sc1
MB 128,8,32
s_add_u32 m0, s58, s62
TR 36,209,4096
TR 38,209,6144
MB 128,12,48
WL 4
TR 40,209,8192
TR 42,209,10240
MB 128,16,64
TR 44,209,12288
TR 46,209,14336
MB 128,20,80
buffer_load_dwordx4 v252, s[12:15], s63 offen offset:0 sc1 lds
MB 128,24,96
s_add_u32 s59, s59, 0x10000
TR 48,209,16384
TR 50,209,18432
MB 128,28,112
s_add_u32 s7, s58, 32768
TR 52,209,20480
TR 54,209,22528
v_mfma_f32_16x16x32_bf16 v[132:135], a[0:3], v[4:7], 0
s_sub_u32 s58, s58, 131072
TR 56,209,24576
TR 58,209,26624
MB 132,4,20
buffer_load_dwordx4 v252, s[12:15], s63 offen offset:1024 sc1 lds
MB 132,8,36
s_min_u32 s58, s7, s58
TR 60,209,28672
TR 62,209,30720
MB 132,12,52
s_add_u32 s6, s57, 32768
PK 170,152,153
PK 171,154,155
MB 132,16,68
PK 174,156,157
PK 175,158,159
MB 132,20,84
v_accvgpr_write_b32 a186, v170
v_accvgpr_write_b32 a187, v171
MB 132,24,100
v_accvgpr_write_b32 a190, v174
v_accvgpr_write_b32 a191, v175
MB 132,28,116
WL 8
v_mfma_f32_16x16x32_bf16 v[136:139], a[32:35], v[0:3], 0
s_sub_u32 s61, s57, 131072
TR 0,210,0
TR 2,210,2048
MB 136,36,16
buffer_load_dwordx4 v252, s[12:15], s63 offen offset:2048 sc1 lds
MB 136,40,32
s_min_u32 s6, s6, s61
TR 4,210,4096
TR 6,210,6144
MB 136,44,48
WL 4
TR 8,210,8192
TR 10,210,10240
MB 136,48,64
s_sub_u32 s61, s6, s57
TR 12,210,12288
TR 14,210,14336
MB 136,52,80
buffer_load_dwordx4 v252, s[12:15], s63 offen offset:3072 sc1 lds
MB 136,56,96
s_add_u32 s63, s63, 4096
TR 16,210,16384
TR 18,210,18432
MB 136,60,112
s_add_u32 m0, m0, 4096
TR 20,210,20480
TR 22,210,22528
v_mfma_f32_16x16x32_bf16 v[140:143], a[32:35], v[4:7], 0
s_mov_b32 s57, s6
TR 24,210,24576
TR 26,210,26624
MB 140,36,20
s_getpc_b64 s[98:99]
.LCT2:
v_add_u32 v208, s61, v208
v_add_u32 v209, s61, v209
MB 140,40,36
s_lshr_b32 s78, s9, 4
TR 28,210,28672
TR 30,210,30720
MB 140,44,52
PK 160,128,129
PK 161,130,131
PK 164,132,133
MB 140,48,68
PK 165,134,135
v_accvgpr_write_b32 a192, v160
MB 140,52,84
buffer_load_dwordx4 v252, s[12:15], s63 offen offset:0 sc1 lds
MB 140,56,100
s_lshl_b32 s78, s78, 12
v_add_u32 v210, s61, v210
v_accvgpr_write_b32 a193, v161
MB 140,60,116
WL 8
v_accvgpr_write_b32 a196, v164
v_accvgpr_write_b32 a197, v165
v_mfma_f32_16x16x32_bf16 v[144:147], a[0:3], v[0:3], 0
s_add_u32 s78, s78, s56
TR 32,211,0
TR 34,211,2048
MB 144,4,16
buffer_load_dwordx4 v252, s[12:15], s63 offen offset:1024 sc1 lds
MB 144,8,32
s_min_u32 s78, s78, .LCEND-.LCT2-1024
TR 36,211,4096
TR 38,211,6144
MB 144,12,48
WL 4
TR 40,211,8192
TR 42,211,10240
MB 144,16,64
TR 44,211,12288
TR 46,211,14336
MB 144,20,80
v_min_u32 v184, 0x70, v253
v_lshl_add_u32 v184, v184, 3, s78
MB 144,24,96
TR 48,211,16384
TR 50,211,18432
MB 144,28,112
TR 52,211,20480
TR 54,211,22528
PK 162,136,137
v_mfma_f32_16x16x32_bf16 v[148:151], a[0:3], v[4:7], 0
TR 56,211,24576
TR 58,211,26624
MB 148,4,20
buffer_load_dwordx4 v252, s[12:15], s63 offen offset:2048 sc1 lds
MB 148,8,36
TR 60,211,28672
)A"
R"A(TR 62,211,30720
MB 148,12,52
global_load_dword v184, v184, s[98:99]
MB 148,16,68
PK 163,138,139
PK 166,140,141
MB 148,20,84
buffer_load_dwordx4 v252, s[12:15], s63 offen offset:3072 sc1 lds
MB 148,24,100
v_add_u32 v211, s61, v211
PK 167,142,143
MB 148,28,116
WL 8
s_barrier
v_accvgpr_write_b32 a194, v162
v_mfma_f32_16x16x32_bf16 v[152:155], a[32:35], v[0:3], 0
TR 0,208,0
TR 2,208,2048
v_accvgpr_write_b32 a195, v163
MB 152,36,16
v_accvgpr_write_b32 a198, v166
v_accvgpr_write_b32 a199, v167
MB 152,40,32
TR 4,208,4096
TR 6,208,6144
MB 152,44,48
WL 4
TR 8,208,8192
TR 10,208,10240
MB 152,48,64
TR 12,208,12288
TR 14,208,14336
MB 152,52,80
PK 168,144,145
PK 169,146,147
MB 152,56,96
TR 16,208,16384
TR 18,208,18432
MB 152,60,112
TR 20,208,20480
TR 22,208,22528
v_mfma_f32_16x16x32_bf16 v[156:159], a[32:35], v[4:7], 0
TR 24,208,24576
TR 26,208,26624
MB 156,36,20
PK 172,148,149
PK 173,150,151
MB 156,40,36
TR 28,208,28672
TR 30,208,30720
MB 156,44,52
buffer_load_dwordx2 v[248:249], v250, s[20:23], s53 offen sc1
MB 156,48,68
v_accvgpr_write_b32 a200, v168
v_accvgpr_write_b32 a201, v169
MB 156,52,84
v_accvgpr_write_b32 a204, v172
v_accvgpr_write_b32 a205, v173
MB 156,56,100
MB 156,60,116
WL 8
s_waitcnt vmcnt(10)
v_cmp_ne_u64 vcc, s[48:49], v[176:177]
s_cbranch_vccnz .LPS8
.LPR8:
v_mfma_f32_16x16x32_bf16 v[128:131], a[0:3], v[0:3], 0
s_add_u32 s63, s59, s62
TR 32,209,0
TR 34,209,2048
s_add_u32 s52, s75, 0
MB 128,4,16
buffer_load_dwordx4 a[64:67], v253, s[20:23], s52 offen offset:0 sc1
MB 128,8,32
s_add_u32 m0, s58, s62
TR 36,209,4096
TR 38,209,6144
MB 128,12,48
WL 4
TR 40,209,8192
TR 42,209,10240
MB 128,16,64
TR 44,209,12288
TR 46,209,14336
MB 128,20,80
buffer_load_dwordx4 v252, s[12:15], s63 offen offset:0 sc1 lds
MB 128,24,96
s_add_u32 s59, s59, 0xffff8000
TR 48,209,16384
TR 50,209,18432
MB 128,28,112
s_add_u32 s7, s58, 32768
TR 52,209,20480
TR 54,209,22528
v_mfma_f32_16x16x32_bf16 v[132:135], a[0:3], v[4:7], 0
s_sub_u32 s58, s58, 131072
TR 56,209,24576
TR 58,209,26624
MB 132,4,20
buffer_load_dwordx4 v252, s[12:15], s63 offen offset:1024 sc1 lds
MB 132,8,36
s_min_u32 s58, s7, s58
TR 60,209,28672
TR 62,209,30720
MB 132,12,52
buffer_load_dwordx4 a[68:71], v253, s[20:23], s52 offen offset:1024 sc1
MB 132,16,68
s_add_u32 s6, s57, 32768
PK 170,152,153
PK 171,154,155
MB 132,20,84
PK 174,156,157
PK 175,158,159
MB 132,24,100
v_accvgpr_write_b32 a202, v170
v_accvgpr_write_b32 a203, v171
MB 132,28,116
WL 8
v_accvgpr_write_b32 a206, v174
v_accvgpr_write_b32 a207, v175
v_mfma_f32_16x16x32_bf16 v[136:139], a[32:35], v[0:3], 0
s_sub_u32 s61, s57, 131072
TR 0,210,0
TR 2,210,2048
MB 136,36,16
buffer_load_dwordx4 v252, s[12:15], s63 offen offset:2048 sc1 lds
MB 136,40,32
s_min_u32 s6, s6, s61
TR 4,210,4096
TR 6,210,6144
MB 136,44,48
WL 4
TR 8,210,8192
TR 10,210,10240
MB 136,48,64
s_sub_u32 s61, s6, s57
TR 12,210,12288
TR 14,210,14336
MB 136,52,80
buffer_load_dwordx4 v252, s[12:15], s63 offen offset:3072 sc1 lds
MB 136,56,96
s_add_u32 s63, s63, 4096
TR 16,210,16384
TR 18,210,18432
PK 160,128,129
MB 136,60,112
s_add_u32 m0, m0, 4096
TR 20,210,20480
TR 22,210,22528
v_mfma_f32_16x16x32_bf16 v[140:143], a[32:35], v[4:7], 0
s_mov_b32 s57, s6
TR 24,210,24576
TR 26,210,26624
PK 161,130,131
MB 140,36,20
buffer_load_dwordx4 a[72:75], v253, s[20:23], s52 offen offset:2048 sc1
MB 140,40,36
TR 28,210,28672
TR 30,210,30720
MB 140,44,52
v_add_u32 v208, s61, v208
v_add_u32 v209, s61, v209
PK 164,132,133
MB 140,48,68
PK 165,134,135
v_accvgpr_write_b32 a208, v160
MB 140,52,84
buffer_load_dwordx4 v252, s[12:15], s63 offen offset:0 sc1 lds
MB 140,56,100
buffer_load_dwordx4 a[76:79], v253, s[20:23], s52 offen offset:3072 sc1
MB 140,60,116
WL 8
v_add_u32 v210, s61, v210
v_accvgpr_write_b32 a209, v161
v_accvgpr_write_b32 a212, v164
v_mfma_f32_16x16x32_bf16 v[144:147], a[0:3], v[0:3], 0
TR 32,211,0
TR 34,211,2048
MB 144,4,16
buffer_load_dwordx4 v252, s[12:15], s63 offen offset:1024 sc1 lds
MB 144,8,32
TR 36,211,4096
TR 38,211,6144
v_accvgpr_write_b32 a213, v165
MB 144,12,48
WL 4
TR 40,211,8192
TR 42,211,10240
MB 144,16,64
s_add_u32 s52, s75, 4096
TR 44,211,12288
TR 46,211,14336
PK 162,136,137
MB 144,20,80
buffer_load_dwordx4 a[80:83], v253, s[20:23], s52 offen offset:0 sc1
MB 144,24,96
TR 48,211,16384
TR 50,211,18432
MB 144,28,112
TR 52,211,20480
TR 54,211,22528
PK 163,138,139
v_mfma_f32_16x16x32_bf16 v[148:151], a[0:3], v[4:7], 0
TR 56,211,24576
TR 58,211,26624
MB 148,4,20
buffer_load_dwordx4 v252, s[12:15], s63 offen offset:2048 sc1 lds
MB 148,8,36
TR 60,211,28672
TR 62,211,30720
PK 166,140,141
MB 148,12,52
buffer_load_dwordx2 v[178:179], off, s[20:23], s55 sc1
MB 148,16,68
buffer_load_dwordx4 a[84:87], v253, s[20:23], s52 offen offset:1024 sc1
MB 148,20,84
buffer_load_dwordx4 v252, s[12:15], s63 offen offset:3072 sc1 lds
MB 148,24,100
v_add_u32 v211, s61, v211
PK 167,142,143
v_accvgpr_write_b32 a210, v162
MB 148,28,116
WL 8
s_barrier
v_accvgpr_write_b32 a211, v163
v_mfma_f32_16x16x32_bf16 v[152:155], a[32:35], v[0:3], 0
TR 0,208,0
TR 2,208,2048
v_accvgpr_write_b32 a214, v166
MB 152,36,16
buffer_load_dwordx4 a[88:91], v253, s[20:23], s52 offen offset:2048 sc1
MB 152,40,32
TR 4,208,4096
TR 6,208,6144
MB 152,44,48
WL 4
TR 8,208,8192
TR 10,208,10240
v_accvgpr_write_b32 a215, v167
MB 152,48,64
TR 12,208,12288
TR 14,208,14336
MB 152,52,80
PK 168,144,145
PK 169,146,147
MB 152,56,96
TR 16,208,16384
TR 18,208,18432
MB 152,60,112
TR 20,208,20480
TR 22,208,22528
v_mfma_f32_16x16x32_bf16 v[156:159], a[32:35], v[4:7], 0
TR 24,208,24576
TR 26,208,26624
MB 156,36,20
buffer_load_dwordx4 a[92:95], v253, s[20:23], s52 offen offset:3072 sc1
MB 156,40,36
TR 28,208,28672
TR 30,208,30720
MB 156,44,52
PK 172,148,149
PK 173,150,151
MB 156,48,68
v_accvgpr_write_b32 a216, v168
v_accvgpr_write_b32 a217, v169
MB 156,52,84
v_accvgpr_write_b32 a220, v172
v_accvgpr_write_b32 a221, v173
MB 156,56,100
MB 156,60,116
WL 8
s_waitcnt vmcnt(17)
v_cmp_ne_u64 vcc, s[48:49], v[248:249]
s_cbranch_vccnz .LPS9
.LPR9:
v_mfma_f32_16x16x32_bf16 v[128:131], a[0:3], v[0:3], 0
s_add_u32 s63, s59, s62
TR 32,209,0
TR 34,209,2048
MB 128,4,16
MB 128,8,32
s_add_u32 m0, s58, s62
TR 36,209,4096
TR 38,209,6144
MB 128,12,48
WL 4
TR 40,209,8192
TR 42,209,10240
MB 128,16,64
TR 44,209,12288
TR 46,209,14336
MB 128,20,80
buffer_load_dwordx4 v252, s[12:15], s63 offen offset:0 sc1 lds
MB 128,24,96
s_add_u32 s59, s59, 0x18000
TR 48,209,16384
TR 50,209,18432
MB 128,28,112
s_add_u32 s7, s58, 32768
TR 52,209,20480
TR 54,209,22528
v_mfma_f32_16x16x32_bf16 v[132:135], a[0:3], v[4:7], 0
s_sub_u32 s58, s58, 131072
TR 56,209,24576
TR 58,209,26624
MB 132,4,20
buffer_load_dwordx4 v252, s[12:15], s63 offen offset:1024 sc1 lds
MB 132,8,36
s_min_u32 s58, s7, s58
TR 60,209,28672
TR 62,209,30720
MB 132,12,52
s_add_u32 s6, s57, 32768
PK 170,152,153
PK 171,154,155
MB 132,16,68
PK 174,156,157
PK 175,158,159
MB 132,20,84
v_accvgpr_write_b32 a218, v170
v_accvgpr_write_b32 a219, v171
MB 132,24,100
v_accvgpr_write_b32 a222, v174
v_accvgpr_write_b32 a223, v175
MB 132,28,116
WL 8
v_mfma_f32_16x16x32_bf16 v[136:139], a[32:35], v[0:3], 0
s_sub_u32 s57, s57, 131072
TR 0,210,0
TR 2,210,2048
MB 136,36,16
buffer_load_dwordx4 v252, s[12:15], s63 offen offset:2048 sc1 lds
MB 136,40,32
s_min_u32 s57, s6, s57
TR 4,210,4096
TR 6,210,6144
MB 136,44,48
WL 4
TR 8,210,8192
TR 10,210,10240
MB 136,48,64
TR 12,210,12288
TR 14,210,14336
PK 160,128,129
MB 136,52,80
buffer_load_dwordx4 v252, s[12:15], s63 offen offset:3072 sc1 lds
MB 136,56,96
s_add_u32 s63, s63, 4096
TR 16,210,16384
TR 18,210,18432
PK 161,130,131
MB 136,60,112
s_add_u32 m0, m0, 4096
TR 20,210,20480
TR 22,210,22528
PK 164,132,133
v_mfma_f32_16x16x32_bf16 v[140:143], a[32:35], v[4:7], 0
TR 24,210,24576
TR 26,210,26624
PK 165,134,135
MB 140,36,20
s_waitcnt vmcnt(8)
v_cmp_ne_u64 vcc, s[48:49], v[178:179]
s_cbranch_vccnz .LPS10
.LPR10:
MB 140,40,36
s_add_u32 s52, s75, 8192
TR 28,210,28672
TR 30,210,30720
v_accvgpr_write_b32 a224, v160
MB 140,44,52
buffer_load_dwordx4 a[96:99], v253, s[20:23], s52 offen offset:0 sc1
MB 140,48,68
buffer_load_dwordx4 a[100:103], v253, s[20:23], s52 offen offset:1024 sc1
MB 140,52,84
buffer_load_dwordx4 v252, s[12:15], s63 offen offset:0 sc1 lds
MB 140,56,100
buffer_load_dwordx4 a[104:107], v253, s[20:23], s52 offen offset:2048 sc1
MB 140,60,116
WL 8
buffer_load_dwordx4 a[108:111], v253, s[20:23], s52 offen offset:3072 sc1
v_mfma_f32_16x16x32_bf16 v[144:147], a[0:3], v[0:3], 0
s_add_u32 s52, s75, 12288
TR 32,211,0
TR 34,211,2048
v_accvgpr_write_b32 a225, v161
MB 144,4,16
buffer_load_dwordx4 v252, s[12:15], s63 offen offset:1024 sc1 lds
MB 144,8,32
TR 36,211,4096
TR 38,211,6144
v_accvgpr_write_b32 a228, v164
MB 144,12,48
WL 4
TR 40,211,8192
TR 42,211,10240
v_accvgpr_write_b32 a229, v165
MB 144,16,64
TR 44,211,12288
TR 46,211,14336
MB 144,20,80
buffer_load_dwordx4 a[112:115], v253, s[20:23], s52 offen offset:0 sc1
MB 144,24,96
TR 48,211,16384
TR 50,211,18432
MB 144,28,112
TR 52,211,20480
TR 54,211,22528
PK 162,136,137
v_mfma_f32_16x16x32_bf16 v[148:151], a[0:3], v[4:7], 0
s_lshl_b32 s82, s77, 2
TR 56,211,24576
TR 58,211,26624
MB 148,4,20
buffer_load_dwordx4 v252, s[12:15], s63 offen offset:2048 sc1 lds
MB 148,8,36
s_add_u32 s82, s82, s8
TR 60,211,28672
TR 62,211,30720
MB 148,12,52
buffer_load_dwordx4 a[116:119], v253, s[20:23], s52 offen offset:1024 sc1
MB 148,16,68
buffer_load_dwordx4 a[120:123], v253, s[20:23], s52 offen offset:2048 sc1
MB 148,20,84
buffer_load_dwordx4 v252, s[12:15], s63 offen offset:3072 sc1 lds
MB 148,24,100
buffer_load_dwordx4 a[124:127], v253, s[20:23], s52 offen offset:3072 sc1
MB 148,28,116
WL 8
s_barrier
v_add_u32 v240, s57, v253
v_mfma_f32_16x16x32_bf16 v[152:155], a[32:35], v[0:3], 0
s_lshl_b32 s82, s82, 6
RD 0,240,0
PK 163,138,139
MB 152,36,16
PK 166,140,141
PK 167,142,143
v_accvgpr_write_b32 a226, v162
MB 152,40,32
s_add_u32 s82, s82, 0x7014000
RD 4,240,8192
v_accvgpr_write_b32 a227, v163
MB 152,44,48
WL 2
v_accvgpr_write_b32 a230, v166
v_accvgpr_write_b32 a231, v167
MB 152,48,64
RD 8,240,16384
PK 168,144,145
MB 152,52,80
PK 169,146,147
PK 172,148,149
MB 152,56,96
RD 12,240,24576
PK 173,150,151
MB 152,60,112
v_accvgpr_write_b32 a232, v168
v_accvgpr_write_b32 a233, v169
v_mfma_f32_16x16x32_bf16 v[156:159], a[32:35], v[4:7], 0
RD 16,240,1024
v_accvgpr_write_b32 a236, v172
MB 156,36,20
v_accvgpr_write_b32 a237, v173
MB 156,40,36
RD 20,240,9216
MB 156,44,52
MB 156,48,68
RD 24,240,17408
MB 156,52,84
MB 156,56,100
RD 28,240,25600
MB 156,60,116
WL 6
s_waitcnt vmcnt(16)
s_nop 4
PK 170,152,153
PK 171,154,155
PK 174,156,157
PK 175,158,159
MZ 128,0,64
s_add_u32 s63, s59, s62
RD 32,240,2048
v_accvgpr_write_b32 a234, v170
MZ 132,0,68
v_accvgpr_write_b32 a235, v171
v_accvgpr_write_b32 a238, v174
MZ 136,4,64
s_add_u32 m0, s58, s62
RD 36,240,10240
v_accvgpr_write_b32 a239, v175
MZ 140,4,68
WL 6
v_bfe_u32 v196, v253, 8, 2
v_lshlrev_b32 v196, 3, v196
MZ 144,8,64
RD 40,240,18432
v_bfe_u32 v194, v253, 4, 4
MZ 148,8,68
buffer_load_dwordx4 v252, s[12:15], s63 offen offset:0 sc1 lds
MZ 152,12,64
s_add_u32 s59, s59, 0xffff8000
RD 44,240,26624
s_add_u32 s6, s57, 32768
MZ 156,12,68
WL 6
v_mul_u32_u24 v194, 0x380, v194
v_mul_u32_u24 v197, 5, v196
MA 128,16,72
s_add_u32 s7, s58, 32768
RD 48,240,3072
v_and_or_b32 v194, v197, 48, v194
MA 132,16,76
buffer_load_dwordx4 v252, s[12:15], s63 offen offset:1024 sc1 lds
MA 136,20,72
s_sub_u32 s58, s58, 131072
RD 52,240,11264
v_bfe_u32 v197, v253, 4, 5
MA 140,20,76
WL 6
v_lshlrev_b32 v197, 7, v197
v_and_b32 v198, 0x380, v197
MA 144,24,72
s_min_u32 s58, s7, s58
RD 56,240,19456
v_bfe_u32 v199, v253, 1, 7
MA 148,24,76
s_sub_u32 s57, s57, 131072
v_and_b32 v198, 0x380, v253
v_bfe_u32 v200, v253, 9, 1
MA 152,28,72
s_min_u32 s57, s6, s57
RD 60,240,27648
v_lshl_add_u32 v197, v200, 30, v197
MA 156,28,76
WL 6
v_mov_b32 v200, 0x7f000000
v_mov_b32 v192, s48
MA 128,32,80
s_mov_b32 s16, s46
RD 0,240,4096
v_mov_b32 v193, s49
MA 132,32,84
buffer_load_dwordx4 v252, s[12:15], s63 offen offset:2048 sc1 lds
MA 136,36,80
s_and_b32 s17, s47, 0xffff
RD 4,240,12288
v_mov_b32 v204, 1
MA 140,36,84
WL 6
v_mov_b32 v201, 0x7f000000
)A"
R"A(v_lshrrev_b32 v216, 6, v253
MA 144,40,80
s_mov_b32 s18, 134217728
RD 8,240,20480
v_lshlrev_b32 v216, 7, v216
MA 148,40,84
buffer_load_dwordx4 v252, s[12:15], s63 offen offset:3072 sc1 lds
MA 152,44,80
s_add_u32 s63, s63, 4096
RD 12,240,28672
v_bfe_u32 v217, v253, 4, 2
MA 156,44,84
WL 6
v_lshl_add_u32 v216, v217, 3, v216
v_bfe_u32 v217, v253, 7, 2
MA 128,48,88
s_add_u32 m0, m0, 4096
RD 16,240,5120
v_xor_b32 v212, 0, v217
MA 132,48,92
v_lshl_add_u32 v212, v212, 5, v216
v_xor_b32 v213, 1, v217
MA 136,52,88
RD 20,240,13312
v_lshl_add_u32 v213, v213, 5, v216
MA 140,52,92
WL 6
v_xor_b32 v214, 2, v217
v_lshl_add_u32 v214, v214, 5, v216
MA 144,56,88
RD 24,240,21504
v_xor_b32 v215, 3, v217
MA 148,56,92
buffer_load_dwordx4 v252, s[12:15], s63 offen offset:0 sc1 lds
MA 152,60,88
s_waitcnt vmcnt(12)
RD 28,240,29696
v_lshl_add_u32 v215, v215, 5, v216
MA 156,60,92
WL 6
s_cmp_eq_u32 s80, 7
s_cselect_b32 s18, 0, s18
MA 128,0,96
s_lshl_b32 s55, s77, 10
RD 32,240,6144
MA 132,0,100
buffer_load_dwordx4 v252, s[12:15], s63 offen offset:1024 sc1 lds
MA 136,4,96
s_add_u32 s55, s55, 0x6fd4000
RD 36,240,14336
MA 140,4,100
WL 6
MA 144,8,96
s_lshr_b32 s53, s74, 2
RD 40,240,22528
MA 148,8,100
MA 152,12,96
s_lshl_b32 s53, s53, 7
RD 44,240,30720
MA 156,12,100
WL 6
MA 128,16,104
s_add_u32 s53, s53, 0x6fd0000
RD 48,240,7168
MA 132,16,108
buffer_load_dwordx4 v252, s[12:15], s63 offen offset:2048 sc1 lds
MA 136,20,104
s_lshl_b32 s73, s81, 3
RD 52,240,15360
MA 140,20,108
WL 6
MA 144,24,104
s_add_u32 s73, s73, s80
RD 56,240,23552
MA 148,24,108
buffer_load_dwordx4 v252, s[12:15], s63 offen offset:3072 sc1 lds
MA 152,28,104
s_waitcnt vmcnt(8)
RD 60,240,31744
v_add_u32 v240, s57, v253
MA 156,28,108
WL 6
s_barrier
MA 128,32,112
s_mul_i32 s73, s73, 0x1c000
RD 0,240,0
RD 4,240,1024
MA 132,32,116
MA 136,36,112
s_lshl_b32 s50, s8, 12
RD 8,240,2048
RD 12,240,3072
MA 140,36,116
WL 8
MA 144,40,112
s_add_u32 s73, s73, s50
RD 16,240,4096
MA 148,40,116
MA 152,44,112
s_add_u32 s73, s73, 0x6000000
RD 20,240,5120
MA 156,44,116
WL 8
MA 128,48,120
s_lshl_b32 s72, s81, 3
RD 24,240,6144
MA 132,48,124
MA 136,52,120
s_add_u32 s72, s72, s80
RD 28,240,7168
MA 140,52,124
WL 8
MA 144,56,120
s_lshl_b32 s72, s72, 7
MA 148,56,124
s_add_u32 s72, s72, 0x6fd4000
MA 152,60,120
s_cmp_eq_u32 s8, 0
s_cselect_b32 s72, s72, 0x7f000000
MA 156,60,124
WL 4
v_mov_b64 v[242:243], 0
s_mov_b32 s69, 0
v_max3_f32 v254, v128, v129, v130
v_max_f32 v254, v254, v131
v_max3_f32 v255, v132, v133, v134
v_max_f32 v255, v255, v135
v_max3_f32 v254, v254, v136, v137
v_max3_f32 v254, v254, v138, v139
v_max3_f32 v255, v255, v140, v141
v_max3_f32 v255, v255, v142, v143
v_max3_f32 v254, v254, v144, v145
v_max3_f32 v254, v254, v146, v147
v_max3_f32 v255, v255, v148, v149
v_max3_f32 v255, v255, v150, v151
v_max3_f32 v254, v254, v152, v153
v_max3_f32 v254, v254, v154, v155
v_max3_f32 v255, v255, v156, v157
v_max3_f32 v255, v255, v158, v159
v_mov_b32 v244, v254
v_mov_b32 v248, v255
s_nop 0
v_permlane16_swap_b32 v254, v244
v_permlane16_swap_b32 v255, v248
v_max_f32 v254, v254, v244
v_max_f32 v255, v255, v248
v_mov_b32 v244, v254
v_mov_b32 v248, v255
s_nop 0
v_permlane32_swap_b32 v254, v244
v_permlane32_swap_b32 v255, v248
v_max_f32 v254, v254, v244
v_max_f32 v255, v255, v248
v_sub_f32 v244, 0, v254
v_sub_f32 v245, 0, v254
v_sub_f32 v246, 0, v254
v_sub_f32 v247, 0, v254
v_sub_f32 v128, v128, v254
v_sub_f32 v129, v129, v254
v_sub_f32 v130, v130, v254
v_sub_f32 v131, v131, v254
v_sub_f32 v136, v136, v254
v_sub_f32 v137, v137, v254
v_sub_f32 v138, v138, v254
v_sub_f32 v139, v139, v254
v_sub_f32 v248, 0, v255
v_sub_f32 v249, 0, v255
v_sub_f32 v250, 0, v255
v_sub_f32 v251, 0, v255
v_sub_f32 v132, v132, v255
v_sub_f32 v133, v133, v255
v_sub_f32 v134, v134, v255
v_sub_f32 v135, v135, v255
v_sub_f32 v140, v140, v255
v_sub_f32 v141, v141, v255
v_sub_f32 v142, v142, v255
v_sub_f32 v143, v143, v255
v_exp_f32 v128, v128
v_exp_f32 v129, v129
v_exp_f32 v130, v130
v_exp_f32 v131, v131
v_exp_f32 v132, v132
v_exp_f32 v133, v133
v_exp_f32 v134, v134
v_exp_f32 v135, v135
v_exp_f32 v136, v136
v_exp_f32 v137, v137
v_exp_f32 v138, v138
v_exp_f32 v139, v139
v_exp_f32 v140, v140
v_exp_f32 v141, v141
v_exp_f32 v142, v142
v_exp_f32 v143, v143
v_add_f32 v242, v242, v128
v_add_f32 v242, v242, v129
v_add_f32 v242, v242, v130
v_add_f32 v242, v242, v131
v_add_f32 v243, v243, v132
v_add_f32 v243, v243, v133
v_add_f32 v243, v243, v134
v_add_f32 v243, v243, v135
v_add_f32 v242, v242, v136
v_add_f32 v242, v242, v137
v_add_f32 v242, v242, v138
v_add_f32 v242, v242, v139
v_add_f32 v243, v243, v140
v_add_f32 v243, v243, v141
v_add_f32 v243, v243, v142
v_add_f32 v243, v243, v143
PK 160,128,129
PK 161,130,131
PK 162,136,137
PK 163,138,139
PK 164,132,133
PK 165,134,135
PK 166,140,141
PK 167,142,143
v_mfma_f32_16x16x32_bf16 v[128:131], a[0:3], a[64:67], v[244:247]
s_add_u32 s63, s59, s62
RD 32,240,8192
RD 36,240,9216
MA 128,4,72
v_add_f32 v144, v144, v244
v_add_f32 v145, v145, v244
MA 128,8,80
s_add_u32 m0, s58, s62
RD 40,240,10240
RD 44,240,11264
v_add_f32 v146, v146, v244
MA 128,12,88
WL 4
v_add_f32 v147, v147, v244
v_add_f32 v148, v148, v248
MA 128,16,96
RD 48,240,12288
v_add_f32 v149, v149, v248
MA 128,20,104
buffer_load_dwordx4 v252, s[12:15], s63 offen offset:0 sc1 lds
MA 128,24,112
s_add_u32 s59, s59, 0x18000
RD 52,240,13312
v_add_f32 v150, v150, v248
MA 128,28,120
v_add_f32 v151, v151, v248
v_add_f32 v152, v152, v244
v_mfma_f32_16x16x32_bf16 v[132:135], a[0:3], a[68:71], v[248:251]
s_add_u32 s7, s58, 32768
RD 56,240,14336
v_add_f32 v153, v153, v244
MA 132,4,76
buffer_load_dwordx4 v252, s[12:15], s63 offen offset:1024 sc1 lds
MA 132,8,84
s_sub_u32 s58, s58, 131072
RD 60,240,15360
v_add_f32 v154, v154, v244
v_add_f32 v155, v155, v244
MA 132,12,92
s_min_u32 s58, s7, s58
v_add_f32 v156, v156, v248
v_add_f32 v157, v157, v248
MA 132,16,100
s_add_u32 s6, s57, 32768
v_add_f32 v158, v158, v248
v_add_f32 v159, v159, v248
MA 132,20,108
v_exp_f32 v144, v144
MA 132,24,116
v_exp_f32 v145, v145
MA 132,28,124
WL 4
v_exp_f32 v146, v146
PK 168,144,145
v_mfma_f32_16x16x32_bf16 v[136:139], a[32:35], a[64:67], v[244:247]
s_sub_u32 s57, s57, 131072
RD 0,240,16384
RD 4,240,17408
MA 136,36,72
buffer_load_dwordx4 v252, s[12:15], s63 offen offset:2048 sc1 lds
MA 136,40,80
s_min_u32 s57, s6, s57
RD 8,240,18432
RD 12,240,19456
MA 136,44,88
WL 4
v_exp_f32 v147, v147
MA 136,48,96
RD 16,240,20480
v_add_f32 v242, v242, v144
PK 169,146,147
MA 136,52,104
buffer_load_dwordx4 v252, s[12:15], s63 offen offset:3072 sc1 lds
MA 136,56,112
s_add_u32 s63, s63, 4096
RD 20,240,21504
v_add_f32 v242, v242, v145
MA 136,60,120
v_exp_f32 v148, v148
v_mfma_f32_16x16x32_bf16 v[140:143], a[32:35], a[68:71], v[248:251]
s_add_u32 m0, m0, 4096
RD 24,240,22528
v_add_f32 v242, v242, v146
MA 140,36,76
v_exp_f32 v149, v149
MA 140,40,84
RD 28,240,23552
v_add_f32 v242, v242, v147
PK 172,148,149
MA 140,44,92
v_exp_f32 v150, v150
MA 140,48,100
v_exp_f32 v151, v151
v_add_f32 v243, v243, v150
MA 140,52,108
buffer_load_dwordx4 v252, s[12:15], s63 offen offset:0 sc1 lds
v_add_f32 v243, v243, v151
MA 140,56,116
v_exp_f32 v152, v152
PK 173,150,151
MA 140,60,124
WL 4
v_exp_f32 v153, v153
v_mfma_f32_16x16x32_bf16 v[144:147], a[0:3], a[64:67], v[244:247]
RD 32,240,24576
RD 36,240,25600
PK 170,152,153
MA 144,4,72
buffer_load_dwordx4 v252, s[12:15], s63 offen offset:1024 sc1 lds
MA 144,8,80
RD 40,240,26624
RD 44,240,27648
MA 144,12,88
WL 4
v_exp_f32 v154, v154
MA 144,16,96
RD 48,240,28672
v_add_f32 v243, v243, v148
MA 144,20,104
v_exp_f32 v155, v155
MA 144,24,112
RD 52,240,29696
v_add_f32 v243, v243, v149
PK 171,154,155
MA 144,28,120
v_exp_f32 v156, v156
v_mfma_f32_16x16x32_bf16 v[148:151], a[0:3], a[68:71], v[248:251]
RD 56,240,30720
v_add_f32 v242, v242, v152
MA 148,4,76
buffer_load_dwordx4 v252, s[12:15], s63 offen offset:2048 sc1 lds
MA 148,8,84
RD 60,240,31744
v_add_f32 v242, v242, v153
MA 148,12,92
v_exp_f32 v157, v157
MA 148,16,100
v_exp_f32 v158, v158
MA 148,20,108
buffer_load_dwordx4 v252, s[12:15], s63 offen offset:3072 sc1 lds
MA 148,24,116
v_add_u32 v240, s57, v253
v_add_f32 v242, v242, v154
MA 148,28,124
WL 4
s_barrier
v_add_f32 v242, v242, v155
v_mfma_f32_16x16x32_bf16 v[152:155], a[32:35], a[64:67], v[244:247]
s_add_u32 s63, s59, s62
RD 0,240,0
v_add_f32 v243, v243, v156
MA 152,36,72
buffer_store_dwordx4 a[128:131], v253, s[16:19], s73 offen offset:0 sc1
MA 152,40,80
s_add_u32 m0, s58, s62
RD 4,240,1024
v_add_f32 v243, v243, v157
MA 152,44,88
WL 2
v_exp_f32 v159, v159
MA 152,48,96
RD 8,240,2048
v_add_f32 v243, v243, v158
MA 152,52,104
buffer_load_dwordx4 v252, s[12:15], s63 offen offset:0 sc1 lds
MA 152,56,112
RD 12,240,3072
v_add_f32 v243, v243, v159
MA 152,60,120
PK 174,156,157
PK 175,158,159
v_mfma_f32_16x16x32_bf16 v[156:159], a[32:35], a[68:71], v[248:251]
RD 16,240,4096
v_max3_f32 v254, v128, v129, v130
MA 156,36,76
buffer_load_dwordx4 v252, s[12:15], s63 offen offset:1024 sc1 lds
MA 156,40,84
RD 20,240,5120
v_max_f32 v254, v254, v131
MA 156,44,92
v_max3_f32 v255, v132, v133, v134
v_max_f32 v255, v255, v135
MA 156,48,100
RD 24,240,6144
v_max3_f32 v254, v254, v136, v137
MA 156,52,108
buffer_load_dwordx4 v252, s[12:15], s63 offen offset:2048 sc1 lds
MA 156,56,116
RD 28,240,7168
v_max3_f32 v254, v254, v138, v139
MA 156,60,124
WL 6
buffer_store_dwordx4 a[132:135], v253, s[16:19], s73 offen offset:1024 sc1
v_max3_f32 v255, v255, v140, v141
v_max3_f32 v255, v255, v142, v143
v_max3_f32 v254, v254, v144, v145
v_max3_f32 v254, v254, v146, v147
v_max3_f32 v255, v255, v148, v149
v_max3_f32 v255, v255, v150, v151
v_mfma_f32_16x16x32_bf16 v[0:3], a[0:3], v[160:163], 0
RD 32,240,8192
RD 36,240,9216
v_mfma_f32_16x16x32_bf16 v[4:7], a[0:3], v[164:167], 0
buffer_load_dwordx4 v252, s[12:15], s63 offen offset:3072 sc1 lds
v_mfma_f32_16x16x32_bf16 v[12:15], a[4:7], v[164:167], 0
s_add_u32 s63, s63, 4096
RD 40,240,10240
v_max3_f32 v254, v254, v152, v153
v_mfma_f32_16x16x32_bf16 v[8:11], a[4:7], v[160:163], 0
WL 7
v_max3_f32 v254, v254, v154, v155
v_max3_f32 v255, v255, v156, v157
v_mfma_f32_16x16x32_bf16 v[16:19], a[8:11], v[160:163], 0
s_add_u32 m0, m0, 4096
RD 44,240,11264
v_max3_f32 v255, v255, v158, v159
v_mfma_f32_16x16x32_bf16 v[20:23], a[8:11], v[164:167], 0
v_max_f32 v241, v254, v255
v_cmp_lt_f32 vcc, 0x41800000, v241
v_mfma_f32_16x16x32_bf16 v[28:31], a[12:15], v[164:167], 0
s_cbranch_vccnz .LRA7
.LRAR7:
RD 48,240,12288
v_mfma_f32_16x16x32_bf16 v[24:27], a[12:15], v[160:163], 0
WL 7
v_exp_f32 v128, v128
v_mfma_f32_16x16x32_bf16 v[32:35], a[16:19], v[160:163], 0
RD 52,240,13312
RD 56,240,14336
v_mfma_f32_16x16x32_bf16 v[36:39], a[16:19], v[164:167], 0
v_exp_f32 v129, v129
v_mfma_f32_16x16x32_bf16 v[44:47], a[20:23], v[164:167], 0
RD 60,240,15360
v_add_f32 v242, v242, v128
v_mfma_f32_16x16x32_bf16 v[40:43], a[20:23], v[160:163], 0
WL 8
v_exp_f32 v130, v130
v_mfma_f32_16x16x32_bf16 v[48:51], a[24:27], v[160:163], 0
RD 0,240,16384
v_add_f32 v242, v242, v129
v_mfma_f32_16x16x32_bf16 v[52:55], a[24:27], v[164:167], 0
buffer_load_dwordx4 v252, s[12:15], s63 offen offset:0 sc1 lds
v_mfma_f32_16x16x32_bf16 v[60:63], a[28:31], v[164:167], 0
s_add_u32 s59, s59, 0xffff8000
RD 4,240,17408
v_add_f32 v242, v242, v130
v_mfma_f32_16x16x32_bf16 v[56:59], a[28:31], v[160:163], 0
WL 8
v_exp_f32 v131, v131
v_mfma_f32_16x16x32_bf16 v[64:67], a[32:35], v[160:163], 0
s_add_u32 s7, s58, 32768
RD 8,240,18432
v_add_f32 v242, v242, v131
v_mfma_f32_16x16x32_bf16 v[68:71], a[32:35], v[164:167], 0
v_exp_f32 v132, v132
v_mfma_f32_16x16x32_bf16 v[76:79], a[36:39], v[164:167], 0
s_sub_u32 s58, s58, 131072
RD 12,240,19456
RD 16,240,20480
v_mfma_f32_16x16x32_bf16 v[72:75], a[36:39], v[160:163], 0
WL 9
v_exp_f32 v133, v133
v_mfma_f32_16x16x32_bf16 v[80:83], a[40:43], v[160:163], 0
s_min_u32 s58, s7, s58
RD 20,240,21504
v_add_f32 v243, v243, v132
v_mfma_f32_16x16x32_bf16 v[84:87], a[40:43], v[164:167], 0
buffer_load_dwordx4 v252, s[12:15], s63 offen offset:1024 sc1 lds
v_mfma_f32_16x16x32_bf16 v[92:95], a[44:47], v[164:167], 0
s_add_u32 s6, s57, 32768
RD 24,240,22528
v_add_f32 v243, v243, v133
v_mfma_f32_16x16x32_bf16 v[88:91], a[44:47], v[160:163], 0
)A"
R"A(WL 9
v_exp_f32 v134, v134
v_mfma_f32_16x16x32_bf16 v[96:99], a[48:51], v[160:163], 0
s_sub_u32 s57, s57, 131072
RD 28,240,23552
v_add_f32 v243, v243, v134
v_mfma_f32_16x16x32_bf16 v[100:103], a[48:51], v[164:167], 0
v_exp_f32 v135, v135
v_mfma_f32_16x16x32_bf16 v[108:111], a[52:55], v[164:167], 0
s_min_u32 s57, s6, s57
RD 32,240,24576
v_add_f32 v243, v243, v135
v_mfma_f32_16x16x32_bf16 v[104:107], a[52:55], v[160:163], 0
WL 9
v_exp_f32 v136, v136
v_mfma_f32_16x16x32_bf16 v[112:115], a[56:59], v[160:163], 0
RD 36,240,25600
RD 40,240,26624
v_mfma_f32_16x16x32_bf16 v[116:119], a[56:59], v[164:167], 0
buffer_load_dwordx4 v252, s[12:15], s63 offen offset:2048 sc1 lds
v_mfma_f32_16x16x32_bf16 v[124:127], a[60:63], v[164:167], 0
RD 44,240,27648
v_add_f32 v242, v242, v136
v_mfma_f32_16x16x32_bf16 v[120:123], a[60:63], v[160:163], 0
WL 10
v_exp_f32 v137, v137
MB 0,0,168
RD 48,240,28672
PK 160,128,129
MB 4,0,172
buffer_store_dwordx4 a[136:139], v253, s[16:19], s73 offen offset:2048 sc1
MB 12,4,172
RD 52,240,29696
PK 161,130,131
MB 8,4,168
WL 10
v_exp_f32 v138, v138
MB 16,8,168
RD 56,240,30720
PK 164,132,133
MB 20,8,172
v_exp_f32 v139, v139
MB 28,12,172
RD 60,240,31744
PK 165,134,135
MB 24,12,168
WL 10
v_exp_f32 v140, v140
MB 32,16,168
v_exp_f32 v141, v141
MB 36,16,172
buffer_load_dwordx4 v252, s[12:15], s63 offen offset:3072 sc1 lds
MB 44,20,172
v_exp_f32 v142, v142
MB 40,20,168
WL 8
v_exp_f32 v143, v143
MB 48,24,168
v_exp_f32 v144, v144
MB 52,24,172
v_exp_f32 v145, v145
MB 60,28,172
v_add_u32 v240, s57, v253
v_add_f32 v242, v242, v137
MB 56,28,168
s_waitcnt vmcnt(11) lgkmcnt(0)
s_barrier
v_add_f32 v242, v242, v138
MB 64,32,168
RD 0,240,0
RD 4,240,1024
MB 68,32,172
v_exp_f32 v146, v146
MB 76,36,172
RD 8,240,2048
RD 12,240,3072
MB 72,36,168
v_exp_f32 v147, v147
MB 80,40,168
RD 16,240,4096
v_add_f32 v242, v242, v139
MB 84,40,172
PK 162,136,137
PK 163,138,139
MB 92,44,172
RD 20,240,5120
v_add_f32 v243, v243, v140
MB 88,44,168
v_add_f32 v243, v243, v141
v_add_f32 v243, v243, v142
MB 96,48,168
RD 24,240,6144
v_add_f32 v243, v243, v143
MB 100,48,172
PK 166,140,141
PK 167,142,143
MB 108,52,172
RD 28,240,7168
v_add_f32 v242, v242, v144
MB 104,52,168
v_add_f32 v242, v242, v145
v_add_f32 v242, v242, v146
MB 112,56,168
v_add_f32 v242, v242, v147
MB 116,56,172
buffer_store_dwordx4 a[140:143], v253, s[16:19], s73 offen offset:3072 sc1
MB 124,60,172
s_add_u32 s73, s73, 16384
MB 120,60,168
WL 4
s_cmp_lg_u32 s69, 0
s_cbranch_scc1 .LRB7
.LRBR7:
v_mfma_f32_16x16x32_bf16 v[128:131], a[0:3], a[64:67], v[244:247]
s_add_u32 s63, s59, s62
RD 32,240,8192
RD 36,240,9216
MA 128,4,72
v_exp_f32 v148, v148
MA 128,8,80
s_add_u32 m0, s58, s62
RD 40,240,10240
RD 44,240,11264
MA 128,12,88
WL 4
v_exp_f32 v149, v149
MA 128,16,96
RD 48,240,12288
PK 168,144,145
MA 128,20,104
buffer_load_dwordx4 v252, s[12:15], s63 offen offset:0 sc1 lds
MA 128,24,112
s_add_u32 s59, s59, 0x18000
RD 52,240,13312
PK 169,146,147
MA 128,28,120
v_exp_f32 v150, v150
v_mfma_f32_16x16x32_bf16 v[132:135], a[0:3], a[68:71], v[248:251]
s_add_u32 s7, s58, 32768
RD 56,240,14336
v_add_f32 v243, v243, v148
MA 132,4,76
buffer_load_dwordx4 v252, s[12:15], s63 offen offset:1024 sc1 lds
MA 132,8,84
s_sub_u32 s58, s58, 131072
RD 60,240,15360
v_add_f32 v243, v243, v149
MA 132,12,92
s_min_u32 s58, s7, s58
v_exp_f32 v151, v151
MA 132,16,100
s_add_u32 s6, s57, 32768
v_exp_f32 v152, v152
MA 132,20,108
buffer_load_dwordx4 v252, s[12:15], s63 offen offset:2048 sc1 lds
MA 132,24,116
v_exp_f32 v153, v153
MA 132,28,124
WL 4
v_exp_f32 v154, v154
v_mfma_f32_16x16x32_bf16 v[136:139], a[32:35], a[64:67], v[244:247]
s_sub_u32 s57, s57, 131072
RD 0,240,16384
RD 4,240,17408
MA 136,36,72
buffer_load_dwordx4 v252, s[12:15], s63 offen offset:3072 sc1 lds
MA 136,40,80
s_add_u32 s63, s63, 4096
RD 8,240,18432
RD 12,240,19456
MA 136,44,88
WL 4
v_exp_f32 v155, v155
MA 136,48,96
s_add_u32 m0, m0, 4096
RD 16,240,20480
v_add_f32 v243, v243, v150
MA 136,52,104
buffer_load_dwordx4 v252, s[12:15], s63 offen offset:0 sc1 lds
MA 136,56,112
s_min_u32 s57, s6, s57
RD 20,240,21504
v_add_f32 v243, v243, v151
MA 136,60,120
v_exp_f32 v156, v156
v_mfma_f32_16x16x32_bf16 v[140:143], a[32:35], a[68:71], v[248:251]
RD 24,240,22528
PK 172,148,149
MA 140,36,76
buffer_load_dwordx4 v252, s[12:15], s63 offen offset:1024 sc1 lds
MA 140,40,84
RD 28,240,23552
PK 173,150,151
MA 140,44,92
v_exp_f32 v157, v157
MA 140,48,100
v_exp_f32 v158, v158
MA 140,52,108
buffer_load_dwordx4 v252, s[12:15], s63 offen offset:2048 sc1 lds
MA 140,56,116
v_exp_f32 v159, v159
MA 140,60,124
WL 4
v_add_f32 v242, v242, v152
v_add_f32 v242, v242, v153
v_mfma_f32_16x16x32_bf16 v[144:147], a[0:3], a[64:67], v[244:247]
RD 32,240,24576
RD 36,240,25600
MA 144,4,72
buffer_load_dwordx4 v252, s[12:15], s63 offen offset:3072 sc1 lds
MA 144,8,80
s_add_u32 s63, s59, s62
RD 40,240,26624
RD 44,240,27648
MA 144,12,88
WL 4
v_add_f32 v242, v242, v154
v_add_f32 v242, v242, v155
MA 144,16,96
s_add_u32 m0, s58, s62
RD 48,240,28672
PK 170,152,153
MA 144,20,104
PK 171,154,155
v_add_f32 v243, v243, v156
MA 144,24,112
RD 52,240,29696
v_add_f32 v243, v243, v157
MA 144,28,120
v_add_f32 v243, v243, v158
v_add_f32 v243, v243, v159
v_mfma_f32_16x16x32_bf16 v[148:151], a[0:3], a[68:71], v[248:251]
RD 56,240,30720
PK 174,156,157
MA 148,4,76
PK 175,158,159
v_max3_f32 v254, v128, v129, v130
MA 148,8,84
RD 60,240,31744
v_max_f32 v254, v254, v131
MA 148,12,92
v_max3_f32 v255, v132, v133, v134
v_max_f32 v255, v255, v135
MA 148,16,100
v_max3_f32 v254, v254, v136, v137
v_max3_f32 v254, v254, v138, v139
MA 148,20,108
v_max3_f32 v255, v255, v140, v141
v_max3_f32 v255, v255, v142, v143
MA 148,24,116
v_add_u32 v240, s57, v253
MA 148,28,124
WL 4
v_mfma_f32_16x16x32_bf16 v[152:155], a[32:35], a[64:67], v[244:247]
RD 0,240,0
MA 152,36,72
buffer_store_dwordx4 a[144:147], v253, s[16:19], s73 offen offset:0 sc1
MA 152,40,80
RD 4,240,1024
MA 152,44,88
WL 2
MA 152,48,96
RD 8,240,2048
MA 152,52,104
buffer_load_dwordx4 v252, s[12:15], s63 offen offset:0 sc1 lds
MA 152,56,112
RD 12,240,3072
v_max3_f32 v254, v254, v144, v145
MA 152,60,120
v_max3_f32 v254, v254, v146, v147
v_max3_f32 v255, v255, v148, v149
v_mfma_f32_16x16x32_bf16 v[156:159], a[32:35], a[68:71], v[248:251]
RD 16,240,4096
v_max3_f32 v255, v255, v150, v151
MA 156,36,76
buffer_load_dwordx4 v252, s[12:15], s63 offen offset:1024 sc1 lds
MA 156,40,84
RD 20,240,5120
MA 156,44,92
MA 156,48,100
RD 24,240,6144
MA 156,52,108
buffer_load_dwordx4 v252, s[12:15], s63 offen offset:2048 sc1 lds
MA 156,56,116
RD 28,240,7168
MA 156,60,124
WL 6
buffer_store_dwordx4 a[148:151], v253, s[16:19], s73 offen offset:1024 sc1
MB 0,0,160
RD 32,240,8192
RD 36,240,9216
MB 4,0,164
buffer_load_dwordx4 v252, s[12:15], s63 offen offset:3072 sc1 lds
MB 12,4,164
s_add_u32 s63, s63, 4096
RD 40,240,10240
v_max3_f32 v254, v254, v152, v153
MB 8,4,160
WL 7
v_max3_f32 v254, v254, v154, v155
v_max3_f32 v255, v255, v156, v157
MB 16,8,160
s_add_u32 m0, m0, 4096
RD 44,240,11264
v_max3_f32 v255, v255, v158, v159
MB 20,8,164
v_max_f32 v241, v254, v255
v_cmp_lt_f32 vcc, 0x41800000, v241
MB 28,12,164
s_cbranch_vccnz .LRA8
.LRAR8:
RD 48,240,12288
MB 24,12,160
WL 7
v_exp_f32 v128, v128
MB 32,16,160
RD 52,240,13312
RD 56,240,14336
MB 36,16,164
v_exp_f32 v129, v129
MB 44,20,164
RD 60,240,15360
v_add_f32 v242, v242, v128
MB 40,20,160
WL 8
v_exp_f32 v130, v130
MB 48,24,160
RD 0,240,16384
v_add_f32 v242, v242, v129
MB 52,24,164
buffer_load_dwordx4 v252, s[12:15], s63 offen offset:0 sc1 lds
MB 60,28,164
s_add_u32 s59, s59, 0xffff8000
RD 4,240,17408
v_add_f32 v242, v242, v130
MB 56,28,160
WL 8
v_exp_f32 v131, v131
MB 64,32,160
s_add_u32 s7, s58, 32768
RD 8,240,18432
v_add_f32 v242, v242, v131
MB 68,32,164
v_exp_f32 v132, v132
MB 76,36,164
s_sub_u32 s58, s58, 131072
RD 12,240,19456
RD 16,240,20480
MB 72,36,160
WL 9
v_exp_f32 v133, v133
MB 80,40,160
s_min_u32 s58, s7, s58
RD 20,240,21504
v_add_f32 v243, v243, v132
MB 84,40,164
buffer_load_dwordx4 v252, s[12:15], s63 offen offset:1024 sc1 lds
MB 92,44,164
s_add_u32 s6, s57, 32768
RD 24,240,22528
v_add_f32 v243, v243, v133
MB 88,44,160
WL 9
v_exp_f32 v134, v134
MB 96,48,160
s_sub_u32 s57, s57, 131072
RD 28,240,23552
v_add_f32 v243, v243, v134
MB 100,48,164
v_exp_f32 v135, v135
MB 108,52,164
s_min_u32 s57, s6, s57
RD 32,240,24576
v_add_f32 v243, v243, v135
MB 104,52,160
WL 9
v_exp_f32 v136, v136
MB 112,56,160
RD 36,240,25600
RD 40,240,26624
MB 116,56,164
buffer_load_dwordx4 v252, s[12:15], s63 offen offset:2048 sc1 lds
MB 124,60,164
RD 44,240,27648
v_add_f32 v242, v242, v136
MB 120,60,160
WL 10
v_exp_f32 v137, v137
MB 0,0,168
RD 48,240,28672
PK 160,128,129
MB 4,0,172
buffer_store_dwordx4 a[152:155], v253, s[16:19], s73 offen offset:2048 sc1
MB 12,4,172
RD 52,240,29696
PK 161,130,131
MB 8,4,168
WL 10
v_exp_f32 v138, v138
MB 16,8,168
RD 56,240,30720
PK 164,132,133
MB 20,8,172
v_exp_f32 v139, v139
MB 28,12,172
RD 60,240,31744
PK 165,134,135
MB 24,12,168
WL 10
v_exp_f32 v140, v140
MB 32,16,168
v_exp_f32 v141, v141
MB 36,16,172
buffer_load_dwordx4 v252, s[12:15], s63 offen offset:3072 sc1 lds
MB 44,20,172
v_exp_f32 v142, v142
MB 40,20,168
WL 8
v_exp_f32 v143, v143
MB 48,24,168
v_exp_f32 v144, v144
MB 52,24,172
v_exp_f32 v145, v145
MB 60,28,172
v_add_u32 v240, s57, v253
v_add_f32 v242, v242, v137
MB 56,28,168
s_waitcnt vmcnt(11) lgkmcnt(0)
s_barrier
v_add_f32 v242, v242, v138
MB 64,32,168
RD 0,240,0
RD 4,240,1024
MB 68,32,172
v_exp_f32 v146, v146
MB 76,36,172
RD 8,240,2048
RD 12,240,3072
MB 72,36,168
v_exp_f32 v147, v147
MB 80,40,168
RD 16,240,4096
v_add_f32 v242, v242, v139
MB 84,40,172
PK 162,136,137
PK 163,138,139
MB 92,44,172
RD 20,240,5120
v_add_f32 v243, v243, v140
MB 88,44,168
v_add_f32 v243, v243, v141
v_add_f32 v243, v243, v142
MB 96,48,168
RD 24,240,6144
v_add_f32 v243, v243, v143
MB 100,48,172
PK 166,140,141
PK 167,142,143
MB 108,52,172
RD 28,240,7168
v_add_f32 v242, v242, v144
MB 104,52,168
v_add_f32 v242, v242, v145
v_add_f32 v242, v242, v146
MB 112,56,168
v_add_f32 v242, v242, v147
MB 116,56,172
buffer_store_dwordx4 a[156:159], v253, s[16:19], s73 offen offset:3072 sc1
MB 124,60,172
s_add_u32 s73, s73, 16384
MB 120,60,168
WL 4
s_cmp_lg_u32 s69, 0
s_cbranch_scc1 .LRB8
.LRBR8:
v_mfma_f32_16x16x32_bf16 v[128:131], a[0:3], a[64:67], v[244:247]
s_add_u32 s63, s59, s62
RD 32,240,8192
RD 36,240,9216
MA 128,4,72
v_exp_f32 v148, v148
MA 128,8,80
s_add_u32 m0, s58, s62
RD 40,240,10240
RD 44,240,11264
MA 128,12,88
WL 4
v_exp_f32 v149, v149
MA 128,16,96
RD 48,240,12288
PK 168,144,145
MA 128,20,104
buffer_load_dwordx4 v252, s[12:15], s63 offen offset:0 sc1 lds
MA 128,24,112
s_add_u32 s59, s59, 0x18000
RD 52,240,13312
PK 169,146,147
MA 128,28,120
v_exp_f32 v150, v150
v_mfma_f32_16x16x32_bf16 v[132:135], a[0:3], a[68:71], v[248:251]
s_add_u32 s7, s58, 32768
RD 56,240,14336
v_add_f32 v243, v243, v148
MA 132,4,76
buffer_load_dwordx4 v252, s[12:15], s63 offen offset:1024 sc1 lds
MA 132,8,84
s_sub_u32 s58, s58, 131072
RD 60,240,15360
v_add_f32 v243, v243, v149
MA 132,12,92
s_min_u32 s58, s7, s58
v_exp_f32 v151, v151
MA 132,16,100
s_add_u32 s6, s57, 32768
v_exp_f32 v152, v152
MA 132,20,108
buffer_load_dwordx4 v252, s[12:15], s63 offen offset:2048 sc1 lds
MA 132,24,116
v_exp_f32 v153, v153
MA 132,28,124
WL 4
v_exp_f32 v154, v154
v_mfma_f32_16x16x32_bf16 v[136:139], a[32:35], a[64:67], v[244:247]
s_sub_u32 s57, s57, 131072
RD 0,240,16384
RD 4,240,17408
MA 136,36,72
buffer_load_dwordx4 v252, s[12:15], s63 offen offset:3072 sc1 lds
MA 136,40,80
s_add_u32 s63, s63, 4096
RD 8,240,18432
RD 12,240,19456
MA 136,44,88
WL 4
v_exp_f32 v155, v155
MA 136,48,96
s_add_u32 m0, m0, 4096
RD 16,240,20480
v_add_f32 v243, v243, v150
MA 136,52,104
buffer_load_dwordx4 v252, s[12:15], s63 offen offset:0 sc1 lds
MA 136,56,112
s_min_u32 s57, s6, s57
RD 20,240,21504
v_add_f32 v243, v243, v151
MA 136,60,120
v_exp_f32 v156, v156
v_mfma_f32_16x16x32_bf16 v[140:143], a[32:35], a[68:71], v[248:251]
RD 24,240,22528
PK 172,148,149
MA 140,36,76
buffer_load_dwordx4 v252, s[12:15], s63 offen offset:1024 sc1 lds
MA 140,40,84
RD 28,240,23552
PK 173,150,151
MA 140,44,92
v_exp_f32 v157, v157
)A"
R"A(MA 140,48,100
v_exp_f32 v158, v158
MA 140,52,108
buffer_load_dwordx4 v252, s[12:15], s63 offen offset:2048 sc1 lds
MA 140,56,116
v_exp_f32 v159, v159
MA 140,60,124
WL 4
v_add_f32 v242, v242, v152
v_add_f32 v242, v242, v153
v_mfma_f32_16x16x32_bf16 v[144:147], a[0:3], a[64:67], v[244:247]
RD 32,240,24576
RD 36,240,25600
MA 144,4,72
buffer_load_dwordx4 v252, s[12:15], s63 offen offset:3072 sc1 lds
MA 144,8,80
s_add_u32 s63, s59, s62
RD 40,240,26624
RD 44,240,27648
MA 144,12,88
WL 4
v_add_f32 v242, v242, v154
v_add_f32 v242, v242, v155
MA 144,16,96
s_add_u32 m0, s58, s62
RD 48,240,28672
PK 170,152,153
MA 144,20,104
PK 171,154,155
v_add_f32 v243, v243, v156
MA 144,24,112
RD 52,240,29696
v_add_f32 v243, v243, v157
MA 144,28,120
v_add_f32 v243, v243, v158
v_add_f32 v243, v243, v159
v_mfma_f32_16x16x32_bf16 v[148:151], a[0:3], a[68:71], v[248:251]
RD 56,240,30720
PK 174,156,157
MA 148,4,76
PK 175,158,159
v_max3_f32 v254, v128, v129, v130
MA 148,8,84
RD 60,240,31744
v_max_f32 v254, v254, v131
MA 148,12,92
v_max3_f32 v255, v132, v133, v134
v_max_f32 v255, v255, v135
MA 148,16,100
v_max3_f32 v254, v254, v136, v137
v_max3_f32 v254, v254, v138, v139
MA 148,20,108
v_max3_f32 v255, v255, v140, v141
v_max3_f32 v255, v255, v142, v143
MA 148,24,116
v_add_u32 v240, s57, v253
MA 148,28,124
WL 4
v_mfma_f32_16x16x32_bf16 v[152:155], a[32:35], a[64:67], v[244:247]
RD 0,240,0
MA 152,36,72
buffer_store_dwordx4 a[160:163], v253, s[16:19], s73 offen offset:0 sc1
MA 152,40,80
RD 4,240,1024
MA 152,44,88
WL 2
MA 152,48,96
RD 8,240,2048
MA 152,52,104
buffer_load_dwordx4 v252, s[12:15], s63 offen offset:0 sc1 lds
MA 152,56,112
RD 12,240,3072
v_max3_f32 v254, v254, v144, v145
MA 152,60,120
v_max3_f32 v254, v254, v146, v147
v_max3_f32 v255, v255, v148, v149
v_mfma_f32_16x16x32_bf16 v[156:159], a[32:35], a[68:71], v[248:251]
RD 16,240,4096
v_max3_f32 v255, v255, v150, v151
MA 156,36,76
buffer_load_dwordx4 v252, s[12:15], s63 offen offset:1024 sc1 lds
MA 156,40,84
RD 20,240,5120
MA 156,44,92
MA 156,48,100
RD 24,240,6144
MA 156,52,108
buffer_load_dwordx4 v252, s[12:15], s63 offen offset:2048 sc1 lds
MA 156,56,116
RD 28,240,7168
MA 156,60,124
WL 6
buffer_store_dwordx4 a[164:167], v253, s[16:19], s73 offen offset:1024 sc1
MB 0,0,160
RD 32,240,8192
RD 36,240,9216
MB 4,0,164
buffer_load_dwordx4 v252, s[12:15], s63 offen offset:3072 sc1 lds
MB 12,4,164
s_add_u32 s63, s63, 4096
RD 40,240,10240
v_max3_f32 v254, v254, v152, v153
MB 8,4,160
WL 7
v_max3_f32 v254, v254, v154, v155
v_max3_f32 v255, v255, v156, v157
MB 16,8,160
s_add_u32 m0, m0, 4096
RD 44,240,11264
v_max3_f32 v255, v255, v158, v159
MB 20,8,164
v_max_f32 v241, v254, v255
v_cmp_lt_f32 vcc, 0x41800000, v241
MB 28,12,164
s_cbranch_vccnz .LRA9
.LRAR9:
RD 48,240,12288
MB 24,12,160
WL 7
v_exp_f32 v128, v128
MB 32,16,160
RD 52,240,13312
RD 56,240,14336
MB 36,16,164
v_exp_f32 v129, v129
MB 44,20,164
RD 60,240,15360
v_add_f32 v242, v242, v128
MB 40,20,160
WL 8
v_exp_f32 v130, v130
MB 48,24,160
RD 0,240,16384
v_add_f32 v242, v242, v129
MB 52,24,164
buffer_load_dwordx4 v252, s[12:15], s63 offen offset:0 sc1 lds
MB 60,28,164
s_add_u32 s59, s59, 0xffff8000
RD 4,240,17408
v_add_f32 v242, v242, v130
MB 56,28,160
WL 8
v_exp_f32 v131, v131
MB 64,32,160
s_add_u32 s7, s58, 32768
RD 8,240,18432
v_add_f32 v242, v242, v131
MB 68,32,164
v_exp_f32 v132, v132
MB 76,36,164
s_sub_u32 s58, s58, 131072
RD 12,240,19456
RD 16,240,20480
MB 72,36,160
WL 9
v_exp_f32 v133, v133
MB 80,40,160
s_min_u32 s58, s7, s58
RD 20,240,21504
v_add_f32 v243, v243, v132
MB 84,40,164
buffer_load_dwordx4 v252, s[12:15], s63 offen offset:1024 sc1 lds
MB 92,44,164
s_add_u32 s6, s57, 32768
RD 24,240,22528
v_add_f32 v243, v243, v133
MB 88,44,160
WL 9
v_exp_f32 v134, v134
MB 96,48,160
s_sub_u32 s57, s57, 131072
RD 28,240,23552
v_add_f32 v243, v243, v134
MB 100,48,164
v_exp_f32 v135, v135
MB 108,52,164
s_min_u32 s57, s6, s57
RD 32,240,24576
v_add_f32 v243, v243, v135
MB 104,52,160
WL 9
v_exp_f32 v136, v136
MB 112,56,160
RD 36,240,25600
RD 40,240,26624
MB 116,56,164
buffer_load_dwordx4 v252, s[12:15], s63 offen offset:2048 sc1 lds
MB 124,60,164
RD 44,240,27648
v_add_f32 v242, v242, v136
MB 120,60,160
WL 10
v_exp_f32 v137, v137
MB 0,0,168
RD 48,240,28672
PK 160,128,129
MB 4,0,172
buffer_store_dwordx4 a[168:171], v253, s[16:19], s73 offen offset:2048 sc1
MB 12,4,172
RD 52,240,29696
PK 161,130,131
MB 8,4,168
WL 10
v_exp_f32 v138, v138
MB 16,8,168
RD 56,240,30720
PK 164,132,133
MB 20,8,172
v_exp_f32 v139, v139
MB 28,12,172
RD 60,240,31744
PK 165,134,135
MB 24,12,168
WL 10
v_exp_f32 v140, v140
MB 32,16,168
v_exp_f32 v141, v141
MB 36,16,172
buffer_load_dwordx4 v252, s[12:15], s63 offen offset:3072 sc1 lds
MB 44,20,172
v_exp_f32 v142, v142
MB 40,20,168
WL 8
v_exp_f32 v143, v143
MB 48,24,168
v_exp_f32 v144, v144
MB 52,24,172
v_exp_f32 v145, v145
MB 60,28,172
v_add_u32 v240, s57, v253
v_add_f32 v242, v242, v137
MB 56,28,168
s_waitcnt vmcnt(11) lgkmcnt(0)
s_barrier
v_add_f32 v242, v242, v138
MB 64,32,168
RD 0,240,0
RD 4,240,1024
MB 68,32,172
v_exp_f32 v146, v146
MB 76,36,172
RD 8,240,2048
RD 12,240,3072
MB 72,36,168
v_exp_f32 v147, v147
MB 80,40,168
RD 16,240,4096
v_add_f32 v242, v242, v139
MB 84,40,172
PK 162,136,137
PK 163,138,139
MB 92,44,172
RD 20,240,5120
v_add_f32 v243, v243, v140
MB 88,44,168
v_add_f32 v243, v243, v141
v_add_f32 v243, v243, v142
MB 96,48,168
RD 24,240,6144
v_add_f32 v243, v243, v143
MB 100,48,172
PK 166,140,141
PK 167,142,143
MB 108,52,172
RD 28,240,7168
v_add_f32 v242, v242, v144
MB 104,52,168
v_add_f32 v242, v242, v145
v_add_f32 v242, v242, v146
MB 112,56,168
v_add_f32 v242, v242, v147
MB 116,56,172
buffer_store_dwordx4 a[172:175], v253, s[16:19], s73 offen offset:3072 sc1
MB 124,60,172
s_add_u32 s73, s73, 16384
MB 120,60,168
WL 4
s_cmp_lg_u32 s69, 0
s_cbranch_scc1 .LRB9
.LRBR9:
v_mfma_f32_16x16x32_bf16 v[128:131], a[0:3], a[64:67], v[244:247]
s_add_u32 s63, s59, s62
RD 32,240,8192
RD 36,240,9216
MA 128,4,72
v_exp_f32 v148, v148
MA 128,8,80
s_add_u32 m0, s58, s62
RD 40,240,10240
RD 44,240,11264
MA 128,12,88
WL 4
v_exp_f32 v149, v149
MA 128,16,96
RD 48,240,12288
PK 168,144,145
MA 128,20,104
buffer_load_dwordx4 v252, s[12:15], s63 offen offset:0 sc1 lds
MA 128,24,112
s_add_u32 s59, s59, 0x18000
RD 52,240,13312
PK 169,146,147
MA 128,28,120
v_exp_f32 v150, v150
v_mfma_f32_16x16x32_bf16 v[132:135], a[0:3], a[68:71], v[248:251]
s_add_u32 s7, s58, 32768
RD 56,240,14336
v_add_f32 v243, v243, v148
MA 132,4,76
buffer_load_dwordx4 v252, s[12:15], s63 offen offset:1024 sc1 lds
MA 132,8,84
s_sub_u32 s58, s58, 131072
RD 60,240,15360
v_add_f32 v243, v243, v149
MA 132,12,92
s_min_u32 s58, s7, s58
v_exp_f32 v151, v151
MA 132,16,100
s_add_u32 s6, s57, 32768
v_exp_f32 v152, v152
MA 132,20,108
buffer_load_dwordx4 v252, s[12:15], s63 offen offset:2048 sc1 lds
MA 132,24,116
v_exp_f32 v153, v153
MA 132,28,124
WL 4
v_exp_f32 v154, v154
v_mfma_f32_16x16x32_bf16 v[136:139], a[32:35], a[64:67], v[244:247]
s_sub_u32 s57, s57, 131072
RD 0,240,16384
RD 4,240,17408
MA 136,36,72
buffer_load_dwordx4 v252, s[12:15], s63 offen offset:3072 sc1 lds
MA 136,40,80
s_add_u32 s63, s63, 4096
RD 8,240,18432
RD 12,240,19456
MA 136,44,88
WL 4
v_exp_f32 v155, v155
MA 136,48,96
s_add_u32 m0, m0, 4096
RD 16,240,20480
v_add_f32 v243, v243, v150
MA 136,52,104
buffer_load_dwordx4 v252, s[12:15], s63 offen offset:0 sc1 lds
MA 136,56,112
s_min_u32 s57, s6, s57
RD 20,240,21504
v_add_f32 v243, v243, v151
MA 136,60,120
v_exp_f32 v156, v156
v_mfma_f32_16x16x32_bf16 v[140:143], a[32:35], a[68:71], v[248:251]
RD 24,240,22528
PK 172,148,149
MA 140,36,76
buffer_load_dwordx4 v252, s[12:15], s63 offen offset:1024 sc1 lds
MA 140,40,84
RD 28,240,23552
PK 173,150,151
MA 140,44,92
v_exp_f32 v157, v157
MA 140,48,100
v_exp_f32 v158, v158
MA 140,52,108
buffer_load_dwordx4 v252, s[12:15], s63 offen offset:2048 sc1 lds
MA 140,56,116
v_exp_f32 v159, v159
MA 140,60,124
WL 4
v_add_f32 v242, v242, v152
v_add_f32 v242, v242, v153
v_mfma_f32_16x16x32_bf16 v[144:147], a[0:3], a[64:67], v[244:247]
RD 32,240,24576
RD 36,240,25600
MA 144,4,72
buffer_load_dwordx4 v252, s[12:15], s63 offen offset:3072 sc1 lds
MA 144,8,80
s_add_u32 s63, s59, s62
RD 40,240,26624
RD 44,240,27648
MA 144,12,88
WL 4
v_add_f32 v242, v242, v154
v_add_f32 v242, v242, v155
MA 144,16,96
s_add_u32 m0, s58, s62
RD 48,240,28672
PK 170,152,153
MA 144,20,104
PK 171,154,155
v_add_f32 v243, v243, v156
MA 144,24,112
RD 52,240,29696
v_add_f32 v243, v243, v157
MA 144,28,120
v_add_f32 v243, v243, v158
v_add_f32 v243, v243, v159
v_mfma_f32_16x16x32_bf16 v[148:151], a[0:3], a[68:71], v[248:251]
RD 56,240,30720
PK 174,156,157
MA 148,4,76
PK 175,158,159
v_max3_f32 v254, v128, v129, v130
MA 148,8,84
RD 60,240,31744
v_max_f32 v254, v254, v131
MA 148,12,92
v_max3_f32 v255, v132, v133, v134
v_max_f32 v255, v255, v135
MA 148,16,100
v_max3_f32 v254, v254, v136, v137
v_max3_f32 v254, v254, v138, v139
MA 148,20,108
v_max3_f32 v255, v255, v140, v141
v_max3_f32 v255, v255, v142, v143
MA 148,24,116
v_add_u32 v240, s57, v253
MA 148,28,124
WL 4
v_mfma_f32_16x16x32_bf16 v[152:155], a[32:35], a[64:67], v[244:247]
RD 0,240,0
MA 152,36,72
buffer_store_dwordx4 a[176:179], v253, s[16:19], s73 offen offset:0 sc1
MA 152,40,80
RD 4,240,1024
MA 152,44,88
WL 2
MA 152,48,96
RD 8,240,2048
MA 152,52,104
buffer_load_dwordx4 v252, s[12:15], s63 offen offset:0 sc1 lds
MA 152,56,112
RD 12,240,3072
v_max3_f32 v254, v254, v144, v145
MA 152,60,120
v_max3_f32 v254, v254, v146, v147
v_max3_f32 v255, v255, v148, v149
v_mfma_f32_16x16x32_bf16 v[156:159], a[32:35], a[68:71], v[248:251]
RD 16,240,4096
v_max3_f32 v255, v255, v150, v151
MA 156,36,76
buffer_load_dwordx4 v252, s[12:15], s63 offen offset:1024 sc1 lds
MA 156,40,84
RD 20,240,5120
MA 156,44,92
MA 156,48,100
RD 24,240,6144
MA 156,52,108
buffer_load_dwordx4 v252, s[12:15], s63 offen offset:2048 sc1 lds
MA 156,56,116
RD 28,240,7168
MA 156,60,124
WL 6
buffer_store_dwordx4 a[180:183], v253, s[16:19], s73 offen offset:1024 sc1
MB 0,0,160
RD 32,240,8192
RD 36,240,9216
MB 4,0,164
buffer_load_dwordx4 v252, s[12:15], s63 offen offset:3072 sc1 lds
MB 12,4,164
s_add_u32 s63, s63, 4096
RD 40,240,10240
v_max3_f32 v254, v254, v152, v153
MB 8,4,160
WL 7
v_max3_f32 v254, v254, v154, v155
v_max3_f32 v255, v255, v156, v157
MB 16,8,160
s_add_u32 m0, m0, 4096
RD 44,240,11264
v_max3_f32 v255, v255, v158, v159
MB 20,8,164
v_max_f32 v241, v254, v255
v_cmp_lt_f32 vcc, 0x41800000, v241
MB 28,12,164
s_cbranch_vccnz .LRA10
.LRAR10:
RD 48,240,12288
MB 24,12,160
WL 7
v_exp_f32 v128, v128
MB 32,16,160
RD 52,240,13312
RD 56,240,14336
MB 36,16,164
v_exp_f32 v129, v129
MB 44,20,164
RD 60,240,15360
v_add_f32 v242, v242, v128
MB 40,20,160
WL 8
v_exp_f32 v130, v130
MB 48,24,160
RD 0,240,16384
v_add_f32 v242, v242, v129
MB 52,24,164
buffer_load_dwordx4 v252, s[12:15], s63 offen offset:0 sc1 lds
MB 60,28,164
s_add_u32 s59, s59, 0xffff8000
RD 4,240,17408
v_add_f32 v242, v242, v130
MB 56,28,160
WL 8
v_exp_f32 v131, v131
MB 64,32,160
s_add_u32 s7, s58, 32768
RD 8,240,18432
v_add_f32 v242, v242, v131
MB 68,32,164
v_exp_f32 v132, v132
MB 76,36,164
s_sub_u32 s58, s58, 131072
RD 12,240,19456
RD 16,240,20480
MB 72,36,160
WL 9
v_exp_f32 v133, v133
MB 80,40,160
s_min_u32 s58, s7, s58
RD 20,240,21504
v_add_f32 v243, v243, v132
MB 84,40,164
buffer_load_dwordx4 v252, s[12:15], s63 offen offset:1024 sc1 lds
MB 92,44,164
s_add_u32 s6, s57, 32768
RD 24,240,22528
v_add_f32 v243, v243, v133
MB 88,44,160
WL 9
v_exp_f32 v134, v134
MB 96,48,160
s_sub_u32 s57, s57, 131072
RD 28,240,23552
v_add_f32 v243, v243, v134
MB 100,48,164
v_exp_f32 v135, v135
MB 108,52,164
s_min_u32 s57, s6, s57
RD 32,240,24576
v_add_f32 v243, v243, v135
MB 104,52,160
WL 9
v_exp_f32 v136, v136
MB 112,56,160
RD 36,240,25600
RD 40,240,26624
MB 116,56,164
buffer_load_dwordx4 v252, s[12:15], s63 offen offset:2048 sc1 lds
MB 124,60,164
RD 44,240,27648
v_add_f32 v242, v242, v136
MB 120,60,160
WL 10
v_exp_f32 v137, v137
MB 0,0,168
RD 48,240,28672
PK 160,128,129
MB 4,0,172
buffer_store_dwordx4 a[184:187], v253, s[16:19], s73 offen offset:2048 sc1
MB 12,4,172
RD 52,240,29696
PK 161,130,131
MB 8,4,168
)A"
R"A(WL 10
v_exp_f32 v138, v138
MB 16,8,168
RD 56,240,30720
PK 164,132,133
MB 20,8,172
v_exp_f32 v139, v139
MB 28,12,172
RD 60,240,31744
PK 165,134,135
MB 24,12,168
WL 10
v_exp_f32 v140, v140
MB 32,16,168
v_exp_f32 v141, v141
MB 36,16,172
buffer_load_dwordx4 v252, s[12:15], s63 offen offset:3072 sc1 lds
MB 44,20,172
v_exp_f32 v142, v142
MB 40,20,168
WL 8
v_exp_f32 v143, v143
MB 48,24,168
v_exp_f32 v144, v144
MB 52,24,172
v_exp_f32 v145, v145
MB 60,28,172
v_add_u32 v240, s57, v253
v_add_f32 v242, v242, v137
MB 56,28,168
s_waitcnt vmcnt(11) lgkmcnt(0)
s_barrier
v_add_f32 v242, v242, v138
MB 64,32,168
RD 0,240,0
RD 4,240,1024
MB 68,32,172
v_exp_f32 v146, v146
MB 76,36,172
RD 8,240,2048
RD 12,240,3072
MB 72,36,168
v_exp_f32 v147, v147
MB 80,40,168
RD 16,240,4096
v_add_f32 v242, v242, v139
MB 84,40,172
PK 162,136,137
PK 163,138,139
MB 92,44,172
RD 20,240,5120
v_add_f32 v243, v243, v140
MB 88,44,168
v_add_f32 v243, v243, v141
v_add_f32 v243, v243, v142
MB 96,48,168
RD 24,240,6144
v_add_f32 v243, v243, v143
MB 100,48,172
PK 166,140,141
PK 167,142,143
MB 108,52,172
RD 28,240,7168
v_add_f32 v242, v242, v144
MB 104,52,168
v_add_f32 v242, v242, v145
v_add_f32 v242, v242, v146
MB 112,56,168
v_add_f32 v242, v242, v147
MB 116,56,172
buffer_store_dwordx4 a[188:191], v253, s[16:19], s73 offen offset:3072 sc1
MB 124,60,172
s_add_u32 s73, s73, 16384
MB 120,60,168
WL 4
s_cmp_lg_u32 s69, 0
s_cbranch_scc1 .LRB10
.LRBR10:
s_mov_b64 exec, 1
buffer_atomic_add v204, off, s[20:23], s82 sc0
s_mov_b64 exec, -1
v_mfma_f32_16x16x32_bf16 v[128:131], a[0:3], a[64:67], v[244:247]
s_add_u32 s63, s59, s62
RD 32,240,8192
RD 36,240,9216
MA 128,4,72
v_exp_f32 v148, v148
MA 128,8,80
s_add_u32 m0, s58, s62
RD 40,240,10240
RD 44,240,11264
MA 128,12,88
WL 4
v_exp_f32 v149, v149
MA 128,16,96
RD 48,240,12288
PK 168,144,145
MA 128,20,104
buffer_load_dwordx4 v252, s[12:15], s63 offen offset:0 sc1 lds
MA 128,24,112
s_add_u32 s59, s59, 0x18000
RD 52,240,13312
PK 169,146,147
MA 128,28,120
v_exp_f32 v150, v150
v_mfma_f32_16x16x32_bf16 v[132:135], a[0:3], a[68:71], v[248:251]
s_add_u32 s7, s58, 32768
RD 56,240,14336
v_add_f32 v243, v243, v148
MA 132,4,76
buffer_load_dwordx4 v252, s[12:15], s63 offen offset:1024 sc1 lds
MA 132,8,84
s_sub_u32 s58, s58, 131072
RD 60,240,15360
v_add_f32 v243, v243, v149
MA 132,12,92
s_min_u32 s58, s7, s58
v_exp_f32 v151, v151
MA 132,16,100
s_add_u32 s6, s57, 32768
v_exp_f32 v152, v152
MA 132,20,108
buffer_load_dwordx4 v252, s[12:15], s63 offen offset:2048 sc1 lds
MA 132,24,116
v_exp_f32 v153, v153
MA 132,28,124
WL 4
v_exp_f32 v154, v154
v_mfma_f32_16x16x32_bf16 v[136:139], a[32:35], a[64:67], v[244:247]
s_sub_u32 s57, s57, 131072
RD 0,240,16384
RD 4,240,17408
MA 136,36,72
buffer_load_dwordx4 v252, s[12:15], s63 offen offset:3072 sc1 lds
MA 136,40,80
s_add_u32 s63, s63, 4096
RD 8,240,18432
RD 12,240,19456
MA 136,44,88
WL 4
v_exp_f32 v155, v155
MA 136,48,96
s_add_u32 m0, m0, 4096
RD 16,240,20480
v_add_f32 v243, v243, v150
MA 136,52,104
buffer_load_dwordx4 v252, s[12:15], s63 offen offset:0 sc1 lds
MA 136,56,112
s_min_u32 s57, s6, s57
RD 20,240,21504
v_add_f32 v243, v243, v151
MA 136,60,120
v_exp_f32 v156, v156
v_mfma_f32_16x16x32_bf16 v[140:143], a[32:35], a[68:71], v[248:251]
RD 24,240,22528
PK 172,148,149
MA 140,36,76
buffer_load_dwordx4 v252, s[12:15], s63 offen offset:1024 sc1 lds
MA 140,40,84
RD 28,240,23552
PK 173,150,151
MA 140,44,92
v_exp_f32 v157, v157
MA 140,48,100
v_exp_f32 v158, v158
MA 140,52,108
buffer_load_dwordx4 v252, s[12:15], s63 offen offset:2048 sc1 lds
MA 140,56,116
v_exp_f32 v159, v159
MA 140,60,124
WL 4
v_add_f32 v242, v242, v152
v_add_f32 v242, v242, v153
v_mfma_f32_16x16x32_bf16 v[144:147], a[0:3], a[64:67], v[244:247]
RD 32,240,24576
RD 36,240,25600
MA 144,4,72
buffer_load_dwordx4 v252, s[12:15], s63 offen offset:3072 sc1 lds
MA 144,8,80
s_add_u32 s63, s59, s62
RD 40,240,26624
RD 44,240,27648
MA 144,12,88
WL 4
v_add_f32 v242, v242, v154
v_add_f32 v242, v242, v155
MA 144,16,96
s_add_u32 m0, s58, s62
RD 48,240,28672
PK 170,152,153
MA 144,20,104
PK 171,154,155
v_add_f32 v243, v243, v156
MA 144,24,112
RD 52,240,29696
v_add_f32 v243, v243, v157
MA 144,28,120
v_add_f32 v243, v243, v158
v_add_f32 v243, v243, v159
v_mfma_f32_16x16x32_bf16 v[148:151], a[0:3], a[68:71], v[248:251]
RD 56,240,30720
PK 174,156,157
MA 148,4,76
PK 175,158,159
v_max3_f32 v254, v128, v129, v130
MA 148,8,84
RD 60,240,31744
v_max_f32 v254, v254, v131
MA 148,12,92
v_max3_f32 v255, v132, v133, v134
v_max_f32 v255, v255, v135
MA 148,16,100
v_max3_f32 v254, v254, v136, v137
v_max3_f32 v254, v254, v138, v139
MA 148,20,108
v_max3_f32 v255, v255, v140, v141
v_max3_f32 v255, v255, v142, v143
MA 148,24,116
v_add_u32 v240, s57, v253
MA 148,28,124
WL 4
v_mfma_f32_16x16x32_bf16 v[152:155], a[32:35], a[64:67], v[244:247]
RD 0,240,0
MA 152,36,72
buffer_store_dwordx4 a[192:195], v253, s[16:19], s73 offen offset:0 sc1
MA 152,40,80
RD 4,240,1024
MA 152,44,88
WL 2
MA 152,48,96
RD 8,240,2048
MA 152,52,104
buffer_load_dwordx4 v252, s[12:15], s63 offen offset:0 sc1 lds
MA 152,56,112
RD 12,240,3072
v_max3_f32 v254, v254, v144, v145
MA 152,60,120
v_max3_f32 v254, v254, v146, v147
v_max3_f32 v255, v255, v148, v149
v_mfma_f32_16x16x32_bf16 v[156:159], a[32:35], a[68:71], v[248:251]
RD 16,240,4096
v_max3_f32 v255, v255, v150, v151
MA 156,36,76
buffer_load_dwordx4 v252, s[12:15], s63 offen offset:1024 sc1 lds
MA 156,40,84
RD 20,240,5120
MA 156,44,92
MA 156,48,100
RD 24,240,6144
MA 156,52,108
buffer_load_dwordx4 v252, s[12:15], s63 offen offset:2048 sc1 lds
MA 156,56,116
RD 28,240,7168
MA 156,60,124
WL 6
buffer_store_dwordx4 a[196:199], v253, s[16:19], s73 offen offset:1024 sc1
MB 0,0,160
RD 32,240,8192
RD 36,240,9216
MB 4,0,164
buffer_load_dwordx4 v252, s[12:15], s63 offen offset:3072 sc1 lds
MB 12,4,164
s_add_u32 s63, s63, 4096
RD 40,240,10240
v_max3_f32 v254, v254, v152, v153
MB 8,4,160
WL 7
v_max3_f32 v254, v254, v154, v155
v_max3_f32 v255, v255, v156, v157
MB 16,8,160
s_add_u32 m0, m0, 4096
RD 44,240,11264
v_max3_f32 v255, v255, v158, v159
MB 20,8,164
v_max_f32 v241, v254, v255
v_cmp_lt_f32 vcc, 0x41800000, v241
MB 28,12,164
s_cbranch_vccnz .LRA11
.LRAR11:
RD 48,240,12288
MB 24,12,160
WL 7
v_exp_f32 v128, v128
MB 32,16,160
RD 52,240,13312
RD 56,240,14336
MB 36,16,164
v_exp_f32 v129, v129
MB 44,20,164
RD 60,240,15360
v_add_f32 v242, v242, v128
MB 40,20,160
WL 8
v_exp_f32 v130, v130
MB 48,24,160
RD 0,240,16384
v_add_f32 v242, v242, v129
MB 52,24,164
buffer_load_dwordx4 v252, s[12:15], s63 offen offset:0 sc1 lds
MB 60,28,164
s_add_u32 s59, s59, 0xffff8000
RD 4,240,17408
v_add_f32 v242, v242, v130
MB 56,28,160
WL 8
v_exp_f32 v131, v131
MB 64,32,160
s_add_u32 s7, s58, 32768
RD 8,240,18432
v_add_f32 v242, v242, v131
MB 68,32,164
v_exp_f32 v132, v132
MB 76,36,164
s_sub_u32 s58, s58, 131072
RD 12,240,19456
RD 16,240,20480
MB 72,36,160
WL 9
v_exp_f32 v133, v133
MB 80,40,160
s_min_u32 s58, s7, s58
RD 20,240,21504
v_add_f32 v243, v243, v132
MB 84,40,164
buffer_load_dwordx4 v252, s[12:15], s63 offen offset:1024 sc1 lds
MB 92,44,164
s_add_u32 s6, s57, 32768
RD 24,240,22528
v_add_f32 v243, v243, v133
MB 88,44,160
WL 9
v_exp_f32 v134, v134
MB 96,48,160
s_sub_u32 s57, s57, 131072
RD 28,240,23552
v_add_f32 v243, v243, v134
MB 100,48,164
v_exp_f32 v135, v135
MB 108,52,164
s_min_u32 s57, s6, s57
RD 32,240,24576
v_add_f32 v243, v243, v135
MB 104,52,160
WL 9
v_exp_f32 v136, v136
MB 112,56,160
RD 36,240,25600
RD 40,240,26624
MB 116,56,164
buffer_load_dwordx4 v252, s[12:15], s63 offen offset:2048 sc1 lds
MB 124,60,164
RD 44,240,27648
v_add_f32 v242, v242, v136
MB 120,60,160
WL 10
v_exp_f32 v137, v137
MB 0,0,168
RD 48,240,28672
PK 160,128,129
MB 4,0,172
buffer_store_dwordx4 a[200:203], v253, s[16:19], s73 offen offset:2048 sc1
MB 12,4,172
RD 52,240,29696
PK 161,130,131
MB 8,4,168
WL 10
v_exp_f32 v138, v138
MB 16,8,168
RD 56,240,30720
PK 164,132,133
MB 20,8,172
v_exp_f32 v139, v139
MB 28,12,172
RD 60,240,31744
PK 165,134,135
MB 24,12,168
WL 10
v_exp_f32 v140, v140
MB 32,16,168
v_exp_f32 v141, v141
MB 36,16,172
buffer_load_dwordx4 v252, s[12:15], s63 offen offset:3072 sc1 lds
MB 44,20,172
v_exp_f32 v142, v142
MB 40,20,168
WL 8
v_exp_f32 v143, v143
MB 48,24,168
v_exp_f32 v144, v144
MB 52,24,172
v_exp_f32 v145, v145
MB 60,28,172
v_add_u32 v240, s57, v253
v_add_f32 v242, v242, v137
MB 56,28,168
s_waitcnt vmcnt(11) lgkmcnt(0)
s_barrier
v_add_f32 v242, v242, v138
MB 64,32,168
RD 0,240,0
RD 4,240,1024
MB 68,32,172
v_exp_f32 v146, v146
MB 76,36,172
RD 8,240,2048
RD 12,240,3072
MB 72,36,168
v_exp_f32 v147, v147
MB 80,40,168
RD 16,240,4096
v_add_f32 v242, v242, v139
MB 84,40,172
PK 162,136,137
PK 163,138,139
MB 92,44,172
RD 20,240,5120
v_add_f32 v243, v243, v140
MB 88,44,168
v_add_f32 v243, v243, v141
v_add_f32 v243, v243, v142
MB 96,48,168
RD 24,240,6144
v_add_f32 v243, v243, v143
MB 100,48,172
PK 166,140,141
PK 167,142,143
MB 108,52,172
RD 28,240,7168
v_add_f32 v242, v242, v144
MB 104,52,168
v_add_f32 v242, v242, v145
v_add_f32 v242, v242, v146
MB 112,56,168
v_add_f32 v242, v242, v147
MB 116,56,172
buffer_store_dwordx4 a[204:207], v253, s[16:19], s73 offen offset:3072 sc1
MB 124,60,172
s_add_u32 s73, s73, 16384
MB 120,60,168
WL 4
s_cmp_lg_u32 s69, 0
s_cbranch_scc1 .LRB11
.LRBR11:
v_mfma_f32_16x16x32_bf16 v[128:131], a[0:3], a[64:67], v[244:247]
s_add_u32 s63, s59, s62
RD 32,240,8192
RD 36,240,9216
MA 128,4,72
v_exp_f32 v148, v148
MA 128,8,80
s_add_u32 m0, s58, s62
RD 40,240,10240
RD 44,240,11264
MA 128,12,88
WL 4
v_exp_f32 v149, v149
MA 128,16,96
RD 48,240,12288
PK 168,144,145
MA 128,20,104
buffer_load_dwordx4 v252, s[12:15], s63 offen offset:0 sc1 lds
MA 128,24,112
s_add_u32 s59, s59, 0x18000
RD 52,240,13312
PK 169,146,147
MA 128,28,120
v_exp_f32 v150, v150
v_mfma_f32_16x16x32_bf16 v[132:135], a[0:3], a[68:71], v[248:251]
s_add_u32 s7, s58, 32768
RD 56,240,14336
v_add_f32 v243, v243, v148
MA 132,4,76
buffer_load_dwordx4 v252, s[12:15], s63 offen offset:1024 sc1 lds
MA 132,8,84
s_sub_u32 s58, s58, 131072
RD 60,240,15360
v_add_f32 v243, v243, v149
MA 132,12,92
s_min_u32 s58, s7, s58
v_exp_f32 v151, v151
MA 132,16,100
s_add_u32 s6, s57, 32768
v_exp_f32 v152, v152
MA 132,20,108
buffer_load_dwordx4 v252, s[12:15], s63 offen offset:2048 sc1 lds
MA 132,24,116
v_exp_f32 v153, v153
MA 132,28,124
WL 4
v_exp_f32 v154, v154
v_mfma_f32_16x16x32_bf16 v[136:139], a[32:35], a[64:67], v[244:247]
s_sub_u32 s57, s57, 131072
RD 0,240,16384
RD 4,240,17408
MA 136,36,72
buffer_load_dwordx4 v252, s[12:15], s63 offen offset:3072 sc1 lds
MA 136,40,80
s_add_u32 s63, s63, 4096
RD 8,240,18432
RD 12,240,19456
MA 136,44,88
WL 4
v_exp_f32 v155, v155
MA 136,48,96
s_add_u32 m0, m0, 4096
RD 16,240,20480
v_add_f32 v243, v243, v150
MA 136,52,104
buffer_load_dwordx4 v252, s[12:15], s63 offen offset:0 sc1 lds
MA 136,56,112
s_min_u32 s57, s6, s57
RD 20,240,21504
v_add_f32 v243, v243, v151
MA 136,60,120
v_exp_f32 v156, v156
v_mfma_f32_16x16x32_bf16 v[140:143], a[32:35], a[68:71], v[248:251]
RD 24,240,22528
PK 172,148,149
MA 140,36,76
buffer_load_dwordx4 v252, s[12:15], s63 offen offset:1024 sc1 lds
MA 140,40,84
RD 28,240,23552
PK 173,150,151
MA 140,44,92
v_exp_f32 v157, v157
MA 140,48,100
v_exp_f32 v158, v158
MA 140,52,108
buffer_load_dwordx4 v252, s[12:15], s63 offen offset:2048 sc1 lds
MA 140,56,116
v_exp_f32 v159, v159
MA 140,60,124
WL 4
v_add_f32 v242, v242, v152
v_add_f32 v242, v242, v153
v_mfma_f32_16x16x32_bf16 v[144:147], a[0:3], a[64:67], v[244:247]
RD 32,240,24576
RD 36,240,25600
MA 144,4,72
buffer_load_dwordx4 v252, s[12:15], s63 offen offset:3072 sc1 lds
MA 144,8,80
s_add_u32 s63, s59, s62
RD 40,240,26624
RD 44,240,27648
MA 144,12,88
WL 4
v_add_f32 v242, v242, v154
v_add_f32 v242, v242, v155
MA 144,16,96
s_add_u32 m0, s58, s62
RD 48,240,28672
PK 170,152,153
MA 144,20,104
s_getpc_b64 s[98:99]
.LCT3:
PK 171,154,155
v_add_f32 v243, v243, v156
MA 144,24,112
s_lshr_b32 s78, s9, 4
RD 52,240,29696
v_add_f32 v243, v243, v157
MA 144,28,120
s_lshl_b32 s78, s78, 12
v_add_f32 v243, v243, v158
v_add_f32 v243, v243, v159
v_mfma_f32_16x16x32_bf16 v[148:151], a[0:3], a[68:71], v[248:251]
s_add_u32 s78, s78, s56
)A"
R"A(RD 56,240,30720
PK 174,156,157
MA 148,4,76
s_min_u32 s78, s78, .LCEND-.LCT3-1024
PK 175,158,159
v_max3_f32 v254, v128, v129, v130
MA 148,8,84
RD 60,240,31744
v_max_f32 v254, v254, v131
MA 148,12,92
v_max3_f32 v255, v132, v133, v134
v_max_f32 v255, v255, v135
MA 148,16,100
v_max3_f32 v254, v254, v136, v137
v_max3_f32 v254, v254, v138, v139
MA 148,20,108
v_max3_f32 v255, v255, v140, v141
v_max3_f32 v255, v255, v142, v143
MA 148,24,116
v_add_u32 v240, s57, v253
v_min_u32 v184, 0x70, v253
MA 148,28,124
WL 4
v_lshl_add_u32 v184, v184, 3, s78
v_mfma_f32_16x16x32_bf16 v[152:155], a[32:35], a[64:67], v[244:247]
RD 0,240,0
MA 152,36,72
buffer_store_dwordx4 a[208:211], v253, s[16:19], s73 offen offset:0 sc1
MA 152,40,80
RD 4,240,1024
MA 152,44,88
WL 2
global_load_dword v184, v184, s[98:99]
MA 152,48,96
RD 8,240,2048
MA 152,52,104
buffer_load_dwordx4 v252, s[12:15], s63 offen offset:0 sc1 lds
MA 152,56,112
RD 12,240,3072
v_max3_f32 v254, v254, v144, v145
MA 152,60,120
v_max3_f32 v254, v254, v146, v147
v_max3_f32 v255, v255, v148, v149
v_mfma_f32_16x16x32_bf16 v[156:159], a[32:35], a[68:71], v[248:251]
RD 16,240,4096
v_max3_f32 v255, v255, v150, v151
MA 156,36,76
buffer_load_dwordx4 v252, s[12:15], s63 offen offset:1024 sc1 lds
MA 156,40,84
RD 20,240,5120
MA 156,44,92
MA 156,48,100
RD 24,240,6144
MA 156,52,108
buffer_load_dwordx4 v252, s[12:15], s63 offen offset:2048 sc1 lds
MA 156,56,116
RD 28,240,7168
MA 156,60,124
WL 6
buffer_store_dwordx4 a[212:215], v253, s[16:19], s73 offen offset:1024 sc1
MB 0,0,160
RD 32,240,8192
RD 36,240,9216
MB 4,0,164
buffer_load_dwordx4 v252, s[12:15], s63 offen offset:3072 sc1 lds
MB 12,4,164
s_add_u32 s63, s63, 4096
RD 40,240,10240
v_max3_f32 v254, v254, v152, v153
MB 8,4,160
WL 7
v_max3_f32 v254, v254, v154, v155
v_max3_f32 v255, v255, v156, v157
MB 16,8,160
s_add_u32 m0, m0, 4096
RD 44,240,11264
v_max3_f32 v255, v255, v158, v159
MB 20,8,164
v_max_f32 v241, v254, v255
v_cmp_lt_f32 vcc, 0x41800000, v241
MB 28,12,164
s_cbranch_vccnz .LRA12
.LRAR12:
RD 48,240,12288
MB 24,12,160
WL 7
v_exp_f32 v128, v128
MB 32,16,160
RD 52,240,13312
RD 56,240,14336
MB 36,16,164
v_exp_f32 v129, v129
MB 44,20,164
RD 60,240,15360
v_add_f32 v242, v242, v128
MB 40,20,160
WL 8
v_exp_f32 v130, v130
MB 48,24,160
RD 0,240,16384
v_add_f32 v242, v242, v129
MB 52,24,164
buffer_load_dwordx4 v252, s[12:15], s63 offen offset:0 sc1 lds
MB 60,28,164
s_add_u32 s59, s59, 0xffff8000
RD 4,240,17408
v_add_f32 v242, v242, v130
MB 56,28,160
WL 8
v_exp_f32 v131, v131
MB 64,32,160
s_add_u32 s7, s58, 32768
RD 8,240,18432
v_add_f32 v242, v242, v131
MB 68,32,164
v_exp_f32 v132, v132
MB 76,36,164
s_sub_u32 s58, s58, 131072
RD 12,240,19456
RD 16,240,20480
MB 72,36,160
WL 9
v_exp_f32 v133, v133
MB 80,40,160
s_min_u32 s58, s7, s58
RD 20,240,21504
v_add_f32 v243, v243, v132
MB 84,40,164
buffer_load_dwordx4 v252, s[12:15], s63 offen offset:1024 sc1 lds
MB 92,44,164
s_add_u32 s6, s57, 32768
RD 24,240,22528
v_add_f32 v243, v243, v133
MB 88,44,160
WL 9
v_exp_f32 v134, v134
MB 96,48,160
s_sub_u32 s57, s57, 131072
RD 28,240,23552
v_add_f32 v243, v243, v134
MB 100,48,164
v_exp_f32 v135, v135
MB 108,52,164
s_min_u32 s57, s6, s57
RD 32,240,24576
v_add_f32 v243, v243, v135
MB 104,52,160
WL 9
v_exp_f32 v136, v136
MB 112,56,160
RD 36,240,25600
RD 40,240,26624
MB 116,56,164
buffer_load_dwordx4 v252, s[12:15], s63 offen offset:2048 sc1 lds
MB 124,60,164
RD 44,240,27648
v_add_f32 v242, v242, v136
MB 120,60,160
WL 10
v_exp_f32 v137, v137
MB 0,0,168
RD 48,240,28672
PK 160,128,129
MB 4,0,172
buffer_store_dwordx4 a[216:219], v253, s[16:19], s73 offen offset:2048 sc1
MB 12,4,172
RD 52,240,29696
PK 161,130,131
MB 8,4,168
WL 10
v_exp_f32 v138, v138
MB 16,8,168
RD 56,240,30720
PK 164,132,133
MB 20,8,172
v_exp_f32 v139, v139
MB 28,12,172
RD 60,240,31744
PK 165,134,135
MB 24,12,168
WL 10
v_exp_f32 v140, v140
MB 32,16,168
v_exp_f32 v141, v141
MB 36,16,172
buffer_load_dwordx4 v252, s[12:15], s63 offen offset:3072 sc1 lds
MB 44,20,172
v_exp_f32 v142, v142
MB 40,20,168
WL 8
v_exp_f32 v143, v143
MB 48,24,168
v_exp_f32 v144, v144
MB 52,24,172
v_exp_f32 v145, v145
MB 60,28,172
v_add_u32 v240, s57, v253
v_add_f32 v242, v242, v137
MB 56,28,168
s_waitcnt vmcnt(12) lgkmcnt(0)
s_barrier
v_add_f32 v242, v242, v138
MB 64,32,168
RD 0,240,0
RD 4,240,1024
MB 68,32,172
v_exp_f32 v146, v146
MB 76,36,172
RD 8,240,2048
RD 12,240,3072
MB 72,36,168
v_exp_f32 v147, v147
MB 80,40,168
RD 16,240,4096
v_add_f32 v242, v242, v139
MB 84,40,172
PK 162,136,137
PK 163,138,139
MB 92,44,172
RD 20,240,5120
v_add_f32 v243, v243, v140
MB 88,44,168
v_add_f32 v243, v243, v141
v_add_f32 v243, v243, v142
MB 96,48,168
RD 24,240,6144
v_add_f32 v243, v243, v143
MB 100,48,172
PK 166,140,141
PK 167,142,143
MB 108,52,172
RD 28,240,7168
v_add_f32 v242, v242, v144
MB 104,52,168
v_add_f32 v242, v242, v145
v_add_f32 v242, v242, v146
MB 112,56,168
v_add_f32 v242, v242, v147
MB 116,56,172
buffer_store_dwordx4 a[220:223], v253, s[16:19], s73 offen offset:3072 sc1
MB 124,60,172
s_add_u32 s73, s73, 16384
MB 120,60,168
WL 4
s_cmp_lg_u32 s69, 0
s_cbranch_scc1 .LRB12
.LRBR12:
v_readfirstlane_b32 s79, v204
v_mfma_f32_16x16x32_bf16 v[128:131], a[0:3], a[64:67], v[244:247]
s_add_u32 s63, s59, s62
RD 32,240,8192
RD 36,240,9216
MA 128,4,72
s_min_u32 s50, s79, 13
v_exp_f32 v148, v148
MA 128,8,80
s_add_u32 m0, s58, s62
RD 40,240,10240
RD 44,240,11264
MA 128,12,88
WL 4
v_exp_f32 v149, v149
MA 128,16,96
s_mul_i32 s54, s8, 14
RD 48,240,12288
PK 168,144,145
MA 128,20,104
buffer_load_dwordx4 v252, s[12:15], s63 offen offset:0 sc1 lds
MA 128,24,112
s_add_u32 s59, s59, 0x18000
RD 52,240,13312
PK 169,146,147
MA 128,28,120
s_add_u32 s54, s54, s50
v_exp_f32 v150, v150
v_mfma_f32_16x16x32_bf16 v[132:135], a[0:3], a[68:71], v[248:251]
s_add_u32 s7, s58, 32768
RD 56,240,14336
v_add_f32 v243, v243, v148
MA 132,4,76
buffer_load_dwordx4 v252, s[12:15], s63 offen offset:1024 sc1 lds
MA 132,8,84
s_sub_u32 s58, s58, 131072
RD 60,240,15360
v_add_f32 v243, v243, v149
MA 132,12,92
s_min_u32 s58, s7, s58
v_exp_f32 v151, v151
MA 132,16,100
s_add_u32 s6, s57, 32768
v_exp_f32 v152, v152
MA 132,20,108
buffer_load_dwordx4 v252, s[12:15], s63 offen offset:2048 sc1 lds
MA 132,24,116
s_lshr_b32 s84, s54, 3
v_exp_f32 v153, v153
MA 132,28,124
WL 4
v_exp_f32 v154, v154
v_mfma_f32_16x16x32_bf16 v[136:139], a[32:35], a[64:67], v[244:247]
s_sub_u32 s57, s57, 131072
RD 0,240,16384
RD 4,240,17408
MA 136,36,72
buffer_load_dwordx4 v252, s[12:15], s63 offen offset:3072 sc1 lds
MA 136,40,80
s_add_u32 s63, s63, 4096
RD 8,240,18432
RD 12,240,19456
MA 136,44,88
WL 4
v_exp_f32 v155, v155
MA 136,48,96
s_add_u32 m0, m0, 4096
RD 16,240,20480
v_add_f32 v243, v243, v150
MA 136,52,104
buffer_load_dwordx4 v252, s[12:15], s63 offen offset:0 sc1 lds
MA 136,56,112
s_min_u32 s57, s6, s57
RD 20,240,21504
v_add_f32 v243, v243, v151
MA 136,60,120
s_lshl_b32 s84, s84, 6
v_exp_f32 v156, v156
v_mfma_f32_16x16x32_bf16 v[140:143], a[32:35], a[68:71], v[248:251]
s_bfe_u32 s83, s54, 0x20001
RD 24,240,22528
PK 172,148,149
MA 140,36,76
buffer_load_dwordx4 v252, s[12:15], s63 offen offset:1024 sc1 lds
MA 140,40,84
s_lshr_b32 s82, s54, 4
RD 28,240,23552
PK 173,150,151
MA 140,44,92
s_lshl_b32 s82, s82, 2
v_exp_f32 v157, v157
MA 140,48,100
s_add_u32 s82, s82, s83
v_exp_f32 v158, v158
MA 140,52,108
buffer_load_dwordx4 v252, s[12:15], s63 offen offset:2048 sc1 lds
MA 140,56,116
s_lshl_b32 s82, s82, 1
v_exp_f32 v159, v159
MA 140,60,124
WL 4
v_add_f32 v242, v242, v152
v_add_f32 v242, v242, v153
v_mfma_f32_16x16x32_bf16 v[144:147], a[0:3], a[64:67], v[244:247]
s_bfe_u32 s50, s54, 0x10003
RD 32,240,24576
RD 36,240,25600
MA 144,4,72
buffer_load_dwordx4 v252, s[12:15], s63 offen offset:3072 sc1 lds
MA 144,8,80
s_add_u32 s63, s59, s62
RD 40,240,26624
RD 44,240,27648
MA 144,12,88
WL 4
v_add_f32 v242, v242, v154
v_add_f32 v242, v242, v155
MA 144,16,96
s_add_u32 m0, s58, s62
RD 48,240,28672
PK 170,152,153
MA 144,20,104
s_add_u32 s82, s82, s50
PK 171,154,155
v_add_f32 v243, v243, v156
MA 144,24,112
s_lshl_b32 s82, s82, 1
RD 52,240,29696
v_add_f32 v243, v243, v157
MA 144,28,120
s_and_b32 s50, s54, 1
v_add_f32 v243, v243, v158
v_add_f32 v243, v243, v159
v_mfma_f32_16x16x32_bf16 v[148:151], a[0:3], a[68:71], v[248:251]
s_add_u32 s82, s82, s50
RD 56,240,30720
PK 174,156,157
MA 148,4,76
s_lshl_b32 s82, s82, 10
PK 175,158,159
v_max3_f32 v254, v128, v129, v130
MA 148,8,84
s_lshl_b32 s50, s50, 2
RD 60,240,31744
v_max_f32 v254, v254, v131
MA 148,12,92
s_add_u32 s83, s83, s50
v_max3_f32 v255, v132, v133, v134
v_max_f32 v255, v255, v135
MA 148,16,100
s_lshl_b32 s50, s77, 3
v_max3_f32 v254, v254, v136, v137
v_max3_f32 v254, v254, v138, v139
MA 148,20,108
s_add_u32 s83, s83, s50
v_max3_f32 v255, v255, v140, v141
v_max3_f32 v255, v255, v142, v143
MA 148,24,116
s_mul_i32 s83, s83, 14336
v_add_u32 v240, s57, v253
MA 148,28,124
WL 4
v_mfma_f32_16x16x32_bf16 v[152:155], a[32:35], a[64:67], v[244:247]
s_add_u32 s83, s83, s84
RD 0,240,0
MA 152,36,72
buffer_store_dwordx4 a[224:227], v253, s[16:19], s73 offen offset:0 sc1
MA 152,40,80
s_add_u32 s54, s54, 56
RD 4,240,1024
MA 152,44,88
WL 2
MA 152,48,96
s_lshr_b32 s64, s54, 3
RD 8,240,2048
MA 152,52,104
buffer_load_dwordx4 v252, s[12:15], s63 offen offset:0 sc1 lds
MA 152,56,112
RD 12,240,3072
v_max3_f32 v254, v254, v144, v145
MA 152,60,120
v_max3_f32 v254, v254, v146, v147
v_max3_f32 v255, v255, v148, v149
v_mfma_f32_16x16x32_bf16 v[156:159], a[32:35], a[68:71], v[248:251]
RD 16,240,4096
v_max3_f32 v255, v255, v150, v151
MA 156,36,76
buffer_load_dwordx4 v252, s[12:15], s63 offen offset:1024 sc1 lds
MA 156,40,84
RD 20,240,5120
MA 156,44,92
MA 156,48,100
RD 24,240,6144
MA 156,52,108
buffer_load_dwordx4 v252, s[12:15], s63 offen offset:2048 sc1 lds
MA 156,56,116
RD 28,240,7168
MA 156,60,124
WL 6
buffer_store_dwordx4 a[228:231], v253, s[16:19], s73 offen offset:1024 sc1
MB 0,0,160
RD 32,240,8192
RD 36,240,9216
MB 4,0,164
buffer_load_dwordx4 v252, s[12:15], s63 offen offset:3072 sc1 lds
MB 12,4,164
s_add_u32 s63, s63, 4096
RD 40,240,10240
v_max3_f32 v254, v254, v152, v153
MB 8,4,160
WL 7
v_max3_f32 v254, v254, v154, v155
v_max3_f32 v255, v255, v156, v157
MB 16,8,160
s_add_u32 m0, m0, 4096
RD 44,240,11264
v_max3_f32 v255, v255, v158, v159
MB 20,8,164
v_max_f32 v241, v254, v255
v_cmp_lt_f32 vcc, 0x41800000, v241
MB 28,12,164
s_cbranch_vccnz .LRA13
.LRAR13:
RD 48,240,12288
v_add_u32 v201, s72, v199
MB 24,12,160
WL 7
v_exp_f32 v128, v128
MB 32,16,160
s_lshl_b32 s64, s64, 6
RD 52,240,13312
RD 56,240,14336
MB 36,16,164
s_bfe_u32 s85, s54, 0x20001
v_exp_f32 v129, v129
MB 44,20,164
s_lshr_b32 s80, s54, 4
RD 60,240,15360
v_add_f32 v242, v242, v128
MB 40,20,160
WL 8
v_exp_f32 v130, v130
MB 48,24,160
s_lshl_b32 s80, s80, 2
RD 0,240,16384
v_add_f32 v242, v242, v129
MB 52,24,164
buffer_load_dwordx4 v252, s[12:15], s63 offen offset:0 sc1 lds
MB 60,28,164
s_add_u32 s59, s59, 0xffff8000
RD 4,240,17408
v_add_f32 v242, v242, v130
MB 56,28,160
WL 8
v_exp_f32 v131, v131
MB 64,32,160
s_add_u32 s7, s58, 32768
RD 8,240,18432
v_add_f32 v242, v242, v131
MB 68,32,164
s_add_u32 s80, s80, s85
v_exp_f32 v132, v132
MB 76,36,164
s_sub_u32 s58, s58, 131072
RD 12,240,19456
RD 16,240,20480
MB 72,36,160
WL 9
v_exp_f32 v133, v133
MB 80,40,160
s_min_u32 s58, s7, s58
RD 20,240,21504
v_add_f32 v243, v243, v132
MB 84,40,164
buffer_load_dwordx4 v252, s[12:15], s63 offen offset:1024 sc1 lds
MB 92,44,164
s_add_u32 s6, s57, 32768
RD 24,240,22528
v_add_f32 v243, v243, v133
MB 88,44,160
WL 9
v_exp_f32 v134, v134
MB 96,48,160
s_sub_u32 s57, s57, 131072
RD 28,240,23552
v_add_f32 v243, v243, v134
MB 100,48,164
s_lshl_b32 s80, s80, 1
v_exp_f32 v135, v135
MB 108,52,164
s_min_u32 s57, s6, s57
RD 32,240,24576
v_add_f32 v243, v243, v135
MB 104,52,160
WL 9
v_exp_f32 v136, v136
MB 112,56,160
s_bfe_u32 s50, s54, 0x10003
RD 36,240,25600
RD 40,240,26624
MB 116,56,164
buffer_load_dwordx4 v252, s[12:15], s63 offen offset:2048 sc1 lds
MB 124,60,164
s_add_u32 s80, s80, s50
RD 44,240,27648
v_add_f32 v242, v242, v136
MB 120,60,160
WL 10
v_exp_f32 v137, v137
MB 0,0,168
s_lshl_b32 s80, s80, 1
RD 48,240,28672
PK 160,128,129
)A"
R"A(MB 4,0,172
buffer_store_dwordx4 a[232:235], v253, s[16:19], s73 offen offset:2048 sc1
MB 12,4,172
s_and_b32 s50, s54, 1
RD 52,240,29696
PK 161,130,131
MB 8,4,168
WL 10
v_exp_f32 v138, v138
MB 16,8,168
s_add_u32 s80, s80, s50
RD 56,240,30720
PK 164,132,133
MB 20,8,172
s_lshl_b32 s80, s80, 10
v_exp_f32 v139, v139
MB 28,12,172
s_lshl_b32 s50, s50, 2
RD 60,240,31744
PK 165,134,135
MB 24,12,168
WL 10
v_exp_f32 v140, v140
MB 32,16,168
s_lshl_b32 s65, s11, 2
v_exp_f32 v141, v141
MB 36,16,172
buffer_load_dwordx4 v252, s[12:15], s63 offen offset:3072 sc1 lds
MB 44,20,172
s_mov_b32 s51, 0x32222222
v_exp_f32 v142, v142
MB 40,20,168
WL 8
v_exp_f32 v143, v143
MB 48,24,168
s_lshr_b32 s65, s51, s65
v_exp_f32 v144, v144
MB 52,24,172
s_and_b32 s65, s65, 15
v_exp_f32 v145, v145
MB 60,28,172
s_add_u32 s65, s65, 4
v_add_u32 v240, s57, v253
v_add_f32 v242, v242, v137
MB 56,28,168
s_waitcnt vmcnt(11) lgkmcnt(0)
s_barrier
v_add_f32 v242, v242, v138
MB 64,32,168
s_add_u32 s85, s85, s50
RD 0,240,0
RD 4,240,1024
MB 68,32,172
s_lshl_b32 s50, s77, 3
v_exp_f32 v146, v146
MB 76,36,172
s_add_u32 s85, s85, s50
RD 8,240,2048
RD 12,240,3072
MB 72,36,168
v_exp_f32 v147, v147
MB 80,40,168
s_mul_i32 s85, s85, 14336
RD 16,240,4096
v_add_f32 v242, v242, v139
MB 84,40,172
s_add_u32 s85, s85, s64
PK 162,136,137
PK 163,138,139
MB 92,44,172
s_mul_i32 s54, s77, 0xe0000
RD 20,240,5120
v_add_f32 v243, v243, v140
MB 88,44,168
v_add_f32 v243, v243, v141
v_add_f32 v243, v243, v142
MB 96,48,168
s_add_u32 s54, s54, 0x6000000
RD 24,240,6144
v_add_f32 v243, v243, v143
MB 100,48,172
s_add_u32 s50, s79, 2
PK 166,140,141
PK 167,142,143
MB 108,52,172
s_lshr_b32 s50, s50, 4
RD 28,240,7168
v_add_f32 v242, v242, v144
MB 104,52,168
v_add_f32 v242, v242, v145
v_add_f32 v242, v242, v146
MB 112,56,168
s_min_u32 s50, s50, 1
v_add_f32 v242, v242, v147
s_lshl_b32 s50, s50, 30
MB 116,56,172
buffer_store_dwordx4 a[236:239], v253, s[16:19], s73 offen offset:3072 sc1
MB 124,60,172
s_add_u32 s73, s73, 16384
s_add_u32 s80, s80, s50
s_add_u32 s50, s82, s50
MB 120,60,168
WL 4
s_cmp_lg_u32 s69, 0
s_cbranch_scc1 .LRB13
.LRBR13:
; attention main loop
.LATLA:
v_mfma_f32_16x16x32_bf16 v[128:131], a[0:3], a[64:67], v[244:247]
s_add_u32 s63, s59, s62
RD 32,240,8192
RD 36,240,9216
MA 128,4,72
buffer_load_dwordx2 v[202:203], v198, s[20:23], s55 offen sc1
MA 128,8,80
s_add_u32 m0, s58, s62
RD 40,240,10240
RD 44,240,11264
MA 128,12,88
WL 4
v_exp_f32 v148, v148
MA 128,16,96
RD 48,240,12288
PK 168,144,145
MA 128,20,104
buffer_load_dwordx4 v252, s[12:15], s63 offen offset:0 sc1 lds
MA 128,24,112
s_add_u32 s59, s59, 0x18000
RD 52,240,13312
PK 169,146,147
MA 128,28,120
v_exp_f32 v149, v149
v_mfma_f32_16x16x32_bf16 v[132:135], a[0:3], a[68:71], v[248:251]
s_add_u32 s7, s58, 32768
RD 56,240,14336
v_add_f32 v243, v243, v148
MA 132,4,76
buffer_load_dwordx4 v252, s[12:15], s63 offen offset:1024 sc1 lds
MA 132,8,84
s_sub_u32 s58, s58, 131072
RD 60,240,15360
v_add_f32 v243, v243, v149
MA 132,12,92
s_min_u32 s58, s7, s58
v_exp_f32 v150, v150
MA 132,16,100
s_add_u32 s6, s57, 32768
v_exp_f32 v151, v151
MA 132,20,108
buffer_load_dwordx4 v252, s[12:15], s63 offen offset:2048 sc1 lds
MA 132,24,116
v_exp_f32 v152, v152
MA 132,28,124
WL 4
v_exp_f32 v153, v153
v_mfma_f32_16x16x32_bf16 v[136:139], a[32:35], a[64:67], v[244:247]
s_sub_u32 s57, s57, 131072
RD 0,240,16384
RD 4,240,17408
MA 136,36,72
buffer_load_dwordx4 v252, s[12:15], s63 offen offset:3072 sc1 lds
MA 136,40,80
s_add_u32 s63, s63, 4096
RD 8,240,18432
RD 12,240,19456
MA 136,44,88
WL 4
v_exp_f32 v154, v154
MA 136,48,96
s_add_u32 m0, m0, 4096
RD 16,240,20480
v_add_f32 v243, v243, v150
MA 136,52,104
buffer_load_dwordx4 v252, s[12:15], s63 offen offset:0 sc1 lds
MA 136,56,112
s_min_u32 s57, s6, s57
RD 20,240,21504
v_add_f32 v243, v243, v151
MA 136,60,120
v_exp_f32 v155, v155
v_mfma_f32_16x16x32_bf16 v[140:143], a[32:35], a[68:71], v[248:251]
RD 24,240,22528
PK 172,148,149
MA 140,36,76
buffer_load_dwordx4 v252, s[12:15], s63 offen offset:1024 sc1 lds
MA 140,40,84
RD 28,240,23552
PK 173,150,151
MA 140,44,92
v_exp_f32 v156, v156
MA 140,48,100
v_exp_f32 v157, v157
MA 140,52,108
buffer_load_dwordx4 v252, s[12:15], s63 offen offset:2048 sc1 lds
MA 140,56,116
v_exp_f32 v158, v158
MA 140,60,124
WL 4
v_exp_f32 v159, v159
v_mfma_f32_16x16x32_bf16 v[144:147], a[0:3], a[64:67], v[244:247]
RD 32,240,24576
RD 36,240,25600
MA 144,4,72
buffer_load_dwordx4 v252, s[12:15], s63 offen offset:3072 sc1 lds
MA 144,8,80
s_add_u32 s63, s59, s62
RD 40,240,26624
RD 44,240,27648
MA 144,12,88
WL 4
v_add_f32 v242, v242, v152
v_add_f32 v242, v242, v153
MA 144,16,96
s_add_u32 m0, s58, s62
RD 48,240,28672
v_add_f32 v242, v242, v154
MA 144,20,104
v_add_f32 v242, v242, v155
PK 170,152,153
MA 144,24,112
RD 52,240,29696
PK 171,154,155
MA 144,28,120
v_add_f32 v243, v243, v156
v_add_f32 v243, v243, v157
v_mfma_f32_16x16x32_bf16 v[148:151], a[0:3], a[68:71], v[248:251]
RD 56,240,30720
v_add_f32 v243, v243, v158
MA 148,4,76
v_add_f32 v243, v243, v159
PK 174,156,157
MA 148,8,84
RD 60,240,31744
PK 175,158,159
MA 148,12,92
v_max3_f32 v254, v128, v129, v130
v_max_f32 v254, v254, v131
MA 148,16,100
v_max3_f32 v255, v132, v133, v134
v_max_f32 v255, v255, v135
MA 148,20,108
v_max3_f32 v254, v254, v136, v137
v_max3_f32 v254, v254, v138, v139
MA 148,24,116
v_add_u32 v240, s57, v253
v_max3_f32 v255, v255, v140, v141
MA 148,28,124
WL 4
v_max3_f32 v255, v255, v142, v143
v_mfma_f32_16x16x32_bf16 v[152:155], a[32:35], a[64:67], v[244:247]
RD 0,240,0
MA 152,36,72
MA 152,40,80
RD 4,240,1024
MA 152,44,88
WL 2
MA 152,48,96
RD 8,240,2048
MA 152,52,104
buffer_load_dwordx4 v252, s[12:15], s63 offen offset:0 sc1 lds
MA 152,56,112
RD 12,240,3072
v_max3_f32 v254, v254, v144, v145
MA 152,60,120
v_max3_f32 v254, v254, v146, v147
v_max3_f32 v255, v255, v148, v149
v_mfma_f32_16x16x32_bf16 v[156:159], a[32:35], a[68:71], v[248:251]
RD 16,240,4096
v_max3_f32 v255, v255, v150, v151
MA 156,36,76
buffer_load_dwordx4 v252, s[12:15], s63 offen offset:1024 sc1 lds
MA 156,40,84
RD 20,240,5120
MA 156,44,92
MA 156,48,100
RD 24,240,6144
MA 156,52,108
buffer_load_dwordx4 v252, s[12:15], s63 offen offset:2048 sc1 lds
MA 156,56,116
RD 28,240,7168
MA 156,60,124
WL 6
MB 0,0,160
RD 32,240,8192
RD 36,240,9216
MB 4,0,164
buffer_load_dwordx4 v252, s[12:15], s63 offen offset:3072 sc1 lds
MB 12,4,164
s_add_u32 s63, s63, 4096
RD 40,240,10240
v_max3_f32 v254, v254, v152, v153
MB 8,4,160
WL 7
v_max3_f32 v254, v254, v154, v155
v_max3_f32 v255, v255, v156, v157
MB 16,8,160
s_add_u32 m0, m0, 4096
RD 44,240,11264
v_max3_f32 v255, v255, v158, v159
MB 20,8,164
v_max_f32 v241, v254, v255
v_cmp_lt_f32 vcc, 0x41800000, v241
MB 28,12,164
s_cbranch_vccnz .LRA14
.LRAR14:
RD 48,240,12288
MB 24,12,160
WL 7
v_exp_f32 v128, v128
MB 32,16,160
RD 52,240,13312
RD 56,240,14336
MB 36,16,164
v_exp_f32 v129, v129
MB 44,20,164
RD 60,240,15360
v_add_f32 v242, v242, v128
MB 40,20,160
WL 8
v_exp_f32 v130, v130
MB 48,24,160
RD 0,240,16384
v_add_f32 v242, v242, v129
MB 52,24,164
buffer_load_dwordx4 v252, s[12:15], s63 offen offset:0 sc1 lds
MB 60,28,164
s_add_u32 s59, s59, 0xffff8000
RD 4,240,17408
v_add_f32 v242, v242, v130
MB 56,28,160
WL 8
v_exp_f32 v131, v131
MB 64,32,160
s_add_u32 s7, s58, 32768
RD 8,240,18432
v_add_f32 v242, v242, v131
MB 68,32,164
v_exp_f32 v132, v132
MB 76,36,164
s_sub_u32 s58, s58, 131072
RD 12,240,19456
RD 16,240,20480
MB 72,36,160
WL 9
v_exp_f32 v133, v133
MB 80,40,160
s_min_u32 s58, s7, s58
RD 20,240,21504
v_add_f32 v243, v243, v132
MB 84,40,164
buffer_load_dwordx4 v252, s[12:15], s63 offen offset:1024 sc1 lds
MB 92,44,164
s_add_u32 s6, s57, 32768
RD 24,240,22528
v_add_f32 v243, v243, v133
MB 88,44,160
WL 9
v_exp_f32 v134, v134
MB 96,48,160
s_sub_u32 s57, s57, 131072
RD 28,240,23552
v_add_f32 v243, v243, v134
MB 100,48,164
v_exp_f32 v135, v135
MB 108,52,164
s_min_u32 s57, s6, s57
RD 32,240,24576
v_add_f32 v243, v243, v135
MB 104,52,160
WL 9
v_exp_f32 v136, v136
MB 112,56,160
RD 36,240,25600
RD 40,240,26624
MB 116,56,164
buffer_load_dwordx4 v252, s[12:15], s63 offen offset:2048 sc1 lds
MB 124,60,164
RD 44,240,27648
v_add_f32 v242, v242, v136
MB 120,60,160
WL 10
v_exp_f32 v137, v137
MB 0,0,168
RD 48,240,28672
PK 160,128,129
MB 4,0,172
v_exp_f32 v138, v138
MB 12,4,172
RD 52,240,29696
PK 161,130,131
MB 8,4,168
WL 10
v_exp_f32 v139, v139
MB 16,8,168
RD 56,240,30720
PK 164,132,133
MB 20,8,172
v_exp_f32 v140, v140
MB 28,12,172
RD 60,240,31744
PK 165,134,135
MB 24,12,168
WL 10
v_exp_f32 v141, v141
MB 32,16,168
v_exp_f32 v142, v142
MB 36,16,172
buffer_load_dwordx4 v252, s[12:15], s63 offen offset:3072 sc1 lds
MB 44,20,172
v_exp_f32 v143, v143
MB 40,20,168
WL 8
v_exp_f32 v144, v144
MB 48,24,168
v_exp_f32 v145, v145
MB 52,24,172
v_exp_f32 v146, v146
MB 60,28,172
v_add_u32 v240, s57, v253
v_add_f32 v242, v242, v137
MB 56,28,168
s_waitcnt vmcnt(8) lgkmcnt(0)
s_barrier
v_add_f32 v242, v242, v138
MB 64,32,168
RD 0,240,0
RD 4,240,1024
MB 68,32,172
v_exp_f32 v147, v147
MB 76,36,172
RD 8,240,2048
RD 12,240,3072
MB 72,36,168
v_add_f32 v242, v242, v139
PK 162,136,137
MB 80,40,168
RD 16,240,4096
PK 163,138,139
MB 84,40,172
v_add_f32 v243, v243, v140
v_add_f32 v243, v243, v141
MB 92,44,172
RD 20,240,5120
v_add_f32 v243, v243, v142
MB 88,44,168
v_add_f32 v243, v243, v143
PK 166,140,141
MB 96,48,168
s_sub_u32 s65, s65, 1
RD 24,240,6144
PK 167,142,143
MB 100,48,172
v_add_f32 v242, v242, v144
v_add_f32 v242, v242, v145
MB 108,52,172
RD 28,240,7168
v_add_f32 v242, v242, v146
MB 104,52,168
v_add_f32 v242, v242, v147
MB 112,56,168
MB 116,56,172
buffer_store_dwordx2 v[192:193], v201, s[20:23], 0 offen sc1
MB 124,60,172
v_mov_b32 v201, 0x7f000000
MB 120,60,168
WL 4
s_cmp_lg_u32 s69, 0
s_cbranch_scc1 .LRB14
.LRBR14:
s_cmp_lg_u32 s65, 0
s_cbranch_scc1 .LATLA
v_cmp_ne_u64 vcc, s[48:49], v[202:203]
s_cbranch_vccnz .LPS11
.LPR11:
v_add_u32 v195, s50, v253
v_add_u32 v205, s80, v253
v_mfma_f32_16x16x32_bf16 v[128:131], a[0:3], a[64:67], v[244:247]
s_add_u32 s63, s59, s62
RD 32,240,8192
RD 36,240,9216
MA 128,4,72
v_exp_f32 v148, v148
MA 128,8,80
s_add_u32 m0, s58, s62
RD 40,240,10240
RD 44,240,11264
MA 128,12,88
WL 4
v_exp_f32 v149, v149
MA 128,16,96
s_add_u32 s52, s54, 0x0
RD 48,240,12288
PK 168,144,145
MA 128,20,104
buffer_load_dwordx4 v252, s[12:15], s63 offen offset:0 sc1 lds
MA 128,24,112
s_add_u32 s59, s59, 0x10000
RD 52,240,13312
PK 169,146,147
MA 128,28,120
v_exp_f32 v150, v150
v_mfma_f32_16x16x32_bf16 v[132:135], a[0:3], a[68:71], v[248:251]
s_add_u32 s7, s58, 32768
RD 56,240,14336
v_add_f32 v243, v243, v148
MA 132,4,76
buffer_load_dwordx4 v252, s[12:15], s63 offen offset:1024 sc1 lds
MA 132,8,84
s_sub_u32 s58, s58, 131072
RD 60,240,15360
v_add_f32 v243, v243, v149
MA 132,12,92
s_min_u32 s58, s7, s58
v_exp_f32 v151, v151
MA 132,16,100
s_add_u32 s6, s57, 32768
v_exp_f32 v152, v152
MA 132,20,108
buffer_load_dwordx4 a[192:195], v195, s[20:23], s52 offen sc1
MA 132,24,116
v_exp_f32 v153, v153
MA 132,28,124
WL 4
v_exp_f32 v154, v154
v_mfma_f32_16x16x32_bf16 v[136:139], a[32:35], a[64:67], v[244:247]
s_sub_u32 s57, s57, 131072
RD 0,240,16384
RD 4,240,17408
MA 136,36,72
buffer_load_dwordx4 v252, s[12:15], s63 offen offset:2048 sc1 lds
MA 136,40,80
s_min_u32 s57, s6, s57
RD 8,240,18432
RD 12,240,19456
MA 136,44,88
WL 4
v_exp_f32 v155, v155
MA 136,48,96
RD 16,240,20480
v_add_f32 v243, v243, v150
MA 136,52,104
buffer_load_dwordx4 v252, s[12:15], s63 offen offset:3072 sc1 lds
MA 136,56,112
s_add_u32 s63, s63, 4096
RD 20,240,21504
v_add_f32 v243, v243, v151
MA 136,60,120
v_exp_f32 v156, v156
v_mfma_f32_16x16x32_bf16 v[140:143], a[32:35], a[68:71], v[248:251]
s_add_u32 m0, m0, 4096
RD 24,240,22528
PK 172,148,149
MA 140,36,76
buffer_load_dwordx4 a[220:223], v205, s[20:23], s52 offen sc1
MA 140,40,84
s_add_u32 s52, s54, 0x1c000
RD 28,240,23552
PK 173,150,151
MA 140,44,92
v_exp_f32 v157, v157
MA 140,48,100
v_exp_f32 v158, v158
MA 140,52,108
buffer_load_dwordx4 v252, s[12:15], s63 offen offset:0 sc1 lds
MA 140,56,116
v_exp_f32 v159, v159
MA 140,60,124
WL 4
v_add_f32 v242, v242, v152
v_add_f32 v242, v242, v153
v_mfma_f32_16x16x32_bf16 v[144:147], a[0:3], a[64:67], v[244:247]
)A"
R"A(RD 32,240,24576
RD 36,240,25600
MA 144,4,72
buffer_load_dwordx4 v252, s[12:15], s63 offen offset:1024 sc1 lds
MA 144,8,80
RD 40,240,26624
RD 44,240,27648
MA 144,12,88
WL 4
v_add_f32 v242, v242, v154
v_add_f32 v242, v242, v155
MA 144,16,96
RD 48,240,28672
PK 170,152,153
MA 144,20,104
buffer_load_dwordx4 a[196:199], v195, s[20:23], s52 offen sc1
MA 144,24,112
RD 52,240,29696
PK 171,154,155
MA 144,28,120
v_add_f32 v243, v243, v156
v_add_f32 v243, v243, v157
v_mfma_f32_16x16x32_bf16 v[148:151], a[0:3], a[68:71], v[248:251]
RD 56,240,30720
v_add_f32 v243, v243, v158
MA 148,4,76
buffer_load_dwordx4 v252, s[12:15], s63 offen offset:2048 sc1 lds
MA 148,8,84
RD 60,240,31744
v_add_f32 v243, v243, v159
MA 148,12,92
buffer_load_dwordx4 a[224:227], v205, s[20:23], s52 offen sc1
MA 148,16,100
PK 174,156,157
PK 175,158,159
MA 148,20,108
buffer_load_dwordx4 v252, s[12:15], s63 offen offset:3072 sc1 lds
MA 148,24,116
s_add_u32 s52, s54, 0x38000
v_add_u32 v240, s57, v253
v_max3_f32 v254, v128, v129, v130
MA 148,28,124
WL 4
s_barrier
v_max_f32 v254, v254, v131
v_mfma_f32_16x16x32_bf16 v[152:155], a[32:35], a[64:67], v[244:247]
s_add_u32 s63, s59, s62
RD 0,240,0
v_max3_f32 v255, v132, v133, v134
MA 152,36,72
buffer_load_dwordx4 a[200:203], v195, s[20:23], s52 offen sc1
MA 152,40,80
s_add_u32 m0, s58, s62
RD 4,240,1024
v_max_f32 v255, v255, v135
MA 152,44,88
WL 2
buffer_load_dwordx4 a[228:231], v205, s[20:23], s52 offen sc1
MA 152,48,96
s_add_u32 s52, s54, 0x54000
RD 8,240,2048
v_max3_f32 v254, v254, v136, v137
MA 152,52,104
buffer_load_dwordx4 v252, s[12:15], s63 offen offset:0 sc1 lds
MA 152,56,112
RD 12,240,3072
v_max3_f32 v254, v254, v138, v139
MA 152,60,120
v_max3_f32 v255, v255, v140, v141
v_max3_f32 v255, v255, v142, v143
v_mfma_f32_16x16x32_bf16 v[156:159], a[32:35], a[68:71], v[248:251]
RD 16,240,4096
v_max3_f32 v254, v254, v144, v145
MA 156,36,76
buffer_load_dwordx4 v252, s[12:15], s63 offen offset:1024 sc1 lds
MA 156,40,84
RD 20,240,5120
v_max3_f32 v254, v254, v146, v147
MA 156,44,92
buffer_load_dwordx4 a[204:207], v195, s[20:23], s52 offen sc1
MA 156,48,100
RD 24,240,6144
v_max3_f32 v255, v255, v148, v149
MA 156,52,108
buffer_load_dwordx4 v252, s[12:15], s63 offen offset:2048 sc1 lds
MA 156,56,116
RD 28,240,7168
v_max3_f32 v255, v255, v150, v151
MA 156,60,124
WL 6
buffer_load_dwordx4 a[232:235], v205, s[20:23], s52 offen sc1
MB 0,0,160
s_add_u32 s52, s54, 0x70000
RD 32,240,8192
MB 4,0,164
buffer_load_dwordx4 v252, s[12:15], s63 offen offset:3072 sc1 lds
MB 12,4,164
s_add_u32 s63, s63, 4096
RD 36,240,9216
v_max3_f32 v254, v254, v152, v153
MB 8,4,160
WL 6
v_max3_f32 v254, v254, v154, v155
v_max3_f32 v255, v255, v156, v157
MB 16,8,160
s_add_u32 m0, m0, 4096
RD 40,240,10240
v_max3_f32 v255, v255, v158, v159
MB 20,8,164
v_max_f32 v241, v254, v255
v_cmp_lt_f32 vcc, 0x41800000, v241
MB 28,12,164
s_cbranch_vccnz .LRA15
.LRAR15:
RD 44,240,11264
MB 24,12,160
WL 6
v_exp_f32 v128, v128
MB 32,16,160
RD 48,240,12288
v_add_f32 v242, v242, v128
MB 36,16,164
v_exp_f32 v129, v129
MB 44,20,164
RD 52,240,13312
v_add_f32 v242, v242, v129
MB 40,20,160
WL 6
v_exp_f32 v130, v130
MB 48,24,160
RD 56,240,14336
v_add_f32 v242, v242, v130
MB 52,24,164
buffer_load_dwordx4 v252, s[12:15], s63 offen offset:0 sc1 lds
MB 60,28,164
RD 60,240,15360
MB 56,28,160
WL 6
v_exp_f32 v131, v131
MB 64,32,160
RD 0,240,16384
v_add_f32 v242, v242, v131
MB 68,32,164
buffer_load_dwordx4 a[208:211], v195, s[20:23], s52 offen sc1
MB 76,36,164
RD 4,240,17408
MB 72,36,160
WL 6
v_exp_f32 v132, v132
MB 80,40,160
RD 8,240,18432
v_add_f32 v243, v243, v132
MB 84,40,164
buffer_load_dwordx4 v252, s[12:15], s63 offen offset:1024 sc1 lds
MB 92,44,164
s_add_u32 s6, s57, 32768
RD 12,240,19456
MB 88,44,160
WL 6
v_exp_f32 v133, v133
MB 96,48,160
s_sub_u32 s57, s57, 131072
RD 16,240,20480
v_add_f32 v243, v243, v133
MB 100,48,164
buffer_load_dwordx4 a[236:239], v205, s[20:23], s52 offen sc1
MB 108,52,164
s_min_u32 s57, s6, s57
RD 20,240,21504
MB 104,52,160
WL 6
v_exp_f32 v134, v134
MB 112,56,160
s_add_u32 s52, s54, 0x8c000
RD 24,240,22528
v_add_f32 v243, v243, v134
MB 116,56,164
buffer_load_dwordx4 v252, s[12:15], s63 offen offset:2048 sc1 lds
MB 124,60,164
RD 28,240,23552
MB 120,60,160
WL 6
v_exp_f32 v135, v135
MB 0,0,168
RD 32,240,24576
PK 160,128,129
MB 4,0,172
v_exp_f32 v136, v136
MB 12,4,172
RD 36,240,25600
PK 161,130,131
MB 8,4,168
WL 6
buffer_load_dwordx4 a[212:215], v195, s[20:23], s52 offen sc1
MB 16,8,168
RD 40,240,26624
v_add_f32 v243, v243, v135
MB 20,8,172
v_exp_f32 v137, v137
MB 28,12,172
RD 44,240,27648
PK 164,132,133
MB 24,12,168
WL 6
v_exp_f32 v138, v138
MB 32,16,168
RD 48,240,28672
PK 165,134,135
MB 36,16,172
buffer_load_dwordx4 v252, s[12:15], s63 offen offset:3072 sc1 lds
MB 44,20,172
s_mov_b32 s12, s34
RD 52,240,29696
v_add_f32 v242, v242, v136
MB 40,20,168
WL 6
v_exp_f32 v139, v139
MB 48,24,168
s_and_b32 s13, s35, 0xffff
RD 56,240,30720
v_add_f32 v242, v242, v137
MB 52,24,172
buffer_load_dwordx4 a[240:243], v205, s[20:23], s52 offen sc1
MB 60,28,172
s_mov_b32 s14, 3670016
RD 60,240,31744
v_add_u32 v240, s57, v253
MB 56,28,168
s_waitcnt vmcnt(29) lgkmcnt(6)
s_barrier
v_add_f32 v242, v242, v138
MB 64,32,168
s_mul_i32 s59, s10, 229376
RD 0,240,0
RD 4,240,1024
MB 68,32,172
s_add_u32 s52, s54, 0xa8000
v_exp_f32 v140, v140
MB 76,36,172
s_add_u32 s7, s58, 32768
RD 8,240,2048
RD 12,240,3072
MB 72,36,168
WL 8
v_exp_f32 v141, v141
MB 80,40,168
s_sub_u32 s58, s58, 131072
RD 16,240,4096
v_add_f32 v242, v242, v139
PK 166,140,141
MB 84,40,172
v_exp_f32 v142, v142
MB 92,44,172
s_min_u32 s58, s7, s58
RD 20,240,5120
PK 162,136,137
MB 88,44,168
WL 8
buffer_load_dwordx4 a[216:219], v195, s[20:23], s52 offen sc1
MB 96,48,168
RD 24,240,6144
PK 163,138,139
MB 100,48,172
v_exp_f32 v143, v143
MB 108,52,172
RD 28,240,7168
v_add_f32 v243, v243, v140
v_add_f32 v243, v243, v143
MB 104,52,168
WL 8
v_exp_f32 v144, v144
PK 167,142,143
MB 112,56,168
v_exp_f32 v145, v145
v_add_f32 v242, v242, v144
MB 116,56,172
v_exp_f32 v146, v146
v_exp_f32 v147, v147
MB 124,60,172
v_add_f32 v243, v243, v141
v_add_f32 v243, v243, v142
v_add_f32 v242, v242, v145
v_add_f32 v242, v242, v147
MB 120,60,168
WL 4
buffer_load_dwordx4 a[244:247], v205, s[20:23], s52 offen sc1
v_add_f32 v242, v242, v146
s_cmp_lg_u32 s69, 0
s_cbranch_scc1 .LRB15
.LRBR15:
v_add_u32 v252, s56, v253
v_lshrrev_b32 v252, 7, v252
v_mul_u32_u24 v252, 0x380, v252
v_lshrrev_b32 v241, 3, v253
v_xor_b32 v241, v241, v253
v_and_b32 v241, 0x60, v241
v_add_u32 v252, v252, v241
v_and_b32 v241, 16, v253
v_add_u32 v252, v252, v241
s_mul_i32 s75, s74, 17408
s_add_u32 s75, s75, 0x2c00000
v_mfma_f32_16x16x32_bf16 v[128:131], a[0:3], a[64:67], v[244:247]
s_add_u32 m0, s58, 0
RD 32,240,8192
RD 36,240,9216
MA 128,4,72
v_exp_f32 v148, v148
MA 128,8,80
RD 40,240,10240
RD 44,240,11264
MA 128,12,88
WL 4
v_exp_f32 v149, v149
MA 128,16,96
RD 48,240,12288
PK 168,144,145
MA 128,20,104
buffer_load_dwordx4 v252, s[12:15], s59 offen lds
MA 128,24,112
s_add_u32 s59, s59, 28672
RD 52,240,13312
PK 169,146,147
MA 128,28,120
v_exp_f32 v150, v150
v_mfma_f32_16x16x32_bf16 v[132:135], a[0:3], a[68:71], v[248:251]
s_add_u32 m0, s58, 4096
RD 56,240,14336
v_add_f32 v243, v243, v148
MA 132,4,76
buffer_load_dwordx4 v252, s[12:15], s59 offen lds
MA 132,8,84
s_add_u32 s59, s59, 28672
RD 60,240,15360
v_add_f32 v243, v243, v149
MA 132,12,92
s_add_u32 m0, s58, 8192
v_exp_f32 v151, v151
MA 132,16,100
s_add_u32 s6, s57, 32768
v_exp_f32 v152, v152
MA 132,20,108
v_exp_f32 v153, v153
MA 132,24,116
v_exp_f32 v154, v154
MA 132,28,124
WL 4
v_exp_f32 v155, v155
v_mfma_f32_16x16x32_bf16 v[136:139], a[32:35], a[64:67], v[244:247]
s_sub_u32 s57, s57, 131072
RD 0,240,16384
RD 4,240,17408
MA 136,36,72
buffer_load_dwordx4 v252, s[12:15], s59 offen lds
MA 136,40,80
s_add_u32 s59, s59, 28672
RD 8,240,18432
RD 12,240,19456
MA 136,44,88
WL 4
v_exp_f32 v156, v156
MA 136,48,96
s_add_u32 m0, s58, 12288
RD 16,240,20480
v_add_f32 v243, v243, v150
MA 136,52,104
buffer_load_dwordx4 v252, s[12:15], s59 offen lds
MA 136,56,112
s_add_u32 s59, s59, 28672
RD 20,240,21504
v_add_f32 v243, v243, v151
MA 136,60,120
s_min_u32 s57, s6, s57
v_exp_f32 v157, v157
v_mfma_f32_16x16x32_bf16 v[140:143], a[32:35], a[68:71], v[248:251]
s_add_u32 m0, s58, 16384
RD 24,240,22528
PK 172,148,149
MA 140,36,76
buffer_load_dwordx2 v[178:179], off, s[20:23], s53 sc1
MA 140,40,84
RD 28,240,23552
PK 173,150,151
MA 140,44,92
v_exp_f32 v158, v158
MA 140,48,100
v_exp_f32 v159, v159
MA 140,52,108
buffer_load_dwordx4 v252, s[12:15], s59 offen lds
MA 140,56,116
v_add_f32 v242, v242, v152
v_add_f32 v242, v242, v153
MA 140,60,124
WL 4
v_add_f32 v242, v242, v154
v_add_f32 v242, v242, v155
v_mfma_f32_16x16x32_bf16 v[144:147], a[0:3], a[64:67], v[244:247]
s_add_u32 s59, s59, 28672
RD 32,240,24576
RD 36,240,25600
s_add_u32 m0, s58, 20480
MA 144,4,72
buffer_load_dwordx4 v252, s[12:15], s59 offen lds
MA 144,8,80
s_add_u32 s59, s59, 28672
RD 40,240,26624
RD 44,240,27648
MA 144,12,88
WL 4
PK 170,152,153
PK 171,154,155
MA 144,16,96
s_add_u32 m0, s58, 24576
RD 48,240,28672
v_add_f32 v243, v243, v156
MA 144,20,104
v_add_f32 v243, v243, v157
v_add_f32 v243, v243, v158
MA 144,24,112
RD 52,240,29696
v_add_f32 v243, v243, v159
MA 144,28,120
PK 174,156,157
PK 175,158,159
v_mfma_f32_16x16x32_bf16 v[148:151], a[0:3], a[68:71], v[248:251]
RD 56,240,30720
v_max3_f32 v254, v128, v129, v130
MA 148,4,76
buffer_load_dwordx4 v252, s[12:15], s59 offen lds
MA 148,8,84
s_add_u32 s59, s59, 28672
RD 60,240,31744
v_max_f32 v254, v254, v131
MA 148,12,92
s_add_u32 m0, s58, 28672
v_max3_f32 v255, v132, v133, v134
v_max_f32 v255, v255, v135
MA 148,16,100
v_max3_f32 v254, v254, v136, v137
v_max3_f32 v254, v254, v138, v139
MA 148,20,108
buffer_load_dwordx4 v252, s[12:15], s59 offen lds
MA 148,24,116
v_add_u32 v240, s57, v253
v_max3_f32 v255, v255, v140, v141
MA 148,28,124
s_waitcnt vmcnt(27) lgkmcnt(4)
s_barrier
v_max3_f32 v255, v255, v142, v143
v_mfma_f32_16x16x32_bf16 v[152:155], a[32:35], a[64:67], v[244:247]
s_add_u32 s59, s59, 0xfffcf080
RD 0,240,0
MA 152,36,72
MA 152,40,80
s_add_u32 s7, s58, 32768
RD 4,240,1024
MA 152,44,88
WL 2
MA 152,48,96
s_sub_u32 s58, s58, 131072
RD 8,240,2048
MA 152,52,104
v_max3_f32 v254, v254, v144, v145
v_max3_f32 v254, v254, v146, v147
MA 152,56,112
s_min_u32 s58, s7, s58
RD 12,240,3072
v_max3_f32 v255, v255, v148, v149
MA 152,60,120
v_max3_f32 v255, v255, v150, v151
v_mfma_f32_16x16x32_bf16 v[156:159], a[32:35], a[68:71], v[248:251]
RD 16,240,4096
MA 156,36,76
MA 156,40,84
RD 20,240,5120
MA 156,44,92
MA 156,48,100
RD 24,240,6144
MA 156,52,108
MA 156,56,116
RD 28,240,7168
MA 156,60,124
WL 6
s_waitcnt vmcnt(4)
v_cmp_ne_u64 vcc, s[48:49], v[178:179]
s_cbranch_vccnz .LPS12
.LPR12:
s_add_u32 s52, s75, 16384
buffer_load_dwordx4 v[180:183], v253, s[20:23], s52 offen sc1
s_add_u32 s52, s75, 0
buffer_load_dwordx4 a[128:131], v253, s[20:23], s52 offen offset:0 sc1
buffer_load_dwordx4 a[132:135], v253, s[20:23], s52 offen offset:1024 sc1
MB 0,0,160
s_add_u32 m0, s58, 0
RD 32,240,8192
MB 4,0,164
buffer_load_dwordx4 v252, s[12:15], s59 offen lds
MB 12,4,164
s_add_u32 s59, s59, 28672
RD 36,240,9216
v_max3_f32 v254, v254, v152, v153
MB 8,4,160
WL 6
v_max3_f32 v254, v254, v154, v155
v_max3_f32 v255, v255, v156, v157
MB 16,8,160
s_add_u32 m0, s58, 4096
RD 40,240,10240
v_max3_f32 v255, v255, v158, v159
MB 20,8,164
v_max_f32 v241, v254, v255
v_cmp_lt_f32 vcc, 0x41800000, v241
MB 28,12,164
s_cbranch_vccnz .LRA16
.LRAR16:
RD 44,240,11264
MB 24,12,160
WL 6
v_exp_f32 v128, v128
MB 32,16,160
RD 48,240,12288
v_add_f32 v242, v242, v128
MB 36,16,164
buffer_load_dwordx4 v252, s[12:15], s59 offen lds
MB 44,20,164
s_add_u32 s59, s59, 28672
RD 52,240,13312
MB 40,20,160
WL 6
v_exp_f32 v129, v129
MB 48,24,160
s_add_u32 m0, s58, 8192
RD 56,240,14336
v_add_f32 v242, v242, v129
MB 52,24,164
v_exp_f32 v130, v130
MB 60,28,164
RD 60,240,15360
v_add_f32 v242, v242, v130
MB 56,28,160
WL 6
v_exp_f32 v131, v131
MB 64,32,160
RD 0,240,16384
v_add_f32 v242, v242, v131
MB 68,32,164
buffer_load_dwordx4 v252, s[12:15], s59 offen lds
MB 76,36,164
s_add_u32 s59, s59, 28672
)A"
R"A(RD 4,240,17408
MB 72,36,160
WL 6
v_exp_f32 v132, v132
MB 80,40,160
s_add_u32 m0, s58, 12288
RD 8,240,18432
v_add_f32 v243, v243, v132
MB 84,40,164
buffer_load_dwordx4 v252, s[12:15], s59 offen lds
MB 92,44,164
s_add_u32 s59, s59, 28672
RD 12,240,19456
MB 88,44,160
WL 6
v_exp_f32 v133, v133
MB 96,48,160
s_add_u32 m0, s58, 16384
RD 16,240,20480
v_add_f32 v243, v243, v133
MB 100,48,164
v_exp_f32 v134, v134
MB 108,52,164
s_add_u32 s6, s57, 32768
RD 20,240,21504
v_add_f32 v243, v243, v134
MB 104,52,160
WL 6
v_exp_f32 v135, v135
MB 112,56,160
s_sub_u32 s57, s57, 131072
RD 24,240,22528
s_min_u32 s57, s6, s57
MB 116,56,164
buffer_load_dwordx4 v252, s[12:15], s59 offen lds
MB 124,60,164
s_add_u32 s59, s59, 28672
RD 28,240,23552
v_add_f32 v243, v243, v135
MB 120,60,160
WL 6
v_exp_f32 v136, v136
MB 0,0,168
s_add_u32 m0, s58, 20480
RD 32,240,24576
PK 160,128,129
MB 4,0,172
buffer_load_dwordx4 v252, s[12:15], s59 offen lds
MB 12,4,172
s_add_u32 s59, s59, 28672
RD 36,240,25600
PK 161,130,131
MB 8,4,168
WL 6
v_exp_f32 v137, v137
MB 16,8,168
s_add_u32 m0, s58, 24576
RD 40,240,26624
PK 164,132,133
MB 20,8,172
v_exp_f32 v138, v138
MB 28,12,172
RD 44,240,27648
PK 165,134,135
MB 24,12,168
WL 6
v_exp_f32 v139, v139
MB 32,16,168
RD 48,240,28672
v_add_f32 v242, v242, v136
MB 36,16,172
buffer_load_dwordx4 v252, s[12:15], s59 offen lds
MB 44,20,172
s_add_u32 s59, s59, 28672
RD 52,240,29696
v_add_f32 v242, v242, v137
MB 40,20,168
WL 6
v_exp_f32 v140, v140
MB 48,24,168
s_add_u32 m0, s58, 28672
RD 56,240,30720
v_add_f32 v242, v242, v138
MB 52,24,172
buffer_load_dwordx4 v252, s[12:15], s59 offen lds
MB 60,28,172
s_add_u32 s59, s59, 0xfffcf080
RD 60,240,31744
v_add_u32 v240, s57, v253
MB 56,28,168
WL 6
s_barrier
v_add_f32 v242, v242, v139
MB 64,32,168
s_add_u32 s7, s58, 32768
RD 0,240,0
PK 162,136,137
MB 68,32,172
v_exp_f32 v141, v141
MB 76,36,172
s_sub_u32 s58, s58, 131072
RD 4,240,1024
PK 163,138,139
MB 72,36,168
WL 6
v_exp_f32 v142, v142
MB 80,40,168
s_min_u32 s58, s7, s58
RD 8,240,2048
v_add_f32 v243, v243, v140
MB 84,40,172
v_exp_f32 v143, v143
MB 92,44,172
RD 12,240,3072
v_add_f32 v243, v243, v141
MB 88,44,168
WL 6
v_exp_f32 v144, v144
MB 96,48,168
RD 16,240,4096
v_add_f32 v243, v243, v142
MB 100,48,172
v_exp_f32 v145, v145
MB 108,52,172
RD 20,240,5120
v_add_f32 v243, v243, v143
MB 104,52,168
WL 6
v_exp_f32 v146, v146
MB 112,56,168
RD 24,240,6144
PK 166,140,141
v_add_f32 v242, v242, v146
MB 116,56,172
v_exp_f32 v147, v147
MB 124,60,172
RD 28,240,7168
PK 167,142,143
v_add_f32 v242, v242, v147
MB 120,60,168
WL 6
v_add_f32 v242, v242, v144
v_add_f32 v242, v242, v145
s_cmp_lg_u32 s69, 0
s_cbranch_scc1 .LRB16
.LRBR16:
buffer_load_dwordx4 a[136:139], v253, s[20:23], s52 offen offset:2048 sc1
buffer_load_dwordx4 a[140:143], v253, s[20:23], s52 offen offset:3072 sc1
s_add_u32 s52, s75, 4096
buffer_load_dwordx4 a[144:147], v253, s[20:23], s52 offen offset:0 sc1
MB 0,0,160
s_add_u32 m0, s58, 0
RD 32,240,8192
PK 168,144,145
MB 4,0,164
v_exp_f32 v152, v152
MB 12,4,164
s_add_u32 s6, s57, 32768
RD 36,240,9216
PK 169,146,147
MB 8,4,160
WL 6
v_exp_f32 v153, v153
MB 16,8,160
s_sub_u32 s57, s57, 131072
RD 40,240,10240
v_add_f32 v242, v242, v152
MB 20,8,164
s_min_u32 s57, s6, s57
v_exp_f32 v154, v154
MB 28,12,164
RD 44,240,11264
v_add_f32 v242, v242, v153
MB 24,12,160
WL 6
v_exp_f32 v155, v155
MB 32,16,160
RD 48,240,12288
v_add_f32 v242, v242, v154
MB 36,16,164
v_exp_f32 v148, v148
MB 44,20,164
RD 52,240,13312
v_add_f32 v242, v242, v155
MB 40,20,160
WL 6
v_exp_f32 v149, v149
MB 48,24,160
RD 56,240,14336
PK 170,152,153
MB 52,24,164
v_exp_f32 v150, v150
MB 60,28,164
RD 60,240,15360
PK 171,154,155
MB 56,28,160
WL 6
v_exp_f32 v151, v151
MB 64,32,160
RD 0,240,16384
v_add_f32 v243, v243, v148
MB 68,32,164
v_exp_f32 v156, v156
MB 76,36,164
RD 4,240,17408
v_add_f32 v243, v243, v149
MB 72,36,160
WL 6
v_exp_f32 v157, v157
MB 80,40,160
RD 8,240,18432
v_add_f32 v243, v243, v150
MB 84,40,164
v_exp_f32 v158, v158
MB 92,44,164
RD 12,240,19456
v_add_f32 v243, v243, v151
MB 88,44,160
WL 6
v_exp_f32 v159, v159
MB 96,48,160
RD 16,240,20480
PK 172,148,149
MB 100,48,164
PK 173,150,151
v_add_f32 v243, v243, v156
MB 108,52,164
RD 20,240,21504
v_add_f32 v243, v243, v157
MB 104,52,160
WL 6
v_add_f32 v243, v243, v158
v_add_f32 v243, v243, v159
MB 112,56,160
RD 24,240,22528
PK 174,156,157
MB 116,56,164
PK 175,158,159
MB 124,60,164
RD 28,240,23552
v_add_u32 v208, s57, v212
MB 120,60,160
WL 6
buffer_load_dwordx4 a[148:151], v253, s[20:23], s52 offen offset:1024 sc1
MB 0,0,168
RD 32,240,24576
v_add_u32 v209, s57, v213
MB 4,0,172
buffer_load_dwordx4 v252, s[12:15], s59 offen lds
MB 12,4,172
s_add_u32 s59, s59, 28672
RD 36,240,25600
v_add_u32 v210, s57, v214
MB 8,4,168
WL 6
buffer_load_dwordx4 a[152:155], v253, s[20:23], s52 offen offset:2048 sc1
MB 16,8,168
s_add_u32 m0, s58, 4096
RD 40,240,26624
v_add_u32 v211, s57, v215
MB 20,8,172
buffer_load_dwordx4 v252, s[12:15], s59 offen lds
MB 28,12,172
s_add_u32 s59, s59, 28672
RD 44,240,27648
v_mov_b32 v128, v244
MB 24,12,168
WL 6
buffer_load_dwordx4 a[156:159], v253, s[20:23], s52 offen offset:3072 sc1
MB 32,16,168
s_add_u32 m0, s58, 8192
RD 48,240,28672
s_add_u32 s52, s75, 8192
MB 36,16,172
buffer_load_dwordx4 v252, s[12:15], s59 offen lds
MB 44,20,172
s_add_u32 s59, s59, 28672
RD 52,240,29696
v_mov_b32 v129, v248
MB 40,20,168
WL 6
buffer_load_dwordx4 a[160:163], v253, s[20:23], s52 offen offset:0 sc1
MB 48,24,168
s_add_u32 m0, s58, 12288
RD 56,240,30720
MB 52,24,172
buffer_load_dwordx4 v252, s[12:15], s59 offen lds
MB 60,28,172
s_add_u32 s59, s59, 28672
RD 60,240,31744
MB 56,28,168
WL 6
buffer_load_dwordx4 a[164:167], v253, s[20:23], s52 offen offset:1024 sc1
MB 64,32,168
s_add_u32 m0, s58, 16384
MB 68,32,172
buffer_load_dwordx4 v252, s[12:15], s59 offen lds
MB 76,36,172
s_add_u32 s59, s59, 28672
MB 72,36,168
WL 4
buffer_load_dwordx4 a[168:171], v253, s[20:23], s52 offen offset:2048 sc1
MB 80,40,168
s_add_u32 m0, s58, 20480
MB 84,40,172
buffer_load_dwordx4 v252, s[12:15], s59 offen lds
MB 92,44,172
s_add_u32 s59, s59, 28672
MB 88,44,168
WL 2
buffer_load_dwordx4 a[172:175], v253, s[20:23], s52 offen offset:3072 sc1
MB 96,48,168
s_add_u32 m0, s58, 24576
s_add_u32 s52, s75, 12288
MB 100,48,172
buffer_load_dwordx4 v252, s[12:15], s59 offen lds
MB 108,52,172
s_add_u32 s59, s59, 28672
s_add_u32 m0, s58, 28672
MB 104,52,168
WL 0
buffer_load_dwordx4 a[176:179], v253, s[20:23], s52 offen offset:0 sc1
MB 112,56,168
buffer_load_dwordx4 v252, s[12:15], s59 offen lds
MB 116,56,172
s_add_u32 s59, s59, 0xfffcf080
MB 124,60,172
s_add_u32 s7, s58, 32768
MB 120,60,168
s_sub_u32 s58, s58, 131072
s_min_u32 s58, s7, s58
buffer_load_dwordx4 a[180:183], v253, s[20:23], s52 offen offset:1024 sc1
buffer_load_dwordx4 a[184:187], v253, s[20:23], s52 offen offset:2048 sc1
buffer_load_dwordx4 a[188:191], v253, s[20:23], s52 offen offset:3072 sc1
s_waitcnt vmcnt(33)
s_barrier
TR 0,208,0
TR 2,208,2048
TR 4,208,4096
TR 6,208,6144
TR 8,208,8192
TR 10,208,10240
TR 12,208,12288
TR 14,208,14336
TR 16,208,16384
TR 18,208,18432
TR 20,208,20480
TR 22,208,22528
TR 24,208,24576
TR 26,208,26624
TR 28,208,28672
TR 30,208,30720
s_waitcnt vmcnt(32)
v_mov_b32 v132, v180
v_mov_b32 v133, v181
v_mov_b32 v134, v182
v_mov_b32 v135, v183
v_sub_f32 v130, v132, v128
v_sub_f32 v131, v133, v129
v_max_f32 v136, v130, v131
v_cmp_lt_f32 vcc, 0x42800000, v136
s_cbranch_vccnz .LMSLOW
v_exp_f32 v130, v130
v_exp_f32 v131, v131
s_nop 0
v_mul_f32 v0, v0, v130
v_mul_f32 v1, v1, v130
v_mul_f32 v2, v2, v130
v_mul_f32 v3, v3, v130
v_mul_f32 v8, v8, v130
v_mul_f32 v9, v9, v130
v_mul_f32 v10, v10, v130
v_mul_f32 v11, v11, v130
v_mul_f32 v4, v4, v131
v_mul_f32 v5, v5, v131
v_mul_f32 v6, v6, v131
v_mul_f32 v7, v7, v131
s_waitcnt vmcnt(30)
MA 0,250,128
v_mul_f32 v12, v12, v131
v_mul_f32 v13, v13, v131
v_mul_f32 v14, v14, v131
v_mul_f32 v15, v15, v131
MA 8,248,128
v_mul_f32 v16, v16, v130
v_mul_f32 v17, v17, v130
v_mul_f32 v18, v18, v130
v_mul_f32 v19, v19, v130
MA 4,250,132
v_mul_f32 v24, v24, v130
v_mul_f32 v25, v25, v130
v_mul_f32 v26, v26, v130
v_mul_f32 v27, v27, v130
MA 12,248,132
v_mul_f32 v20, v20, v131
v_mul_f32 v21, v21, v131
v_mul_f32 v22, v22, v131
v_mul_f32 v23, v23, v131
s_waitcnt vmcnt(20)
MA 16,250,136
v_mul_f32 v28, v28, v131
v_mul_f32 v29, v29, v131
v_mul_f32 v30, v30, v131
v_mul_f32 v31, v31, v131
PK 0,0,1
PK 1,2,3
MA 24,248,136
v_mul_f32 v32, v32, v130
v_mul_f32 v33, v33, v130
v_mul_f32 v34, v34, v130
v_mul_f32 v35, v35, v130
PK 2,8,9
PK 3,10,11
MA 20,250,140
v_mul_f32 v40, v40, v130
v_mul_f32 v41, v41, v130
v_mul_f32 v42, v42, v130
v_mul_f32 v43, v43, v130
PK 4,4,5
PK 5,6,7
MA 28,248,140
v_mul_f32 v36, v36, v131
v_mul_f32 v37, v37, v131
v_mul_f32 v38, v38, v131
v_mul_f32 v39, v39, v131
PK 6,12,13
PK 7,14,15
s_waitcnt vmcnt(18)
MA 32,250,144
v_mul_f32 v44, v44, v131
v_mul_f32 v45, v45, v131
v_mul_f32 v46, v46, v131
v_mul_f32 v47, v47, v131
PK 16,16,17
PK 17,18,19
MA 40,248,144
v_mul_f32 v48, v48, v130
v_mul_f32 v49, v49, v130
v_mul_f32 v50, v50, v130
v_mul_f32 v51, v51, v130
PK 18,24,25
PK 19,26,27
MA 36,250,148
v_mul_f32 v56, v56, v130
v_mul_f32 v57, v57, v130
v_mul_f32 v58, v58, v130
v_mul_f32 v59, v59, v130
PK 20,20,21
PK 21,22,23
MA 44,248,148
v_mul_f32 v52, v52, v131
v_mul_f32 v53, v53, v131
v_mul_f32 v54, v54, v131
v_mul_f32 v55, v55, v131
PK 22,28,29
PK 23,30,31
s_waitcnt vmcnt(14)
MA 48,250,152
v_mul_f32 v60, v60, v131
v_mul_f32 v61, v61, v131
v_mul_f32 v62, v62, v131
v_mul_f32 v63, v63, v131
PK 32,32,33
PK 33,34,35
MA 56,248,152
v_mul_f32 v64, v64, v130
v_mul_f32 v65, v65, v130
v_mul_f32 v66, v66, v130
v_mul_f32 v67, v67, v130
PK 34,40,41
PK 35,42,43
MA 52,250,156
v_mul_f32 v72, v72, v130
v_mul_f32 v73, v73, v130
v_mul_f32 v74, v74, v130
v_mul_f32 v75, v75, v130
PK 36,36,37
PK 37,38,39
MA 60,248,156
v_mul_f32 v68, v68, v131
v_mul_f32 v69, v69, v131
v_mul_f32 v70, v70, v131
v_mul_f32 v71, v71, v131
PK 38,44,45
PK 39,46,47
s_waitcnt vmcnt(10)
MA 64,250,160
v_mul_f32 v76, v76, v131
v_mul_f32 v77, v77, v131
v_mul_f32 v78, v78, v131
v_mul_f32 v79, v79, v131
PK 48,48,49
PK 49,50,51
MA 72,248,160
v_mul_f32 v80, v80, v130
v_mul_f32 v81, v81, v130
v_mul_f32 v82, v82, v130
v_mul_f32 v83, v83, v130
PK 50,56,57
PK 51,58,59
MA 68,250,164
v_mul_f32 v88, v88, v130
v_mul_f32 v89, v89, v130
v_mul_f32 v90, v90, v130
v_mul_f32 v91, v91, v130
PK 52,52,53
PK 53,54,55
MA 76,248,164
v_mul_f32 v84, v84, v131
v_mul_f32 v85, v85, v131
v_mul_f32 v86, v86, v131
v_mul_f32 v87, v87, v131
PK 54,60,61
PK 55,62,63
s_waitcnt vmcnt(6)
MA 80,250,168
v_mul_f32 v92, v92, v131
v_mul_f32 v93, v93, v131
v_mul_f32 v94, v94, v131
v_mul_f32 v95, v95, v131
PK 64,64,65
PK 65,66,67
MA 88,248,168
v_mul_f32 v96, v96, v130
v_mul_f32 v97, v97, v130
v_mul_f32 v98, v98, v130
v_mul_f32 v99, v99, v130
PK 66,72,73
PK 67,74,75
MA 84,250,172
v_mul_f32 v104, v104, v130
v_mul_f32 v105, v105, v130
v_mul_f32 v106, v106, v130
v_mul_f32 v107, v107, v130
PK 68,68,69
PK 69,70,71
MA 92,248,172
v_mul_f32 v100, v100, v131
v_mul_f32 v101, v101, v131
v_mul_f32 v102, v102, v131
v_mul_f32 v103, v103, v131
PK 70,76,77
PK 71,78,79
s_waitcnt vmcnt(2)
MA 96,250,176
v_mul_f32 v108, v108, v131
v_mul_f32 v109, v109, v131
v_mul_f32 v110, v110, v131
v_mul_f32 v111, v111, v131
PK 80,80,81
PK 81,82,83
MA 104,248,176
v_mul_f32 v112, v112, v130
v_mul_f32 v113, v113, v130
v_mul_f32 v114, v114, v130
v_mul_f32 v115, v115, v130
PK 82,88,89
PK 83,90,91
MA 100,250,180
v_mul_f32 v120, v120, v130
v_mul_f32 v121, v121, v130
v_mul_f32 v122, v122, v130
v_mul_f32 v123, v123, v130
PK 84,84,85
PK 85,86,87
MA 108,248,180
v_mul_f32 v116, v116, v131
v_mul_f32 v117, v117, v131
v_mul_f32 v118, v118, v131
v_mul_f32 v119, v119, v131
PK 86,92,93
PK 87,94,95
s_waitcnt vmcnt(0)
MA 112,250,184
v_mul_f32 v124, v124, v131
v_mul_f32 v125, v125, v131
v_mul_f32 v126, v126, v131
v_mul_f32 v127, v127, v131
PK 96,96,97
PK 97,98,99
MA 120,248,184
PK 98,104,105
PK 99,106,107
MA 116,250,188
v_fma_f32 v242, v242, v130, v134
v_fma_f32 v243, v243, v131, v135
PK 100,100,101
PK 101,102,103
MA 124,248,188
PK 102,108,109
PK 103,110,111
PK 112,112,113
PK 113,114,115
PK 114,120,121
)A"
R"A(PK 115,122,123
s_nop 1
PK 116,116,117
PK 117,118,119
PK 118,124,125
PK 119,126,127
; output projection of this head, then the cross-head reduction
.LDWO:
s_lshl_b32 s73, s77, 4
s_add_u32 s73, s73, s10
s_mul_i32 s73, s73, 0x1c000
s_lshl_b32 s50, s8, 12
s_add_u32 s73, s73, s50
s_add_u32 s73, s73, 0x4000000
WL 8
v_mfma_f32_16x16x32_bf16 v[128:131], a[0:3], v[0:3], 0
s_add_u32 m0, s58, 0
TR 32,209,0
TR 34,209,2048
MB 128,4,16
v_mov_b32 v254, v242
v_mov_b32 v255, v243
MB 128,8,32
s_lshl_b32 s53, s77, 12
TR 36,209,4096
TR 38,209,6144
MB 128,12,48
WL 4
TR 40,209,8192
TR 42,209,10240
v_permlane16_swap_b32 v242, v254
v_permlane16_swap_b32 v243, v255
MB 128,16,64
s_add_u32 s53, s53, 0x6fe0000
TR 44,209,12288
TR 46,209,14336
v_add_f32 v242, v242, v254
v_add_f32 v243, v243, v255
MB 128,20,80
buffer_load_dwordx4 v252, s[12:15], s59 offen lds
MB 128,24,96
s_add_u32 s59, s59, 28672
TR 48,209,16384
TR 50,209,18432
v_mov_b32 v254, v242
v_mov_b32 v255, v243
MB 128,28,112
s_add_u32 m0, s58, 4096
TR 52,209,20480
TR 54,209,22528
v_mfma_f32_16x16x32_bf16 v[132:135], a[0:3], v[4:7], 0
s_lshl_b32 s72, s10, 7
TR 56,209,24576
TR 58,209,26624
v_permlane32_swap_b32 v242, v254
v_permlane32_swap_b32 v243, v255
MB 132,4,20
buffer_load_dwordx4 v252, s[12:15], s59 offen lds
MB 132,8,36
s_add_u32 s59, s59, 28672
TR 60,209,28672
TR 62,209,30720
v_add_f32 v242, v242, v254
v_add_f32 v243, v243, v255
MB 132,12,52
s_add_u32 m0, s58, 8192
MB 132,16,68
s_add_u32 s6, s57, 32768
v_rcp_f32 v248, v242
v_rcp_f32 v249, v243
MB 132,20,84
buffer_load_dwordx2 v[202:203], v197, s[20:23], s53 offen sc1
MB 132,24,100
s_add_u32 s72, s72, s53
s_cmp_eq_u32 s8, 0
s_cselect_b32 s72, s72, 0x7f000000
MB 132,28,116
WL 8
s_mul_i32 s74, s77, 0x1c0000
s_add_u32 s74, s74, 0x4000000
v_mfma_f32_16x16x32_bf16 v[136:139], a[32:35], v[0:3], 0
s_sub_u32 s61, s57, 131072
TR 0,210,0
TR 2,210,2048
MB 136,36,16
buffer_load_dwordx4 v252, s[12:15], s59 offen lds
MB 136,40,32
s_add_u32 s59, s59, 28672
TR 4,210,4096
TR 6,210,6144
MB 136,44,48
WL 4
TR 8,210,8192
TR 10,210,10240
MB 136,48,64
s_add_u32 m0, s58, 12288
TR 12,210,12288
TR 14,210,14336
MB 136,52,80
buffer_load_dwordx4 v252, s[12:15], s59 offen lds
MB 136,56,96
s_add_u32 s59, s59, 28672
TR 16,210,16384
TR 18,210,18432
v_mul_f32 v128, v128, v248
MB 136,60,112
s_add_u32 m0, s58, 16384
TR 20,210,20480
TR 22,210,22528
v_mfma_f32_16x16x32_bf16 v[140:143], a[32:35], v[4:7], 0
s_min_u32 s6, s6, s61
TR 24,210,24576
TR 26,210,26624
v_mul_f32 v129, v129, v248
MB 140,36,20
v_mul_f32 v130, v130, v248
v_mul_f32 v131, v131, v248
MB 140,40,36
s_sub_u32 s61, s6, s57
TR 28,210,28672
TR 30,210,30720
MB 140,44,52
s_mov_b32 s57, s6
v_add_u32 v208, s61, v208
v_add_u32 v209, s61, v209
PK 160,128,129
MB 140,48,68
PK 161,130,131
v_mul_f32 v132, v132, v249
MB 140,52,84
buffer_load_dwordx4 v252, s[12:15], s59 offen lds
MB 140,56,100
v_add_u32 v210, s61, v210
v_mul_f32 v133, v133, v249
MB 140,60,116
WL 8
v_mul_f32 v134, v134, v249
v_mul_f32 v135, v135, v249
PK 164,132,133
v_mfma_f32_16x16x32_bf16 v[144:147], a[0:3], v[0:3], 0
s_add_u32 s59, s59, 28672
TR 32,211,0
TR 34,211,2048
s_add_u32 m0, s58, 20480
MB 144,4,16
buffer_load_dwordx4 v252, s[12:15], s59 offen lds
MB 144,8,32
s_add_u32 s59, s59, 28672
TR 36,211,4096
TR 38,211,6144
PK 165,134,135
MB 144,12,48
WL 4
TR 40,211,8192
TR 42,211,10240
MB 144,16,64
s_add_u32 m0, s58, 24576
TR 44,211,12288
TR 46,211,14336
MB 144,20,80
v_mul_f32 v136, v136, v248
v_mul_f32 v137, v137, v248
MB 144,24,96
TR 48,211,16384
TR 50,211,18432
MB 144,28,112
TR 52,211,20480
TR 54,211,22528
v_mul_f32 v138, v138, v248
v_mfma_f32_16x16x32_bf16 v[148:151], a[0:3], v[4:7], 0
TR 56,211,24576
TR 58,211,26624
MB 148,4,20
buffer_load_dwordx4 v252, s[12:15], s59 offen lds
MB 148,8,36
s_add_u32 s59, s59, 28672
TR 60,211,28672
TR 62,211,30720
MB 148,12,52
s_add_u32 m0, s58, 28672
v_mul_f32 v139, v139, v248
PK 162,136,137
MB 148,16,68
PK 163,138,139
v_mul_f32 v140, v140, v249
MB 148,20,84
buffer_load_dwordx4 v252, s[12:15], s59 offen lds
MB 148,24,100
v_add_u32 v211, s61, v211
v_mul_f32 v141, v141, v249
MB 148,28,116
s_waitcnt vmcnt(17) lgkmcnt(8)
s_barrier
v_mul_f32 v142, v142, v249
v_mfma_f32_16x16x32_bf16 v[152:155], a[32:35], v[0:3], 0
s_add_u32 s59, s59, 0xfffcf080
TR 0,208,0
TR 2,208,2048
MB 152,36,16
v_mul_f32 v143, v143, v249
PK 166,140,141
PK 167,142,143
MB 152,40,32
s_add_u32 s7, s58, 32768
TR 4,208,4096
TR 6,208,6144
MB 152,44,48
WL 4
TR 8,208,8192
TR 10,208,10240
MB 152,48,64
s_sub_u32 s58, s58, 131072
TR 12,208,12288
TR 14,208,14336
MB 152,52,80
buffer_store_dwordx4 v[160:163], v253, s[20:23], s73 offen offset:0 sc1
MB 152,56,96
s_min_u32 s58, s7, s58
TR 16,208,16384
TR 18,208,18432
MB 152,60,112
TR 20,208,20480
TR 22,208,22528
v_mul_f32 v144, v144, v248
v_mfma_f32_16x16x32_bf16 v[156:159], a[32:35], v[4:7], 0
TR 24,208,24576
TR 26,208,26624
MB 156,36,20
v_mul_f32 v145, v145, v248
v_mul_f32 v146, v146, v248
MB 156,40,36
TR 28,208,28672
TR 30,208,30720
MB 156,44,52
buffer_store_dwordx4 v[164:167], v253, s[20:23], s73 offen offset:1024 sc1
MB 156,48,68
v_mul_f32 v147, v147, v248
PK 168,144,145
MB 156,52,84
PK 169,146,147
v_mul_f32 v148, v148, v249
v_mul_f32 v149, v149, v249
MB 156,56,100
v_mul_f32 v150, v150, v249
v_mul_f32 v151, v151, v249
MB 156,60,116
WL 8
PK 172,148,149
PK 173,150,151
s_mov_b32 s76, s73
s_add_u32 s73, s73, 16384
v_mfma_f32_16x16x32_bf16 v[128:131], a[0:3], v[0:3], 0
s_add_u32 m0, s58, 0
TR 32,209,0
TR 34,209,2048
MB 128,4,16
MB 128,8,32
TR 36,209,4096
TR 38,209,6144
MB 128,12,48
WL 4
TR 40,209,8192
TR 42,209,10240
MB 128,16,64
TR 44,209,12288
TR 46,209,14336
MB 128,20,80
buffer_load_dwordx4 v252, s[12:15], s59 offen lds
MB 128,24,96
s_add_u32 s59, s59, 28672
TR 48,209,16384
TR 50,209,18432
MB 128,28,112
s_add_u32 m0, s58, 4096
TR 52,209,20480
TR 54,209,22528
v_mfma_f32_16x16x32_bf16 v[132:135], a[0:3], v[4:7], 0
TR 56,209,24576
TR 58,209,26624
v_mul_f32 v152, v152, v248
MB 132,4,20
buffer_load_dwordx4 v252, s[12:15], s59 offen lds
MB 132,8,36
s_add_u32 s59, s59, 28672
TR 60,209,28672
TR 62,209,30720
MB 132,12,52
s_add_u32 m0, s58, 8192
v_mul_f32 v153, v153, v248
v_mul_f32 v154, v154, v248
MB 132,16,68
s_add_u32 s6, s57, 32768
v_mul_f32 v155, v155, v248
PK 170,152,153
MB 132,20,84
PK 171,154,155
v_mul_f32 v156, v156, v249
MB 132,24,100
v_mul_f32 v157, v157, v249
v_mul_f32 v158, v158, v249
MB 132,28,116
WL 8
v_mul_f32 v159, v159, v249
PK 174,156,157
v_mfma_f32_16x16x32_bf16 v[136:139], a[32:35], v[0:3], 0
s_sub_u32 s61, s57, 131072
TR 0,210,0
TR 2,210,2048
MB 136,36,16
buffer_load_dwordx4 v252, s[12:15], s59 offen lds
MB 136,40,32
s_add_u32 s59, s59, 28672
TR 4,210,4096
TR 6,210,6144
MB 136,44,48
WL 4
TR 8,210,8192
TR 10,210,10240
PK 175,158,159
MB 136,48,64
s_add_u32 m0, s58, 12288
TR 12,210,12288
TR 14,210,14336
v_mul_f32 v128, v128, v248
MB 136,52,80
buffer_load_dwordx4 v252, s[12:15], s59 offen lds
MB 136,56,96
s_add_u32 s59, s59, 28672
TR 16,210,16384
TR 18,210,18432
v_mul_f32 v129, v129, v248
MB 136,60,112
s_add_u32 m0, s58, 16384
TR 20,210,20480
TR 22,210,22528
v_mfma_f32_16x16x32_bf16 v[140:143], a[32:35], v[4:7], 0
s_min_u32 s6, s6, s61
TR 24,210,24576
TR 26,210,26624
v_mul_f32 v130, v130, v248
MB 140,36,20
buffer_store_dwordx4 v[168:171], v253, s[20:23], s76 offen offset:2048 sc1
MB 140,40,36
s_sub_u32 s61, s6, s57
TR 28,210,28672
TR 30,210,30720
v_mul_f32 v131, v131, v248
MB 140,44,52
s_mov_b32 s57, s6
v_add_u32 v208, s61, v208
v_add_u32 v209, s61, v209
PK 160,128,129
MB 140,48,68
buffer_store_dwordx4 v[172:175], v253, s[20:23], s76 offen offset:3072 sc1
MB 140,52,84
buffer_load_dwordx4 v252, s[12:15], s59 offen lds
MB 140,56,100
v_add_u32 v210, s61, v210
PK 161,130,131
v_mul_f32 v132, v132, v249
MB 140,60,116
WL 8
v_mul_f32 v133, v133, v249
v_mul_f32 v134, v134, v249
v_mul_f32 v135, v135, v249
v_mfma_f32_16x16x32_bf16 v[144:147], a[0:3], v[0:3], 0
s_add_u32 s59, s59, 28672
TR 32,211,0
TR 34,211,2048
s_add_u32 m0, s58, 20480
MB 144,4,16
buffer_load_dwordx4 v252, s[12:15], s59 offen lds
MB 144,8,32
s_add_u32 s59, s59, 28672
TR 36,211,4096
TR 38,211,6144
PK 164,132,133
MB 144,12,48
WL 4
TR 40,211,8192
TR 42,211,10240
PK 165,134,135
MB 144,16,64
s_add_u32 m0, s58, 24576
TR 44,211,12288
TR 46,211,14336
MB 144,20,80
v_mul_f32 v136, v136, v248
v_mul_f32 v137, v137, v248
MB 144,24,96
TR 48,211,16384
TR 50,211,18432
MB 144,28,112
TR 52,211,20480
TR 54,211,22528
v_mul_f32 v138, v138, v248
v_mfma_f32_16x16x32_bf16 v[148:151], a[0:3], v[4:7], 0
TR 56,211,24576
TR 58,211,26624
MB 148,4,20
buffer_load_dwordx4 v252, s[12:15], s59 offen lds
MB 148,8,36
s_add_u32 s59, s59, 28672
TR 60,211,28672
TR 62,211,30720
MB 148,12,52
s_add_u32 m0, s58, 28672
v_mul_f32 v139, v139, v248
PK 162,136,137
MB 148,16,68
PK 163,138,139
v_mul_f32 v140, v140, v249
MB 148,20,84
buffer_load_dwordx4 v252, s[12:15], s59 offen lds
MB 148,24,100
v_add_u32 v211, s61, v211
v_mul_f32 v141, v141, v249
MB 148,28,116
s_waitcnt vmcnt(21) lgkmcnt(8)
s_barrier
v_mul_f32 v142, v142, v249
v_mfma_f32_16x16x32_bf16 v[152:155], a[32:35], v[0:3], 0
s_add_u32 s59, s59, 0xfffcf080
TR 0,208,0
TR 2,208,2048
MB 152,36,16
v_mul_f32 v143, v143, v249
PK 166,140,141
PK 167,142,143
MB 152,40,32
s_add_u32 s7, s58, 32768
TR 4,208,4096
TR 6,208,6144
MB 152,44,48
WL 4
TR 8,208,8192
TR 10,208,10240
MB 152,48,64
s_sub_u32 s58, s58, 131072
TR 12,208,12288
TR 14,208,14336
MB 152,52,80
buffer_store_dwordx4 v[160:163], v253, s[20:23], s73 offen offset:0 sc1
MB 152,56,96
s_min_u32 s58, s7, s58
TR 16,208,16384
TR 18,208,18432
MB 152,60,112
TR 20,208,20480
TR 22,208,22528
v_mul_f32 v144, v144, v248
v_mfma_f32_16x16x32_bf16 v[156:159], a[32:35], v[4:7], 0
TR 24,208,24576
TR 26,208,26624
MB 156,36,20
v_mul_f32 v145, v145, v248
v_mul_f32 v146, v146, v248
MB 156,40,36
TR 28,208,28672
TR 30,208,30720
MB 156,44,52
buffer_store_dwordx4 v[164:167], v253, s[20:23], s73 offen offset:1024 sc1
MB 156,48,68
v_mul_f32 v147, v147, v248
PK 168,144,145
MB 156,52,84
PK 169,146,147
v_mul_f32 v148, v148, v249
v_mul_f32 v149, v149, v249
MB 156,56,100
v_mul_f32 v150, v150, v249
v_mul_f32 v151, v151, v249
MB 156,60,116
WL 8
PK 172,148,149
PK 173,150,151
s_mov_b32 s76, s73
s_add_u32 s73, s73, 16384
v_mfma_f32_16x16x32_bf16 v[128:131], a[0:3], v[0:3], 0
s_add_u32 m0, s58, 0
TR 32,209,0
TR 34,209,2048
MB 128,4,16
MB 128,8,32
TR 36,209,4096
TR 38,209,6144
MB 128,12,48
WL 4
TR 40,209,8192
TR 42,209,10240
MB 128,16,64
TR 44,209,12288
TR 46,209,14336
MB 128,20,80
buffer_load_dwordx4 v252, s[12:15], s59 offen lds
MB 128,24,96
s_add_u32 s59, s59, 28672
TR 48,209,16384
TR 50,209,18432
MB 128,28,112
s_add_u32 m0, s58, 4096
TR 52,209,20480
TR 54,209,22528
v_mfma_f32_16x16x32_bf16 v[132:135], a[0:3], v[4:7], 0
TR 56,209,24576
TR 58,209,26624
v_mul_f32 v152, v152, v248
MB 132,4,20
buffer_load_dwordx4 v252, s[12:15], s59 offen lds
MB 132,8,36
s_add_u32 s59, s59, 28672
TR 60,209,28672
TR 62,209,30720
MB 132,12,52
s_add_u32 m0, s58, 8192
v_mul_f32 v153, v153, v248
v_mul_f32 v154, v154, v248
MB 132,16,68
s_add_u32 s6, s57, 32768
v_mul_f32 v155, v155, v248
PK 170,152,153
MB 132,20,84
PK 171,154,155
v_mul_f32 v156, v156, v249
MB 132,24,100
v_mul_f32 v157, v157, v249
v_mul_f32 v158, v158, v249
MB 132,28,116
WL 8
v_mul_f32 v159, v159, v249
PK 174,156,157
v_mfma_f32_16x16x32_bf16 v[136:139], a[32:35], v[0:3], 0
s_sub_u32 s61, s57, 131072
TR 0,210,0
TR 2,210,2048
MB 136,36,16
buffer_load_dwordx4 v252, s[12:15], s59 offen lds
MB 136,40,32
s_add_u32 s59, s59, 28672
TR 4,210,4096
TR 6,210,6144
MB 136,44,48
WL 4
TR 8,210,8192
TR 10,210,10240
PK 175,158,159
MB 136,48,64
s_add_u32 m0, s58, 12288
TR 12,210,12288
TR 14,210,14336
v_mul_f32 v128, v128, v248
MB 136,52,80
buffer_load_dwordx4 v252, s[12:15], s59 offen lds
MB 136,56,96
s_add_u32 s59, s59, 28672
TR 16,210,16384
TR 18,210,18432
v_mul_f32 v129, v129, v248
MB 136,60,112
s_add_u32 m0, s58, 16384
TR 20,210,20480
TR 22,210,22528
v_mfma_f32_16x16x32_bf16 v[140:143], a[32:35], v[4:7], 0
s_min_u32 s6, s6, s61
TR 24,210,24576
TR 26,210,26624
v_mul_f32 v130, v130, v248
MB 140,36,20
buffer_store_dwordx4 v[168:171], v253, s[20:23], s76 offen offset:2048 sc1
)A"
R"A(MB 140,40,36
s_sub_u32 s61, s6, s57
TR 28,210,28672
TR 30,210,30720
v_mul_f32 v131, v131, v248
MB 140,44,52
s_mov_b32 s57, s6
v_add_u32 v208, s61, v208
v_add_u32 v209, s61, v209
PK 160,128,129
MB 140,48,68
buffer_store_dwordx4 v[172:175], v253, s[20:23], s76 offen offset:3072 sc1
MB 140,52,84
buffer_load_dwordx4 v252, s[12:15], s59 offen lds
MB 140,56,100
v_add_u32 v210, s61, v210
PK 161,130,131
v_mul_f32 v132, v132, v249
MB 140,60,116
WL 8
v_mul_f32 v133, v133, v249
v_mul_f32 v134, v134, v249
v_mul_f32 v135, v135, v249
v_mfma_f32_16x16x32_bf16 v[144:147], a[0:3], v[0:3], 0
s_add_u32 s59, s59, 28672
TR 32,211,0
TR 34,211,2048
s_add_u32 m0, s58, 20480
MB 144,4,16
buffer_load_dwordx4 v252, s[12:15], s59 offen lds
MB 144,8,32
s_add_u32 s59, s59, 28672
TR 36,211,4096
TR 38,211,6144
PK 164,132,133
MB 144,12,48
WL 4
TR 40,211,8192
TR 42,211,10240
PK 165,134,135
MB 144,16,64
s_add_u32 m0, s58, 24576
TR 44,211,12288
TR 46,211,14336
MB 144,20,80
v_mul_f32 v136, v136, v248
v_mul_f32 v137, v137, v248
MB 144,24,96
TR 48,211,16384
TR 50,211,18432
MB 144,28,112
TR 52,211,20480
TR 54,211,22528
v_mul_f32 v138, v138, v248
v_mfma_f32_16x16x32_bf16 v[148:151], a[0:3], v[4:7], 0
TR 56,211,24576
TR 58,211,26624
MB 148,4,20
buffer_load_dwordx4 v252, s[12:15], s59 offen lds
MB 148,8,36
s_add_u32 s59, s59, 28672
TR 60,211,28672
TR 62,211,30720
MB 148,12,52
s_add_u32 m0, s58, 28672
v_mul_f32 v139, v139, v248
PK 162,136,137
MB 148,16,68
PK 163,138,139
v_mul_f32 v140, v140, v249
MB 148,20,84
buffer_load_dwordx4 v252, s[12:15], s59 offen lds
MB 148,24,100
v_add_u32 v211, s61, v211
v_mul_f32 v141, v141, v249
MB 148,28,116
s_waitcnt vmcnt(24) lgkmcnt(8)
s_barrier
v_mul_f32 v142, v142, v249
v_mfma_f32_16x16x32_bf16 v[152:155], a[32:35], v[0:3], 0
s_add_u32 s59, s59, 0xfffcf080
TR 0,208,0
TR 2,208,2048
MB 152,36,16
v_mul_f32 v143, v143, v249
PK 166,140,141
PK 167,142,143
MB 152,40,32
s_add_u32 s7, s58, 32768
TR 4,208,4096
TR 6,208,6144
MB 152,44,48
WL 4
TR 8,208,8192
TR 10,208,10240
MB 152,48,64
s_sub_u32 s58, s58, 131072
TR 12,208,12288
TR 14,208,14336
MB 152,52,80
buffer_store_dwordx4 v[160:163], v253, s[20:23], s73 offen offset:0 sc1
MB 152,56,96
s_min_u32 s58, s7, s58
TR 16,208,16384
TR 18,208,18432
MB 152,60,112
TR 20,208,20480
TR 22,208,22528
v_mul_f32 v144, v144, v248
v_mfma_f32_16x16x32_bf16 v[156:159], a[32:35], v[4:7], 0
TR 24,208,24576
TR 26,208,26624
MB 156,36,20
v_mul_f32 v145, v145, v248
v_mul_f32 v146, v146, v248
MB 156,40,36
TR 28,208,28672
TR 30,208,30720
MB 156,44,52
buffer_store_dwordx4 v[164:167], v253, s[20:23], s73 offen offset:1024 sc1
MB 156,48,68
v_mul_f32 v147, v147, v248
PK 168,144,145
MB 156,52,84
PK 169,146,147
v_mul_f32 v148, v148, v249
v_mul_f32 v149, v149, v249
MB 156,56,100
v_mul_f32 v150, v150, v249
v_mul_f32 v151, v151, v249
MB 156,60,116
WL 8
PK 172,148,149
PK 173,150,151
s_mov_b32 s76, s73
s_add_u32 s73, s73, 16384
v_mfma_f32_16x16x32_bf16 v[128:131], a[0:3], v[0:3], 0
s_add_u32 m0, s58, 0
TR 32,209,0
TR 34,209,2048
MB 128,4,16
MB 128,8,32
TR 36,209,4096
TR 38,209,6144
MB 128,12,48
WL 4
TR 40,209,8192
TR 42,209,10240
MB 128,16,64
TR 44,209,12288
TR 46,209,14336
MB 128,20,80
buffer_load_dwordx4 v252, s[12:15], s59 offen lds
MB 128,24,96
s_add_u32 s59, s59, 28672
TR 48,209,16384
TR 50,209,18432
MB 128,28,112
s_add_u32 m0, s58, 4096
TR 52,209,20480
TR 54,209,22528
v_mfma_f32_16x16x32_bf16 v[132:135], a[0:3], v[4:7], 0
TR 56,209,24576
TR 58,209,26624
v_mul_f32 v152, v152, v248
MB 132,4,20
buffer_load_dwordx4 v252, s[12:15], s59 offen lds
MB 132,8,36
s_add_u32 s59, s59, 28672
TR 60,209,28672
TR 62,209,30720
MB 132,12,52
s_add_u32 m0, s58, 8192
v_mul_f32 v153, v153, v248
v_mul_f32 v154, v154, v248
MB 132,16,68
s_add_u32 s6, s57, 32768
v_mul_f32 v155, v155, v248
PK 170,152,153
MB 132,20,84
PK 171,154,155
v_mul_f32 v156, v156, v249
MB 132,24,100
v_mul_f32 v157, v157, v249
v_mul_f32 v158, v158, v249
MB 132,28,116
WL 8
v_mul_f32 v159, v159, v249
PK 174,156,157
v_mfma_f32_16x16x32_bf16 v[136:139], a[32:35], v[0:3], 0
s_sub_u32 s61, s57, 131072
TR 0,210,0
TR 2,210,2048
MB 136,36,16
buffer_load_dwordx4 v252, s[12:15], s59 offen lds
MB 136,40,32
s_add_u32 s59, s59, 28672
TR 4,210,4096
TR 6,210,6144
MB 136,44,48
WL 4
TR 8,210,8192
TR 10,210,10240
PK 175,158,159
MB 136,48,64
s_add_u32 m0, s58, 12288
TR 12,210,12288
TR 14,210,14336
v_mul_f32 v128, v128, v248
MB 136,52,80
buffer_load_dwordx4 v252, s[12:15], s59 offen lds
MB 136,56,96
s_add_u32 s59, s59, 28672
TR 16,210,16384
TR 18,210,18432
v_mul_f32 v129, v129, v248
MB 136,60,112
s_add_u32 m0, s58, 16384
TR 20,210,20480
TR 22,210,22528
v_mfma_f32_16x16x32_bf16 v[140:143], a[32:35], v[4:7], 0
s_min_u32 s6, s6, s61
TR 24,210,24576
TR 26,210,26624
v_mul_f32 v130, v130, v248
MB 140,36,20
buffer_store_dwordx4 v[168:171], v253, s[20:23], s76 offen offset:2048 sc1
MB 140,40,36
s_sub_u32 s61, s6, s57
TR 28,210,28672
TR 30,210,30720
v_mul_f32 v131, v131, v248
MB 140,44,52
s_mov_b32 s57, s6
v_add_u32 v208, s61, v208
v_add_u32 v209, s61, v209
PK 160,128,129
MB 140,48,68
buffer_store_dwordx4 v[172:175], v253, s[20:23], s76 offen offset:3072 sc1
MB 140,52,84
buffer_load_dwordx4 v252, s[12:15], s59 offen lds
MB 140,56,100
v_add_u32 v210, s61, v210
PK 161,130,131
v_mul_f32 v132, v132, v249
MB 140,60,116
WL 8
v_mul_f32 v133, v133, v249
v_mul_f32 v134, v134, v249
v_mul_f32 v135, v135, v249
v_mfma_f32_16x16x32_bf16 v[144:147], a[0:3], v[0:3], 0
s_add_u32 s59, s59, 28672
TR 32,211,0
TR 34,211,2048
s_add_u32 m0, s58, 20480
MB 144,4,16
buffer_load_dwordx4 v252, s[12:15], s59 offen lds
MB 144,8,32
s_add_u32 s59, s59, 28672
TR 36,211,4096
TR 38,211,6144
PK 164,132,133
MB 144,12,48
WL 4
TR 40,211,8192
TR 42,211,10240
PK 165,134,135
MB 144,16,64
s_add_u32 m0, s58, 24576
TR 44,211,12288
TR 46,211,14336
MB 144,20,80
v_mul_f32 v136, v136, v248
v_mul_f32 v137, v137, v248
MB 144,24,96
TR 48,211,16384
TR 50,211,18432
MB 144,28,112
TR 52,211,20480
TR 54,211,22528
v_mfma_f32_16x16x32_bf16 v[148:151], a[0:3], v[4:7], 0
TR 56,211,24576
TR 58,211,26624
MB 148,4,20
buffer_load_dwordx4 v252, s[12:15], s59 offen lds
MB 148,8,36
s_add_u32 s59, s59, 28672
TR 60,211,28672
TR 62,211,30720
MB 148,12,52
s_add_u32 m0, s58, 28672
v_mul_f32 v138, v138, v248
v_mul_f32 v139, v139, v248
MB 148,16,68
PK 162,136,137
PK 163,138,139
MB 148,20,84
buffer_load_dwordx4 v252, s[12:15], s59 offen lds
MB 148,24,100
v_add_u32 v211, s61, v211
v_mul_f32 v140, v140, v249
MB 148,28,116
s_waitcnt vmcnt(24) lgkmcnt(8)
s_barrier
v_mul_f32 v141, v141, v249
v_mfma_f32_16x16x32_bf16 v[152:155], a[32:35], v[0:3], 0
s_add_u32 s59, s59, 0xfffcf080
TR 0,208,0
TR 2,208,2048
MB 152,36,16
v_mul_f32 v142, v142, v249
v_mul_f32 v143, v143, v249
MB 152,40,32
s_add_u32 s7, s58, 32768
TR 4,208,4096
TR 6,208,6144
MB 152,44,48
WL 4
TR 8,208,8192
TR 10,208,10240
MB 152,48,64
s_sub_u32 s58, s58, 131072
TR 12,208,12288
TR 14,208,14336
MB 152,52,80
s_mov_b32 s16, s42
PK 166,140,141
PK 167,142,143
v_mul_f32 v144, v144, v248
MB 152,56,96
s_min_u32 s58, s7, s58
TR 16,208,16384
TR 18,208,18432
MB 152,60,112
s_and_b32 s17, s43, 0xffff
TR 20,208,20480
TR 22,208,22528
v_mul_f32 v145, v145, v248
v_mfma_f32_16x16x32_bf16 v[156:159], a[32:35], v[4:7], 0
s_mov_b32 s18, 896
TR 24,208,24576
TR 26,208,26624
MB 156,36,20
buffer_store_dwordx4 v[160:163], v253, s[20:23], s73 offen offset:0 sc1
MB 156,40,36
s_mov_b32 s78, 200000
TR 28,208,28672
TR 30,208,30720
MB 156,44,52
buffer_store_dwordx4 v[164:167], v253, s[20:23], s73 offen offset:1024 sc1
MB 156,48,68
v_mul_f32 v146, v146, v248
v_mul_f32 v147, v147, v248
PK 168,144,145
MB 156,52,84
PK 169,146,147
v_mul_f32 v148, v148, v249
MB 156,56,100
v_mul_f32 v149, v149, v249
v_mul_f32 v150, v150, v249
v_mul_f32 v151, v151, v249
MB 156,60,116
WL 8
PK 172,148,149
PK 173,150,151
s_mov_b32 s76, s73
s_add_u32 s73, s73, 16384
v_mfma_f32_16x16x32_bf16 v[128:131], a[0:3], v[0:3], 0
s_add_u32 s6, s57, 32768
TR 32,209,0
TR 34,209,2048
MB 128,4,16
v_add_u32 v201, s72, v199
MB 128,8,32
s_sub_u32 s61, s57, 131072
TR 36,209,4096
TR 38,209,6144
MB 128,12,48
WL 4
TR 40,209,8192
TR 42,209,10240
MB 128,16,64
s_min_u32 s6, s6, s61
TR 44,209,12288
TR 46,209,14336
MB 128,20,80
buffer_load_dwordx2 v[224:225], v196, s[16:19], s84 offen offset:0
MB 128,24,96
s_sub_u32 s61, s6, s57
TR 48,209,16384
TR 50,209,18432
MB 128,28,112
s_mov_b32 s57, s6
TR 52,209,20480
TR 54,209,22528
v_mfma_f32_16x16x32_bf16 v[132:135], a[0:3], v[4:7], 0
TR 56,209,24576
TR 58,209,26624
v_mul_f32 v152, v152, v248
MB 132,4,20
v_add_u32 v208, s61, v208
v_mul_f32 v153, v153, v248
MB 132,8,36
TR 60,209,28672
TR 62,209,30720
MB 132,12,52
v_mul_f32 v154, v154, v248
v_mul_f32 v155, v155, v248
MB 132,16,68
PK 170,152,153
PK 171,154,155
MB 132,20,84
v_mul_f32 v156, v156, v249
v_mul_f32 v157, v157, v249
MB 132,24,100
s_mov_b32 s12, s24
v_add_u32 v209, s61, v209
v_mul_f32 v158, v158, v249
MB 132,28,116
WL 8
v_mul_f32 v159, v159, v249
PK 174,156,157
v_mfma_f32_16x16x32_bf16 v[136:139], a[32:35], v[0:3], 0
s_and_b32 s13, s25, 0xffff
TR 0,210,0
TR 2,210,2048
PK 175,158,159
MB 136,36,16
buffer_load_dwordx2 v[226:227], v196, s[16:19], s84 offen offset:32
MB 136,40,32
s_mov_b32 s14, 1835008
TR 4,210,4096
TR 6,210,6144
MB 136,44,48
WL 4
TR 8,210,8192
TR 10,210,10240
MB 136,48,64
TR 12,210,12288
TR 14,210,14336
MB 136,52,80
buffer_store_dwordx4 v[168:171], v253, s[20:23], s76 offen offset:2048 sc1
MB 136,56,96
TR 16,210,16384
TR 18,210,18432
MB 136,60,112
TR 20,210,20480
TR 22,210,22528
v_mul_f32 v128, v128, v248
v_mfma_f32_16x16x32_bf16 v[140:143], a[32:35], v[4:7], 0
TR 24,210,24576
TR 26,210,26624
MB 140,36,20
v_mul_f32 v129, v129, v248
v_mul_f32 v130, v130, v248
MB 140,40,36
TR 28,210,28672
TR 30,210,30720
MB 140,44,52
buffer_store_dwordx4 v[172:175], v253, s[20:23], s76 offen offset:3072 sc1
MB 140,48,68
v_mul_f32 v131, v131, v248
PK 160,128,129
PK 161,130,131
MB 140,52,84
buffer_load_dwordx2 v[228:229], v196, s[16:19], s64 offen offset:0
MB 140,56,100
v_add_u32 v210, s61, v210
v_mul_f32 v132, v132, v249
MB 140,60,116
WL 8
v_mul_f32 v133, v133, v249
v_mul_f32 v134, v134, v249
v_mfma_f32_16x16x32_bf16 v[144:147], a[0:3], v[0:3], 0
TR 32,211,0
TR 34,211,2048
MB 144,4,16
v_mul_f32 v135, v135, v249
PK 164,132,133
PK 165,134,135
MB 144,8,32
TR 36,211,4096
TR 38,211,6144
MB 144,12,48
WL 4
TR 40,211,8192
TR 42,211,10240
MB 144,16,64
TR 44,211,12288
TR 46,211,14336
MB 144,20,80
v_mul_f32 v136, v136, v248
v_mul_f32 v137, v137, v248
MB 144,24,96
TR 48,211,16384
TR 50,211,18432
MB 144,28,112
TR 52,211,20480
TR 54,211,22528
v_mul_f32 v138, v138, v248
v_mfma_f32_16x16x32_bf16 v[148:151], a[0:3], v[4:7], 0
TR 56,211,24576
TR 58,211,26624
MB 148,4,20
buffer_load_dwordx2 v[230:231], v196, s[16:19], s64 offen offset:32
MB 148,8,36
TR 60,211,28672
TR 62,211,30720
MB 148,12,52
v_mul_f32 v139, v139, v248
PK 162,136,137
MB 148,16,68
PK 163,138,139
v_mul_f32 v140, v140, v249
MB 148,20,84
v_mul_f32 v141, v141, v249
v_mul_f32 v142, v142, v249
MB 148,24,100
v_add_u32 v211, s61, v211
v_mul_f32 v143, v143, v249
MB 148,28,116
s_waitcnt vmcnt(6) lgkmcnt(8)
s_barrier
PK 166,140,141
v_mfma_f32_16x16x32_bf16 v[152:155], a[32:35], v[0:3], 0
TR 0,208,0
TR 2,208,2048
PK 167,142,143
MB 152,36,16
buffer_store_dwordx2 v[192:193], v201, s[20:23], 0 offen sc1
MB 152,40,32
TR 4,208,4096
TR 6,208,6144
MB 152,44,48
WL 4
TR 8,208,8192
TR 10,208,10240
MB 152,48,64
TR 12,208,12288
TR 14,208,14336
MB 152,52,80
buffer_store_dwordx4 v[160:163], v253, s[20:23], s73 offen offset:0 sc1
MB 152,56,96
TR 16,208,16384
TR 18,208,18432
MB 152,60,112
TR 20,208,20480
TR 22,208,22528
v_mul_f32 v144, v144, v248
v_mfma_f32_16x16x32_bf16 v[156:159], a[32:35], v[4:7], 0
TR 24,208,24576
TR 26,208,26624
MB 156,36,20
v_mul_f32 v145, v145, v248
v_mul_f32 v146, v146, v248
MB 156,40,36
TR 28,208,28672
TR 30,208,30720
MB 156,44,52
buffer_store_dwordx4 v[164:167], v253, s[20:23], s73 offen offset:1024 sc1
MB 156,48,68
v_mul_f32 v147, v147, v248
PK 168,144,145
MB 156,52,84
PK 169,146,147
v_mul_f32 v148, v148, v249
v_mul_f32 v149, v149, v249
MB 156,56,100
v_mul_f32 v150, v150, v249
v_mul_f32 v151, v151, v249
MB 156,60,116
WL 8
PK 172,148,149
)A"
R"A(PK 173,150,151
s_mov_b32 s76, s73
s_add_u32 s73, s73, 16384
v_mfma_f32_16x16x32_bf16 v[128:131], a[0:3], v[0:3], 0
s_add_u32 s6, s57, 32768
TR 32,209,0
TR 34,209,2048
MB 128,4,16
MB 128,8,32
s_sub_u32 s61, s57, 131072
TR 36,209,4096
TR 38,209,6144
MB 128,12,48
WL 4
TR 40,209,8192
TR 42,209,10240
MB 128,16,64
s_min_u32 s6, s6, s61
TR 44,209,12288
TR 46,209,14336
MB 128,20,80
buffer_load_dwordx4 v[232:235], v194, s[12:15], s83 offen
MB 128,24,96
s_sub_u32 s61, s6, s57
TR 48,209,16384
TR 50,209,18432
MB 128,28,112
s_mov_b32 s57, s6
TR 52,209,20480
TR 54,209,22528
v_mfma_f32_16x16x32_bf16 v[132:135], a[0:3], v[4:7], 0
TR 56,209,24576
TR 58,209,26624
v_mul_f32 v152, v152, v248
MB 132,4,20
v_add_u32 v208, s61, v208
v_mul_f32 v153, v153, v248
MB 132,8,36
TR 60,209,28672
TR 62,209,30720
MB 132,12,52
v_mul_f32 v154, v154, v248
v_mul_f32 v155, v155, v248
MB 132,16,68
PK 170,152,153
PK 171,154,155
MB 132,20,84
v_mul_f32 v156, v156, v249
v_mul_f32 v157, v157, v249
MB 132,24,100
v_add_u32 v209, s61, v209
v_mul_f32 v158, v158, v249
MB 132,28,116
WL 8
v_mul_f32 v159, v159, v249
PK 174,156,157
v_mfma_f32_16x16x32_bf16 v[136:139], a[32:35], v[0:3], 0
TR 0,210,0
TR 2,210,2048
PK 175,158,159
MB 136,36,16
buffer_load_dwordx4 v[236:239], v194, s[12:15], s85 offen
MB 136,40,32
TR 4,210,4096
TR 6,210,6144
MB 136,44,48
WL 4
TR 8,210,8192
TR 10,210,10240
MB 136,48,64
TR 12,210,12288
TR 14,210,14336
MB 136,52,80
buffer_store_dwordx4 v[168:171], v253, s[20:23], s76 offen offset:2048 sc1
MB 136,56,96
TR 16,210,16384
TR 18,210,18432
MB 136,60,112
TR 20,210,20480
TR 22,210,22528
v_mfma_f32_16x16x32_bf16 v[140:143], a[32:35], v[4:7], 0
TR 24,210,24576
TR 26,210,26624
MB 140,36,20
v_mul_f32 v128, v128, v248
v_mul_f32 v129, v129, v248
MB 140,40,36
TR 28,210,28672
TR 30,210,30720
MB 140,44,52
buffer_store_dwordx4 v[172:175], v253, s[20:23], s76 offen offset:3072 sc1
MB 140,48,68
v_mul_f32 v130, v130, v248
v_mul_f32 v131, v131, v248
MB 140,52,84
PK 160,128,129
PK 161,130,131
v_mul_f32 v132, v132, v249
MB 140,56,100
v_add_u32 v210, s61, v210
v_mul_f32 v133, v133, v249
MB 140,60,116
WL 8
v_mul_f32 v134, v134, v249
v_mul_f32 v135, v135, v249
v_mfma_f32_16x16x32_bf16 v[144:147], a[0:3], v[0:3], 0
TR 32,211,0
TR 34,211,2048
MB 144,4,16
PK 164,132,133
PK 165,134,135
MB 144,8,32
TR 36,211,4096
TR 38,211,6144
MB 144,12,48
WL 4
TR 40,211,8192
TR 42,211,10240
MB 144,16,64
TR 44,211,12288
TR 46,211,14336
MB 144,20,80
v_mul_f32 v136, v136, v248
v_mul_f32 v137, v137, v248
MB 144,24,96
TR 48,211,16384
TR 50,211,18432
MB 144,28,112
TR 52,211,20480
TR 54,211,22528
v_mfma_f32_16x16x32_bf16 v[148:151], a[0:3], v[4:7], 0
TR 56,211,24576
TR 58,211,26624
MB 148,4,20
v_mul_f32 v138, v138, v248
v_mul_f32 v139, v139, v248
MB 148,8,36
TR 60,211,28672
TR 62,211,30720
MB 148,12,52
PK 162,136,137
PK 163,138,139
MB 148,16,68
v_mul_f32 v140, v140, v249
v_mul_f32 v141, v141, v249
MB 148,20,84
v_mul_f32 v142, v142, v249
v_mul_f32 v143, v143, v249
MB 148,24,100
v_add_u32 v211, s61, v211
PK 166,140,141
MB 148,28,116
WL 8
s_barrier
PK 167,142,143
v_mfma_f32_16x16x32_bf16 v[152:155], a[32:35], v[0:3], 0
TR 0,208,0
TR 2,208,2048
MB 152,36,16
MB 152,40,32
TR 4,208,4096
TR 6,208,6144
MB 152,44,48
WL 4
TR 8,208,8192
TR 10,208,10240
MB 152,48,64
TR 12,208,12288
TR 14,208,14336
MB 152,52,80
buffer_store_dwordx4 v[160:163], v253, s[20:23], s73 offen offset:0 sc1
MB 152,56,96
TR 16,208,16384
TR 18,208,18432
MB 152,60,112
TR 20,208,20480
TR 22,208,22528
v_mul_f32 v144, v144, v248
v_mfma_f32_16x16x32_bf16 v[156:159], a[32:35], v[4:7], 0
TR 24,208,24576
TR 26,208,26624
MB 156,36,20
v_mul_f32 v145, v145, v248
v_mul_f32 v146, v146, v248
MB 156,40,36
TR 28,208,28672
TR 30,208,30720
MB 156,44,52
buffer_store_dwordx4 v[164:167], v253, s[20:23], s73 offen offset:1024 sc1
MB 156,48,68
v_mul_f32 v147, v147, v248
PK 168,144,145
MB 156,52,84
PK 169,146,147
v_mul_f32 v148, v148, v249
v_mul_f32 v149, v149, v249
MB 156,56,100
v_mul_f32 v150, v150, v249
v_mul_f32 v151, v151, v249
MB 156,60,116
WL 8
PK 172,148,149
PK 173,150,151
s_mov_b32 s76, s73
s_add_u32 s73, s73, 16384
v_mfma_f32_16x16x32_bf16 v[128:131], a[0:3], v[0:3], 0
TR 32,209,0
TR 34,209,2048
MB 128,4,16
MB 128,8,32
TR 36,209,4096
TR 38,209,6144
MB 128,12,48
WL 4
TR 40,209,8192
TR 42,209,10240
MB 128,16,64
TR 44,209,12288
TR 46,209,14336
MB 128,20,80
v_mul_f32 v152, v152, v248
v_mul_f32 v153, v153, v248
MB 128,24,96
TR 48,209,16384
TR 50,209,18432
MB 128,28,112
TR 52,209,20480
TR 54,209,22528
v_mfma_f32_16x16x32_bf16 v[132:135], a[0:3], v[4:7], 0
TR 56,209,24576
TR 58,209,26624
MB 132,4,20
v_mul_f32 v154, v154, v248
v_mul_f32 v155, v155, v248
MB 132,8,36
TR 60,209,28672
TR 62,209,30720
MB 132,12,52
PK 170,152,153
PK 171,154,155
MB 132,16,68
v_mul_f32 v156, v156, v249
v_mul_f32 v157, v157, v249
MB 132,20,84
v_mul_f32 v158, v158, v249
v_mul_f32 v159, v159, v249
MB 132,24,100
PK 174,156,157
PK 175,158,159
MB 132,28,116
WL 8
v_mfma_f32_16x16x32_bf16 v[136:139], a[32:35], v[0:3], 0
TR 0,210,0
TR 2,210,2048
MB 136,36,16
MB 136,40,32
TR 4,210,4096
TR 6,210,6144
MB 136,44,48
WL 4
TR 8,210,8192
TR 10,210,10240
MB 136,48,64
TR 12,210,12288
TR 14,210,14336
MB 136,52,80
buffer_store_dwordx4 v[168:171], v253, s[20:23], s76 offen offset:2048 sc1
MB 136,56,96
TR 16,210,16384
TR 18,210,18432
MB 136,60,112
TR 20,210,20480
TR 22,210,22528
v_mfma_f32_16x16x32_bf16 v[140:143], a[32:35], v[4:7], 0
TR 24,210,24576
TR 26,210,26624
MB 140,36,20
v_mul_f32 v128, v128, v248
v_mul_f32 v129, v129, v248
MB 140,40,36
TR 28,210,28672
TR 30,210,30720
MB 140,44,52
buffer_store_dwordx4 v[172:175], v253, s[20:23], s76 offen offset:3072 sc1
MB 140,48,68
v_mul_f32 v130, v130, v248
v_mul_f32 v131, v131, v248
MB 140,52,84
PK 160,128,129
PK 161,130,131
MB 140,56,100
v_mul_f32 v132, v132, v249
v_mul_f32 v133, v133, v249
MB 140,60,116
WL 8
v_mul_f32 v134, v134, v249
v_mul_f32 v135, v135, v249
v_mfma_f32_16x16x32_bf16 v[144:147], a[0:3], v[0:3], 0
TR 32,211,0
TR 34,211,2048
MB 144,4,16
PK 164,132,133
PK 165,134,135
MB 144,8,32
TR 36,211,4096
TR 38,211,6144
MB 144,12,48
WL 4
TR 40,211,8192
TR 42,211,10240
MB 144,16,64
TR 44,211,12288
TR 46,211,14336
MB 144,20,80
v_mul_f32 v136, v136, v248
v_mul_f32 v137, v137, v248
MB 144,24,96
TR 48,211,16384
TR 50,211,18432
MB 144,28,112
TR 52,211,20480
TR 54,211,22528
v_mfma_f32_16x16x32_bf16 v[148:151], a[0:3], v[4:7], 0
s_mov_b32 s16, s44
TR 56,211,24576
TR 58,211,26624
MB 148,4,20
v_mul_f32 v138, v138, v248
v_mul_f32 v139, v139, v248
MB 148,8,36
s_and_b32 s17, s45, 0xffff
TR 60,211,28672
TR 62,211,30720
MB 148,12,52
s_mov_b32 s18, 1835008
PK 162,136,137
PK 163,138,139
MB 148,16,68
v_mul_f32 v140, v140, v249
v_mul_f32 v141, v141, v249
MB 148,20,84
v_mul_f32 v142, v142, v249
v_mul_f32 v143, v143, v249
MB 148,24,100
PK 166,140,141
PK 167,142,143
MB 148,28,116
WL 8
v_mfma_f32_16x16x32_bf16 v[152:155], a[32:35], v[0:3], 0
MB 152,36,16
MB 152,40,32
MB 152,44,48
WL 0
MB 152,48,64
v_mul_f32 v144, v144, v248
v_mul_f32 v145, v145, v248
MB 152,52,80
buffer_store_dwordx4 v[160:163], v253, s[20:23], s73 offen offset:0 sc1
MB 152,56,96
v_mul_f32 v146, v146, v248
v_mul_f32 v147, v147, v248
MB 152,60,112
PK 168,144,145
PK 169,146,147
v_mfma_f32_16x16x32_bf16 v[156:159], a[32:35], v[4:7], 0
v_mul_f32 v148, v148, v249
v_mul_f32 v149, v149, v249
MB 156,36,20
v_mul_f32 v150, v150, v249
v_mul_f32 v151, v151, v249
MB 156,40,36
PK 172,148,149
PK 173,150,151
MB 156,44,52
buffer_store_dwordx4 v[164:167], v253, s[20:23], s73 offen offset:1024 sc1
MB 156,48,68
MB 156,52,84
MB 156,56,100
MB 156,60,116
s_mov_b32 s76, s73
s_add_u32 s73, s73, 16384
s_nop 5
v_mul_f32 v152, v152, v248
v_mul_f32 v153, v153, v248
v_mul_f32 v154, v154, v248
v_mul_f32 v155, v155, v248
PK 170,152,153
PK 171,154,155
v_mul_f32 v156, v156, v249
v_mul_f32 v157, v157, v249
v_mul_f32 v158, v158, v249
v_mul_f32 v159, v159, v249
PK 174,156,157
PK 175,158,159
buffer_store_dwordx4 v[168:171], v253, s[20:23], s76 offen offset:2048 sc1
buffer_store_dwordx4 v[172:175], v253, s[20:23], s76 offen offset:3072 sc1
buffer_load_dwordx2 v[202:203], v197, s[20:23], s53 offen sc1
s_mov_b32 s66, 0
s_mov_b32 s67, 12
s_mov_b32 s68, 0xffff
s_waitcnt vmcnt(1)
s_barrier
buffer_store_dwordx2 v[192:193], v201, s[20:23], 0 offen offset:2048 sc1
s_cmp_ge_u32 s79, 14
s_cbranch_scc1 .LFINI
s_waitcnt vmcnt(1)
v_cmp_eq_u64 vcc, s[48:49], v[202:203]
s_andn2_b32 s51, vcc_lo, s66
s_or_b32 s66, s66, vcc_lo
s_and_b32 s50, vcc_hi, 0xffff
s_andn2_b32 s55, s50, s68
s_or_b32 s68, s68, s50
v_cndmask_b32 v197, v197, v200, vcc
s_and_b32 s50, s51, 0xffff
s_cbranch_scc0 .LRO15
s_and_b32 s50, s51, 0xf
s_cbranch_scc0 .LRO3
s_bitcmp1_b32 s51, 0
s_cbranch_scc0 .LRO0
s_add_u32 s52, s74, 0x0
buffer_load_dwordx4 a[64:67], v195, s[20:23], s52 offen sc1
.LRO0:
s_bitcmp1_b32 s51, 1
s_cbranch_scc0 .LRO1
s_add_u32 s52, s74, 0x1c000
buffer_load_dwordx4 a[68:71], v195, s[20:23], s52 offen sc1
.LRO1:
s_bitcmp1_b32 s51, 2
s_cbranch_scc0 .LRO2
s_add_u32 s52, s74, 0x38000
buffer_load_dwordx4 a[72:75], v195, s[20:23], s52 offen sc1
.LRO2:
s_bitcmp1_b32 s51, 3
s_cbranch_scc0 .LRO3
s_add_u32 s52, s74, 0x54000
buffer_load_dwordx4 a[76:79], v195, s[20:23], s52 offen sc1
.LRO3:
s_and_b32 s50, s51, 0xf0
s_cbranch_scc0 .LRO7
s_bitcmp1_b32 s51, 4
s_cbranch_scc0 .LRO4
s_add_u32 s52, s74, 0x70000
buffer_load_dwordx4 a[80:83], v195, s[20:23], s52 offen sc1
.LRO4:
s_bitcmp1_b32 s51, 5
s_cbranch_scc0 .LRO5
s_add_u32 s52, s74, 0x8c000
buffer_load_dwordx4 a[84:87], v195, s[20:23], s52 offen sc1
.LRO5:
s_bitcmp1_b32 s51, 6
s_cbranch_scc0 .LRO6
s_add_u32 s52, s74, 0xa8000
buffer_load_dwordx4 a[88:91], v195, s[20:23], s52 offen sc1
.LRO6:
s_bitcmp1_b32 s51, 7
s_cbranch_scc0 .LRO7
s_add_u32 s52, s74, 0xc4000
buffer_load_dwordx4 a[92:95], v195, s[20:23], s52 offen sc1
.LRO7:
s_and_b32 s50, s51, 0xf00
s_cbranch_scc0 .LRO11
s_bitcmp1_b32 s51, 8
s_cbranch_scc0 .LRO8
s_add_u32 s52, s74, 0xe0000
buffer_load_dwordx4 a[96:99], v195, s[20:23], s52 offen sc1
.LRO8:
s_bitcmp1_b32 s51, 9
s_cbranch_scc0 .LRO9
s_add_u32 s52, s74, 0xfc000
buffer_load_dwordx4 a[100:103], v195, s[20:23], s52 offen sc1
.LRO9:
s_bitcmp1_b32 s51, 10
s_cbranch_scc0 .LRO10
s_add_u32 s52, s74, 0x118000
buffer_load_dwordx4 a[104:107], v195, s[20:23], s52 offen sc1
.LRO10:
s_bitcmp1_b32 s51, 11
s_cbranch_scc0 .LRO11
s_add_u32 s52, s74, 0x134000
buffer_load_dwordx4 a[108:111], v195, s[20:23], s52 offen sc1
.LRO11:
s_and_b32 s50, s51, 0xf000
s_cbranch_scc0 .LRO15
s_bitcmp1_b32 s51, 12
s_cbranch_scc0 .LRO12
s_add_u32 s52, s74, 0x150000
buffer_load_dwordx4 a[112:115], v195, s[20:23], s52 offen sc1
.LRO12:
s_bitcmp1_b32 s51, 13
s_cbranch_scc0 .LRO13
s_add_u32 s52, s74, 0x16c000
buffer_load_dwordx4 a[116:119], v195, s[20:23], s52 offen sc1
.LRO13:
s_bitcmp1_b32 s51, 14
s_cbranch_scc0 .LRO14
s_add_u32 s52, s74, 0x188000
buffer_load_dwordx4 a[120:123], v195, s[20:23], s52 offen sc1
.LRO14:
s_bitcmp1_b32 s51, 15
s_cbranch_scc0 .LRO15
s_add_u32 s52, s74, 0x1a4000
buffer_load_dwordx4 a[124:127], v195, s[20:23], s52 offen sc1
.LRO15:
s_and_b32 s50, s51, 0xffff0000
s_cbranch_scc0 .LRO31
s_and_b32 s50, s51, 0xf0000
s_cbranch_scc0 .LRO19
s_bitcmp1_b32 s51, 16
s_cbranch_scc0 .LRO16
s_add_u32 s52, s74, 0x0
buffer_load_dwordx4 a[128:131], v205, s[20:23], s52 offen sc1
.LRO16:
s_bitcmp1_b32 s51, 17
s_cbranch_scc0 .LRO17
s_add_u32 s52, s74, 0x1c000
buffer_load_dwordx4 a[132:135], v205, s[20:23], s52 offen sc1
.LRO17:
s_bitcmp1_b32 s51, 18
s_cbranch_scc0 .LRO18
s_add_u32 s52, s74, 0x38000
buffer_load_dwordx4 a[136:139], v205, s[20:23], s52 offen sc1
.LRO18:
s_bitcmp1_b32 s51, 19
s_cbranch_scc0 .LRO19
s_add_u32 s52, s74, 0x54000
buffer_load_dwordx4 a[140:143], v205, s[20:23], s52 offen sc1
.LRO19:
s_and_b32 s50, s51, 0xf00000
s_cbranch_scc0 .LRO23
s_bitcmp1_b32 s51, 20
s_cbranch_scc0 .LRO20
s_add_u32 s52, s74, 0x70000
buffer_load_dwordx4 a[144:147], v205, s[20:23], s52 offen sc1
.LRO20:
s_bitcmp1_b32 s51, 21
s_cbranch_scc0 .LRO21
s_add_u32 s52, s74, 0x8c000
buffer_load_dwordx4 a[148:151], v205, s[20:23], s52 offen sc1
.LRO21:
s_bitcmp1_b32 s51, 22
s_cbranch_scc0 .LRO22
s_add_u32 s52, s74, 0xa8000
buffer_load_dwordx4 a[152:155], v205, s[20:23], s52 offen sc1
)A"
R"A(.LRO22:
s_bitcmp1_b32 s51, 23
s_cbranch_scc0 .LRO23
s_add_u32 s52, s74, 0xc4000
buffer_load_dwordx4 a[156:159], v205, s[20:23], s52 offen sc1
.LRO23:
s_and_b32 s50, s51, 0xf000000
s_cbranch_scc0 .LRO27
s_bitcmp1_b32 s51, 24
s_cbranch_scc0 .LRO24
s_add_u32 s52, s74, 0xe0000
buffer_load_dwordx4 a[160:163], v205, s[20:23], s52 offen sc1
.LRO24:
s_bitcmp1_b32 s51, 25
s_cbranch_scc0 .LRO25
s_add_u32 s52, s74, 0xfc000
buffer_load_dwordx4 a[164:167], v205, s[20:23], s52 offen sc1
.LRO25:
s_bitcmp1_b32 s51, 26
s_cbranch_scc0 .LRO26
s_add_u32 s52, s74, 0x118000
buffer_load_dwordx4 a[168:171], v205, s[20:23], s52 offen sc1
.LRO26:
s_bitcmp1_b32 s51, 27
s_cbranch_scc0 .LRO27
s_add_u32 s52, s74, 0x134000
buffer_load_dwordx4 a[172:175], v205, s[20:23], s52 offen sc1
.LRO27:
s_and_b32 s50, s51, 0xf0000000
s_cbranch_scc0 .LRO31
s_bitcmp1_b32 s51, 28
s_cbranch_scc0 .LRO28
s_add_u32 s52, s74, 0x150000
buffer_load_dwordx4 a[176:179], v205, s[20:23], s52 offen sc1
.LRO28:
s_bitcmp1_b32 s51, 29
s_cbranch_scc0 .LRO29
s_add_u32 s52, s74, 0x16c000
buffer_load_dwordx4 a[180:183], v205, s[20:23], s52 offen sc1
.LRO29:
s_bitcmp1_b32 s51, 30
s_cbranch_scc0 .LRO30
s_add_u32 s52, s74, 0x188000
buffer_load_dwordx4 a[184:187], v205, s[20:23], s52 offen sc1
.LRO30:
s_bitcmp1_b32 s51, 31
s_cbranch_scc0 .LRO31
s_add_u32 s52, s74, 0x1a4000
buffer_load_dwordx4 a[188:191], v205, s[20:23], s52 offen sc1
.LRO31:
v_permlane16_swap_b32 v232, v234
v_permlane16_swap_b32 v233, v235
v_permlane16_swap_b32 v236, v238
v_permlane16_swap_b32 v237, v239
v_lshlrev_b32 v0, 16, v224
v_and_b32 v1, 0xffff0000, v224
v_lshlrev_b32 v2, 16, v225
v_and_b32 v3, 0xffff0000, v225
v_lshlrev_b32 v4, 16, v226
v_and_b32 v5, 0xffff0000, v226
v_lshlrev_b32 v6, 16, v227
v_and_b32 v7, 0xffff0000, v227
v_lshlrev_b32 v8, 16, v228
v_and_b32 v9, 0xffff0000, v228
v_lshlrev_b32 v10, 16, v229
v_and_b32 v11, 0xffff0000, v229
v_lshlrev_b32 v12, 16, v230
v_and_b32 v13, 0xffff0000, v230
v_lshlrev_b32 v14, 16, v231
v_and_b32 v15, 0xffff0000, v231
s_nop 0
MA 0,250,192
v_lshlrev_b32 v24, 16, v232
MA 0,250,196
v_and_b32 v25, 0xffff0000, v232
MA 0,250,200
v_lshlrev_b32 v26, 16, v233
MA 0,250,204
v_and_b32 v27, 0xffff0000, v233
MA 0,250,208
v_lshlrev_b32 v28, 16, v234
MA 0,250,212
v_and_b32 v29, 0xffff0000, v234
MA 0,250,216
v_lshlrev_b32 v30, 16, v235
MA 4,248,192
v_and_b32 v31, 0xffff0000, v235
MA 4,248,196
v_lshlrev_b32 v32, 16, v236
MA 4,248,200
v_and_b32 v33, 0xffff0000, v236
MA 4,248,204
v_lshlrev_b32 v34, 16, v237
MA 4,248,208
v_and_b32 v35, 0xffff0000, v237
MA 4,248,212
v_lshlrev_b32 v36, 16, v238
MA 4,248,216
v_and_b32 v37, 0xffff0000, v238
MA 8,250,220
v_lshlrev_b32 v38, 16, v239
MA 8,250,224
v_and_b32 v39, 0xffff0000, v239
MA 8,250,228
MA 8,250,232
MA 8,250,236
MA 8,250,240
MA 8,250,244
MA 12,248,220
MA 12,248,224
MA 12,248,228
MA 12,248,232
MA 12,248,236
MA 12,248,240
MA 12,248,244
buffer_load_dwordx2 v[202:203], v197, s[20:23], s53 offen sc1
s_nop 7
v_mul_f32 v48, 0xbfb8aa3b, v0
v_mul_f32 v49, 0xbfb8aa3b, v1
v_mul_f32 v50, 0xbfb8aa3b, v2
v_mul_f32 v51, 0xbfb8aa3b, v3
v_mul_f32 v52, 0xbfb8aa3b, v4
v_mul_f32 v53, 0xbfb8aa3b, v5
v_mul_f32 v54, 0xbfb8aa3b, v6
v_mul_f32 v55, 0xbfb8aa3b, v7
v_mul_f32 v56, 0xbfb8aa3b, v8
v_mul_f32 v57, 0xbfb8aa3b, v9
v_mul_f32 v58, 0xbfb8aa3b, v10
v_mul_f32 v59, 0xbfb8aa3b, v11
v_mul_f32 v60, 0xbfb8aa3b, v12
v_mul_f32 v61, 0xbfb8aa3b, v13
v_mul_f32 v62, 0xbfb8aa3b, v14
v_mul_f32 v63, 0xbfb8aa3b, v15
v_exp_f32 v48, v48
v_exp_f32 v49, v49
v_exp_f32 v50, v50
v_exp_f32 v51, v51
v_exp_f32 v52, v52
v_exp_f32 v53, v53
v_exp_f32 v54, v54
v_exp_f32 v55, v55
v_exp_f32 v56, v56
v_exp_f32 v57, v57
v_exp_f32 v58, v58
v_exp_f32 v59, v59
v_exp_f32 v60, v60
v_exp_f32 v61, v61
v_exp_f32 v62, v62
v_exp_f32 v63, v63
v_add_f32 v48, 1.0, v48
v_add_f32 v49, 1.0, v49
v_add_f32 v50, 1.0, v50
v_add_f32 v51, 1.0, v51
v_add_f32 v52, 1.0, v52
v_add_f32 v53, 1.0, v53
v_add_f32 v54, 1.0, v54
v_add_f32 v55, 1.0, v55
v_add_f32 v56, 1.0, v56
v_add_f32 v57, 1.0, v57
v_add_f32 v58, 1.0, v58
v_add_f32 v59, 1.0, v59
v_add_f32 v60, 1.0, v60
v_add_f32 v61, 1.0, v61
v_add_f32 v62, 1.0, v62
v_add_f32 v63, 1.0, v63
v_rcp_f32 v48, v48
v_rcp_f32 v49, v49
v_rcp_f32 v50, v50
v_rcp_f32 v51, v51
v_rcp_f32 v52, v52
v_rcp_f32 v53, v53
v_rcp_f32 v54, v54
v_rcp_f32 v55, v55
v_rcp_f32 v56, v56
v_rcp_f32 v57, v57
v_rcp_f32 v58, v58
v_rcp_f32 v59, v59
v_rcp_f32 v60, v60
v_rcp_f32 v61, v61
v_rcp_f32 v62, v62
v_rcp_f32 v63, v63
s_nop 0
v_mul_f32 v0, v0, v48
v_mul_f32 v1, v1, v49
v_mul_f32 v2, v2, v50
v_mul_f32 v3, v3, v51
v_mul_f32 v4, v4, v52
v_mul_f32 v5, v5, v53
v_mul_f32 v6, v6, v54
v_mul_f32 v7, v7, v55
v_mul_f32 v8, v8, v56
v_mul_f32 v9, v9, v57
v_mul_f32 v10, v10, v58
v_mul_f32 v11, v11, v59
v_mul_f32 v12, v12, v60
v_mul_f32 v13, v13, v61
v_mul_f32 v14, v14, v62
v_mul_f32 v15, v15, v63
; load the other heads fragments as their flags show
.LP3LOOP:
s_waitcnt vmcnt(0)
s_sub_u32 s78, s78, 1
s_cselect_b32 s66, -1, s66
s_cselect_b32 s68, 0xffff, s68
s_mov_b32 s70, s66
s_or_b32 s71, s68, 0xffff0000
s_and_b32 s50, s70, s71
s_cmp_eq_u32 s50, -1
s_cbranch_scc1 .LP3NP
v_cmp_eq_u64 vcc, s[48:49], v[202:203]
s_andn2_b32 s51, vcc_lo, s66
s_or_b32 s66, s66, vcc_lo
s_and_b32 s50, vcc_hi, 0xffff
s_andn2_b32 s55, s50, s68
s_or_b32 s68, s68, s50
v_cndmask_b32 v197, v197, v200, vcc
s_and_b32 s50, s51, 0xffff
s_cbranch_scc0 .LRP15
s_and_b32 s50, s51, 0xf
s_cbranch_scc0 .LRP3
s_bitcmp1_b32 s51, 0
s_cbranch_scc0 .LRP0
s_add_u32 s52, s74, 0x0
buffer_load_dwordx4 a[64:67], v195, s[20:23], s52 offen sc1
.LRP0:
s_bitcmp1_b32 s51, 1
s_cbranch_scc0 .LRP1
s_add_u32 s52, s74, 0x1c000
buffer_load_dwordx4 a[68:71], v195, s[20:23], s52 offen sc1
.LRP1:
s_bitcmp1_b32 s51, 2
s_cbranch_scc0 .LRP2
s_add_u32 s52, s74, 0x38000
buffer_load_dwordx4 a[72:75], v195, s[20:23], s52 offen sc1
.LRP2:
s_bitcmp1_b32 s51, 3
s_cbranch_scc0 .LRP3
s_add_u32 s52, s74, 0x54000
buffer_load_dwordx4 a[76:79], v195, s[20:23], s52 offen sc1
.LRP3:
s_and_b32 s50, s51, 0xf0
s_cbranch_scc0 .LRP7
s_bitcmp1_b32 s51, 4
s_cbranch_scc0 .LRP4
s_add_u32 s52, s74, 0x70000
buffer_load_dwordx4 a[80:83], v195, s[20:23], s52 offen sc1
.LRP4:
s_bitcmp1_b32 s51, 5
s_cbranch_scc0 .LRP5
s_add_u32 s52, s74, 0x8c000
buffer_load_dwordx4 a[84:87], v195, s[20:23], s52 offen sc1
.LRP5:
s_bitcmp1_b32 s51, 6
s_cbranch_scc0 .LRP6
s_add_u32 s52, s74, 0xa8000
buffer_load_dwordx4 a[88:91], v195, s[20:23], s52 offen sc1
.LRP6:
s_bitcmp1_b32 s51, 7
s_cbranch_scc0 .LRP7
s_add_u32 s52, s74, 0xc4000
buffer_load_dwordx4 a[92:95], v195, s[20:23], s52 offen sc1
.LRP7:
s_and_b32 s50, s51, 0xf00
s_cbranch_scc0 .LRP11
s_bitcmp1_b32 s51, 8
s_cbranch_scc0 .LRP8
s_add_u32 s52, s74, 0xe0000
buffer_load_dwordx4 a[96:99], v195, s[20:23], s52 offen sc1
.LRP8:
s_bitcmp1_b32 s51, 9
s_cbranch_scc0 .LRP9
s_add_u32 s52, s74, 0xfc000
buffer_load_dwordx4 a[100:103], v195, s[20:23], s52 offen sc1
.LRP9:
s_bitcmp1_b32 s51, 10
s_cbranch_scc0 .LRP10
s_add_u32 s52, s74, 0x118000
buffer_load_dwordx4 a[104:107], v195, s[20:23], s52 offen sc1
.LRP10:
s_bitcmp1_b32 s51, 11
s_cbranch_scc0 .LRP11
s_add_u32 s52, s74, 0x134000
buffer_load_dwordx4 a[108:111], v195, s[20:23], s52 offen sc1
.LRP11:
s_and_b32 s50, s51, 0xf000
s_cbranch_scc0 .LRP15
s_bitcmp1_b32 s51, 12
s_cbranch_scc0 .LRP12
s_add_u32 s52, s74, 0x150000
buffer_load_dwordx4 a[112:115], v195, s[20:23], s52 offen sc1
.LRP12:
s_bitcmp1_b32 s51, 13
s_cbranch_scc0 .LRP13
s_add_u32 s52, s74, 0x16c000
buffer_load_dwordx4 a[116:119], v195, s[20:23], s52 offen sc1
.LRP13:
s_bitcmp1_b32 s51, 14
s_cbranch_scc0 .LRP14
s_add_u32 s52, s74, 0x188000
buffer_load_dwordx4 a[120:123], v195, s[20:23], s52 offen sc1
.LRP14:
s_bitcmp1_b32 s51, 15
s_cbranch_scc0 .LRP15
s_add_u32 s52, s74, 0x1a4000
buffer_load_dwordx4 a[124:127], v195, s[20:23], s52 offen sc1
.LRP15:
s_and_b32 s50, s51, 0xffff0000
s_cbranch_scc0 .LRP31
s_and_b32 s50, s51, 0xf0000
s_cbranch_scc0 .LRP19
s_bitcmp1_b32 s51, 16
s_cbranch_scc0 .LRP16
s_add_u32 s52, s74, 0x0
buffer_load_dwordx4 a[128:131], v205, s[20:23], s52 offen sc1
.LRP16:
s_bitcmp1_b32 s51, 17
s_cbranch_scc0 .LRP17
s_add_u32 s52, s74, 0x1c000
buffer_load_dwordx4 a[132:135], v205, s[20:23], s52 offen sc1
.LRP17:
s_bitcmp1_b32 s51, 18
s_cbranch_scc0 .LRP18
s_add_u32 s52, s74, 0x38000
buffer_load_dwordx4 a[136:139], v205, s[20:23], s52 offen sc1
.LRP18:
s_bitcmp1_b32 s51, 19
s_cbranch_scc0 .LRP19
s_add_u32 s52, s74, 0x54000
buffer_load_dwordx4 a[140:143], v205, s[20:23], s52 offen sc1
.LRP19:
s_and_b32 s50, s51, 0xf00000
s_cbranch_scc0 .LRP23
s_bitcmp1_b32 s51, 20
s_cbranch_scc0 .LRP20
s_add_u32 s52, s74, 0x70000
buffer_load_dwordx4 a[144:147], v205, s[20:23], s52 offen sc1
.LRP20:
s_bitcmp1_b32 s51, 21
s_cbranch_scc0 .LRP21
s_add_u32 s52, s74, 0x8c000
buffer_load_dwordx4 a[148:151], v205, s[20:23], s52 offen sc1
.LRP21:
s_bitcmp1_b32 s51, 22
s_cbranch_scc0 .LRP22
s_add_u32 s52, s74, 0xa8000
buffer_load_dwordx4 a[152:155], v205, s[20:23], s52 offen sc1
.LRP22:
s_bitcmp1_b32 s51, 23
s_cbranch_scc0 .LRP23
s_add_u32 s52, s74, 0xc4000
buffer_load_dwordx4 a[156:159], v205, s[20:23], s52 offen sc1
.LRP23:
s_and_b32 s50, s51, 0xf000000
s_cbranch_scc0 .LRP27
s_bitcmp1_b32 s51, 24
s_cbranch_scc0 .LRP24
s_add_u32 s52, s74, 0xe0000
buffer_load_dwordx4 a[160:163], v205, s[20:23], s52 offen sc1
.LRP24:
s_bitcmp1_b32 s51, 25
s_cbranch_scc0 .LRP25
s_add_u32 s52, s74, 0xfc000
buffer_load_dwordx4 a[164:167], v205, s[20:23], s52 offen sc1
.LRP25:
s_bitcmp1_b32 s51, 26
s_cbranch_scc0 .LRP26
s_add_u32 s52, s74, 0x118000
buffer_load_dwordx4 a[168:171], v205, s[20:23], s52 offen sc1
.LRP26:
s_bitcmp1_b32 s51, 27
s_cbranch_scc0 .LRP27
s_add_u32 s52, s74, 0x134000
buffer_load_dwordx4 a[172:175], v205, s[20:23], s52 offen sc1
.LRP27:
s_and_b32 s50, s51, 0xf0000000
s_cbranch_scc0 .LRP31
s_bitcmp1_b32 s51, 28
s_cbranch_scc0 .LRP28
s_add_u32 s52, s74, 0x150000
buffer_load_dwordx4 a[176:179], v205, s[20:23], s52 offen sc1
.LRP28:
s_bitcmp1_b32 s51, 29
s_cbranch_scc0 .LRP29
s_add_u32 s52, s74, 0x16c000
buffer_load_dwordx4 a[180:183], v205, s[20:23], s52 offen sc1
.LRP29:
s_bitcmp1_b32 s51, 30
s_cbranch_scc0 .LRP30
s_add_u32 s52, s74, 0x188000
buffer_load_dwordx4 a[184:187], v205, s[20:23], s52 offen sc1
.LRP30:
s_bitcmp1_b32 s51, 31
s_cbranch_scc0 .LRP31
s_add_u32 s52, s74, 0x1a4000
buffer_load_dwordx4 a[188:191], v205, s[20:23], s52 offen sc1
.LRP31:
s_or_b32 s51, s51, s55
s_cbranch_scc1 .LP3NS
s_sleep 2
.LP3NS:
buffer_load_dwordx2 v[202:203], v197, s[20:23], s53 offen sc1
.LP3NP:
s_bitcmp1_b32 s67, 0
s_cbranch_scc1 .LP3F1
s_andn2_b32 s50, 0xffff, s70
s_cbranch_scc1 .LP3F1
MZ 64,250,64
MA 64,250,68
MA 64,250,72
MA 64,250,76
MA 64,250,80
MA 64,250,84
MA 64,250,88
MA 64,250,92
MA 64,250,96
MA 64,250,100
MA 64,250,104
MA 64,250,108
MA 64,250,112
MA 64,250,116
MA 64,250,120
MA 64,250,124
MZ 68,248,64
MA 68,248,68
MA 68,248,72
MA 68,248,76
MA 68,248,80
MA 68,248,84
MA 68,248,88
MA 68,248,92
MA 68,248,96
MA 68,248,100
MA 68,248,104
MA 68,248,108
MA 68,248,112
MA 68,248,116
MA 68,248,120
MA 68,248,124
s_nop 7
v_fma_f32 v64, v64, v0, v24
v_fma_f32 v65, v65, v1, v25
v_fma_f32 v66, v66, v2, v26
v_fma_f32 v67, v67, v3, v27
v_fma_f32 v68, v68, v4, v28
v_fma_f32 v69, v69, v5, v29
v_fma_f32 v70, v70, v6, v30
v_fma_f32 v71, v71, v7, v31
PK 72,64,65
PK 73,66,67
PK 74,68,69
PK 75,70,71
s_nop 0
v_permlane16_swap_b32 v72, v74
v_permlane16_swap_b32 v73, v75
buffer_store_dwordx4 v[72:75], v194, s[16:19], s83 offen
s_or_b32 s67, s67, 1
.LP3F1:
s_bitcmp1_b32 s67, 1
s_cbranch_scc1 .LP3F2
s_andn2_b32 s50, 0xffff0000, s70
s_cbranch_scc1 .LP3F2
MZ 64,250,128
MA 64,250,132
MA 64,250,136
MA 64,250,140
MA 64,250,144
MA 64,250,148
MA 64,250,152
MA 64,250,156
MA 64,250,160
MA 64,250,164
MA 64,250,168
MA 64,250,172
MA 64,250,176
MA 64,250,180
MA 64,250,184
MA 64,250,188
MZ 68,248,128
MA 68,248,132
MA 68,248,136
MA 68,248,140
MA 68,248,144
MA 68,248,148
MA 68,248,152
MA 68,248,156
MA 68,248,160
MA 68,248,164
MA 68,248,168
MA 68,248,172
MA 68,248,176
MA 68,248,180
MA 68,248,184
MA 68,248,188
s_nop 7
v_fma_f32 v64, v64, v8, v32
v_fma_f32 v65, v65, v9, v33
v_fma_f32 v66, v66, v10, v34
v_fma_f32 v67, v67, v11, v35
)A"
R"A(v_fma_f32 v68, v68, v12, v36
v_fma_f32 v69, v69, v13, v37
v_fma_f32 v70, v70, v14, v38
v_fma_f32 v71, v71, v15, v39
PK 76,64,65
PK 77,66,67
PK 78,68,69
PK 79,70,71
s_nop 0
v_permlane16_swap_b32 v76, v78
v_permlane16_swap_b32 v77, v79
buffer_store_dwordx4 v[76:79], v194, s[16:19], s85 offen
s_or_b32 s67, s67, 2
s_xor_b32 s50, s77, 15
s_or_b32 s50, s50, s82
s_cbranch_scc1 .LP3F2
s_mov_b32 s72, 0x7000000
buffer_store_dwordx2 v[192:193], off, s[20:23], s72 sc1
.LP3F2:
s_cmp_lg_u32 s67, 15
s_cbranch_scc1 .LP3LOOP
.LFINI:
s_endpgm
.LPS5:
s_mov_b32 s78, 200000
.LPL5:
s_sleep 2
buffer_load_dwordx2 v[254:255], v253, s[20:23], s67 offen sc1
s_waitcnt vmcnt(0)
v_cmp_ne_u64 vcc, s[48:49], v[254:255]
s_sub_u32 s78, s78, 1
s_cbranch_scc1 .LPX5
s_cbranch_vccnz .LPL5
.LPX5:
s_branch .LPR5
.LPS6:
s_mov_b32 s78, 200000
.LPL6:
s_sleep 2
buffer_load_dwordx2 v[254:255], v253, s[20:23], s67 offen sc1
s_waitcnt vmcnt(0)
v_cmp_ne_u64 vcc, s[48:49], v[254:255]
s_sub_u32 s78, s78, 1
s_cbranch_scc1 .LPX6
s_cbranch_vccnz .LPL6
.LPX6:
s_branch .LPR6
.LPS7:
s_mov_b32 s78, 200000
.LPL7:
s_sleep 2
buffer_load_dwordx2 v[248:249], v250, s[20:23], s53 offen sc1
s_waitcnt vmcnt(0)
v_cmp_ne_u64 vcc, s[48:49], v[248:249]
s_sub_u32 s78, s78, 1
s_cbranch_scc1 .LPX7
s_cbranch_vccnz .LPL7
.LPX7:
s_branch .LPR7
.LPS8:
s_mov_b32 s78, 200000
.LPL8:
s_sleep 2
buffer_load_dwordx2 v[176:177], off, s[20:23], s54 sc1
s_waitcnt vmcnt(0)
v_cmp_ne_u64 vcc, s[48:49], v[176:177]
s_sub_u32 s78, s78, 1
s_cbranch_scc1 .LPX8
s_cbranch_vccnz .LPL8
.LPX8:
s_branch .LPR8
.LPS9:
s_mov_b32 s78, 200000
.LPL9:
s_sleep 2
buffer_load_dwordx2 v[248:249], v250, s[20:23], s53 offen sc1
s_waitcnt vmcnt(0)
v_cmp_ne_u64 vcc, s[48:49], v[248:249]
s_sub_u32 s78, s78, 1
s_cbranch_scc1 .LPX9
s_cbranch_vccnz .LPL9
.LPX9:
s_branch .LPR9
.LPS10:
s_mov_b32 s78, 200000
.LPL10:
s_sleep 2
buffer_load_dwordx2 v[178:179], off, s[20:23], s55 sc1
s_waitcnt vmcnt(0)
v_cmp_ne_u64 vcc, s[48:49], v[178:179]
s_sub_u32 s78, s78, 1
s_cbranch_scc1 .LPX10
s_cbranch_vccnz .LPL10
.LPX10:
s_branch .LPR10
.LRA7:
v_mov_b32 v241, v254
s_nop 1
v_permlane16_swap_b32 v254, v241
v_max_f32 v254, v254, v241
v_mov_b32 v241, v254
s_nop 1
v_permlane32_swap_b32 v254, v241
v_max_f32 v254, v254, v241
v_max_f32 v254, 0, v254
v_sub_f32 v128, v128, v254
v_sub_f32 v129, v129, v254
v_sub_f32 v130, v130, v254
v_sub_f32 v131, v131, v254
v_sub_f32 v136, v136, v254
v_sub_f32 v137, v137, v254
v_sub_f32 v138, v138, v254
v_sub_f32 v139, v139, v254
v_sub_f32 v144, v144, v254
v_sub_f32 v145, v145, v254
v_sub_f32 v146, v146, v254
v_sub_f32 v147, v147, v254
v_sub_f32 v152, v152, v254
v_sub_f32 v153, v153, v254
v_sub_f32 v154, v154, v254
v_sub_f32 v155, v155, v254
v_sub_f32 v244, v244, v254
v_sub_f32 v245, v245, v254
v_sub_f32 v246, v246, v254
v_sub_f32 v247, v247, v254
v_exp_f32 v254, -v254
s_nop 0
v_mul_f32 v242, v242, v254
v_mov_b32 v241, v255
s_nop 1
v_permlane16_swap_b32 v255, v241
v_max_f32 v255, v255, v241
v_mov_b32 v241, v255
s_nop 1
v_permlane32_swap_b32 v255, v241
v_max_f32 v255, v255, v241
v_max_f32 v255, 0, v255
v_sub_f32 v132, v132, v255
v_sub_f32 v133, v133, v255
v_sub_f32 v134, v134, v255
v_sub_f32 v135, v135, v255
v_sub_f32 v140, v140, v255
v_sub_f32 v141, v141, v255
v_sub_f32 v142, v142, v255
v_sub_f32 v143, v143, v255
v_sub_f32 v148, v148, v255
v_sub_f32 v149, v149, v255
v_sub_f32 v150, v150, v255
v_sub_f32 v151, v151, v255
v_sub_f32 v156, v156, v255
v_sub_f32 v157, v157, v255
v_sub_f32 v158, v158, v255
v_sub_f32 v159, v159, v255
v_sub_f32 v248, v248, v255
v_sub_f32 v249, v249, v255
v_sub_f32 v250, v250, v255
v_sub_f32 v251, v251, v255
v_exp_f32 v255, -v255
s_nop 0
v_mul_f32 v243, v243, v255
s_mov_b32 s69, 1
s_branch .LRAR7
.LRB7:
s_nop 7
v_mul_f32 v0, v0, v254
v_mul_f32 v1, v1, v254
v_mul_f32 v2, v2, v254
v_mul_f32 v3, v3, v254
v_mul_f32 v8, v8, v254
v_mul_f32 v9, v9, v254
v_mul_f32 v10, v10, v254
v_mul_f32 v11, v11, v254
v_mul_f32 v16, v16, v254
v_mul_f32 v17, v17, v254
v_mul_f32 v18, v18, v254
v_mul_f32 v19, v19, v254
v_mul_f32 v24, v24, v254
v_mul_f32 v25, v25, v254
v_mul_f32 v26, v26, v254
v_mul_f32 v27, v27, v254
v_mul_f32 v32, v32, v254
v_mul_f32 v33, v33, v254
v_mul_f32 v34, v34, v254
v_mul_f32 v35, v35, v254
v_mul_f32 v40, v40, v254
v_mul_f32 v41, v41, v254
v_mul_f32 v42, v42, v254
v_mul_f32 v43, v43, v254
v_mul_f32 v48, v48, v254
v_mul_f32 v49, v49, v254
v_mul_f32 v50, v50, v254
v_mul_f32 v51, v51, v254
v_mul_f32 v56, v56, v254
v_mul_f32 v57, v57, v254
v_mul_f32 v58, v58, v254
v_mul_f32 v59, v59, v254
v_mul_f32 v64, v64, v254
v_mul_f32 v65, v65, v254
v_mul_f32 v66, v66, v254
v_mul_f32 v67, v67, v254
v_mul_f32 v72, v72, v254
v_mul_f32 v73, v73, v254
v_mul_f32 v74, v74, v254
v_mul_f32 v75, v75, v254
v_mul_f32 v80, v80, v254
v_mul_f32 v81, v81, v254
v_mul_f32 v82, v82, v254
v_mul_f32 v83, v83, v254
v_mul_f32 v88, v88, v254
v_mul_f32 v89, v89, v254
v_mul_f32 v90, v90, v254
v_mul_f32 v91, v91, v254
v_mul_f32 v96, v96, v254
v_mul_f32 v97, v97, v254
v_mul_f32 v98, v98, v254
v_mul_f32 v99, v99, v254
v_mul_f32 v104, v104, v254
v_mul_f32 v105, v105, v254
v_mul_f32 v106, v106, v254
v_mul_f32 v107, v107, v254
v_mul_f32 v112, v112, v254
v_mul_f32 v113, v113, v254
v_mul_f32 v114, v114, v254
v_mul_f32 v115, v115, v254
v_mul_f32 v120, v120, v254
v_mul_f32 v121, v121, v254
v_mul_f32 v122, v122, v254
v_mul_f32 v123, v123, v254
v_mul_f32 v4, v4, v255
v_mul_f32 v5, v5, v255
v_mul_f32 v6, v6, v255
v_mul_f32 v7, v7, v255
v_mul_f32 v12, v12, v255
v_mul_f32 v13, v13, v255
v_mul_f32 v14, v14, v255
v_mul_f32 v15, v15, v255
v_mul_f32 v20, v20, v255
v_mul_f32 v21, v21, v255
v_mul_f32 v22, v22, v255
v_mul_f32 v23, v23, v255
v_mul_f32 v28, v28, v255
v_mul_f32 v29, v29, v255
v_mul_f32 v30, v30, v255
v_mul_f32 v31, v31, v255
v_mul_f32 v36, v36, v255
v_mul_f32 v37, v37, v255
v_mul_f32 v38, v38, v255
v_mul_f32 v39, v39, v255
v_mul_f32 v44, v44, v255
v_mul_f32 v45, v45, v255
v_mul_f32 v46, v46, v255
v_mul_f32 v47, v47, v255
v_mul_f32 v52, v52, v255
v_mul_f32 v53, v53, v255
v_mul_f32 v54, v54, v255
v_mul_f32 v55, v55, v255
v_mul_f32 v60, v60, v255
v_mul_f32 v61, v61, v255
v_mul_f32 v62, v62, v255
v_mul_f32 v63, v63, v255
v_mul_f32 v68, v68, v255
v_mul_f32 v69, v69, v255
v_mul_f32 v70, v70, v255
v_mul_f32 v71, v71, v255
v_mul_f32 v76, v76, v255
v_mul_f32 v77, v77, v255
v_mul_f32 v78, v78, v255
v_mul_f32 v79, v79, v255
v_mul_f32 v84, v84, v255
v_mul_f32 v85, v85, v255
v_mul_f32 v86, v86, v255
v_mul_f32 v87, v87, v255
v_mul_f32 v92, v92, v255
v_mul_f32 v93, v93, v255
v_mul_f32 v94, v94, v255
v_mul_f32 v95, v95, v255
v_mul_f32 v100, v100, v255
v_mul_f32 v101, v101, v255
v_mul_f32 v102, v102, v255
v_mul_f32 v103, v103, v255
v_mul_f32 v108, v108, v255
v_mul_f32 v109, v109, v255
v_mul_f32 v110, v110, v255
v_mul_f32 v111, v111, v255
v_mul_f32 v116, v116, v255
v_mul_f32 v117, v117, v255
v_mul_f32 v118, v118, v255
v_mul_f32 v119, v119, v255
v_mul_f32 v124, v124, v255
v_mul_f32 v125, v125, v255
v_mul_f32 v126, v126, v255
v_mul_f32 v127, v127, v255
s_mov_b32 s69, 0
s_nop 0
s_branch .LRBR7
.LRA8:
v_mov_b32 v241, v254
s_nop 1
v_permlane16_swap_b32 v254, v241
v_max_f32 v254, v254, v241
v_mov_b32 v241, v254
s_nop 1
v_permlane32_swap_b32 v254, v241
v_max_f32 v254, v254, v241
v_max_f32 v254, 0, v254
v_sub_f32 v128, v128, v254
v_sub_f32 v129, v129, v254
v_sub_f32 v130, v130, v254
v_sub_f32 v131, v131, v254
v_sub_f32 v136, v136, v254
v_sub_f32 v137, v137, v254
v_sub_f32 v138, v138, v254
v_sub_f32 v139, v139, v254
v_sub_f32 v144, v144, v254
v_sub_f32 v145, v145, v254
v_sub_f32 v146, v146, v254
v_sub_f32 v147, v147, v254
v_sub_f32 v152, v152, v254
v_sub_f32 v153, v153, v254
v_sub_f32 v154, v154, v254
v_sub_f32 v155, v155, v254
v_sub_f32 v244, v244, v254
v_sub_f32 v245, v245, v254
v_sub_f32 v246, v246, v254
v_sub_f32 v247, v247, v254
v_exp_f32 v254, -v254
s_nop 0
v_mul_f32 v242, v242, v254
v_mov_b32 v241, v255
s_nop 1
v_permlane16_swap_b32 v255, v241
v_max_f32 v255, v255, v241
v_mov_b32 v241, v255
s_nop 1
v_permlane32_swap_b32 v255, v241
v_max_f32 v255, v255, v241
v_max_f32 v255, 0, v255
v_sub_f32 v132, v132, v255
v_sub_f32 v133, v133, v255
v_sub_f32 v134, v134, v255
v_sub_f32 v135, v135, v255
v_sub_f32 v140, v140, v255
v_sub_f32 v141, v141, v255
v_sub_f32 v142, v142, v255
v_sub_f32 v143, v143, v255
v_sub_f32 v148, v148, v255
v_sub_f32 v149, v149, v255
v_sub_f32 v150, v150, v255
v_sub_f32 v151, v151, v255
v_sub_f32 v156, v156, v255
v_sub_f32 v157, v157, v255
v_sub_f32 v158, v158, v255
v_sub_f32 v159, v159, v255
v_sub_f32 v248, v248, v255
v_sub_f32 v249, v249, v255
v_sub_f32 v250, v250, v255
v_sub_f32 v251, v251, v255
v_exp_f32 v255, -v255
s_nop 0
v_mul_f32 v243, v243, v255
s_mov_b32 s69, 1
s_branch .LRAR8
.LRB8:
s_nop 7
v_mul_f32 v0, v0, v254
v_mul_f32 v1, v1, v254
v_mul_f32 v2, v2, v254
v_mul_f32 v3, v3, v254
v_mul_f32 v8, v8, v254
v_mul_f32 v9, v9, v254
v_mul_f32 v10, v10, v254
v_mul_f32 v11, v11, v254
v_mul_f32 v16, v16, v254
v_mul_f32 v17, v17, v254
v_mul_f32 v18, v18, v254
v_mul_f32 v19, v19, v254
v_mul_f32 v24, v24, v254
v_mul_f32 v25, v25, v254
v_mul_f32 v26, v26, v254
v_mul_f32 v27, v27, v254
v_mul_f32 v32, v32, v254
v_mul_f32 v33, v33, v254
v_mul_f32 v34, v34, v254
v_mul_f32 v35, v35, v254
v_mul_f32 v40, v40, v254
v_mul_f32 v41, v41, v254
v_mul_f32 v42, v42, v254
v_mul_f32 v43, v43, v254
v_mul_f32 v48, v48, v254
v_mul_f32 v49, v49, v254
v_mul_f32 v50, v50, v254
v_mul_f32 v51, v51, v254
v_mul_f32 v56, v56, v254
v_mul_f32 v57, v57, v254
v_mul_f32 v58, v58, v254
v_mul_f32 v59, v59, v254
v_mul_f32 v64, v64, v254
v_mul_f32 v65, v65, v254
v_mul_f32 v66, v66, v254
v_mul_f32 v67, v67, v254
v_mul_f32 v72, v72, v254
v_mul_f32 v73, v73, v254
v_mul_f32 v74, v74, v254
v_mul_f32 v75, v75, v254
v_mul_f32 v80, v80, v254
v_mul_f32 v81, v81, v254
v_mul_f32 v82, v82, v254
v_mul_f32 v83, v83, v254
v_mul_f32 v88, v88, v254
v_mul_f32 v89, v89, v254
v_mul_f32 v90, v90, v254
v_mul_f32 v91, v91, v254
v_mul_f32 v96, v96, v254
v_mul_f32 v97, v97, v254
v_mul_f32 v98, v98, v254
v_mul_f32 v99, v99, v254
v_mul_f32 v104, v104, v254
v_mul_f32 v105, v105, v254
v_mul_f32 v106, v106, v254
v_mul_f32 v107, v107, v254
v_mul_f32 v112, v112, v254
v_mul_f32 v113, v113, v254
v_mul_f32 v114, v114, v254
v_mul_f32 v115, v115, v254
v_mul_f32 v120, v120, v254
v_mul_f32 v121, v121, v254
v_mul_f32 v122, v122, v254
v_mul_f32 v123, v123, v254
v_mul_f32 v4, v4, v255
v_mul_f32 v5, v5, v255
v_mul_f32 v6, v6, v255
v_mul_f32 v7, v7, v255
v_mul_f32 v12, v12, v255
v_mul_f32 v13, v13, v255
v_mul_f32 v14, v14, v255
v_mul_f32 v15, v15, v255
v_mul_f32 v20, v20, v255
v_mul_f32 v21, v21, v255
v_mul_f32 v22, v22, v255
v_mul_f32 v23, v23, v255
v_mul_f32 v28, v28, v255
v_mul_f32 v29, v29, v255
v_mul_f32 v30, v30, v255
v_mul_f32 v31, v31, v255
v_mul_f32 v36, v36, v255
v_mul_f32 v37, v37, v255
v_mul_f32 v38, v38, v255
v_mul_f32 v39, v39, v255
v_mul_f32 v44, v44, v255
v_mul_f32 v45, v45, v255
v_mul_f32 v46, v46, v255
v_mul_f32 v47, v47, v255
v_mul_f32 v52, v52, v255
v_mul_f32 v53, v53, v255
v_mul_f32 v54, v54, v255
v_mul_f32 v55, v55, v255
v_mul_f32 v60, v60, v255
v_mul_f32 v61, v61, v255
v_mul_f32 v62, v62, v255
v_mul_f32 v63, v63, v255
v_mul_f32 v68, v68, v255
v_mul_f32 v69, v69, v255
v_mul_f32 v70, v70, v255
v_mul_f32 v71, v71, v255
v_mul_f32 v76, v76, v255
v_mul_f32 v77, v77, v255
v_mul_f32 v78, v78, v255
v_mul_f32 v79, v79, v255
v_mul_f32 v84, v84, v255
v_mul_f32 v85, v85, v255
v_mul_f32 v86, v86, v255
v_mul_f32 v87, v87, v255
v_mul_f32 v92, v92, v255
v_mul_f32 v93, v93, v255
v_mul_f32 v94, v94, v255
v_mul_f32 v95, v95, v255
v_mul_f32 v100, v100, v255
v_mul_f32 v101, v101, v255
v_mul_f32 v102, v102, v255
v_mul_f32 v103, v103, v255
v_mul_f32 v108, v108, v255
v_mul_f32 v109, v109, v255
v_mul_f32 v110, v110, v255
v_mul_f32 v111, v111, v255
v_mul_f32 v116, v116, v255
v_mul_f32 v117, v117, v255
v_mul_f32 v118, v118, v255
v_mul_f32 v119, v119, v255
v_mul_f32 v124, v124, v255
v_mul_f32 v125, v125, v255
v_mul_f32 v126, v126, v255
v_mul_f32 v127, v127, v255
s_mov_b32 s69, 0
s_nop 0
s_branch .LRBR8
.LRA9:
v_mov_b32 v241, v254
s_nop 1
v_permlane16_swap_b32 v254, v241
v_max_f32 v254, v254, v241
v_mov_b32 v241, v254
)A"
R"A(s_nop 1
v_permlane32_swap_b32 v254, v241
v_max_f32 v254, v254, v241
v_max_f32 v254, 0, v254
v_sub_f32 v128, v128, v254
v_sub_f32 v129, v129, v254
v_sub_f32 v130, v130, v254
v_sub_f32 v131, v131, v254
v_sub_f32 v136, v136, v254
v_sub_f32 v137, v137, v254
v_sub_f32 v138, v138, v254
v_sub_f32 v139, v139, v254
v_sub_f32 v144, v144, v254
v_sub_f32 v145, v145, v254
v_sub_f32 v146, v146, v254
v_sub_f32 v147, v147, v254
v_sub_f32 v152, v152, v254
v_sub_f32 v153, v153, v254
v_sub_f32 v154, v154, v254
v_sub_f32 v155, v155, v254
v_sub_f32 v244, v244, v254
v_sub_f32 v245, v245, v254
v_sub_f32 v246, v246, v254
v_sub_f32 v247, v247, v254
v_exp_f32 v254, -v254
s_nop 0
v_mul_f32 v242, v242, v254
v_mov_b32 v241, v255
s_nop 1
v_permlane16_swap_b32 v255, v241
v_max_f32 v255, v255, v241
v_mov_b32 v241, v255
s_nop 1
v_permlane32_swap_b32 v255, v241
v_max_f32 v255, v255, v241
v_max_f32 v255, 0, v255
v_sub_f32 v132, v132, v255
v_sub_f32 v133, v133, v255
v_sub_f32 v134, v134, v255
v_sub_f32 v135, v135, v255
v_sub_f32 v140, v140, v255
v_sub_f32 v141, v141, v255
v_sub_f32 v142, v142, v255
v_sub_f32 v143, v143, v255
v_sub_f32 v148, v148, v255
v_sub_f32 v149, v149, v255
v_sub_f32 v150, v150, v255
v_sub_f32 v151, v151, v255
v_sub_f32 v156, v156, v255
v_sub_f32 v157, v157, v255
v_sub_f32 v158, v158, v255
v_sub_f32 v159, v159, v255
v_sub_f32 v248, v248, v255
v_sub_f32 v249, v249, v255
v_sub_f32 v250, v250, v255
v_sub_f32 v251, v251, v255
v_exp_f32 v255, -v255
s_nop 0
v_mul_f32 v243, v243, v255
s_mov_b32 s69, 1
s_branch .LRAR9
.LRB9:
s_nop 7
v_mul_f32 v0, v0, v254
v_mul_f32 v1, v1, v254
v_mul_f32 v2, v2, v254
v_mul_f32 v3, v3, v254
v_mul_f32 v8, v8, v254
v_mul_f32 v9, v9, v254
v_mul_f32 v10, v10, v254
v_mul_f32 v11, v11, v254
v_mul_f32 v16, v16, v254
v_mul_f32 v17, v17, v254
v_mul_f32 v18, v18, v254
v_mul_f32 v19, v19, v254
v_mul_f32 v24, v24, v254
v_mul_f32 v25, v25, v254
v_mul_f32 v26, v26, v254
v_mul_f32 v27, v27, v254
v_mul_f32 v32, v32, v254
v_mul_f32 v33, v33, v254
v_mul_f32 v34, v34, v254
v_mul_f32 v35, v35, v254
v_mul_f32 v40, v40, v254
v_mul_f32 v41, v41, v254
v_mul_f32 v42, v42, v254
v_mul_f32 v43, v43, v254
v_mul_f32 v48, v48, v254
v_mul_f32 v49, v49, v254
v_mul_f32 v50, v50, v254
v_mul_f32 v51, v51, v254
v_mul_f32 v56, v56, v254
v_mul_f32 v57, v57, v254
v_mul_f32 v58, v58, v254
v_mul_f32 v59, v59, v254
v_mul_f32 v64, v64, v254
v_mul_f32 v65, v65, v254
v_mul_f32 v66, v66, v254
v_mul_f32 v67, v67, v254
v_mul_f32 v72, v72, v254
v_mul_f32 v73, v73, v254
v_mul_f32 v74, v74, v254
v_mul_f32 v75, v75, v254
v_mul_f32 v80, v80, v254
v_mul_f32 v81, v81, v254
v_mul_f32 v82, v82, v254
v_mul_f32 v83, v83, v254
v_mul_f32 v88, v88, v254
v_mul_f32 v89, v89, v254
v_mul_f32 v90, v90, v254
v_mul_f32 v91, v91, v254
v_mul_f32 v96, v96, v254
v_mul_f32 v97, v97, v254
v_mul_f32 v98, v98, v254
v_mul_f32 v99, v99, v254
v_mul_f32 v104, v104, v254
v_mul_f32 v105, v105, v254
v_mul_f32 v106, v106, v254
v_mul_f32 v107, v107, v254
v_mul_f32 v112, v112, v254
v_mul_f32 v113, v113, v254
v_mul_f32 v114, v114, v254
v_mul_f32 v115, v115, v254
v_mul_f32 v120, v120, v254
v_mul_f32 v121, v121, v254
v_mul_f32 v122, v122, v254
v_mul_f32 v123, v123, v254
v_mul_f32 v4, v4, v255
v_mul_f32 v5, v5, v255
v_mul_f32 v6, v6, v255
v_mul_f32 v7, v7, v255
v_mul_f32 v12, v12, v255
v_mul_f32 v13, v13, v255
v_mul_f32 v14, v14, v255
v_mul_f32 v15, v15, v255
v_mul_f32 v20, v20, v255
v_mul_f32 v21, v21, v255
v_mul_f32 v22, v22, v255
v_mul_f32 v23, v23, v255
v_mul_f32 v28, v28, v255
v_mul_f32 v29, v29, v255
v_mul_f32 v30, v30, v255
v_mul_f32 v31, v31, v255
v_mul_f32 v36, v36, v255
v_mul_f32 v37, v37, v255
v_mul_f32 v38, v38, v255
v_mul_f32 v39, v39, v255
v_mul_f32 v44, v44, v255
v_mul_f32 v45, v45, v255
v_mul_f32 v46, v46, v255
v_mul_f32 v47, v47, v255
v_mul_f32 v52, v52, v255
v_mul_f32 v53, v53, v255
v_mul_f32 v54, v54, v255
v_mul_f32 v55, v55, v255
v_mul_f32 v60, v60, v255
v_mul_f32 v61, v61, v255
v_mul_f32 v62, v62, v255
v_mul_f32 v63, v63, v255
v_mul_f32 v68, v68, v255
v_mul_f32 v69, v69, v255
v_mul_f32 v70, v70, v255
v_mul_f32 v71, v71, v255
v_mul_f32 v76, v76, v255
v_mul_f32 v77, v77, v255
v_mul_f32 v78, v78, v255
v_mul_f32 v79, v79, v255
v_mul_f32 v84, v84, v255
v_mul_f32 v85, v85, v255
v_mul_f32 v86, v86, v255
v_mul_f32 v87, v87, v255
v_mul_f32 v92, v92, v255
v_mul_f32 v93, v93, v255
v_mul_f32 v94, v94, v255
v_mul_f32 v95, v95, v255
v_mul_f32 v100, v100, v255
v_mul_f32 v101, v101, v255
v_mul_f32 v102, v102, v255
v_mul_f32 v103, v103, v255
v_mul_f32 v108, v108, v255
v_mul_f32 v109, v109, v255
v_mul_f32 v110, v110, v255
v_mul_f32 v111, v111, v255
v_mul_f32 v116, v116, v255
v_mul_f32 v117, v117, v255
v_mul_f32 v118, v118, v255
v_mul_f32 v119, v119, v255
v_mul_f32 v124, v124, v255
v_mul_f32 v125, v125, v255
v_mul_f32 v126, v126, v255
v_mul_f32 v127, v127, v255
s_mov_b32 s69, 0
s_nop 0
s_branch .LRBR9
.LRA10:
v_mov_b32 v241, v254
s_nop 1
v_permlane16_swap_b32 v254, v241
v_max_f32 v254, v254, v241
v_mov_b32 v241, v254
s_nop 1
v_permlane32_swap_b32 v254, v241
v_max_f32 v254, v254, v241
v_max_f32 v254, 0, v254
v_sub_f32 v128, v128, v254
v_sub_f32 v129, v129, v254
v_sub_f32 v130, v130, v254
v_sub_f32 v131, v131, v254
v_sub_f32 v136, v136, v254
v_sub_f32 v137, v137, v254
v_sub_f32 v138, v138, v254
v_sub_f32 v139, v139, v254
v_sub_f32 v144, v144, v254
v_sub_f32 v145, v145, v254
v_sub_f32 v146, v146, v254
v_sub_f32 v147, v147, v254
v_sub_f32 v152, v152, v254
v_sub_f32 v153, v153, v254
v_sub_f32 v154, v154, v254
v_sub_f32 v155, v155, v254
v_sub_f32 v244, v244, v254
v_sub_f32 v245, v245, v254
v_sub_f32 v246, v246, v254
v_sub_f32 v247, v247, v254
v_exp_f32 v254, -v254
s_nop 0
v_mul_f32 v242, v242, v254
v_mov_b32 v241, v255
s_nop 1
v_permlane16_swap_b32 v255, v241
v_max_f32 v255, v255, v241
v_mov_b32 v241, v255
s_nop 1
v_permlane32_swap_b32 v255, v241
v_max_f32 v255, v255, v241
v_max_f32 v255, 0, v255
v_sub_f32 v132, v132, v255
v_sub_f32 v133, v133, v255
v_sub_f32 v134, v134, v255
v_sub_f32 v135, v135, v255
v_sub_f32 v140, v140, v255
v_sub_f32 v141, v141, v255
v_sub_f32 v142, v142, v255
v_sub_f32 v143, v143, v255
v_sub_f32 v148, v148, v255
v_sub_f32 v149, v149, v255
v_sub_f32 v150, v150, v255
v_sub_f32 v151, v151, v255
v_sub_f32 v156, v156, v255
v_sub_f32 v157, v157, v255
v_sub_f32 v158, v158, v255
v_sub_f32 v159, v159, v255
v_sub_f32 v248, v248, v255
v_sub_f32 v249, v249, v255
v_sub_f32 v250, v250, v255
v_sub_f32 v251, v251, v255
v_exp_f32 v255, -v255
s_nop 0
v_mul_f32 v243, v243, v255
s_mov_b32 s69, 1
s_branch .LRAR10
.LRB10:
s_nop 7
v_mul_f32 v0, v0, v254
v_mul_f32 v1, v1, v254
v_mul_f32 v2, v2, v254
v_mul_f32 v3, v3, v254
v_mul_f32 v8, v8, v254
v_mul_f32 v9, v9, v254
v_mul_f32 v10, v10, v254
v_mul_f32 v11, v11, v254
v_mul_f32 v16, v16, v254
v_mul_f32 v17, v17, v254
v_mul_f32 v18, v18, v254
v_mul_f32 v19, v19, v254
v_mul_f32 v24, v24, v254
v_mul_f32 v25, v25, v254
v_mul_f32 v26, v26, v254
v_mul_f32 v27, v27, v254
v_mul_f32 v32, v32, v254
v_mul_f32 v33, v33, v254
v_mul_f32 v34, v34, v254
v_mul_f32 v35, v35, v254
v_mul_f32 v40, v40, v254
v_mul_f32 v41, v41, v254
v_mul_f32 v42, v42, v254
v_mul_f32 v43, v43, v254
v_mul_f32 v48, v48, v254
v_mul_f32 v49, v49, v254
v_mul_f32 v50, v50, v254
v_mul_f32 v51, v51, v254
v_mul_f32 v56, v56, v254
v_mul_f32 v57, v57, v254
v_mul_f32 v58, v58, v254
v_mul_f32 v59, v59, v254
v_mul_f32 v64, v64, v254
v_mul_f32 v65, v65, v254
v_mul_f32 v66, v66, v254
v_mul_f32 v67, v67, v254
v_mul_f32 v72, v72, v254
v_mul_f32 v73, v73, v254
v_mul_f32 v74, v74, v254
v_mul_f32 v75, v75, v254
v_mul_f32 v80, v80, v254
v_mul_f32 v81, v81, v254
v_mul_f32 v82, v82, v254
v_mul_f32 v83, v83, v254
v_mul_f32 v88, v88, v254
v_mul_f32 v89, v89, v254
v_mul_f32 v90, v90, v254
v_mul_f32 v91, v91, v254
v_mul_f32 v96, v96, v254
v_mul_f32 v97, v97, v254
v_mul_f32 v98, v98, v254
v_mul_f32 v99, v99, v254
v_mul_f32 v104, v104, v254
v_mul_f32 v105, v105, v254
v_mul_f32 v106, v106, v254
v_mul_f32 v107, v107, v254
v_mul_f32 v112, v112, v254
v_mul_f32 v113, v113, v254
v_mul_f32 v114, v114, v254
v_mul_f32 v115, v115, v254
v_mul_f32 v120, v120, v254
v_mul_f32 v121, v121, v254
v_mul_f32 v122, v122, v254
v_mul_f32 v123, v123, v254
v_mul_f32 v4, v4, v255
v_mul_f32 v5, v5, v255
v_mul_f32 v6, v6, v255
v_mul_f32 v7, v7, v255
v_mul_f32 v12, v12, v255
v_mul_f32 v13, v13, v255
v_mul_f32 v14, v14, v255
v_mul_f32 v15, v15, v255
v_mul_f32 v20, v20, v255
v_mul_f32 v21, v21, v255
v_mul_f32 v22, v22, v255
v_mul_f32 v23, v23, v255
v_mul_f32 v28, v28, v255
v_mul_f32 v29, v29, v255
v_mul_f32 v30, v30, v255
v_mul_f32 v31, v31, v255
v_mul_f32 v36, v36, v255
v_mul_f32 v37, v37, v255
v_mul_f32 v38, v38, v255
v_mul_f32 v39, v39, v255
v_mul_f32 v44, v44, v255
v_mul_f32 v45, v45, v255
v_mul_f32 v46, v46, v255
v_mul_f32 v47, v47, v255
v_mul_f32 v52, v52, v255
v_mul_f32 v53, v53, v255
v_mul_f32 v54, v54, v255
v_mul_f32 v55, v55, v255
v_mul_f32 v60, v60, v255
v_mul_f32 v61, v61, v255
v_mul_f32 v62, v62, v255
v_mul_f32 v63, v63, v255
v_mul_f32 v68, v68, v255
v_mul_f32 v69, v69, v255
v_mul_f32 v70, v70, v255
v_mul_f32 v71, v71, v255
v_mul_f32 v76, v76, v255
v_mul_f32 v77, v77, v255
v_mul_f32 v78, v78, v255
v_mul_f32 v79, v79, v255
v_mul_f32 v84, v84, v255
v_mul_f32 v85, v85, v255
v_mul_f32 v86, v86, v255
v_mul_f32 v87, v87, v255
v_mul_f32 v92, v92, v255
v_mul_f32 v93, v93, v255
v_mul_f32 v94, v94, v255
v_mul_f32 v95, v95, v255
v_mul_f32 v100, v100, v255
v_mul_f32 v101, v101, v255
v_mul_f32 v102, v102, v255
v_mul_f32 v103, v103, v255
v_mul_f32 v108, v108, v255
v_mul_f32 v109, v109, v255
v_mul_f32 v110, v110, v255
v_mul_f32 v111, v111, v255
v_mul_f32 v116, v116, v255
v_mul_f32 v117, v117, v255
v_mul_f32 v118, v118, v255
v_mul_f32 v119, v119, v255
v_mul_f32 v124, v124, v255
v_mul_f32 v125, v125, v255
v_mul_f32 v126, v126, v255
v_mul_f32 v127, v127, v255
s_mov_b32 s69, 0
s_nop 0
s_branch .LRBR10
.LRA11:
v_mov_b32 v241, v254
s_nop 1
v_permlane16_swap_b32 v254, v241
v_max_f32 v254, v254, v241
v_mov_b32 v241, v254
s_nop 1
v_permlane32_swap_b32 v254, v241
v_max_f32 v254, v254, v241
v_max_f32 v254, 0, v254
v_sub_f32 v128, v128, v254
v_sub_f32 v129, v129, v254
v_sub_f32 v130, v130, v254
v_sub_f32 v131, v131, v254
v_sub_f32 v136, v136, v254
v_sub_f32 v137, v137, v254
v_sub_f32 v138, v138, v254
v_sub_f32 v139, v139, v254
v_sub_f32 v144, v144, v254
v_sub_f32 v145, v145, v254
v_sub_f32 v146, v146, v254
v_sub_f32 v147, v147, v254
v_sub_f32 v152, v152, v254
v_sub_f32 v153, v153, v254
v_sub_f32 v154, v154, v254
v_sub_f32 v155, v155, v254
v_sub_f32 v244, v244, v254
v_sub_f32 v245, v245, v254
v_sub_f32 v246, v246, v254
v_sub_f32 v247, v247, v254
v_exp_f32 v254, -v254
s_nop 0
v_mul_f32 v242, v242, v254
v_mov_b32 v241, v255
s_nop 1
v_permlane16_swap_b32 v255, v241
v_max_f32 v255, v255, v241
v_mov_b32 v241, v255
s_nop 1
v_permlane32_swap_b32 v255, v241
v_max_f32 v255, v255, v241
v_max_f32 v255, 0, v255
v_sub_f32 v132, v132, v255
v_sub_f32 v133, v133, v255
v_sub_f32 v134, v134, v255
v_sub_f32 v135, v135, v255
v_sub_f32 v140, v140, v255
v_sub_f32 v141, v141, v255
v_sub_f32 v142, v142, v255
v_sub_f32 v143, v143, v255
v_sub_f32 v148, v148, v255
v_sub_f32 v149, v149, v255
v_sub_f32 v150, v150, v255
v_sub_f32 v151, v151, v255
v_sub_f32 v156, v156, v255
v_sub_f32 v157, v157, v255
v_sub_f32 v158, v158, v255
v_sub_f32 v159, v159, v255
v_sub_f32 v248, v248, v255
v_sub_f32 v249, v249, v255
v_sub_f32 v250, v250, v255
v_sub_f32 v251, v251, v255
v_exp_f32 v255, -v255
s_nop 0
v_mul_f32 v243, v243, v255
s_mov_b32 s69, 1
s_branch .LRAR11
.LRB11:
s_nop 7
v_mul_f32 v0, v0, v254
v_mul_f32 v1, v1, v254
v_mul_f32 v2, v2, v254
v_mul_f32 v3, v3, v254
v_mul_f32 v8, v8, v254
v_mul_f32 v9, v9, v254
v_mul_f32 v10, v10, v254
v_mul_f32 v11, v11, v254
v_mul_f32 v16, v16, v254
v_mul_f32 v17, v17, v254
v_mul_f32 v18, v18, v254
v_mul_f32 v19, v19, v254
v_mul_f32 v24, v24, v254
v_mul_f32 v25, v25, v254
v_mul_f32 v26, v26, v254
v_mul_f32 v27, v27, v254
v_mul_f32 v32, v32, v254
v_mul_f32 v33, v33, v254
v_mul_f32 v34, v34, v254
v_mul_f32 v35, v35, v254
v_mul_f32 v40, v40, v254
v_mul_f32 v41, v41, v254
v_mul_f32 v42, v42, v254
v_mul_f32 v43, v43, v254
v_mul_f32 v48, v48, v254
)A"
R"A(v_mul_f32 v49, v49, v254
v_mul_f32 v50, v50, v254
v_mul_f32 v51, v51, v254
v_mul_f32 v56, v56, v254
v_mul_f32 v57, v57, v254
v_mul_f32 v58, v58, v254
v_mul_f32 v59, v59, v254
v_mul_f32 v64, v64, v254
v_mul_f32 v65, v65, v254
v_mul_f32 v66, v66, v254
v_mul_f32 v67, v67, v254
v_mul_f32 v72, v72, v254
v_mul_f32 v73, v73, v254
v_mul_f32 v74, v74, v254
v_mul_f32 v75, v75, v254
v_mul_f32 v80, v80, v254
v_mul_f32 v81, v81, v254
v_mul_f32 v82, v82, v254
v_mul_f32 v83, v83, v254
v_mul_f32 v88, v88, v254
v_mul_f32 v89, v89, v254
v_mul_f32 v90, v90, v254
v_mul_f32 v91, v91, v254
v_mul_f32 v96, v96, v254
v_mul_f32 v97, v97, v254
v_mul_f32 v98, v98, v254
v_mul_f32 v99, v99, v254
v_mul_f32 v104, v104, v254
v_mul_f32 v105, v105, v254
v_mul_f32 v106, v106, v254
v_mul_f32 v107, v107, v254
v_mul_f32 v112, v112, v254
v_mul_f32 v113, v113, v254
v_mul_f32 v114, v114, v254
v_mul_f32 v115, v115, v254
v_mul_f32 v120, v120, v254
v_mul_f32 v121, v121, v254
v_mul_f32 v122, v122, v254
v_mul_f32 v123, v123, v254
v_mul_f32 v4, v4, v255
v_mul_f32 v5, v5, v255
v_mul_f32 v6, v6, v255
v_mul_f32 v7, v7, v255
v_mul_f32 v12, v12, v255
v_mul_f32 v13, v13, v255
v_mul_f32 v14, v14, v255
v_mul_f32 v15, v15, v255
v_mul_f32 v20, v20, v255
v_mul_f32 v21, v21, v255
v_mul_f32 v22, v22, v255
v_mul_f32 v23, v23, v255
v_mul_f32 v28, v28, v255
v_mul_f32 v29, v29, v255
v_mul_f32 v30, v30, v255
v_mul_f32 v31, v31, v255
v_mul_f32 v36, v36, v255
v_mul_f32 v37, v37, v255
v_mul_f32 v38, v38, v255
v_mul_f32 v39, v39, v255
v_mul_f32 v44, v44, v255
v_mul_f32 v45, v45, v255
v_mul_f32 v46, v46, v255
v_mul_f32 v47, v47, v255
v_mul_f32 v52, v52, v255
v_mul_f32 v53, v53, v255
v_mul_f32 v54, v54, v255
v_mul_f32 v55, v55, v255
v_mul_f32 v60, v60, v255
v_mul_f32 v61, v61, v255
v_mul_f32 v62, v62, v255
v_mul_f32 v63, v63, v255
v_mul_f32 v68, v68, v255
v_mul_f32 v69, v69, v255
v_mul_f32 v70, v70, v255
v_mul_f32 v71, v71, v255
v_mul_f32 v76, v76, v255
v_mul_f32 v77, v77, v255
v_mul_f32 v78, v78, v255
v_mul_f32 v79, v79, v255
v_mul_f32 v84, v84, v255
v_mul_f32 v85, v85, v255
v_mul_f32 v86, v86, v255
v_mul_f32 v87, v87, v255
v_mul_f32 v92, v92, v255
v_mul_f32 v93, v93, v255
v_mul_f32 v94, v94, v255
v_mul_f32 v95, v95, v255
v_mul_f32 v100, v100, v255
v_mul_f32 v101, v101, v255
v_mul_f32 v102, v102, v255
v_mul_f32 v103, v103, v255
v_mul_f32 v108, v108, v255
v_mul_f32 v109, v109, v255
v_mul_f32 v110, v110, v255
v_mul_f32 v111, v111, v255
v_mul_f32 v116, v116, v255
v_mul_f32 v117, v117, v255
v_mul_f32 v118, v118, v255
v_mul_f32 v119, v119, v255
v_mul_f32 v124, v124, v255
v_mul_f32 v125, v125, v255
v_mul_f32 v126, v126, v255
v_mul_f32 v127, v127, v255
s_mov_b32 s69, 0
s_nop 0
s_branch .LRBR11
.LRA12:
v_mov_b32 v241, v254
s_nop 1
v_permlane16_swap_b32 v254, v241
v_max_f32 v254, v254, v241
v_mov_b32 v241, v254
s_nop 1
v_permlane32_swap_b32 v254, v241
v_max_f32 v254, v254, v241
v_max_f32 v254, 0, v254
v_sub_f32 v128, v128, v254
v_sub_f32 v129, v129, v254
v_sub_f32 v130, v130, v254
v_sub_f32 v131, v131, v254
v_sub_f32 v136, v136, v254
v_sub_f32 v137, v137, v254
v_sub_f32 v138, v138, v254
v_sub_f32 v139, v139, v254
v_sub_f32 v144, v144, v254
v_sub_f32 v145, v145, v254
v_sub_f32 v146, v146, v254
v_sub_f32 v147, v147, v254
v_sub_f32 v152, v152, v254
v_sub_f32 v153, v153, v254
v_sub_f32 v154, v154, v254
v_sub_f32 v155, v155, v254
v_sub_f32 v244, v244, v254
v_sub_f32 v245, v245, v254
v_sub_f32 v246, v246, v254
v_sub_f32 v247, v247, v254
v_exp_f32 v254, -v254
s_nop 0
v_mul_f32 v242, v242, v254
v_mov_b32 v241, v255
s_nop 1
v_permlane16_swap_b32 v255, v241
v_max_f32 v255, v255, v241
v_mov_b32 v241, v255
s_nop 1
v_permlane32_swap_b32 v255, v241
v_max_f32 v255, v255, v241
v_max_f32 v255, 0, v255
v_sub_f32 v132, v132, v255
v_sub_f32 v133, v133, v255
v_sub_f32 v134, v134, v255
v_sub_f32 v135, v135, v255
v_sub_f32 v140, v140, v255
v_sub_f32 v141, v141, v255
v_sub_f32 v142, v142, v255
v_sub_f32 v143, v143, v255
v_sub_f32 v148, v148, v255
v_sub_f32 v149, v149, v255
v_sub_f32 v150, v150, v255
v_sub_f32 v151, v151, v255
v_sub_f32 v156, v156, v255
v_sub_f32 v157, v157, v255
v_sub_f32 v158, v158, v255
v_sub_f32 v159, v159, v255
v_sub_f32 v248, v248, v255
v_sub_f32 v249, v249, v255
v_sub_f32 v250, v250, v255
v_sub_f32 v251, v251, v255
v_exp_f32 v255, -v255
s_nop 0
v_mul_f32 v243, v243, v255
s_mov_b32 s69, 1
s_branch .LRAR12
.LRB12:
s_nop 7
v_mul_f32 v0, v0, v254
v_mul_f32 v1, v1, v254
v_mul_f32 v2, v2, v254
v_mul_f32 v3, v3, v254
v_mul_f32 v8, v8, v254
v_mul_f32 v9, v9, v254
v_mul_f32 v10, v10, v254
v_mul_f32 v11, v11, v254
v_mul_f32 v16, v16, v254
v_mul_f32 v17, v17, v254
v_mul_f32 v18, v18, v254
v_mul_f32 v19, v19, v254
v_mul_f32 v24, v24, v254
v_mul_f32 v25, v25, v254
v_mul_f32 v26, v26, v254
v_mul_f32 v27, v27, v254
v_mul_f32 v32, v32, v254
v_mul_f32 v33, v33, v254
v_mul_f32 v34, v34, v254
v_mul_f32 v35, v35, v254
v_mul_f32 v40, v40, v254
v_mul_f32 v41, v41, v254
v_mul_f32 v42, v42, v254
v_mul_f32 v43, v43, v254
v_mul_f32 v48, v48, v254
v_mul_f32 v49, v49, v254
v_mul_f32 v50, v50, v254
v_mul_f32 v51, v51, v254
v_mul_f32 v56, v56, v254
v_mul_f32 v57, v57, v254
v_mul_f32 v58, v58, v254
v_mul_f32 v59, v59, v254
v_mul_f32 v64, v64, v254
v_mul_f32 v65, v65, v254
v_mul_f32 v66, v66, v254
v_mul_f32 v67, v67, v254
v_mul_f32 v72, v72, v254
v_mul_f32 v73, v73, v254
v_mul_f32 v74, v74, v254
v_mul_f32 v75, v75, v254
v_mul_f32 v80, v80, v254
v_mul_f32 v81, v81, v254
v_mul_f32 v82, v82, v254
v_mul_f32 v83, v83, v254
v_mul_f32 v88, v88, v254
v_mul_f32 v89, v89, v254
v_mul_f32 v90, v90, v254
v_mul_f32 v91, v91, v254
v_mul_f32 v96, v96, v254
v_mul_f32 v97, v97, v254
v_mul_f32 v98, v98, v254
v_mul_f32 v99, v99, v254
v_mul_f32 v104, v104, v254
v_mul_f32 v105, v105, v254
v_mul_f32 v106, v106, v254
v_mul_f32 v107, v107, v254
v_mul_f32 v112, v112, v254
v_mul_f32 v113, v113, v254
v_mul_f32 v114, v114, v254
v_mul_f32 v115, v115, v254
v_mul_f32 v120, v120, v254
v_mul_f32 v121, v121, v254
v_mul_f32 v122, v122, v254
v_mul_f32 v123, v123, v254
v_mul_f32 v4, v4, v255
v_mul_f32 v5, v5, v255
v_mul_f32 v6, v6, v255
v_mul_f32 v7, v7, v255
v_mul_f32 v12, v12, v255
v_mul_f32 v13, v13, v255
v_mul_f32 v14, v14, v255
v_mul_f32 v15, v15, v255
v_mul_f32 v20, v20, v255
v_mul_f32 v21, v21, v255
v_mul_f32 v22, v22, v255
v_mul_f32 v23, v23, v255
v_mul_f32 v28, v28, v255
v_mul_f32 v29, v29, v255
v_mul_f32 v30, v30, v255
v_mul_f32 v31, v31, v255
v_mul_f32 v36, v36, v255
v_mul_f32 v37, v37, v255
v_mul_f32 v38, v38, v255
v_mul_f32 v39, v39, v255
v_mul_f32 v44, v44, v255
v_mul_f32 v45, v45, v255
v_mul_f32 v46, v46, v255
v_mul_f32 v47, v47, v255
v_mul_f32 v52, v52, v255
v_mul_f32 v53, v53, v255
v_mul_f32 v54, v54, v255
v_mul_f32 v55, v55, v255
v_mul_f32 v60, v60, v255
v_mul_f32 v61, v61, v255
v_mul_f32 v62, v62, v255
v_mul_f32 v63, v63, v255
v_mul_f32 v68, v68, v255
v_mul_f32 v69, v69, v255
v_mul_f32 v70, v70, v255
v_mul_f32 v71, v71, v255
v_mul_f32 v76, v76, v255
v_mul_f32 v77, v77, v255
v_mul_f32 v78, v78, v255
v_mul_f32 v79, v79, v255
v_mul_f32 v84, v84, v255
v_mul_f32 v85, v85, v255
v_mul_f32 v86, v86, v255
v_mul_f32 v87, v87, v255
v_mul_f32 v92, v92, v255
v_mul_f32 v93, v93, v255
v_mul_f32 v94, v94, v255
v_mul_f32 v95, v95, v255
v_mul_f32 v100, v100, v255
v_mul_f32 v101, v101, v255
v_mul_f32 v102, v102, v255
v_mul_f32 v103, v103, v255
v_mul_f32 v108, v108, v255
v_mul_f32 v109, v109, v255
v_mul_f32 v110, v110, v255
v_mul_f32 v111, v111, v255
v_mul_f32 v116, v116, v255
v_mul_f32 v117, v117, v255
v_mul_f32 v118, v118, v255
v_mul_f32 v119, v119, v255
v_mul_f32 v124, v124, v255
v_mul_f32 v125, v125, v255
v_mul_f32 v126, v126, v255
v_mul_f32 v127, v127, v255
s_mov_b32 s69, 0
s_nop 0
s_branch .LRBR12
.LRA13:
v_mov_b32 v241, v254
s_nop 1
v_permlane16_swap_b32 v254, v241
v_max_f32 v254, v254, v241
v_mov_b32 v241, v254
s_nop 1
v_permlane32_swap_b32 v254, v241
v_max_f32 v254, v254, v241
v_max_f32 v254, 0, v254
v_sub_f32 v128, v128, v254
v_sub_f32 v129, v129, v254
v_sub_f32 v130, v130, v254
v_sub_f32 v131, v131, v254
v_sub_f32 v136, v136, v254
v_sub_f32 v137, v137, v254
v_sub_f32 v138, v138, v254
v_sub_f32 v139, v139, v254
v_sub_f32 v144, v144, v254
v_sub_f32 v145, v145, v254
v_sub_f32 v146, v146, v254
v_sub_f32 v147, v147, v254
v_sub_f32 v152, v152, v254
v_sub_f32 v153, v153, v254
v_sub_f32 v154, v154, v254
v_sub_f32 v155, v155, v254
v_sub_f32 v244, v244, v254
v_sub_f32 v245, v245, v254
v_sub_f32 v246, v246, v254
v_sub_f32 v247, v247, v254
v_exp_f32 v254, -v254
s_nop 0
v_mul_f32 v242, v242, v254
v_mov_b32 v241, v255
s_nop 1
v_permlane16_swap_b32 v255, v241
v_max_f32 v255, v255, v241
v_mov_b32 v241, v255
s_nop 1
v_permlane32_swap_b32 v255, v241
v_max_f32 v255, v255, v241
v_max_f32 v255, 0, v255
v_sub_f32 v132, v132, v255
v_sub_f32 v133, v133, v255
v_sub_f32 v134, v134, v255
v_sub_f32 v135, v135, v255
v_sub_f32 v140, v140, v255
v_sub_f32 v141, v141, v255
v_sub_f32 v142, v142, v255
v_sub_f32 v143, v143, v255
v_sub_f32 v148, v148, v255
v_sub_f32 v149, v149, v255
v_sub_f32 v150, v150, v255
v_sub_f32 v151, v151, v255
v_sub_f32 v156, v156, v255
v_sub_f32 v157, v157, v255
v_sub_f32 v158, v158, v255
v_sub_f32 v159, v159, v255
v_sub_f32 v248, v248, v255
v_sub_f32 v249, v249, v255
v_sub_f32 v250, v250, v255
v_sub_f32 v251, v251, v255
v_exp_f32 v255, -v255
s_nop 0
v_mul_f32 v243, v243, v255
s_mov_b32 s69, 1
s_branch .LRAR13
.LRB13:
s_nop 7
v_mul_f32 v0, v0, v254
v_mul_f32 v1, v1, v254
v_mul_f32 v2, v2, v254
v_mul_f32 v3, v3, v254
v_mul_f32 v8, v8, v254
v_mul_f32 v9, v9, v254
v_mul_f32 v10, v10, v254
v_mul_f32 v11, v11, v254
v_mul_f32 v16, v16, v254
v_mul_f32 v17, v17, v254
v_mul_f32 v18, v18, v254
v_mul_f32 v19, v19, v254
v_mul_f32 v24, v24, v254
v_mul_f32 v25, v25, v254
v_mul_f32 v26, v26, v254
v_mul_f32 v27, v27, v254
v_mul_f32 v32, v32, v254
v_mul_f32 v33, v33, v254
v_mul_f32 v34, v34, v254
v_mul_f32 v35, v35, v254
v_mul_f32 v40, v40, v254
v_mul_f32 v41, v41, v254
v_mul_f32 v42, v42, v254
v_mul_f32 v43, v43, v254
v_mul_f32 v48, v48, v254
v_mul_f32 v49, v49, v254
v_mul_f32 v50, v50, v254
v_mul_f32 v51, v51, v254
v_mul_f32 v56, v56, v254
v_mul_f32 v57, v57, v254
v_mul_f32 v58, v58, v254
v_mul_f32 v59, v59, v254
v_mul_f32 v64, v64, v254
v_mul_f32 v65, v65, v254
v_mul_f32 v66, v66, v254
v_mul_f32 v67, v67, v254
v_mul_f32 v72, v72, v254
v_mul_f32 v73, v73, v254
v_mul_f32 v74, v74, v254
v_mul_f32 v75, v75, v254
v_mul_f32 v80, v80, v254
v_mul_f32 v81, v81, v254
v_mul_f32 v82, v82, v254
v_mul_f32 v83, v83, v254
v_mul_f32 v88, v88, v254
v_mul_f32 v89, v89, v254
v_mul_f32 v90, v90, v254
v_mul_f32 v91, v91, v254
v_mul_f32 v96, v96, v254
v_mul_f32 v97, v97, v254
v_mul_f32 v98, v98, v254
v_mul_f32 v99, v99, v254
v_mul_f32 v104, v104, v254
v_mul_f32 v105, v105, v254
v_mul_f32 v106, v106, v254
v_mul_f32 v107, v107, v254
v_mul_f32 v112, v112, v254
v_mul_f32 v113, v113, v254
v_mul_f32 v114, v114, v254
v_mul_f32 v115, v115, v254
v_mul_f32 v120, v120, v254
v_mul_f32 v121, v121, v254
v_mul_f32 v122, v122, v254
v_mul_f32 v123, v123, v254
v_mul_f32 v4, v4, v255
v_mul_f32 v5, v5, v255
v_mul_f32 v6, v6, v255
v_mul_f32 v7, v7, v255
v_mul_f32 v12, v12, v255
v_mul_f32 v13, v13, v255
v_mul_f32 v14, v14, v255
v_mul_f32 v15, v15, v255
v_mul_f32 v20, v20, v255
v_mul_f32 v21, v21, v255
v_mul_f32 v22, v22, v255
v_mul_f32 v23, v23, v255
v_mul_f32 v28, v28, v255
v_mul_f32 v29, v29, v255
v_mul_f32 v30, v30, v255
v_mul_f32 v31, v31, v255
v_mul_f32 v36, v36, v255
v_mul_f32 v37, v37, v255
v_mul_f32 v38, v38, v255
v_mul_f32 v39, v39, v255
v_mul_f32 v44, v44, v255
v_mul_f32 v45, v45, v255
v_mul_f32 v46, v46, v255
v_mul_f32 v47, v47, v255
v_mul_f32 v52, v52, v255
v_mul_f32 v53, v53, v255
v_mul_f32 v54, v54, v255
v_mul_f32 v55, v55, v255
v_mul_f32 v60, v60, v255
v_mul_f32 v61, v61, v255
v_mul_f32 v62, v62, v255
v_mul_f32 v63, v63, v255
v_mul_f32 v68, v68, v255
v_mul_f32 v69, v69, v255
v_mul_f32 v70, v70, v255
v_mul_f32 v71, v71, v255
v_mul_f32 v76, v76, v255
v_mul_f32 v77, v77, v255
v_mul_f32 v78, v78, v255
v_mul_f32 v79, v79, v255
v_mul_f32 v84, v84, v255
v_mul_f32 v85, v85, v255
v_mul_f32 v86, v86, v255
v_mul_f32 v87, v87, v255
v_mul_f32 v92, v92, v255
v_mul_f32 v93, v93, v255
)A"
R"A(v_mul_f32 v94, v94, v255
v_mul_f32 v95, v95, v255
v_mul_f32 v100, v100, v255
v_mul_f32 v101, v101, v255
v_mul_f32 v102, v102, v255
v_mul_f32 v103, v103, v255
v_mul_f32 v108, v108, v255
v_mul_f32 v109, v109, v255
v_mul_f32 v110, v110, v255
v_mul_f32 v111, v111, v255
v_mul_f32 v116, v116, v255
v_mul_f32 v117, v117, v255
v_mul_f32 v118, v118, v255
v_mul_f32 v119, v119, v255
v_mul_f32 v124, v124, v255
v_mul_f32 v125, v125, v255
v_mul_f32 v126, v126, v255
v_mul_f32 v127, v127, v255
s_mov_b32 s69, 0
s_nop 0
s_branch .LRBR13
.LRA14:
v_mov_b32 v241, v254
s_nop 1
v_permlane16_swap_b32 v254, v241
v_max_f32 v254, v254, v241
v_mov_b32 v241, v254
s_nop 1
v_permlane32_swap_b32 v254, v241
v_max_f32 v254, v254, v241
v_max_f32 v254, 0, v254
v_sub_f32 v128, v128, v254
v_sub_f32 v129, v129, v254
v_sub_f32 v130, v130, v254
v_sub_f32 v131, v131, v254
v_sub_f32 v136, v136, v254
v_sub_f32 v137, v137, v254
v_sub_f32 v138, v138, v254
v_sub_f32 v139, v139, v254
v_sub_f32 v144, v144, v254
v_sub_f32 v145, v145, v254
v_sub_f32 v146, v146, v254
v_sub_f32 v147, v147, v254
v_sub_f32 v152, v152, v254
v_sub_f32 v153, v153, v254
v_sub_f32 v154, v154, v254
v_sub_f32 v155, v155, v254
v_sub_f32 v244, v244, v254
v_sub_f32 v245, v245, v254
v_sub_f32 v246, v246, v254
v_sub_f32 v247, v247, v254
v_exp_f32 v254, -v254
s_nop 0
v_mul_f32 v242, v242, v254
v_mov_b32 v241, v255
s_nop 1
v_permlane16_swap_b32 v255, v241
v_max_f32 v255, v255, v241
v_mov_b32 v241, v255
s_nop 1
v_permlane32_swap_b32 v255, v241
v_max_f32 v255, v255, v241
v_max_f32 v255, 0, v255
v_sub_f32 v132, v132, v255
v_sub_f32 v133, v133, v255
v_sub_f32 v134, v134, v255
v_sub_f32 v135, v135, v255
v_sub_f32 v140, v140, v255
v_sub_f32 v141, v141, v255
v_sub_f32 v142, v142, v255
v_sub_f32 v143, v143, v255
v_sub_f32 v148, v148, v255
v_sub_f32 v149, v149, v255
v_sub_f32 v150, v150, v255
v_sub_f32 v151, v151, v255
v_sub_f32 v156, v156, v255
v_sub_f32 v157, v157, v255
v_sub_f32 v158, v158, v255
v_sub_f32 v159, v159, v255
v_sub_f32 v248, v248, v255
v_sub_f32 v249, v249, v255
v_sub_f32 v250, v250, v255
v_sub_f32 v251, v251, v255
v_exp_f32 v255, -v255
s_nop 0
v_mul_f32 v243, v243, v255
s_mov_b32 s69, 1
s_branch .LRAR14
.LRB14:
s_nop 7
v_mul_f32 v0, v0, v254
v_mul_f32 v1, v1, v254
v_mul_f32 v2, v2, v254
v_mul_f32 v3, v3, v254
v_mul_f32 v8, v8, v254
v_mul_f32 v9, v9, v254
v_mul_f32 v10, v10, v254
v_mul_f32 v11, v11, v254
v_mul_f32 v16, v16, v254
v_mul_f32 v17, v17, v254
v_mul_f32 v18, v18, v254
v_mul_f32 v19, v19, v254
v_mul_f32 v24, v24, v254
v_mul_f32 v25, v25, v254
v_mul_f32 v26, v26, v254
v_mul_f32 v27, v27, v254
v_mul_f32 v32, v32, v254
v_mul_f32 v33, v33, v254
v_mul_f32 v34, v34, v254
v_mul_f32 v35, v35, v254
v_mul_f32 v40, v40, v254
v_mul_f32 v41, v41, v254
v_mul_f32 v42, v42, v254
v_mul_f32 v43, v43, v254
v_mul_f32 v48, v48, v254
v_mul_f32 v49, v49, v254
v_mul_f32 v50, v50, v254
v_mul_f32 v51, v51, v254
v_mul_f32 v56, v56, v254
v_mul_f32 v57, v57, v254
v_mul_f32 v58, v58, v254
v_mul_f32 v59, v59, v254
v_mul_f32 v64, v64, v254
v_mul_f32 v65, v65, v254
v_mul_f32 v66, v66, v254
v_mul_f32 v67, v67, v254
v_mul_f32 v72, v72, v254
v_mul_f32 v73, v73, v254
v_mul_f32 v74, v74, v254
v_mul_f32 v75, v75, v254
v_mul_f32 v80, v80, v254
v_mul_f32 v81, v81, v254
v_mul_f32 v82, v82, v254
v_mul_f32 v83, v83, v254
v_mul_f32 v88, v88, v254
v_mul_f32 v89, v89, v254
v_mul_f32 v90, v90, v254
v_mul_f32 v91, v91, v254
v_mul_f32 v96, v96, v254
v_mul_f32 v97, v97, v254
v_mul_f32 v98, v98, v254
v_mul_f32 v99, v99, v254
v_mul_f32 v104, v104, v254
v_mul_f32 v105, v105, v254
v_mul_f32 v106, v106, v254
v_mul_f32 v107, v107, v254
v_mul_f32 v112, v112, v254
v_mul_f32 v113, v113, v254
v_mul_f32 v114, v114, v254
v_mul_f32 v115, v115, v254
v_mul_f32 v120, v120, v254
v_mul_f32 v121, v121, v254
v_mul_f32 v122, v122, v254
v_mul_f32 v123, v123, v254
v_mul_f32 v4, v4, v255
v_mul_f32 v5, v5, v255
v_mul_f32 v6, v6, v255
v_mul_f32 v7, v7, v255
v_mul_f32 v12, v12, v255
v_mul_f32 v13, v13, v255
v_mul_f32 v14, v14, v255
v_mul_f32 v15, v15, v255
v_mul_f32 v20, v20, v255
v_mul_f32 v21, v21, v255
v_mul_f32 v22, v22, v255
v_mul_f32 v23, v23, v255
v_mul_f32 v28, v28, v255
v_mul_f32 v29, v29, v255
v_mul_f32 v30, v30, v255
v_mul_f32 v31, v31, v255
v_mul_f32 v36, v36, v255
v_mul_f32 v37, v37, v255
v_mul_f32 v38, v38, v255
v_mul_f32 v39, v39, v255
v_mul_f32 v44, v44, v255
v_mul_f32 v45, v45, v255
v_mul_f32 v46, v46, v255
v_mul_f32 v47, v47, v255
v_mul_f32 v52, v52, v255
v_mul_f32 v53, v53, v255
v_mul_f32 v54, v54, v255
v_mul_f32 v55, v55, v255
v_mul_f32 v60, v60, v255
v_mul_f32 v61, v61, v255
v_mul_f32 v62, v62, v255
v_mul_f32 v63, v63, v255
v_mul_f32 v68, v68, v255
v_mul_f32 v69, v69, v255
v_mul_f32 v70, v70, v255
v_mul_f32 v71, v71, v255
v_mul_f32 v76, v76, v255
v_mul_f32 v77, v77, v255
v_mul_f32 v78, v78, v255
v_mul_f32 v79, v79, v255
v_mul_f32 v84, v84, v255
v_mul_f32 v85, v85, v255
v_mul_f32 v86, v86, v255
v_mul_f32 v87, v87, v255
v_mul_f32 v92, v92, v255
v_mul_f32 v93, v93, v255
v_mul_f32 v94, v94, v255
v_mul_f32 v95, v95, v255
v_mul_f32 v100, v100, v255
v_mul_f32 v101, v101, v255
v_mul_f32 v102, v102, v255
v_mul_f32 v103, v103, v255
v_mul_f32 v108, v108, v255
v_mul_f32 v109, v109, v255
v_mul_f32 v110, v110, v255
v_mul_f32 v111, v111, v255
v_mul_f32 v116, v116, v255
v_mul_f32 v117, v117, v255
v_mul_f32 v118, v118, v255
v_mul_f32 v119, v119, v255
v_mul_f32 v124, v124, v255
v_mul_f32 v125, v125, v255
v_mul_f32 v126, v126, v255
v_mul_f32 v127, v127, v255
s_mov_b32 s69, 0
s_nop 0
s_branch .LRBR14
.LPS11:
s_mov_b32 s78, 200000
.LPL11:
s_sleep 2
buffer_load_dwordx2 v[202:203], v198, s[20:23], s55 offen sc1
s_waitcnt vmcnt(0)
v_cmp_ne_u64 vcc, s[48:49], v[202:203]
s_sub_u32 s78, s78, 1
s_cbranch_scc1 .LPX11
s_cbranch_vccnz .LPL11
.LPX11:
s_branch .LPR11
.LRA15:
v_mov_b32 v241, v254
s_nop 1
v_permlane16_swap_b32 v254, v241
v_max_f32 v254, v254, v241
v_mov_b32 v241, v254
s_nop 1
v_permlane32_swap_b32 v254, v241
v_max_f32 v254, v254, v241
v_max_f32 v254, 0, v254
v_sub_f32 v128, v128, v254
v_sub_f32 v129, v129, v254
v_sub_f32 v130, v130, v254
v_sub_f32 v131, v131, v254
v_sub_f32 v136, v136, v254
v_sub_f32 v137, v137, v254
v_sub_f32 v138, v138, v254
v_sub_f32 v139, v139, v254
v_sub_f32 v144, v144, v254
v_sub_f32 v145, v145, v254
v_sub_f32 v146, v146, v254
v_sub_f32 v147, v147, v254
v_sub_f32 v152, v152, v254
v_sub_f32 v153, v153, v254
v_sub_f32 v154, v154, v254
v_sub_f32 v155, v155, v254
v_sub_f32 v244, v244, v254
v_sub_f32 v245, v245, v254
v_sub_f32 v246, v246, v254
v_sub_f32 v247, v247, v254
v_exp_f32 v254, -v254
s_nop 0
v_mul_f32 v242, v242, v254
v_mov_b32 v241, v255
s_nop 1
v_permlane16_swap_b32 v255, v241
v_max_f32 v255, v255, v241
v_mov_b32 v241, v255
s_nop 1
v_permlane32_swap_b32 v255, v241
v_max_f32 v255, v255, v241
v_max_f32 v255, 0, v255
v_sub_f32 v132, v132, v255
v_sub_f32 v133, v133, v255
v_sub_f32 v134, v134, v255
v_sub_f32 v135, v135, v255
v_sub_f32 v140, v140, v255
v_sub_f32 v141, v141, v255
v_sub_f32 v142, v142, v255
v_sub_f32 v143, v143, v255
v_sub_f32 v148, v148, v255
v_sub_f32 v149, v149, v255
v_sub_f32 v150, v150, v255
v_sub_f32 v151, v151, v255
v_sub_f32 v156, v156, v255
v_sub_f32 v157, v157, v255
v_sub_f32 v158, v158, v255
v_sub_f32 v159, v159, v255
v_sub_f32 v248, v248, v255
v_sub_f32 v249, v249, v255
v_sub_f32 v250, v250, v255
v_sub_f32 v251, v251, v255
v_exp_f32 v255, -v255
s_nop 0
v_mul_f32 v243, v243, v255
s_mov_b32 s69, 1
s_branch .LRAR15
.LRB15:
s_nop 7
v_mul_f32 v0, v0, v254
v_mul_f32 v1, v1, v254
v_mul_f32 v2, v2, v254
v_mul_f32 v3, v3, v254
v_mul_f32 v8, v8, v254
v_mul_f32 v9, v9, v254
v_mul_f32 v10, v10, v254
v_mul_f32 v11, v11, v254
v_mul_f32 v16, v16, v254
v_mul_f32 v17, v17, v254
v_mul_f32 v18, v18, v254
v_mul_f32 v19, v19, v254
v_mul_f32 v24, v24, v254
v_mul_f32 v25, v25, v254
v_mul_f32 v26, v26, v254
v_mul_f32 v27, v27, v254
v_mul_f32 v32, v32, v254
v_mul_f32 v33, v33, v254
v_mul_f32 v34, v34, v254
v_mul_f32 v35, v35, v254
v_mul_f32 v40, v40, v254
v_mul_f32 v41, v41, v254
v_mul_f32 v42, v42, v254
v_mul_f32 v43, v43, v254
v_mul_f32 v48, v48, v254
v_mul_f32 v49, v49, v254
v_mul_f32 v50, v50, v254
v_mul_f32 v51, v51, v254
v_mul_f32 v56, v56, v254
v_mul_f32 v57, v57, v254
v_mul_f32 v58, v58, v254
v_mul_f32 v59, v59, v254
v_mul_f32 v64, v64, v254
v_mul_f32 v65, v65, v254
v_mul_f32 v66, v66, v254
v_mul_f32 v67, v67, v254
v_mul_f32 v72, v72, v254
v_mul_f32 v73, v73, v254
v_mul_f32 v74, v74, v254
v_mul_f32 v75, v75, v254
v_mul_f32 v80, v80, v254
v_mul_f32 v81, v81, v254
v_mul_f32 v82, v82, v254
v_mul_f32 v83, v83, v254
v_mul_f32 v88, v88, v254
v_mul_f32 v89, v89, v254
v_mul_f32 v90, v90, v254
v_mul_f32 v91, v91, v254
v_mul_f32 v96, v96, v254
v_mul_f32 v97, v97, v254
v_mul_f32 v98, v98, v254
v_mul_f32 v99, v99, v254
v_mul_f32 v104, v104, v254
v_mul_f32 v105, v105, v254
v_mul_f32 v106, v106, v254
v_mul_f32 v107, v107, v254
v_mul_f32 v112, v112, v254
v_mul_f32 v113, v113, v254
v_mul_f32 v114, v114, v254
v_mul_f32 v115, v115, v254
v_mul_f32 v120, v120, v254
v_mul_f32 v121, v121, v254
v_mul_f32 v122, v122, v254
v_mul_f32 v123, v123, v254
v_mul_f32 v4, v4, v255
v_mul_f32 v5, v5, v255
v_mul_f32 v6, v6, v255
v_mul_f32 v7, v7, v255
v_mul_f32 v12, v12, v255
v_mul_f32 v13, v13, v255
v_mul_f32 v14, v14, v255
v_mul_f32 v15, v15, v255
v_mul_f32 v20, v20, v255
v_mul_f32 v21, v21, v255
v_mul_f32 v22, v22, v255
v_mul_f32 v23, v23, v255
v_mul_f32 v28, v28, v255
v_mul_f32 v29, v29, v255
v_mul_f32 v30, v30, v255
v_mul_f32 v31, v31, v255
v_mul_f32 v36, v36, v255
v_mul_f32 v37, v37, v255
v_mul_f32 v38, v38, v255
v_mul_f32 v39, v39, v255
v_mul_f32 v44, v44, v255
v_mul_f32 v45, v45, v255
v_mul_f32 v46, v46, v255
v_mul_f32 v47, v47, v255
v_mul_f32 v52, v52, v255
v_mul_f32 v53, v53, v255
v_mul_f32 v54, v54, v255
v_mul_f32 v55, v55, v255
v_mul_f32 v60, v60, v255
v_mul_f32 v61, v61, v255
v_mul_f32 v62, v62, v255
v_mul_f32 v63, v63, v255
v_mul_f32 v68, v68, v255
v_mul_f32 v69, v69, v255
v_mul_f32 v70, v70, v255
v_mul_f32 v71, v71, v255
v_mul_f32 v76, v76, v255
v_mul_f32 v77, v77, v255
v_mul_f32 v78, v78, v255
v_mul_f32 v79, v79, v255
v_mul_f32 v84, v84, v255
v_mul_f32 v85, v85, v255
v_mul_f32 v86, v86, v255
v_mul_f32 v87, v87, v255
v_mul_f32 v92, v92, v255
v_mul_f32 v93, v93, v255
v_mul_f32 v94, v94, v255
v_mul_f32 v95, v95, v255
v_mul_f32 v100, v100, v255
v_mul_f32 v101, v101, v255
v_mul_f32 v102, v102, v255
v_mul_f32 v103, v103, v255
v_mul_f32 v108, v108, v255
v_mul_f32 v109, v109, v255
v_mul_f32 v110, v110, v255
v_mul_f32 v111, v111, v255
v_mul_f32 v116, v116, v255
v_mul_f32 v117, v117, v255
v_mul_f32 v118, v118, v255
v_mul_f32 v119, v119, v255
v_mul_f32 v124, v124, v255
v_mul_f32 v125, v125, v255
v_mul_f32 v126, v126, v255
v_mul_f32 v127, v127, v255
s_mov_b32 s69, 0
s_nop 0
s_branch .LRBR15
.LPS12:
s_mov_b32 s78, 200000
.LPL12:
s_sleep 2
buffer_load_dwordx2 v[178:179], off, s[20:23], s53 sc1
s_waitcnt vmcnt(0)
v_cmp_ne_u64 vcc, s[48:49], v[178:179]
s_sub_u32 s78, s78, 1
s_cbranch_scc1 .LPX12
s_cbranch_vccnz .LPL12
.LPX12:
s_branch .LPR12
.LRA16:
v_mov_b32 v241, v254
s_nop 1
v_permlane16_swap_b32 v254, v241
v_max_f32 v254, v254, v241
v_mov_b32 v241, v254
s_nop 1
v_permlane32_swap_b32 v254, v241
v_max_f32 v254, v254, v241
v_max_f32 v254, 0, v254
v_sub_f32 v128, v128, v254
v_sub_f32 v129, v129, v254
v_sub_f32 v130, v130, v254
v_sub_f32 v131, v131, v254
v_sub_f32 v136, v136, v254
v_sub_f32 v137, v137, v254
v_sub_f32 v138, v138, v254
v_sub_f32 v139, v139, v254
v_sub_f32 v144, v144, v254
v_sub_f32 v145, v145, v254
v_sub_f32 v146, v146, v254
v_sub_f32 v147, v147, v254
v_sub_f32 v152, v152, v254
v_sub_f32 v153, v153, v254
v_sub_f32 v154, v154, v254
v_sub_f32 v155, v155, v254
v_sub_f32 v244, v244, v254
v_sub_f32 v245, v245, v254
v_sub_f32 v246, v246, v254
v_sub_f32 v247, v247, v254
v_exp_f32 v254, -v254
s_nop 0
v_mul_f32 v242, v242, v254
v_mov_b32 v241, v255
s_nop 1
v_permlane16_swap_b32 v255, v241
v_max_f32 v255, v255, v241
v_mov_b32 v241, v255
s_nop 1
v_permlane32_swap_b32 v255, v241
v_max_f32 v255, v255, v241
v_max_f32 v255, 0, v255
v_sub_f32 v132, v132, v255
v_sub_f32 v133, v133, v255
v_sub_f32 v134, v134, v255
v_sub_f32 v135, v135, v255
)A"
R"A(v_sub_f32 v140, v140, v255
v_sub_f32 v141, v141, v255
v_sub_f32 v142, v142, v255
v_sub_f32 v143, v143, v255
v_sub_f32 v148, v148, v255
v_sub_f32 v149, v149, v255
v_sub_f32 v150, v150, v255
v_sub_f32 v151, v151, v255
v_sub_f32 v156, v156, v255
v_sub_f32 v157, v157, v255
v_sub_f32 v158, v158, v255
v_sub_f32 v159, v159, v255
v_sub_f32 v248, v248, v255
v_sub_f32 v249, v249, v255
v_sub_f32 v250, v250, v255
v_sub_f32 v251, v251, v255
v_exp_f32 v255, -v255
s_nop 0
v_mul_f32 v243, v243, v255
s_mov_b32 s69, 1
s_branch .LRAR16
.LRB16:
s_nop 7
v_mul_f32 v0, v0, v254
v_mul_f32 v1, v1, v254
v_mul_f32 v2, v2, v254
v_mul_f32 v3, v3, v254
v_mul_f32 v8, v8, v254
v_mul_f32 v9, v9, v254
v_mul_f32 v10, v10, v254
v_mul_f32 v11, v11, v254
v_mul_f32 v16, v16, v254
v_mul_f32 v17, v17, v254
v_mul_f32 v18, v18, v254
v_mul_f32 v19, v19, v254
v_mul_f32 v24, v24, v254
v_mul_f32 v25, v25, v254
v_mul_f32 v26, v26, v254
v_mul_f32 v27, v27, v254
v_mul_f32 v32, v32, v254
v_mul_f32 v33, v33, v254
v_mul_f32 v34, v34, v254
v_mul_f32 v35, v35, v254
v_mul_f32 v40, v40, v254
v_mul_f32 v41, v41, v254
v_mul_f32 v42, v42, v254
v_mul_f32 v43, v43, v254
v_mul_f32 v48, v48, v254
v_mul_f32 v49, v49, v254
v_mul_f32 v50, v50, v254
v_mul_f32 v51, v51, v254
v_mul_f32 v56, v56, v254
v_mul_f32 v57, v57, v254
v_mul_f32 v58, v58, v254
v_mul_f32 v59, v59, v254
v_mul_f32 v64, v64, v254
v_mul_f32 v65, v65, v254
v_mul_f32 v66, v66, v254
v_mul_f32 v67, v67, v254
v_mul_f32 v72, v72, v254
v_mul_f32 v73, v73, v254
v_mul_f32 v74, v74, v254
v_mul_f32 v75, v75, v254
v_mul_f32 v80, v80, v254
v_mul_f32 v81, v81, v254
v_mul_f32 v82, v82, v254
v_mul_f32 v83, v83, v254
v_mul_f32 v88, v88, v254
v_mul_f32 v89, v89, v254
v_mul_f32 v90, v90, v254
v_mul_f32 v91, v91, v254
v_mul_f32 v96, v96, v254
v_mul_f32 v97, v97, v254
v_mul_f32 v98, v98, v254
v_mul_f32 v99, v99, v254
v_mul_f32 v104, v104, v254
v_mul_f32 v105, v105, v254
v_mul_f32 v106, v106, v254
v_mul_f32 v107, v107, v254
v_mul_f32 v112, v112, v254
v_mul_f32 v113, v113, v254
v_mul_f32 v114, v114, v254
v_mul_f32 v115, v115, v254
v_mul_f32 v120, v120, v254
v_mul_f32 v121, v121, v254
v_mul_f32 v122, v122, v254
v_mul_f32 v123, v123, v254
v_mul_f32 v4, v4, v255
v_mul_f32 v5, v5, v255
v_mul_f32 v6, v6, v255
v_mul_f32 v7, v7, v255
v_mul_f32 v12, v12, v255
v_mul_f32 v13, v13, v255
v_mul_f32 v14, v14, v255
v_mul_f32 v15, v15, v255
v_mul_f32 v20, v20, v255
v_mul_f32 v21, v21, v255
v_mul_f32 v22, v22, v255
v_mul_f32 v23, v23, v255
v_mul_f32 v28, v28, v255
v_mul_f32 v29, v29, v255
v_mul_f32 v30, v30, v255
v_mul_f32 v31, v31, v255
v_mul_f32 v36, v36, v255
v_mul_f32 v37, v37, v255
v_mul_f32 v38, v38, v255
v_mul_f32 v39, v39, v255
v_mul_f32 v44, v44, v255
v_mul_f32 v45, v45, v255
v_mul_f32 v46, v46, v255
v_mul_f32 v47, v47, v255
v_mul_f32 v52, v52, v255
v_mul_f32 v53, v53, v255
v_mul_f32 v54, v54, v255
v_mul_f32 v55, v55, v255
v_mul_f32 v60, v60, v255
v_mul_f32 v61, v61, v255
v_mul_f32 v62, v62, v255
v_mul_f32 v63, v63, v255
v_mul_f32 v68, v68, v255
v_mul_f32 v69, v69, v255
v_mul_f32 v70, v70, v255
v_mul_f32 v71, v71, v255
v_mul_f32 v76, v76, v255
v_mul_f32 v77, v77, v255
v_mul_f32 v78, v78, v255
v_mul_f32 v79, v79, v255
v_mul_f32 v84, v84, v255
v_mul_f32 v85, v85, v255
v_mul_f32 v86, v86, v255
v_mul_f32 v87, v87, v255
v_mul_f32 v92, v92, v255
v_mul_f32 v93, v93, v255
v_mul_f32 v94, v94, v255
v_mul_f32 v95, v95, v255
v_mul_f32 v100, v100, v255
v_mul_f32 v101, v101, v255
v_mul_f32 v102, v102, v255
v_mul_f32 v103, v103, v255
v_mul_f32 v108, v108, v255
v_mul_f32 v109, v109, v255
v_mul_f32 v110, v110, v255
v_mul_f32 v111, v111, v255
v_mul_f32 v116, v116, v255
v_mul_f32 v117, v117, v255
v_mul_f32 v118, v118, v255
v_mul_f32 v119, v119, v255
v_mul_f32 v124, v124, v255
v_mul_f32 v125, v125, v255
v_mul_f32 v126, v126, v255
v_mul_f32 v127, v127, v255
s_mov_b32 s69, 0
s_nop 0
s_branch .LRBR16
; merge with distant references
.LMSLOW:
v_min_f32 v136, v128, v132
v_sub_f32 v128, v136, v128
v_sub_f32 v132, v136, v132
v_exp_f32 v128, v128
v_exp_f32 v132, v132
s_nop 0
v_mul_f32 v242, v242, v128
v_fmac_f32 v242, v134, v132
v_min_f32 v136, v129, v133
v_sub_f32 v129, v136, v129
v_sub_f32 v133, v136, v133
v_exp_f32 v129, v129
v_exp_f32 v133, v133
s_nop 0
v_mul_f32 v243, v243, v129
v_fmac_f32 v243, v135, v133
s_waitcnt vmcnt(0)
v_accvgpr_read_b32 v141, a128
v_lshlrev_b32 v140, 16, v141
v_and_b32 v141, 0xffff0000, v141
v_mul_f32 v0, v0, v128
v_mul_f32 v1, v1, v128
v_fmac_f32 v0, v140, v132
v_fmac_f32 v1, v141, v132
PK 0,0,1
v_accvgpr_read_b32 v141, a129
v_lshlrev_b32 v140, 16, v141
v_and_b32 v141, 0xffff0000, v141
v_mul_f32 v2, v2, v128
v_mul_f32 v3, v3, v128
v_fmac_f32 v2, v140, v132
v_fmac_f32 v3, v141, v132
PK 1,2,3
v_accvgpr_read_b32 v141, a130
v_lshlrev_b32 v140, 16, v141
v_and_b32 v141, 0xffff0000, v141
v_mul_f32 v8, v8, v128
v_mul_f32 v9, v9, v128
v_fmac_f32 v8, v140, v132
v_fmac_f32 v9, v141, v132
PK 2,8,9
v_accvgpr_read_b32 v141, a131
v_lshlrev_b32 v140, 16, v141
v_and_b32 v141, 0xffff0000, v141
v_mul_f32 v10, v10, v128
v_mul_f32 v11, v11, v128
v_fmac_f32 v10, v140, v132
v_fmac_f32 v11, v141, v132
PK 3,10,11
v_accvgpr_read_b32 v141, a132
v_lshlrev_b32 v140, 16, v141
v_and_b32 v141, 0xffff0000, v141
v_mul_f32 v4, v4, v129
v_mul_f32 v5, v5, v129
v_fmac_f32 v4, v140, v133
v_fmac_f32 v5, v141, v133
PK 4,4,5
v_accvgpr_read_b32 v141, a133
v_lshlrev_b32 v140, 16, v141
v_and_b32 v141, 0xffff0000, v141
v_mul_f32 v6, v6, v129
v_mul_f32 v7, v7, v129
v_fmac_f32 v6, v140, v133
v_fmac_f32 v7, v141, v133
PK 5,6,7
v_accvgpr_read_b32 v141, a134
v_lshlrev_b32 v140, 16, v141
v_and_b32 v141, 0xffff0000, v141
v_mul_f32 v12, v12, v129
v_mul_f32 v13, v13, v129
v_fmac_f32 v12, v140, v133
v_fmac_f32 v13, v141, v133
PK 6,12,13
v_accvgpr_read_b32 v141, a135
v_lshlrev_b32 v140, 16, v141
v_and_b32 v141, 0xffff0000, v141
v_mul_f32 v14, v14, v129
v_mul_f32 v15, v15, v129
v_fmac_f32 v14, v140, v133
v_fmac_f32 v15, v141, v133
PK 7,14,15
v_accvgpr_read_b32 v141, a136
v_lshlrev_b32 v140, 16, v141
v_and_b32 v141, 0xffff0000, v141
v_mul_f32 v16, v16, v128
v_mul_f32 v17, v17, v128
v_fmac_f32 v16, v140, v132
v_fmac_f32 v17, v141, v132
PK 16,16,17
v_accvgpr_read_b32 v141, a137
v_lshlrev_b32 v140, 16, v141
v_and_b32 v141, 0xffff0000, v141
v_mul_f32 v18, v18, v128
v_mul_f32 v19, v19, v128
v_fmac_f32 v18, v140, v132
v_fmac_f32 v19, v141, v132
PK 17,18,19
v_accvgpr_read_b32 v141, a138
v_lshlrev_b32 v140, 16, v141
v_and_b32 v141, 0xffff0000, v141
v_mul_f32 v24, v24, v128
v_mul_f32 v25, v25, v128
v_fmac_f32 v24, v140, v132
v_fmac_f32 v25, v141, v132
PK 18,24,25
v_accvgpr_read_b32 v141, a139
v_lshlrev_b32 v140, 16, v141
v_and_b32 v141, 0xffff0000, v141
v_mul_f32 v26, v26, v128
v_mul_f32 v27, v27, v128
v_fmac_f32 v26, v140, v132
v_fmac_f32 v27, v141, v132
PK 19,26,27
v_accvgpr_read_b32 v141, a140
v_lshlrev_b32 v140, 16, v141
v_and_b32 v141, 0xffff0000, v141
v_mul_f32 v20, v20, v129
v_mul_f32 v21, v21, v129
v_fmac_f32 v20, v140, v133
v_fmac_f32 v21, v141, v133
PK 20,20,21
v_accvgpr_read_b32 v141, a141
v_lshlrev_b32 v140, 16, v141
v_and_b32 v141, 0xffff0000, v141
v_mul_f32 v22, v22, v129
v_mul_f32 v23, v23, v129
v_fmac_f32 v22, v140, v133
v_fmac_f32 v23, v141, v133
PK 21,22,23
v_accvgpr_read_b32 v141, a142
v_lshlrev_b32 v140, 16, v141
v_and_b32 v141, 0xffff0000, v141
v_mul_f32 v28, v28, v129
v_mul_f32 v29, v29, v129
v_fmac_f32 v28, v140, v133
v_fmac_f32 v29, v141, v133
PK 22,28,29
v_accvgpr_read_b32 v141, a143
v_lshlrev_b32 v140, 16, v141
v_and_b32 v141, 0xffff0000, v141
v_mul_f32 v30, v30, v129
v_mul_f32 v31, v31, v129
v_fmac_f32 v30, v140, v133
v_fmac_f32 v31, v141, v133
PK 23,30,31
v_accvgpr_read_b32 v141, a144
v_lshlrev_b32 v140, 16, v141
v_and_b32 v141, 0xffff0000, v141
v_mul_f32 v32, v32, v128
v_mul_f32 v33, v33, v128
v_fmac_f32 v32, v140, v132
v_fmac_f32 v33, v141, v132
PK 32,32,33
v_accvgpr_read_b32 v141, a145
v_lshlrev_b32 v140, 16, v141
v_and_b32 v141, 0xffff0000, v141
v_mul_f32 v34, v34, v128
v_mul_f32 v35, v35, v128
v_fmac_f32 v34, v140, v132
v_fmac_f32 v35, v141, v132
PK 33,34,35
v_accvgpr_read_b32 v141, a146
v_lshlrev_b32 v140, 16, v141
v_and_b32 v141, 0xffff0000, v141
v_mul_f32 v40, v40, v128
v_mul_f32 v41, v41, v128
v_fmac_f32 v40, v140, v132
v_fmac_f32 v41, v141, v132
PK 34,40,41
v_accvgpr_read_b32 v141, a147
v_lshlrev_b32 v140, 16, v141
v_and_b32 v141, 0xffff0000, v141
v_mul_f32 v42, v42, v128
v_mul_f32 v43, v43, v128
v_fmac_f32 v42, v140, v132
v_fmac_f32 v43, v141, v132
PK 35,42,43
v_accvgpr_read_b32 v141, a148
v_lshlrev_b32 v140, 16, v141
v_and_b32 v141, 0xffff0000, v141
v_mul_f32 v36, v36, v129
v_mul_f32 v37, v37, v129
v_fmac_f32 v36, v140, v133
v_fmac_f32 v37, v141, v133
PK 36,36,37
v_accvgpr_read_b32 v141, a149
v_lshlrev_b32 v140, 16, v141
v_and_b32 v141, 0xffff0000, v141
v_mul_f32 v38, v38, v129
v_mul_f32 v39, v39, v129
v_fmac_f32 v38, v140, v133
v_fmac_f32 v39, v141, v133
PK 37,38,39
v_accvgpr_read_b32 v141, a150
v_lshlrev_b32 v140, 16, v141
v_and_b32 v141, 0xffff0000, v141
v_mul_f32 v44, v44, v129
v_mul_f32 v45, v45, v129
v_fmac_f32 v44, v140, v133
v_fmac_f32 v45, v141, v133
PK 38,44,45
v_accvgpr_read_b32 v141, a151
v_lshlrev_b32 v140, 16, v141
v_and_b32 v141, 0xffff0000, v141
v_mul_f32 v46, v46, v129
v_mul_f32 v47, v47, v129
v_fmac_f32 v46, v140, v133
v_fmac_f32 v47, v141, v133
PK 39,46,47
v_accvgpr_read_b32 v141, a152
v_lshlrev_b32 v140, 16, v141
v_and_b32 v141, 0xffff0000, v141
v_mul_f32 v48, v48, v128
v_mul_f32 v49, v49, v128
v_fmac_f32 v48, v140, v132
v_fmac_f32 v49, v141, v132
PK 48,48,49
v_accvgpr_read_b32 v141, a153
v_lshlrev_b32 v140, 16, v141
v_and_b32 v141, 0xffff0000, v141
v_mul_f32 v50, v50, v128
v_mul_f32 v51, v51, v128
v_fmac_f32 v50, v140, v132
v_fmac_f32 v51, v141, v132
PK 49,50,51
v_accvgpr_read_b32 v141, a154
v_lshlrev_b32 v140, 16, v141
v_and_b32 v141, 0xffff0000, v141
v_mul_f32 v56, v56, v128
v_mul_f32 v57, v57, v128
v_fmac_f32 v56, v140, v132
v_fmac_f32 v57, v141, v132
PK 50,56,57
v_accvgpr_read_b32 v141, a155
v_lshlrev_b32 v140, 16, v141
v_and_b32 v141, 0xffff0000, v141
v_mul_f32 v58, v58, v128
v_mul_f32 v59, v59, v128
v_fmac_f32 v58, v140, v132
v_fmac_f32 v59, v141, v132
PK 51,58,59
v_accvgpr_read_b32 v141, a156
v_lshlrev_b32 v140, 16, v141
v_and_b32 v141, 0xffff0000, v141
v_mul_f32 v52, v52, v129
v_mul_f32 v53, v53, v129
v_fmac_f32 v52, v140, v133
v_fmac_f32 v53, v141, v133
PK 52,52,53
v_accvgpr_read_b32 v141, a157
v_lshlrev_b32 v140, 16, v141
v_and_b32 v141, 0xffff0000, v141
v_mul_f32 v54, v54, v129
v_mul_f32 v55, v55, v129
v_fmac_f32 v54, v140, v133
v_fmac_f32 v55, v141, v133
PK 53,54,55
v_accvgpr_read_b32 v141, a158
v_lshlrev_b32 v140, 16, v141
v_and_b32 v141, 0xffff0000, v141
v_mul_f32 v60, v60, v129
v_mul_f32 v61, v61, v129
v_fmac_f32 v60, v140, v133
v_fmac_f32 v61, v141, v133
PK 54,60,61
v_accvgpr_read_b32 v141, a159
v_lshlrev_b32 v140, 16, v141
v_and_b32 v141, 0xffff0000, v141
v_mul_f32 v62, v62, v129
v_mul_f32 v63, v63, v129
v_fmac_f32 v62, v140, v133
v_fmac_f32 v63, v141, v133
PK 55,62,63
v_accvgpr_read_b32 v141, a160
v_lshlrev_b32 v140, 16, v141
v_and_b32 v141, 0xffff0000, v141
v_mul_f32 v64, v64, v128
v_mul_f32 v65, v65, v128
v_fmac_f32 v64, v140, v132
v_fmac_f32 v65, v141, v132
PK 64,64,65
v_accvgpr_read_b32 v141, a161
v_lshlrev_b32 v140, 16, v141
v_and_b32 v141, 0xffff0000, v141
v_mul_f32 v66, v66, v128
v_mul_f32 v67, v67, v128
v_fmac_f32 v66, v140, v132
v_fmac_f32 v67, v141, v132
PK 65,66,67
v_accvgpr_read_b32 v141, a162
v_lshlrev_b32 v140, 16, v141
v_and_b32 v141, 0xffff0000, v141
v_mul_f32 v72, v72, v128
v_mul_f32 v73, v73, v128
v_fmac_f32 v72, v140, v132
v_fmac_f32 v73, v141, v132
PK 66,72,73
v_accvgpr_read_b32 v141, a163
v_lshlrev_b32 v140, 16, v141
v_and_b32 v141, 0xffff0000, v141
v_mul_f32 v74, v74, v128
v_mul_f32 v75, v75, v128
v_fmac_f32 v74, v140, v132
v_fmac_f32 v75, v141, v132
PK 67,74,75
v_accvgpr_read_b32 v141, a164
v_lshlrev_b32 v140, 16, v141
v_and_b32 v141, 0xffff0000, v141
v_mul_f32 v68, v68, v129
v_mul_f32 v69, v69, v129
v_fmac_f32 v68, v140, v133
v_fmac_f32 v69, v141, v133
PK 68,68,69
v_accvgpr_read_b32 v141, a165
v_lshlrev_b32 v140, 16, v141
v_and_b32 v141, 0xffff0000, v141
v_mul_f32 v70, v70, v129
)A"
R"A(v_mul_f32 v71, v71, v129
v_fmac_f32 v70, v140, v133
v_fmac_f32 v71, v141, v133
PK 69,70,71
v_accvgpr_read_b32 v141, a166
v_lshlrev_b32 v140, 16, v141
v_and_b32 v141, 0xffff0000, v141
v_mul_f32 v76, v76, v129
v_mul_f32 v77, v77, v129
v_fmac_f32 v76, v140, v133
v_fmac_f32 v77, v141, v133
PK 70,76,77
v_accvgpr_read_b32 v141, a167
v_lshlrev_b32 v140, 16, v141
v_and_b32 v141, 0xffff0000, v141
v_mul_f32 v78, v78, v129
v_mul_f32 v79, v79, v129
v_fmac_f32 v78, v140, v133
v_fmac_f32 v79, v141, v133
PK 71,78,79
v_accvgpr_read_b32 v141, a168
v_lshlrev_b32 v140, 16, v141
v_and_b32 v141, 0xffff0000, v141
v_mul_f32 v80, v80, v128
v_mul_f32 v81, v81, v128
v_fmac_f32 v80, v140, v132
v_fmac_f32 v81, v141, v132
PK 80,80,81
v_accvgpr_read_b32 v141, a169
v_lshlrev_b32 v140, 16, v141
v_and_b32 v141, 0xffff0000, v141
v_mul_f32 v82, v82, v128
v_mul_f32 v83, v83, v128
v_fmac_f32 v82, v140, v132
v_fmac_f32 v83, v141, v132
PK 81,82,83
v_accvgpr_read_b32 v141, a170
v_lshlrev_b32 v140, 16, v141
v_and_b32 v141, 0xffff0000, v141
v_mul_f32 v88, v88, v128
v_mul_f32 v89, v89, v128
v_fmac_f32 v88, v140, v132
v_fmac_f32 v89, v141, v132
PK 82,88,89
v_accvgpr_read_b32 v141, a171
v_lshlrev_b32 v140, 16, v141
v_and_b32 v141, 0xffff0000, v141
v_mul_f32 v90, v90, v128
v_mul_f32 v91, v91, v128
v_fmac_f32 v90, v140, v132
v_fmac_f32 v91, v141, v132
PK 83,90,91
v_accvgpr_read_b32 v141, a172
v_lshlrev_b32 v140, 16, v141
v_and_b32 v141, 0xffff0000, v141
v_mul_f32 v84, v84, v129
v_mul_f32 v85, v85, v129
v_fmac_f32 v84, v140, v133
v_fmac_f32 v85, v141, v133
PK 84,84,85
v_accvgpr_read_b32 v141, a173
v_lshlrev_b32 v140, 16, v141
v_and_b32 v141, 0xffff0000, v141
v_mul_f32 v86, v86, v129
v_mul_f32 v87, v87, v129
v_fmac_f32 v86, v140, v133
v_fmac_f32 v87, v141, v133
PK 85,86,87
v_accvgpr_read_b32 v141, a174
v_lshlrev_b32 v140, 16, v141
v_and_b32 v141, 0xffff0000, v141
v_mul_f32 v92, v92, v129
v_mul_f32 v93, v93, v129
v_fmac_f32 v92, v140, v133
v_fmac_f32 v93, v141, v133
PK 86,92,93
v_accvgpr_read_b32 v141, a175
v_lshlrev_b32 v140, 16, v141
v_and_b32 v141, 0xffff0000, v141
v_mul_f32 v94, v94, v129
v_mul_f32 v95, v95, v129
v_fmac_f32 v94, v140, v133
v_fmac_f32 v95, v141, v133
PK 87,94,95
v_accvgpr_read_b32 v141, a176
v_lshlrev_b32 v140, 16, v141
v_and_b32 v141, 0xffff0000, v141
v_mul_f32 v96, v96, v128
v_mul_f32 v97, v97, v128
v_fmac_f32 v96, v140, v132
v_fmac_f32 v97, v141, v132
PK 96,96,97
v_accvgpr_read_b32 v141, a177
v_lshlrev_b32 v140, 16, v141
v_and_b32 v141, 0xffff0000, v141
v_mul_f32 v98, v98, v128
v_mul_f32 v99, v99, v128
v_fmac_f32 v98, v140, v132
v_fmac_f32 v99, v141, v132
PK 97,98,99
v_accvgpr_read_b32 v141, a178
v_lshlrev_b32 v140, 16, v141
v_and_b32 v141, 0xffff0000, v141
v_mul_f32 v104, v104, v128
v_mul_f32 v105, v105, v128
v_fmac_f32 v104, v140, v132
v_fmac_f32 v105, v141, v132
PK 98,104,105
v_accvgpr_read_b32 v141, a179
v_lshlrev_b32 v140, 16, v141
v_and_b32 v141, 0xffff0000, v141
v_mul_f32 v106, v106, v128
v_mul_f32 v107, v107, v128
v_fmac_f32 v106, v140, v132
v_fmac_f32 v107, v141, v132
PK 99,106,107
v_accvgpr_read_b32 v141, a180
v_lshlrev_b32 v140, 16, v141
v_and_b32 v141, 0xffff0000, v141
v_mul_f32 v100, v100, v129
v_mul_f32 v101, v101, v129
v_fmac_f32 v100, v140, v133
v_fmac_f32 v101, v141, v133
PK 100,100,101
v_accvgpr_read_b32 v141, a181
v_lshlrev_b32 v140, 16, v141
v_and_b32 v141, 0xffff0000, v141
v_mul_f32 v102, v102, v129
v_mul_f32 v103, v103, v129
v_fmac_f32 v102, v140, v133
v_fmac_f32 v103, v141, v133
PK 101,102,103
v_accvgpr_read_b32 v141, a182
v_lshlrev_b32 v140, 16, v141
v_and_b32 v141, 0xffff0000, v141
v_mul_f32 v108, v108, v129
v_mul_f32 v109, v109, v129
v_fmac_f32 v108, v140, v133
v_fmac_f32 v109, v141, v133
PK 102,108,109
v_accvgpr_read_b32 v141, a183
v_lshlrev_b32 v140, 16, v141
v_and_b32 v141, 0xffff0000, v141
v_mul_f32 v110, v110, v129
v_mul_f32 v111, v111, v129
v_fmac_f32 v110, v140, v133
v_fmac_f32 v111, v141, v133
PK 103,110,111
v_accvgpr_read_b32 v141, a184
v_lshlrev_b32 v140, 16, v141
v_and_b32 v141, 0xffff0000, v141
v_mul_f32 v112, v112, v128
v_mul_f32 v113, v113, v128
v_fmac_f32 v112, v140, v132
v_fmac_f32 v113, v141, v132
PK 112,112,113
v_accvgpr_read_b32 v141, a185
v_lshlrev_b32 v140, 16, v141
v_and_b32 v141, 0xffff0000, v141
v_mul_f32 v114, v114, v128
v_mul_f32 v115, v115, v128
v_fmac_f32 v114, v140, v132
v_fmac_f32 v115, v141, v132
PK 113,114,115
v_accvgpr_read_b32 v141, a186
v_lshlrev_b32 v140, 16, v141
v_and_b32 v141, 0xffff0000, v141
v_mul_f32 v120, v120, v128
v_mul_f32 v121, v121, v128
v_fmac_f32 v120, v140, v132
v_fmac_f32 v121, v141, v132
PK 114,120,121
v_accvgpr_read_b32 v141, a187
v_lshlrev_b32 v140, 16, v141
v_and_b32 v141, 0xffff0000, v141
v_mul_f32 v122, v122, v128
v_mul_f32 v123, v123, v128
v_fmac_f32 v122, v140, v132
v_fmac_f32 v123, v141, v132
PK 115,122,123
v_accvgpr_read_b32 v141, a188
v_lshlrev_b32 v140, 16, v141
v_and_b32 v141, 0xffff0000, v141
v_mul_f32 v116, v116, v129
v_mul_f32 v117, v117, v129
v_fmac_f32 v116, v140, v133
v_fmac_f32 v117, v141, v133
PK 116,116,117
v_accvgpr_read_b32 v141, a189
v_lshlrev_b32 v140, 16, v141
v_and_b32 v141, 0xffff0000, v141
v_mul_f32 v118, v118, v129
v_mul_f32 v119, v119, v129
v_fmac_f32 v118, v140, v133
v_fmac_f32 v119, v141, v133
PK 117,118,119
v_accvgpr_read_b32 v141, a190
v_lshlrev_b32 v140, 16, v141
v_and_b32 v141, 0xffff0000, v141
v_mul_f32 v124, v124, v129
v_mul_f32 v125, v125, v129
v_fmac_f32 v124, v140, v133
v_fmac_f32 v125, v141, v133
PK 118,124,125
v_accvgpr_read_b32 v141, a191
v_lshlrev_b32 v140, 16, v141
v_and_b32 v141, 0xffff0000, v141
v_mul_f32 v126, v126, v129
v_mul_f32 v127, v127, v129
v_fmac_f32 v126, v140, v133
v_fmac_f32 v127, v141, v133
PK 119,126,127
s_branch .LDWO
.LCEND:
.purgem MA
.purgem MB
.purgem MZ
.purgem TR
.purgem RD
.purgem WL
.purgem PK
)A"
  :: "s"(__builtin_amdgcn_kernarg_segment_ptr()), "s"(blockIdx.x), "s"(wid), "s"((unsigned)(size_t)lds) : "v0","v1","v2","v3","v4","v5","v6","v7","v8","v9","v10","v11","v12","v13","v14","v15","v16","v17","v18","v19","v20","v21","v22","v23","v24","v25","v26","v27","v28","v29","v30","v31","v32","v33","v34","v35","v36","v37","v38","v39","v40","v41","v42","v43","v44","v45","v46","v47","v48","v49","v50","v51","v52","v53","v54","v55","v56","v57","v58","v59","v60","v61","v62","v63","v64","v65","v66","v67","v68","v69","v70","v71","v72","v73","v74","v75","v76","v77","v78","v79","v80","v81","v82","v83","v84","v85","v86","v87","v88","v89","v90","v91","v92","v93","v94","v95","v96","v97","v98","v99","v100","v101","v102","v103","v104","v105","v106","v107","v108","v109","v110","v111","v112","v113","v114","v115","v116","v117","v118","v119","v120","v121","v122","v123","v124","v125","v126","v127","v128","v129","v130","v131","v132","v133","v134","v135","v136","v137","v138","v139","v140","v141","v142","v143","v144","v145","v146","v147","v148","v149","v150","v151","v152","v153","v154","v155","v156","v157","v158","v159","v160","v161","v162","v163","v164","v165","v166","v167","v168","v169","v170","v171","v172","v173","v174","v175","v176","v177","v178","v179","v180","v181","v182","v183","v184","v185","v186","v187","v188","v189","v190","v191","v192","v193","v194","v195","v196","v197","v198","v199","v200","v201","v202","v203","v204","v205","v206","v207","v208","v209","v210","v211","v212","v213","v214","v215","v216","v217","v218","v219","v220","v221","v222","v223","v224","v225","v226","v227","v228","v229","v230","v231","v232","v233","v234","v235","v236","v237","v238","v239","v240","v241","v242","v243","v244","v245","v246","v247","v248","v249","v250","v251","v252","v253","v254","v255","a0","a1","a2","a3","a4","a5","a6","a7","a8","a9","a10","a11","a12","a13","a14","a15","a16","a17","a18","a19","a20","a21","a22","a23","a24","a25","a26","a27","a28","a29","a30","a31","a32","a33","a34","a35","a36","a37","a38","a39","a40","a41","a42","a43","a44","a45","a46","a47","a48","a49","a50","a51","a52","a53","a54","a55","a56","a57","a58","a59","a60","a61","a62","a63","a64","a65","a66","a67","a68","a69","a70","a71","a72","a73","a74","a75","a76","a77","a78","a79","a80","a81","a82","a83","a84","a85","a86","a87","a88","a89","a90","a91","a92","a93","a94","a95","a96","a97","a98","a99","a100","a101","a102","a103","a104","a105","a106","a107","a108","a109","a110","a111","a112","a113","a114","a115","a116","a117","a118","a119","a120","a121","a122","a123","a124","a125","a126","a127","a128","a129","a130","a131","a132","a133","a134","a135","a136","a137","a138","a139","a140","a141","a142","a143","a144","a145","a146","a147","a148","a149","a150","a151","a152","a153","a154","a155","a156","a157","a158","a159","a160","a161","a162","a163","a164","a165","a166","a167","a168","a169","a170","a171","a172","a173","a174","a175","a176","a177","a178","a179","a180","a181","a182","a183","a184","a185","a186","a187","a188","a189","a190","a191","a192","a193","a194","a195","a196","a197","a198","a199","a200","a201","a202","a203","a204","a205","a206","a207","a208","a209","a210","a211","a212","a213","a214","a215","a216","a217","a218","a219","a220","a221","a222","a223","a224","a225","a226","a227","a228","a229","a230","a231","a232","a233","a234","a235","a236","a237","a238","a239","a240","a241","a242","a243","a244","a245","a246","a247","a248","a249","a250","a251","a252","a253","a254","a255","s5","s6","s7","s8","s9","s10","s11","s12","s13","s14","s15","s16","s17","s18","s19","s20","s21","s22","s23","s24","s25","s26","s27","s28","s29","s30","s31","s32","s33","s34","s35","s36","s37","s38","s39","s40","s41","s42","s43","s44","s45","s46","s47","s48","s49","s50","s51","s52","s53","s54","s55","s56","s57","s58","s59","s60","s61","s62","s63","s64","s65","s66","s67","s68","s69","s70","s71","s72","s73","s74","s75","s76","s77","s78","s79","s80","s81","s82","s83","s84","s85","s86","s87","s88","s89","s90","s91","s92","s93","s94","s95","s96","s97","s98","s99","s100","s101","vcc","scc","memory");
}
extern "C" void kernel_launch(void* const* d_in, const int* in_sizes, int n_in, void* d_out, int out_size, void* d_ws, size_t ws_size, hipStream_t stream) {
  MkArgs a;
  a.x = d_in[0]; a.ckv = d_in[1]; a.wq = d_in[2]; a.wuk = d_in[3]; a.wuv = d_in[4]; a.wo = d_in[5];
  a.w1 = d_in[7]; a.b1 = d_in[8]; a.w2 = d_in[9]; a.b2 = d_in[10]; a.out = d_out; a.ws = d_ws;
  mk_kernel<<<256, 256, 0, stream>>>(a);
}
```
